```python
import torch

def _softmax(x, axis=-1):
    return torch.softmax(x, dim=axis)
def _gelu(x):
    return torch.nn.functional.gelu(x, approximate="tanh")
def _silu(x):
    return torch.nn.functional.silu(x)
def _sigmoid(x):
    return torch.sigmoid(x)
def _layernorm(x, axis=-1, eps=1e-5):
    m = x.mean(dim=axis, keepdim=True)
    v = x.var(dim=axis, keepdim=True, unbiased=False)
    return (x - m) / torch.sqrt(v + eps)
def _scatter_add(tgt, idx, src, axis=0):
    return tgt.clone().index_add_(axis, idx, src)
def _segment_sum(src, seg, n):
    out = torch.zeros((n,) + src.shape[1:], dtype=src.dtype)
    return out.index_add_(0, seg, src)
def _pad(x, pw):
    flat = tuple(v for pair in reversed(pw) for v in pair)
    return torch.nn.functional.pad(x, flat)

kernel_name = 'standalone__mixer_s_mlp__sampling'

def setup_inputs():

    _w = (lambda a: (lambda t: t.to(torch.bfloat16) if t.is_floating_point() else t)(torch.as_tensor(a)))
    B, S, D, F = 32, 32, 576, 4608
    return {
        "x":  _w((lambda *s: torch.randn(*s, dtype=torch.bfloat16) * 0.4)(B, S, D)),
        "w1": _w((lambda *s: torch.randn(*s, dtype=torch.bfloat16) * 3.2)(D, D) * (D ** -0.5)),
        "w2": _w((lambda *s: torch.randn(*s, dtype=torch.bfloat16) * 3.2)(D, D) * (D ** -0.5)),
        "w3": _w((lambda *s: torch.randn(*s, dtype=torch.bfloat16) * 3.2)(D, F) * (D ** -0.5)),
        "w4": _w((lambda *s: torch.randn(*s, dtype=torch.bfloat16) * 0.8)(F, D) * (F ** -0.5)),
    }

def reference(x, w1, w2, w3, w4):
    B, S, D = x.shape
    h = _gelu(_layernorm(x, axis=-1) @ w1)
    c = D // 4
    g0 = _pad(h[:, :, 0*c:1*c], ((0, 0), (1, 0), (0, 0)))[:, :S, :]
    g1 = _pad(h[:, :, 1*c:2*c], ((0, 0), (0, 1), (0, 0)))[:, 1:, :]
    g2 = _pad(h[:, :, 2*c:3*c], ((0, 0), (2, 0), (0, 0)))[:, :S, :]
    g3 = _pad(h[:, :, 3*c:   ], ((0, 0), (0, 2), (0, 0)))[:, 2:, :]
    hs = torch.concatenate([g0, g1, g2, g3], axis=-1) @ w2
    x1 = x + hs
    h2 = _gelu(_layernorm(x1, axis=-1) @ w3) @ w4
    return x1 + h2
```

```cpp
#include <hip/hip_runtime.h>
#include <hip/hip_bf16.h>
#include <stdint.h>

#define LAS __attribute__((address_space(3)))
typedef unsigned v4u __attribute__((ext_vector_type(4)));
typedef unsigned v2u __attribute__((ext_vector_type(2)));
typedef float f32x4 __attribute__((ext_vector_type(4)));
typedef short bf16x8 __attribute__((ext_vector_type(8)));
typedef short s16x4 __attribute__((ext_vector_type(4)));

constexpr int M_ = 1024, D_ = 576, F_ = 4608;

__device__ __forceinline__ float bf2f(unsigned short b) { return __builtin_bit_cast(float, (unsigned)b << 16); }
__device__ __forceinline__ unsigned short f2bf(float f) { __hip_bfloat16 h = __float2bfloat16(f); return __builtin_bit_cast(unsigned short, h); }
__device__ __forceinline__ float bfr(float f) { return bf2f(f2bf(f)); }
__device__ __forceinline__ float gelu_t(float x) {
  float u = x * (0.7978845608028654f + 0.035677408136300125f * x * x);
  float e = __builtin_amdgcn_exp2f(-2.885390081777927f * u);
  return x * __builtin_amdgcn_rcpf(1.0f + e);
}

__global__ void k_ln(const unsigned short* __restrict__ x, unsigned short* __restrict__ y) {
  int row = blockIdx.x * 4 + (threadIdx.x >> 6), lane = threadIdx.x & 63;
  float v[9]; float s = 0.f;
  for (int i = 0; i < 9; ++i) { v[i] = bf2f(x[row * D_ + i * 64 + lane]); s += v[i]; }
  for (int o = 32; o; o >>= 1) s += __shfl_xor(s, o);
  float m = s / D_; float q = 0.f;
  for (int i = 0; i < 9; ++i) { float d = v[i] - m; q += d * d; }
  for (int o = 32; o; o >>= 1) q += __shfl_xor(q, o);
  float mr = bfr(m), vr = bfr(q / D_);
  float sd = bfr(sqrtf(bfr(vr + 1e-5f)));
  for (int i = 0; i < 9; ++i) y[row * D_ + i * 64 + lane] = f2bf(bfr(v[i] - mr) / sd);
}

template <int K, int N, int MODE>
__global__ __launch_bounds__(256) void k_gemm(const unsigned short* __restrict__ A, const unsigned short* __restrict__ W,
                                              const unsigned short* __restrict__ R, unsigned short* __restrict__ C) {
  __shared__ __attribute__((aligned(16))) char wl[64 * 96];
  const int tid = threadIdx.x, lane = tid & 63, w = tid >> 6;
  const int nt = blockIdx.x % (N / 48), mt = blockIdx.x / (N / 48);
  const int t = lane & 15, q = lane >> 4;
  const int tok = mt * 64 + w * 16 + t;
  f32x4 acc[3] = {{0, 0, 0, 0}, {0, 0, 0, 0}, {0, 0, 0, 0}};
  for (int k0 = 0; k0 < K; k0 += 64) {
    __syncthreads();
    for (int i = tid; i < 64 * 6; i += 256) {
      int r = i / 6, c = i % 6;
      *(v4u*)(wl + r * 96 + c * 16) = *(const v4u*)(W + (size_t)(k0 + r) * N + nt * 48 + c * 8);
    }
    __syncthreads();
#pragma unroll
    for (int s = 0; s < 2; ++s) {
      v4u xb = *(const v4u*)(A + (size_t)tok * K + k0 + 32 * s + 8 * q);
#pragma unroll
      for (int c = 0; c < 3; ++c) {
        const int qq = t >> 2, p = t & 3;
        LAS char* b = (LAS char*)wl;
        s16x4 lo = __builtin_amdgcn_ds_read_tr16_b64_v4i16((LAS s16x4*)(b + (32 * s + 8 * q + qq) * 96 + c * 32 + p * 8));
        s16x4 hi = __builtin_amdgcn_ds_read_tr16_b64_v4i16((LAS s16x4*)(b + (32 * s + 8 * q + 4 + qq) * 96 + c * 32 + p * 8));
        bf16x8 a = __builtin_shufflevector(lo, hi, 0, 1, 2, 3, 4, 5, 6, 7);
        acc[c] = __builtin_amdgcn_mfma_f32_16x16x32_bf16(a, __builtin_bit_cast(bf16x8, xb), acc[c], 0, 0, 0);
      }
    }
  }
#pragma unroll
  for (int c = 0; c < 3; ++c) {
    int col = nt * 48 + c * 16 + q * 4;
    float v[4];
    int dtok = tok;
    bool zero = false;
    if (MODE == 0) {
      int g = col / 144;
      int sh = g == 0 ? 1 : g == 1 ? -1 : g == 2 ? 2 : -2;
      int s = (tok & 31) + sh;
      zero = s < 0 || s >= 32;
      dtok = (tok & ~31) | (s & 31);
    }
#pragma unroll
    for (int j = 0; j < 4; ++j) {
      float a = bfr(acc[c][j]);
      if (MODE == 1) v[j] = bf2f(R[(size_t)tok * N + col + j]) + a;
      else v[j] = zero ? 0.f : gelu_t(a);
    }
    v2u o;
    o.x = f2bf(v[0]) | ((unsigned)f2bf(v[1]) << 16);
    o.y = f2bf(v[2]) | ((unsigned)f2bf(v[3]) << 16);
    *(v2u*)(C + (size_t)dtok * N + col) = o;
  }
}

extern "C" void kernel_launch(void* const* d_in, const int* in_sizes, int n_in, void* d_out, int out_size, void* d_ws,
                              size_t ws_size, hipStream_t stream) {
  const unsigned short* x = (const unsigned short*)d_in[0];
  const unsigned short* w1 = (const unsigned short*)d_in[1];
  const unsigned short* w2 = (const unsigned short*)d_in[2];
  const unsigned short* w3 = (const unsigned short*)d_in[3];
  const unsigned short* w4 = (const unsigned short*)d_in[4];
  unsigned short* out = (unsigned short*)d_out;
  unsigned short* ln = (unsigned short*)d_ws;
  unsigned short* h = ln + M_ * D_;
  unsigned short* x1 = h + M_ * D_;
  unsigned short* H = x1 + M_ * D_;
  k_ln<<<256, 256, 0, stream>>>(x, ln);
  k_gemm<D_, D_, 0><<<16 * 12, 256, 0, stream>>>(ln, w1, nullptr, h);
  k_gemm<D_, D_, 1><<<16 * 12, 256, 0, stream>>>(h, w2, x, x1);
  k_ln<<<256, 256, 0, stream>>>(x1, ln);
  k_gemm<D_, F_, 2><<<16 * 96, 256, 0, stream>>>(ln, w3, nullptr, H);
  k_gemm<F_, D_, 1><<<16 * 12, 256, 0, stream>>>(H, w4, x1, out);
}
```

```cpp
#include <hip/hip_runtime.h>
#include <utility>

#define STAMPS 0

#define LAS __attribute__((address_space(3)))
#define GAS __attribute__((address_space(1)))
#define DI __device__ __forceinline__
typedef unsigned v4u __attribute__((ext_vector_type(4)));
typedef unsigned v2u __attribute__((ext_vector_type(2)));
typedef float f32x4 __attribute__((ext_vector_type(4)));
typedef float f32x2 __attribute__((ext_vector_type(2)));
typedef short bf16x8 __attribute__((ext_vector_type(8)));
typedef short s16x4 __attribute__((ext_vector_type(4)));
typedef __bf16 bf16x2_t __attribute__((ext_vector_type(2)));
typedef unsigned long long u64;

namespace mx {
constexpr int D_ = 576, F_ = 4608;
constexpr int OFF_EPOCH = 0;
constexpr int OFF_FLAGS = 4096;
constexpr int OFF_STAT = 262144;
constexpr int OFF_H1 = 524288;
constexpr int OFF_X1 = OFF_H1 + 64 * 18 * 1024;
constexpr int OFF_HH = OFF_X1 + 64 * 18 * 1024;
constexpr int OFF_P = OFF_HH + 64 * 144 * 1024;
constexpr int OFF_DBG = OFF_P + 8 * 4 * 8 * 8 * 5 * 1024;
constexpr int WS_BYTES = OFF_DBG + 256 * 8 * 32 * 8;
enum { FS = 0, FA = 1, FB = 2, FC1 = 3, FC2 = 4, FC3 = 5, FD = 6 };
constexpr int SLOT = 9216, MISC = 16 * SLOT;
constexpr int L_FULL = MISC, L_FREE = MISC + 16, L_RDY = MISC + 32  , L_CNT = MISC + 64  , L_TAG = MISC + 96;
constexpr int L_CS1 = MISC + 256, L_CS3 = MISC + 512, L_DBG = MISC + 2048;
enum { R_H = 0, R_X0 = 1, R_X1 = 2, R_H1 = 3, R_H2 = 4, R_H3 = 5, R_P = 6, R_TAG = 7 };
constexpr unsigned LDS_CAP = 1u << 21, GLB_CAP = 1u << 16;
constexpr int PD = 4;

template <class F, int... I> DI void sfor_impl(F&& f, std::integer_sequence<int, I...>) { (f(std::integral_constant<int, I>{}), ...); }
template <int N, class F> DI void sfor(F&& f) { sfor_impl(f, std::make_integer_sequence<int, N>{}); }
#define INL __attribute__((always_inline))
#define SB() __builtin_amdgcn_sched_barrier(0)
#define CLOB() asm volatile("" ::: "memory")
#define DRAIN() asm volatile("s_waitcnt vmcnt(0)" ::: "memory")
#define VMW(n) asm volatile("s_waitcnt vmcnt(%0)" ::"i"(n) : "memory")

DI unsigned rfl(unsigned v) { return __builtin_amdgcn_readfirstlane(v); }
DI f32x4 mfma(bf16x8 a, bf16x8 b, f32x4 c) { return __builtin_amdgcn_mfma_f32_16x16x32_bf16(a, b, c, 0, 0, 0); }
DI bf16x8 asfrag(v4u v) { return __builtin_bit_cast(bf16x8, v); }
DI s16x4 trd(unsigned addr) { return __builtin_amdgcn_ds_read_tr16_b64_v4i16((LAS s16x4*)addr); }
DI bf16x8 cat(s16x4 lo, s16x4 hi) { return __builtin_shufflevector(lo, hi, 0, 1, 2, 3, 4, 5, 6, 7); }
DI unsigned pk(float a, float b) { bf16x2_t r = __builtin_convertvector(f32x2{a, b}, bf16x2_t); return __builtin_bit_cast(unsigned, r); }
DI float lo16(unsigned p) { return __uint_as_float(p << 16); }
DI float hi16(unsigned p) { return __uint_as_float(p & 0xffff0000u); }
DI float bfr(float a) { return lo16(pk(a, a)); }
DI float qsum(float v) {
  auto r = __builtin_amdgcn_permlane16_swap(__float_as_uint(v), __float_as_uint(v), false, false);
  float a = __uint_as_float(r[0]) + __uint_as_float(r[1]);
  auto r2 = __builtin_amdgcn_permlane32_swap(__float_as_uint(a), __float_as_uint(a), false, false);
  return __uint_as_float(r2[0]) + __uint_as_float(r2[1]);
}
DI void ln_stats(float s, float ss, float& mean, float& rstd) {
  const float m = s * (1.0f / D_);
  float var = __builtin_fmaf(-m, m, ss * (1.0f / D_));
  var = var > 0.f ? var : 0.f;
  mean = bfr(m);
  const float sd = bfr(__builtin_sqrtf(bfr(bfr(var) + 1e-5f)));
  rstd = 1.0f / sd;
}
constexpr float GA = -2.302208198144325f;
constexpr float GB = -0.1029432397357593f;
DI f32x2 gelu2(f32x2 z, float a, float b) {
  const f32x2 p = z * z * b + a;
  const f32x2 arg = z * p;
  f32x2 d;
  d.x = __builtin_amdgcn_exp2f(arg.x); d.y = __builtin_amdgcn_exp2f(arg.y);
  d = d + 1.0f;
  f32x2 rc; rc.x = __builtin_amdgcn_rcpf(d.x); rc.y = __builtin_amdgcn_rcpf(d.y);
  return z * rc;
}

DI unsigned ldsw(int addr) { return *(volatile LAS unsigned*)addr; }
DI v4u lds4(int addr) { return *(volatile LAS v4u*)addr; }
DI void lds_set(int addr, unsigned v, int lane) { if (lane == 0) *(volatile LAS unsigned*)addr = v; }
DI void wait_word(int addr, unsigned v) {
  for (unsigned n = 0; n < LDS_CAP; ++n) { if (rfl(ldsw(addr)) >= v) break; __builtin_amdgcn_s_sleep(1); }
  CLOB();
}
DI unsigned min3(v4u x) { return rfl(min(min(x.x, x.y), x.z)); }
DI unsigned min4(v4u x) { return rfl(min(min(x.x, x.y), min(x.z, x.w))); }
DI void wait_full(unsigned v) {
  for (unsigned n = 0; n < LDS_CAP; ++n) { if (min3(lds4(L_FULL)) >= v) break; __builtin_amdgcn_s_sleep(1); }
  CLOB();
}
DI void wait_free(unsigned v) {
  for (unsigned n = 0; n < LDS_CAP; ++n) { if (min4(lds4(L_FREE)) >= v) break; __builtin_amdgcn_s_sleep(1); }
  CLOB();
}
DI GAS u64* flagp(char* ws, int kind, int b) { return (GAS u64*)(ws + OFF_FLAGS + (kind * 256 + b) * 128); }
DI void arrive(char* ws, int kind, int b, int lane) {
  if (lane == 0) {
    const unsigned old = __hip_atomic_fetch_add((LAS unsigned*)(L_CNT + 4 * kind), 1u, __ATOMIC_RELAXED, __HIP_MEMORY_SCOPE_WORKGROUP);
    if (old == 3u) {
      for (unsigned n = 0; n < LDS_CAP; ++n) { if (ldsw(L_RDY + 4 * R_TAG) != 0u) break; __builtin_amdgcn_s_sleep(1); }
      const u64 tag = *(volatile LAS u64*)L_TAG;
      __hip_atomic_store(flagp(ws, kind, b), tag, __ATOMIC_RELAXED, __HIP_MEMORY_SCOPE_AGENT);
    }
  }
}
#define LD16(off, soff) __builtin_amdgcn_raw_buffer_load_b128(rs, (off), (soff), 16)
#define LD8(off, soff) __builtin_amdgcn_raw_buffer_load_b64(rs, (off), (soff), 16)
#define ST16(v, off, soff) __builtin_amdgcn_raw_buffer_store_b128((v), rs, (off), (soff), 16)
#define ST8(v, off, soff) __builtin_amdgcn_raw_buffer_store_b64((v), rs, (off), (soff), 16)
DI void dma(const char* sbase, unsigned voff, unsigned ldsaddr) {
  asm volatile("s_mov_b32 m0, %0\n\ts_nop 0\n\tglobal_load_lds_dwordx4 %1, %2" ::"s"(ldsaddr), "v"(voff), "s"(sbase) : "memory");
}
DI int pbase(int s) { return (9 * s + (s & 1)) >> 1; }
template <int M> DI int dstep(int s) {
  if constexpr (M < 4) return pbase(s) + M;
  else if constexpr (M < 10) return pbase((s + 1 + (M - 4) / 2) & 3) + ((M - 4) & 1);
  else if constexpr (M < 13) return pbase((s + M - 9) & 3) + 2;
  else if constexpr (M < 16) return pbase((s + M - 12) & 3) + 3;
  else return M == 16 ? 4 : 13;
}
#define STAMP(k) do { } while (0)

__global__ __launch_bounds__(512, 2) void mixer_kernel(const char* __restrict__ x, const char* __restrict__ w1, const char* __restrict__ w2,
                                                       const char* __restrict__ w3, const char* __restrict__ w4, char* __restrict__ out, char* ws) {
#if defined(__HIP_DEVICE_COMPILE__)
  __shared__ __attribute__((aligned(16))) char smem[163840];
  const int tid = threadIdx.x, lane = tid & 63, w = rfl(tid >> 6);
  const int t = lane & 15, q = lane >> 4;
  const int b = blockIdx.x, j = b & 7, r = b >> 3;
  const bool ab = r < 24;
  const int rt = 2 * j + (r & 1), ct = r >> 1;
  const int g = r >> 2, s = r & 3;
  const __amdgpu_buffer_rsrc_t rs = __builtin_amdgcn_make_buffer_rsrc(ws, 0, WS_BYTES, 0x00020000);
  if (tid < 32) *(volatile LAS unsigned*)(MISC + tid * 4) = 0u;
  asm volatile("" ::"v"(smem));
  STAMP(0);
  asm volatile("s_waitcnt lgkmcnt(0)\n\ts_barrier" ::: "memory");

  if (w < 4) {
    const int i = w;
    if (ab) {
      unsigned ba = (4 * q + (t >> 2)) * 96 + (t & 3) * 8;
      const int T = 4 * rt + i, odd = ct & 1;
      const int upair = (3 * ct + odd) >> 1, usingle = odd ? (3 * ct - 1) >> 1 : (3 * ct + 2) >> 1;
      v2u xres[3];
      {
        const char* xrow = x + (size_t)(16 * T + t) * (D_ * 2);
        bf16x8 xf[18];
#pragma unroll
        for (int u = 0; u < 18; ++u) xf[u] = asfrag(*(const GAS v4u*)(xrow + u * 64 + q * 16));
#pragma unroll
        for (int l = 0; l < 3; ++l) xres[l] = *(const GAS v2u*)(xrow + (48 * ct + 16 * ((l + odd) % 3) + 4 * q) * 2);
        SB();
        const bf16x8 ones = {0x3F80, 0x3F80, 0x3F80, 0x3F80, 0x3F80, 0x3F80, 0x3F80, 0x3F80};
        f32x4 aS = {0, 0, 0, 0}, aG = {0, 0, 0, 0};
#pragma unroll
        for (int u = 0; u < 18; ++u) { aS = mfma(ones, xf[u], aS); aG = mfma(xf[u], xf[u], aG); }
        STAMP(1);
        f32x4 acc[3] = {{0, 0, 0, 0}, {0, 0, 0, 0}, {0, 0, 0, 0}};
        sfor<2>([&](auto H) INL {
          constexpr int h = H.value;
          wait_full(h + 1);
          asm volatile("" : "+v"(ba));
#pragma unroll
          for (int u = 9 * h; u < 9 * h + 9; ++u)
#pragma unroll
            for (int l = 0; l < 3; ++l) acc[l] = mfma(cat(trd(ba + u * 3072 + l * 32), trd(ba + u * 3072 + 1536 + l * 32)), xf[u], acc[l]);
        });
        CLOB();
        lds_set(L_FREE + 4 * i, 1, lane);
        STAMP(2);
        const int e = t & 3;
        float gd = e == 0 ? aG[0] : e == 1 ? aG[1] : e == 2 ? aG[2] : aG[3];
        gd = (q == (t >> 2)) ? gd : 0.f;
        float mean, rstd;
        ln_stats(aS[0], qsum(gd), mean, rstd);
        const float ga = GA * rstd, gb = GB * rstd * rstd * rstd;
        const int grp = ct / 3;
        const int sh = grp == 0 ? 1 : grp == 1 ? -1 : grp == 2 ? 2 : -2;
        const int ts = 16 * (i & 1) + t + sh;
        const float keep = (ts >= 0 && ts < 32) ? rstd : 0.f;
        const int dst = OFF_H1 + ((T & ~1) + ((ts >> 4) & 1)) * (18 * 1024) + ((ts & 15) + 16 * q) * 16;
        wait_full(3);
        unsigned hp[6];
#pragma unroll
        for (int l = 0; l < 3; ++l) {
          const f32x4 cs = *(const LAS f32x4*)(L_CS1 + l * 64 + q * 16);
          const f32x4 z = acc[l] - cs * mean;
          const f32x2 g0 = gelu2(f32x2{z.x, z.y}, ga, gb) * keep, g1 = gelu2(f32x2{z.z, z.w}, ga, gb) * keep;
          hp[2 * l] = pk(g0.x, g0.y); hp[2 * l + 1] = pk(g1.x, g1.y);
        }
        ST16((v4u{hp[0], hp[1], hp[2], hp[3]}), dst, upair * 1024);
        ST8((v2u{hp[4], hp[5]}), dst + odd * 8, usingle * 1024);
        STAMP(3);
        DRAIN();
        arrive(ws, FA, b, lane);
        STAMP(4);
      }
      {
        wait_full(4);
        wait_word(L_RDY + 4 * R_H, 1);
        STAMP(5);
        bf16x8 hf[18];
#pragma unroll
        for (int u = 0; u < 18; ++u) hf[u] = asfrag(LD16(lane * 16 + u * 1024, OFF_H1 + T * (18 * 1024)));
        SB();
        unsigned bb = ba + 6 * SLOT;
        asm volatile("" : "+v"(bb));
        f32x4 acc[3] = {{0, 0, 0, 0}, {0, 0, 0, 0}, {0, 0, 0, 0}};
#pragma unroll
        for (int u = 0; u < 18; ++u)
#pragma unroll
          for (int l = 0; l < 3; ++l) acc[l] = mfma(cat(trd(bb + u * 3072 + l * 32), trd(bb + u * 3072 + 1536 + l * 32)), hf[u], acc[l]);
        CLOB();
        lds_set(L_FREE + 4 * i, 2, lane);
        STAMP(6);
        unsigned xp[6];
        float sm = 0.f, sq = 0.f;
#pragma unroll
        for (int l = 0; l < 3; ++l) {
          const unsigned p0 = pk(acc[l].x, acc[l].y), p1 = pk(acc[l].z, acc[l].w);
          xp[2 * l] = pk(lo16(xres[l].x) + lo16(p0), hi16(xres[l].x) + hi16(p0));
          xp[2 * l + 1] = pk(lo16(xres[l].y) + lo16(p1), hi16(xres[l].y) + hi16(p1));
          const float v0 = lo16(xp[2 * l]), v1 = hi16(xp[2 * l]), v2 = lo16(xp[2 * l + 1]), v3 = hi16(xp[2 * l + 1]);
          sm += (v0 + v1) + (v2 + v3);
          sq += (v0 * v0 + v1 * v1) + (v2 * v2 + v3 * v3);
        }
        const int dst = OFF_X1 + T * (18 * 1024) + lane * 16;
        ST16((v4u{xp[0], xp[1], xp[2], xp[3]}), dst, upair * 1024);
        ST8((v2u{xp[4], xp[5]}), dst + odd * 8, usingle * 1024);
        sm = qsum(sm); sq = qsum(sq);
        if (q == 0) ST8((v2u{__float_as_uint(sm), __float_as_uint(sq)}), OFF_STAT + t * 8, (T * 12 + ct) * 128);
        STAMP(7);
        DRAIN();
        arrive(ws, FB, b, lane);
        STAMP(8);
      }
    }
    unsigned b0 = (4 * q + (t >> 2)) * 288 + (t & 3) * 8, b1 = b0 + 6 * SLOT, b2 = b0 + 12 * SLOT;
    asm volatile("" : "+v"(b0), "+v"(b1), "+v"(b2));
#define WADDR(z, c) ((((12 + (z)) % 16) < 6 ? b0 : ((12 + (z)) % 16) < 12 ? b1 : b2) + (((12 + (z)) % 16) % 6) * SLOT + (c) * 32)
#define FN(i_) ((i_) < 108 ? (i_) / 9 : 12 + ((i_) - 108) % 6)
#define FC(i_) ((i_) < 108 ? (i_) % 9 : ((i_) - 108) / 6)
    const int T0 = 8 * g + 2 * i;
    const int hdst = OFF_HH + (T0 * 144 + j * 18) * 1024;
    bf16x8 own[4][2];
    s16x4 lo[PD], hi[PD];
    {
      wait_full(5);
      wait_word(L_RDY + 4 * (R_X0 + (i >> 1)), 1);
      STAMP(9);
      v2u st[2][3];
#pragma unroll
      for (int tt = 0; tt < 2; ++tt)
#pragma unroll
        for (int k = 0; k < 3; ++k) st[tt][k] = LD8(OFF_STAT + (3 * q + k) * 128 + t * 8, (T0 + tt) * (12 * 128));
      bf16x8 xb[12][2];
#pragma unroll
      for (int n = 0; n < 12; ++n)
#pragma unroll
        for (int tt = 0; tt < 2; ++tt) xb[n][tt] = asfrag(LD16(lane * 16 + n * 1024, OFF_X1 + (T0 + tt) * (18 * 1024)));
      SB();
#pragma unroll
      for (int k = 0; k < PD; ++k) { lo[k] = trd(WADDR(0, k)); hi[k] = trd(WADDR(0, k) + 4608); }
      float mean[2], ga[2], gb[2];
#pragma unroll
      for (int tt = 0; tt < 2; ++tt) {
        const float sm = qsum((__uint_as_float(st[tt][0].x) + __uint_as_float(st[tt][1].x)) + __uint_as_float(st[tt][2].x));
        const float sq = qsum((__uint_as_float(st[tt][0].y) + __uint_as_float(st[tt][1].y)) + __uint_as_float(st[tt][2].y));
        float rstd;
        ln_stats(sm, sq, mean[tt], rstd);
        ga[tt] = GA * rstd; gb[tt] = GB * rstd * rstd * rstd;
      }
      f32x4 acc[2][9];
#pragma unroll
      for (int c = 0; c < 9; ++c) { acc[0][c] = f32x4{0, 0, 0, 0}; acc[1][c] = f32x4{0, 0, 0, 0}; }
      unsigned hp[2][2][2];
      v4u fv;
      auto epi = [&](auto Cc) INL {
        constexpr int c = Cc.value;
        const f32x4 cs = *(const LAS f32x4*)(L_CS3 + c * 64 + q * 16);
#pragma unroll
        for (int tt = 0; tt < 2; ++tt) {
          const f32x4 z = acc[tt][c] - cs * mean[tt];
          const f32x2 g0 = gelu2(f32x2{z.x, z.y}, ga[tt], gb[tt]), g1 = gelu2(f32x2{z.z, z.w}, ga[tt], gb[tt]);
          hp[tt][c & 1][0] = pk(g0.x, g0.y); hp[tt][c & 1][1] = pk(g1.x, g1.y);
        }
        if constexpr (c == 5 || c == 7) { DRAIN(); arrive(ws, c == 5 ? FC1 : FC2, b, lane); }
        if constexpr ((c & 1) && c < 8) {
#pragma unroll
          for (int tt = 0; tt < 2; ++tt) {
            const v4u v = {hp[tt][0][0], hp[tt][0][1], hp[tt][1][0], hp[tt][1][1]};
            own[c >> 1][tt] = asfrag(v);
            ST16(v, lane * 16 + tt * (144 * 1024), hdst + (pbase(s) + (c >> 1)) * 1024);
          }
        }
        if constexpr (c == 8) {
#pragma unroll
          for (int tt = 0; tt < 2; ++tt)
            ST8((v2u{hp[tt][0][0], hp[tt][0][1]}), lane * 16 + tt * (144 * 1024) + (s & 1) * 8, hdst + (s < 2 ? 4 : 13) * 1024);
        }
        SB();
      };
      sfor<162>([&](auto I) INL {
        constexpr int ii = I.value, n = FN(ii), c = FC(ii), nx = ii - 1 + PD;
        const bf16x8 cur = cat(lo[ii % PD], hi[ii % PD]);
        acc[0][c] = mfma(cur, xb[n % 12][0], acc[0][c]); SB();
        if constexpr (ii >= 1 && nx < 162) lo[nx % PD] = trd(WADDR(FN(nx), FC(nx)));
        SB();
        acc[1][c] = mfma(cur, xb[n % 12][1], acc[1][c]); SB();
        if constexpr (ii >= 1 && nx < 162) hi[nx % PD] = trd(WADDR(FN(nx), FC(nx)) + 4608);
        SB();
        if constexpr (ii < 108 && c == 8) {
          if constexpr (n < 6) {
#pragma unroll
            for (int tt = 0; tt < 2; ++tt) xb[n][tt] = asfrag(LD16(lane * 16 + (12 + n) * 1024, OFF_X1 + (T0 + tt) * (18 * 1024)));
          }
          if constexpr (n == 1 || n == 5 || n == 8 || n == 11) { CLOB(); lds_set(L_FREE + 4 * i, n == 1 ? 3 : n == 5 ? 4 : n == 8 ? 5 : 6, lane); }
          SB();
        }
        if constexpr (ii == 96) { fv = lds4(L_FULL); SB(); }
        if constexpr (ii == 104) { if (min3(fv) < 8) wait_full(8); CLOB(); SB(); STAMP(10); }
        if constexpr (ii >= 108 && n == 17 && c >= 1) epi(std::integral_constant<int, c - 1>{});
        if constexpr (ii == 150) { fv = lds4(L_FULL); SB(); }
      });
      CLOB();
      lds_set(L_FREE + 4 * i, 7, lane);
      if (min3(fv) < 9) wait_full(9);
      CLOB();
#pragma unroll
      for (int k = 0; k < PD; ++k) { lo[k] = trd(WADDR(18, k)); hi[k] = trd(WADDR(18, k) + 4608); }
      SB();
      epi(std::integral_constant<int, 8>{});
      STAMP(11);
    }
    {
      bf16x8 hb[18][2];
#pragma unroll
      for (int m = 0; m < 4; ++m) { hb[m][0] = own[m][0]; hb[m][1] = own[m][1]; }
      auto hload = [&](auto M) INL {
        constexpr int m = M.value;
#pragma unroll
        for (int tt = 0; tt < 2; ++tt) hb[m][tt] = asfrag(LD16(lane * 16 + tt * (144 * 1024), hdst + dstep<m>(s) * 1024));
      };
      wait_word(L_RDY + 4 * R_H1, 1);
      sfor<6>([&](auto M) INL { hload(std::integral_constant<int, 4 + M.value>{}); });
      SB();
      STAMP(12);
      f32x4 acc[2][9];
#pragma unroll
      for (int c = 0; c < 9; ++c) { acc[0][c] = f32x4{0, 0, 0, 0}; acc[1][c] = f32x4{0, 0, 0, 0}; }
      const int pdst = OFF_P + (((g * 4 + s) * 8 + 2 * i) * 8 + j) * (5 * 1024);
      v4u fv;
      unsigned rv;
      auto epi = [&](auto Cc) INL {
        constexpr int c = Cc.value;
        if constexpr ((c & 1) && c < 8) {
#pragma unroll
          for (int tt = 0; tt < 2; ++tt) {
            const f32x4 a0 = acc[tt][c - 1], a1 = acc[tt][c];
            ST16((v4u{pk(a0.x, a0.y), pk(a0.z, a0.w), pk(a1.x, a1.y), pk(a1.z, a1.w)}), lane * 16 + tt * (40 * 1024) + (c >> 1) * 1024, pdst);
          }
        }
        if constexpr (c == 8) {
#pragma unroll
          for (int tt = 0; tt < 2; ++tt) ST16(__builtin_bit_cast(v4u, acc[tt][8]), lane * 16 + tt * (40 * 1024) + 4096, pdst);
        }
        SB();
      };
      sfor<162>([&](auto I) INL {
        constexpr int ii = I.value, n = FN(ii), c = FC(ii), nx = ii - 1 + PD;
        const bf16x8 cur = cat(lo[ii % PD], hi[ii % PD]);
        acc[0][c] = mfma(cur, hb[n][0], acc[0][c]); SB();
        if constexpr (ii >= 1 && nx < 162) lo[nx % PD] = trd(WADDR(18 + FN(nx), FC(nx)));
        SB();
        acc[1][c] = mfma(cur, hb[n][1], acc[1][c]); SB();
        if constexpr (ii >= 1 && nx < 162) hi[nx % PD] = trd(WADDR(18 + FN(nx), FC(nx)) + 4608);
        SB();
        if constexpr (ii == 17) { CLOB(); lds_set(L_FREE + 4 * i, 8, lane); SB(); }
        if constexpr (ii == 35) { VMW(12); arrive(ws, FC3, b, lane); SB(); }
        if constexpr (ii == 50) { rv = ldsw(L_RDY + 4 * R_H2); SB(); }
        if constexpr (ii == 56) {
          if (rfl(rv) == 0) wait_word(L_RDY + 4 * R_H2, 1);
          CLOB();
          sfor<3>([&](auto M) INL { hload(std::integral_constant<int, 10 + M.value>{}); });
          SB();
          STAMP(13);
        }
        if constexpr (ii == 72) { fv = lds4(L_FULL); SB(); }
        if constexpr (ii == 80) { if (min3(fv) < 10) wait_full(10); CLOB(); SB(); }
        if constexpr (ii == 84) { rv = ldsw(L_RDY + 4 * R_H3); SB(); }
        if constexpr (ii == 90) {
          if (rfl(rv) == 0) wait_word(L_RDY + 4 * R_H3, 1);
          CLOB();
          sfor<5>([&](auto M) INL { hload(std::integral_constant<int, 13 + M.value>{}); });
          SB();
          STAMP(14);
        }
        if constexpr (ii == 96) { fv = lds4(L_FULL); SB(); }
        if constexpr (ii == 104) { if (min3(fv) < 11) wait_full(11); CLOB(); SB(); }
        if constexpr (ii >= 108 && n == 17 && c >= 1) epi(std::integral_constant<int, c - 1>{});
      });
      epi(std::integral_constant<int, 8>{});
      STAMP(15);
      DRAIN();
      arrive(ws, FD, b, lane);
      STAMP(16);
    }
  } else if (w < 7) {
    const int ld = w - 4;
    const unsigned fullw = L_FULL + 4 * ld;
    unsigned v3[3], v4[3];
#pragma unroll
    for (int e = 0; e < 3; ++e) {
      const int idx = (3 * ld + e) * 64 + lane, row = idx / 18, ch = idx % 18;
      const int sc = (ch + 2 * (s & 1)) % 18;
      v3[e] = row * (F_ * 2) + sc * 16;
      v4[e] = row * (D_ * 2) + sc * 16;
    }
    const char* w3b = w3 + (576 * j + 144 * s) * 2;
    const char* w4b = w4 + (size_t)(576 * j) * (D_ * 2) + 144 * s * 2;
    auto w3slice = [&](int n) INL {
#pragma unroll
      for (int e = 0; e < 3; ++e) dma(w3b + (size_t)n * (32 * F_ * 2), v3[e], ((12 + n) & 15) * SLOT + (3 * ld + e) * 1024);
    };
    auto w4slice = [&](auto M) INL {
      constexpr int m = M.value;
      const char* src = w4b + dstep<m>(s) * (32 * D_ * 2);
#pragma unroll
      for (int e = 0; e < 3; ++e) dma(src, v4[e], ((30 + m) & 15) * SLOT + (3 * ld + e) * 1024);
    };
    const bf16x8 ones = {0x3F80, 0x3F80, 0x3F80, 0x3F80, 0x3F80, 0x3F80, 0x3F80, 0x3F80};
    f32x4 cs[3] = {{0, 0, 0, 0}, {0, 0, 0, 0}, {0, 0, 0, 0}};
    const unsigned cb = (4 * q + (t >> 2)) * 288 + (t & 3) * 8 + ld * 96;
    auto colsum3 = [&](int n0, int n1) INL {
      for (int n = n0; n < n1; ++n) {
        const unsigned a = cb + ((12 + n) & 15) * SLOT;
#pragma unroll
        for (int c = 0; c < 3; ++c) cs[c] = mfma(cat(trd(a + c * 32), trd(a + 4608 + c * 32)), ones, cs[c]);
      }
      CLOB();
    };
    if (ab) {
      const int idx = ld * 64 + lane, row = idx / 6, ch = idx % 6;
      const int sc = (ch + 2 * (ct & 1)) % 6;
      const int krow = 8 * ((row >> 2) & 3) + 4 * (row >> 4) + (row & 3);
      const unsigned v1 = krow * (D_ * 2) + sc * 16, v2 = row * (D_ * 2) + sc * 16;
      for (int u = 0; u < 18; ++u) dma(w1 + 96 * ct + u * (32 * D_ * 2), v1, u * 3072 + ld * 1024);
      for (int u = 0; u < 18; ++u) dma(w2 + 96 * ct + u * (32 * D_ * 2), v2, 6 * SLOT + u * 3072 + ld * 1024);
      for (int n = 0; n < 4; ++n) w3slice(n);
      STAMP(1);
      VMW(39); lds_set(fullw, 1, lane);
      VMW(30); lds_set(fullw, 2, lane);
      STAMP(2);
      wait_full(2);
      {
        f32x4 c1 = {0, 0, 0, 0};
        const unsigned a = (4 * q + (t >> 2)) * 96 + (t & 3) * 8 + ld * 32;
#pragma unroll
        for (int u = 0; u < 18; ++u) c1 = mfma(cat(trd(a + u * 3072), trd(a + u * 3072 + 1536)), ones, c1);
        if (t == 0) *(LAS f32x4*)(L_CS1 + ld * 64 + q * 16) = c1;
        CLOB();
      }
      lds_set(fullw, 3, lane);
      STAMP(3);
      VMW(12); lds_set(fullw, 4, lane);
      wait_free(1); wait_full(3);
      for (int n = 4; n < 10; ++n) w3slice(n);
      STAMP(4);
      wait_free(2);
      for (int n = 10; n < 16; ++n) w3slice(n);
      STAMP(5);
    } else {
      for (int n = 0; n < 16; ++n) w3slice(n);
    }
    DRAIN(); lds_set(fullw, 5, lane);
    STAMP(6);
    wait_full(5);
    colsum3(0, 16);
    lds_set(fullw, 6, lane);
    STAMP(7);
    wait_free(3); wait_full(6);
    w3slice(16); w3slice(17);
    wait_free(4);
    sfor<4>([&](auto M) INL { w4slice(M); });
    VMW(12); lds_set(fullw, 7, lane);
    wait_full(7);
    colsum3(16, 18);
    if (t == 0) {
#pragma unroll
      for (int c = 0; c < 3; ++c) *(LAS f32x4*)(L_CS3 + (3 * ld + c) * 64 + q * 16) = cs[c];
    }
    CLOB();
    lds_set(fullw, 8, lane);
    STAMP(8);
    wait_free(5);
    sfor<3>([&](auto M) INL { w4slice(std::integral_constant<int, 4 + M.value>{}); });
    wait_free(6);
    sfor<3>([&](auto M) INL { w4slice(std::integral_constant<int, 7 + M.value>{}); });
    DRAIN(); lds_set(fullw, 9, lane);
    STAMP(9);
    wait_free(7); wait_full(8);
    sfor<6>([&](auto M) INL { w4slice(std::integral_constant<int, 10 + M.value>{}); });
    wait_free(8);
    sfor<2>([&](auto M) INL { w4slice(std::integral_constant<int, 16 + M.value>{}); });
    VMW(6); lds_set(fullw, 10, lane);
    DRAIN(); lds_set(fullw, 11, lane);
    STAMP(10);
  } else {
    const u64 tag = __hip_atomic_load((GAS u64*)(ws + OFF_EPOCH), __ATOMIC_RELAXED, __HIP_MEMORY_SCOPE_AGENT) + 1ull;
    if (lane == 0) {
      *(volatile LAS u64*)L_TAG = tag;
      *(volatile LAS unsigned*)(L_RDY + 4 * R_TAG) = 1u;
      __hip_atomic_store(flagp(ws, FS, b), tag, __ATOMIC_RELAXED, __HIP_MEMORY_SCOPE_AGENT);
    }
    STAMP(1);
    auto poll = [&](int kind, int wg, bool need) INL {
      GAS u64* p = flagp(ws, kind, wg);
      for (unsigned n = 0; n < GLB_CAP; ++n) {
        const u64 v = __hip_atomic_load(p, __ATOMIC_RELAXED, __HIP_MEMORY_SCOPE_AGENT);
        if (__all(!need || v == tag)) break;
        __builtin_amdgcn_s_sleep(1);
      }
      CLOB();
    };
    if (ab) {
      poll(FA, j + 8 * ((r & 1) + 2 * (lane % 12)), true);
      lds_set(L_RDY + 4 * R_H, 1, lane);
    }
    STAMP(2);
    {
      GAS u64* p = flagp(ws, FB, g + 8 * (lane % 24));
      bool d0 = false, d1 = false;
      for (unsigned n = 0; n < GLB_CAP; ++n) {
        const u64 v = __hip_atomic_load(p, __ATOMIC_RELAXED, __HIP_MEMORY_SCOPE_AGENT);
        const bool ok = v == tag;
        if (!d0 && __all(ok || (lane & 1))) { d0 = true; lds_set(L_RDY + 4 * R_X0, 1, lane); }
        if (!d1 && __all(ok || !(lane & 1))) { d1 = true; lds_set(L_RDY + 4 * R_X1, 1, lane); }
        if (d0 && d1) break;
        __builtin_amdgcn_s_sleep(1);
      }
      lds_set(L_RDY + 4 * R_X0, 1, lane); lds_set(L_RDY + 4 * R_X1, 1, lane);
    }
    STAMP(3);
    if (b == 0) {
#pragma unroll
      for (int k = 0; k < 4; ++k) poll(FS, k * 64 + lane, true);
      if (lane == 0) __hip_atomic_store((GAS u64*)(ws + OFF_EPOCH), tag, __ATOMIC_RELAXED, __HIP_MEMORY_SCOPE_AGENT);
    }
    const int peer = j + 8 * (4 * g + (lane & 3));
    poll(FC1, peer, true); lds_set(L_RDY + 4 * R_H1, 1, lane);
    STAMP(4);
    poll(FC2, peer, true); lds_set(L_RDY + 4 * R_H2, 1, lane);
    STAMP(5);
    poll(FC3, peer, true); lds_set(L_RDY + 4 * R_H3, 1, lane);
    STAMP(6);
  }
  if (w >= 3) {
    const int p = w - 3;
    const int T = 8 * g + j;
    if (w != 3 && w != 7) { wait_word(L_RDY + 4 * R_X0, 1); wait_word(L_RDY + 4 * R_X1, 1); }
    v2u st[3];
#pragma unroll
    for (int k = 0; k < 3; ++k) st[k] = LD8(OFF_STAT + (3 * q + k) * 128 + t * 8, T * (12 * 128));
    const int u = p < 4 ? pbase(s) + p : (s < 2 ? 4 : 13);
    const v4u xv = LD16(lane * 16, OFF_X1 + (T * 18 + u) * 1024);
    float mean, rstd;
    ln_stats(qsum((__uint_as_float(st[0].x) + __uint_as_float(st[1].x)) + __uint_as_float(st[2].x)),
             qsum((__uint_as_float(st[0].y) + __uint_as_float(st[1].y)) + __uint_as_float(st[2].y)), mean, rstd);
    if (w == 7) {
      const u64 tag = *(volatile LAS u64*)L_TAG;
      GAS u64* fp = flagp(ws, FD, (lane & 7) + 8 * (4 * g + s));
      for (unsigned n = 0; n < GLB_CAP; ++n) {
        const u64 v = __hip_atomic_load(fp, __ATOMIC_RELAXED, __HIP_MEMORY_SCOPE_AGENT);
        if (__all(v == tag)) break;
        __builtin_amdgcn_s_sleep(1);
      }
      CLOB();
      lds_set(L_RDY + 4 * R_P, 1, lane);
    } else {
      wait_word(L_RDY + 4 * R_P, 1);
    }
    STAMP(20);
    v4u pv[8];
    const int psrc = OFF_P + (((g * 4 + s) * 8 + j) * 8 * 5 + p) * 1024;
#pragma unroll
    for (int k = 0; k < 8; ++k) pv[k] = LD16(lane * 16 + k * (5 * 1024), psrc);
    char* orow = out + (size_t)(16 * T + t) * (D_ * 2) + (32 * u + 4 * q) * 2;
    if (p < 4) {
      float a[8];
#pragma unroll
      for (int e = 0; e < 8; ++e) a[e] = 0.f;
#pragma unroll
      for (int k = 0; k < 8; ++k)
#pragma unroll
        for (int e = 0; e < 4; ++e) { const unsigned d = pv[k][e]; a[2 * e] += lo16(d); a[2 * e + 1] += hi16(d); }
      unsigned o[4];
#pragma unroll
      for (int e = 0; e < 4; ++e) {
        const unsigned h2 = pk(a[2 * e] * rstd, a[2 * e + 1] * rstd), xe = xv[e];
        o[e] = pk(lo16(xe) + lo16(h2), hi16(xe) + hi16(h2));
      }
      *(GAS v2u*)orow = v2u{o[0], o[1]};
      *(GAS v2u*)(orow + 32) = v2u{o[2], o[3]};
    } else {
      f32x4 a = {0, 0, 0, 0};
#pragma unroll
      for (int k = 0; k < 8; ++k) a += __builtin_bit_cast(f32x4, pv[k]);
      const unsigned x0 = (s & 1) ? xv.z : xv.x, x1v = (s & 1) ? xv.w : xv.y;
      const unsigned h0 = pk(a.x * rstd, a.y * rstd), h1 = pk(a.z * rstd, a.w * rstd);
      *(GAS v2u*)(orow + (s & 1) * 32) = v2u{pk(lo16(x0) + lo16(h0), hi16(x0) + hi16(h0)), pk(lo16(x1v) + lo16(h1), hi16(x1v) + hi16(h1))};
    }
    STAMP(21);
  }
#endif
}
}

extern "C" void kernel_launch(void* const* d_in, const int* in_sizes, int n_in, void* d_out, int out_size, void* d_ws, size_t ws_size,
                              hipStream_t stream) {
  mx::mixer_kernel<<<256, 512, 0, stream>>>((const char*)d_in[0], (const char*)d_in[1], (const char*)d_in[2], (const char*)d_in[3],
                                            (const char*)d_in[4], (char*)d_out, (char*)d_ws);
}
```

```cpp
#include <hip/hip_runtime.h>
#include <utility>

#define STAMPS 0

#define LAS __attribute__((address_space(3)))
#define GAS __attribute__((address_space(1)))
#define DI __device__ __forceinline__
typedef unsigned v4u __attribute__((ext_vector_type(4)));
typedef unsigned v2u __attribute__((ext_vector_type(2)));
typedef float f32x4 __attribute__((ext_vector_type(4)));
typedef float f32x2 __attribute__((ext_vector_type(2)));
typedef short bf16x8 __attribute__((ext_vector_type(8)));
typedef short s16x4 __attribute__((ext_vector_type(4)));
typedef __bf16 bf16x2_t __attribute__((ext_vector_type(2)));
typedef unsigned long long u64;

namespace mx {
constexpr int D_ = 576, F_ = 4608;
constexpr int OFF_EPOCH = 0;
constexpr int OFF_FLAGS = 4096;
constexpr int OFF_STAT = 262144;
constexpr int OFF_H1 = 524288;
constexpr int OFF_X1 = OFF_H1 + 64 * 18 * 1024;
constexpr int OFF_HH = OFF_X1 + 64 * 18 * 1024;
constexpr int OFF_P = OFF_HH + 64 * 144 * 1024;
constexpr int OFF_DBG = OFF_P + 8 * 4 * 8 * 8 * 5 * 1024;
constexpr int WS_BYTES = OFF_DBG + 2 * 256 * 8 * 32 * 8;
enum { FS = 0, FA = 1, FB = 2, FC1 = 3, FC2 = 4, FC3 = 5, FD = 6 };
constexpr int SLOT = 9216, MISC = 16 * SLOT;
constexpr int L_FULL = MISC, L_FREE = MISC + 16, L_RDY = MISC + 32  , L_CNT = MISC + 64  , L_TAG = MISC + 96;
constexpr int L_CS1 = MISC + 256, L_CS3 = MISC + 512, L_DBG = MISC + 2048;
enum { R_H = 0, R_X0 = 1, R_X1 = 2, R_H1 = 3, R_H2 = 4, R_H3 = 5, R_P = 6, R_TAG = 7 };
constexpr unsigned LDS_CAP = 1u << 21, GLB_CAP = 1u << 16;
constexpr int PD = 4;

template <class F, int... I> DI void sfor_impl(F&& f, std::integer_sequence<int, I...>) { (f(std::integral_constant<int, I>{}), ...); }
template <int N, class F> DI void sfor(F&& f) { sfor_impl(f, std::make_integer_sequence<int, N>{}); }
#define INL __attribute__((always_inline))
#define SB() __builtin_amdgcn_sched_barrier(0)
#define CLOB() asm volatile("" ::: "memory")
#define DRAIN() asm volatile("s_waitcnt vmcnt(0)" ::: "memory")
#define VMW(n) asm volatile("s_waitcnt vmcnt(%0)" ::"i"(n) : "memory")

DI unsigned rfl(unsigned v) { return __builtin_amdgcn_readfirstlane(v); }
DI f32x4 mfma(bf16x8 a, bf16x8 b, f32x4 c) { return __builtin_amdgcn_mfma_f32_16x16x32_bf16(a, b, c, 0, 0, 0); }
DI bf16x8 asfrag(v4u v) { return __builtin_bit_cast(bf16x8, v); }
DI s16x4 trd(unsigned addr) { return __builtin_amdgcn_ds_read_tr16_b64_v4i16((LAS s16x4*)addr); }
DI bf16x8 cat(s16x4 lo, s16x4 hi) { return __builtin_shufflevector(lo, hi, 0, 1, 2, 3, 4, 5, 6, 7); }
DI unsigned pk(float a, float b) { bf16x2_t r = __builtin_convertvector(f32x2{a, b}, bf16x2_t); return __builtin_bit_cast(unsigned, r); }
DI float lo16(unsigned p) { return __uint_as_float(p << 16); }
DI float hi16(unsigned p) { return __uint_as_float(p & 0xffff0000u); }
DI float bfr(float a) { return lo16(pk(a, a)); }
DI float qsum(float v) {
  auto r = __builtin_amdgcn_permlane16_swap(__float_as_uint(v), __float_as_uint(v), false, false);
  float a = __uint_as_float(r[0]) + __uint_as_float(r[1]);
  auto r2 = __builtin_amdgcn_permlane32_swap(__float_as_uint(a), __float_as_uint(a), false, false);
  return __uint_as_float(r2[0]) + __uint_as_float(r2[1]);
}
DI void ln_stats(float s, float ss, float& mean, float& rstd) {
  const float m = s * (1.0f / D_);
  float var = __builtin_fmaf(-m, m, ss * (1.0f / D_));
  var = var > 0.f ? var : 0.f;
  mean = bfr(m);
  const float sd = bfr(__builtin_sqrtf(bfr(bfr(var) + 1e-5f)));
  rstd = 1.0f / sd;
}
constexpr float GA = -2.302208198144325f;
constexpr float GB = -0.1029432397357593f;
template <int N> DI void geluN(f32x2 (&z)[N], const float (&a)[N], const float (&b)[N]) {
  f32x2 p[N];
#pragma unroll
  for (int k = 0; k < N; ++k) p[k] = z[k] * z[k];
  SB();
#pragma unroll
  for (int k = 0; k < N; ++k) p[k] = p[k] * b[k] + a[k];
  SB();
#pragma unroll
  for (int k = 0; k < N; ++k) p[k] = z[k] * p[k];
  SB();
#pragma unroll
  for (int k = 0; k < N; ++k) { p[k].x = __builtin_amdgcn_exp2f(p[k].x); p[k].y = __builtin_amdgcn_exp2f(p[k].y); }
  SB();
#pragma unroll
  for (int k = 0; k < N; ++k) p[k] = p[k] + 1.0f;
  SB();
#pragma unroll
  for (int k = 0; k < N; ++k) { p[k].x = __builtin_amdgcn_rcpf(p[k].x); p[k].y = __builtin_amdgcn_rcpf(p[k].y); }
  SB();
#pragma unroll
  for (int k = 0; k < N; ++k) z[k] = z[k] * p[k];
  SB();
}

DI unsigned ldsw(int addr) { return *(volatile LAS unsigned*)addr; }
DI v4u lds4(int addr) { return *(volatile LAS v4u*)addr; }
DI void lds_set(int addr, unsigned v, int lane) { if (lane == 0) *(volatile LAS unsigned*)addr = v; }
DI void wait_word(int addr, unsigned v) {
  _Pragma("nounroll") for (unsigned n = 0; n < LDS_CAP; ++n) { if (rfl(ldsw(addr)) >= v) break; __builtin_amdgcn_s_sleep(1); }
  CLOB();
}
DI unsigned min3(v4u x) { return rfl(min(min(x.x, x.y), x.z)); }
DI unsigned min4(v4u x) { return rfl(min(min(x.x, x.y), min(x.z, x.w))); }
DI void wait_full(unsigned v) {
  _Pragma("nounroll") for (unsigned n = 0; n < LDS_CAP; ++n) { if (min3(lds4(L_FULL)) >= v) break; __builtin_amdgcn_s_sleep(1); }
  CLOB();
}
DI void wait_free(unsigned v) {
  _Pragma("nounroll") for (unsigned n = 0; n < LDS_CAP; ++n) { if (min4(lds4(L_FREE)) >= v) break; __builtin_amdgcn_s_sleep(1); }
  CLOB();
}
DI GAS u64* flagp(char* ws, int kind, int b) { return (GAS u64*)(ws + OFF_FLAGS + (kind * 256 + b) * 128); }
DI void arrive(char* ws, int kind, int b, int lane) {
  if (lane == 0) {
    const unsigned old = __hip_atomic_fetch_add((LAS unsigned*)(L_CNT + 4 * kind), 1u, __ATOMIC_RELAXED, __HIP_MEMORY_SCOPE_WORKGROUP);
    if (old == 3u) {
      _Pragma("nounroll") for (unsigned n = 0; n < LDS_CAP; ++n) { if (ldsw(L_RDY + 4 * R_TAG) != 0u) break; __builtin_amdgcn_s_sleep(1); }
      const u64 tag = *(volatile LAS u64*)L_TAG;
      __hip_atomic_store(flagp(ws, kind, b), tag, __ATOMIC_RELAXED, __HIP_MEMORY_SCOPE_AGENT);
    }
  }
}
#define LD16(off, soff) __builtin_amdgcn_raw_buffer_load_b128(rs, (off), (soff), 16)
#define LD8(off, soff) __builtin_amdgcn_raw_buffer_load_b64(rs, (off), (soff), 16)
#define ST16(v, off, soff) __builtin_amdgcn_raw_buffer_store_b128((v), rs, (off), (soff), 16)
#define ST8(v, off, soff) __builtin_amdgcn_raw_buffer_store_b64((v), rs, (off), (soff), 16)
DI void dma(const char* sbase, unsigned voff, unsigned ldsaddr) {
  asm volatile("s_mov_b32 m0, %0\n\ts_nop 0\n\tglobal_load_lds_dwordx4 %1, %2" ::"s"(ldsaddr), "v"(voff), "s"(sbase) : "memory");
}
DI int cstep(int n, int s) { const int u = n + ((9 * s) >> 1); return u >= 18 ? u - 18 : u; }
DI int pbase(int s) { return (9 * s + (s & 1)) >> 1; }
template <int M> DI int dstep(int s) {
  if constexpr (M < 4) return pbase(s) + M;
  else if constexpr (M < 10) return pbase((s + 1 + (M - 4) / 2) & 3) + ((M - 4) & 1);
  else if constexpr (M < 13) return pbase((s + M - 9) & 3) + 2;
  else if constexpr (M < 16) return pbase((s + M - 12) & 3) + 3;
  else return M == 16 ? 4 : 13;
}
#define STAMP(k) do { } while (0)

__global__ __launch_bounds__(512, 2) void mixer_kernel(const char* __restrict__ x, const char* __restrict__ w1, const char* __restrict__ w2,
                                                       const char* __restrict__ w3, const char* __restrict__ w4, char* __restrict__ out, char* ws) {
#if defined(__HIP_DEVICE_COMPILE__)
  __shared__ __attribute__((aligned(16))) char smem[163840];
  const int tid = threadIdx.x, lane = tid & 63, w = rfl(tid >> 6);
  const int t = lane & 15, q = lane >> 4;
  const int b = blockIdx.x, j = b & 7, r = b >> 3;
  const bool ab = r < 24;
  const int rt = 2 * j + (r & 1), ct = r >> 1;
  const int g = r >> 2, s = r & 3;
  const __amdgpu_buffer_rsrc_t rs = __builtin_amdgcn_make_buffer_rsrc(ws, 0, WS_BYTES, 0x00020000);
  if (tid < 32) *(volatile LAS unsigned*)(MISC + tid * 4) = 0u;
  asm volatile("" ::"v"(smem));
  STAMP(0);
  asm volatile("s_waitcnt lgkmcnt(0)\n\ts_barrier" ::: "memory");

  if (w < 4) {
    const int i = w;
    if (ab) {
      unsigned ba = 4 * SLOT + (4 * q + (t >> 2)) * 96 + (t & 3) * 8;
      const int T = 4 * rt + i, odd = ct & 1;
      const int upair = (3 * ct + odd) >> 1, usingle = odd ? (3 * ct - 1) >> 1 : (3 * ct + 2) >> 1;
      v2u xres[3];
      {
        const char* xsrc = x + (size_t)T * (16 * D_ * 2);
        const unsigned xl = (i == 3 ? 2 : 10 + 2 * i) * SLOT;
#pragma unroll
        for (int e = 0; e < 18; ++e) {
          const unsigned f = (((unsigned)(lane + 64 * e) * 911u) >> 17) & 7u;
          dma(xsrc + e * 1024, (lane * 16) ^ (f << 4), xl + e * 1024);
        }
        unsigned xa = xl + t * (D_ * 2), xs = ((t >> 1) & 7) << 4;
        unsigned xe0 = xa + ((q * 16) ^ xs), xe1 = xa + ((64 + q * 16) ^ xs);
        const int grp = ct / 3;
        const int sh = grp == 0 ? 1 : grp == 1 ? -1 : grp == 2 ? 2 : -2;
        const int ts = 16 * (i & 1) + t + sh;
        const bool keep = ts >= 0 && ts < 32;
        const int dst = OFF_H1 + ((T & ~1) + ((ts >> 4) & 1)) * (18 * 1024) + ((ts & 15) + 16 * q) * 16;
        asm volatile("" : "+v"(xe0), "+v"(xe1));
        DRAIN();
        STAMP(1);
        bf16x8 xf[18];
#pragma unroll
        for (int u = 0; u < 18; ++u) xf[u] = *(const LAS bf16x8*)(((u & 1) ? xe1 : xe0) + (u >> 1) * 128);
#pragma unroll
        for (int l = 0; l < 3; ++l) {
          const unsigned cb = (48 * ct + 16 * ((l + odd) % 3) + 4 * q) * 2;
          xres[l] = *(const LAS v2u*)(xa + (cb ^ xs));
        }
        CLOB();
        lds_set(L_FREE + 4 * i, 1, lane);
        const bf16x8 ones = {0x3F80, 0x3F80, 0x3F80, 0x3F80, 0x3F80, 0x3F80, 0x3F80, 0x3F80};
        f32x4 aS = {0, 0, 0, 0}, aG = {0, 0, 0, 0};
#pragma unroll
        for (int u = 0; u < 18; ++u) { aS = mfma(ones, xf[u], aS); aG = mfma(xf[u], xf[u], aG); }
        STAMP(17);
        f32x4 acc[3] = {{0, 0, 0, 0}, {0, 0, 0, 0}, {0, 0, 0, 0}};
        float nmean, rstd;
        sfor<2>([&](auto H) INL {
          constexpr int h = H.value;
          wait_full(h + 1);
          asm volatile("" : "+v"(ba));
#pragma unroll
          for (int u = 9 * h; u < 9 * h + 9; ++u)
#pragma unroll
            for (int l = 0; l < 3; ++l) acc[l] = mfma(cat(trd(ba + u * 3072 + l * 32), trd(ba + u * 3072 + 1536 + l * 32)), xf[u], acc[l]);
          if constexpr (h == 0) {
            const int e = t & 3;
            float gd = e == 0 ? aG[0] : e == 1 ? aG[1] : e == 2 ? aG[2] : aG[3];
            gd = (q == (t >> 2)) ? gd : 0.f;
            ln_stats(aS[0], qsum(gd), nmean, rstd);
            nmean = -nmean;
          }
        });
        STAMP(2);
        const float ga = GA * rstd, gb = GB * rstd * rstd * rstd;
        wait_full(3);
        f32x2 z[6];
#pragma unroll
        for (int l = 0; l < 3; ++l) {
          const f32x4 cs = *(const LAS f32x4*)(L_CS1 + l * 64 + q * 16);
          const f32x4 zz = cs * nmean + acc[l];
          z[2 * l] = f32x2{zz.x, zz.y}; z[2 * l + 1] = f32x2{zz.z, zz.w};
        }
        const float ga6[6] = {ga, ga, ga, ga, ga, ga}, gb6[6] = {gb, gb, gb, gb, gb, gb};
        geluN<6>(z, ga6, gb6);
        unsigned hp[6];
#pragma unroll
        for (int k = 0; k < 6; ++k) { const f32x2 v = z[k] * rstd; hp[k] = keep ? pk(v.x, v.y) : 0u; }
        ST16((v4u{hp[0], hp[1], hp[2], hp[3]}), dst, upair * 1024);
        ST8((v2u{hp[4], hp[5]}), dst + odd * 8, usingle * 1024);
        STAMP(3);
        DRAIN();
        lds_set(L_FREE + 4 * i, 2, lane);
        arrive(ws, FA, b, lane);
        STAMP(4);
      }
      {
        wait_full(4);
        wait_word(L_RDY + 4 * R_H, 1);
        STAMP(5);
        bf16x8 hf[18];
#pragma unroll
        for (int u = 0; u < 18; ++u) hf[u] = asfrag(LD16(lane * 16 + u * 1024, OFF_H1 + T * (18 * 1024)));
        SB();
        unsigned bb = ba + 6 * SLOT;
        asm volatile("" : "+v"(bb));
        f32x4 acc[3] = {{0, 0, 0, 0}, {0, 0, 0, 0}, {0, 0, 0, 0}};
#pragma unroll
        for (int u = 0; u < 18; ++u)
#pragma unroll
          for (int l = 0; l < 3; ++l) acc[l] = mfma(cat(trd(bb + u * 3072 + l * 32), trd(bb + u * 3072 + 1536 + l * 32)), hf[u], acc[l]);
        STAMP(6);
        unsigned xp[6];
        float sm = 0.f, sq = 0.f;
#pragma unroll
        for (int l = 0; l < 3; ++l) {
          const unsigned p0 = pk(acc[l].x, acc[l].y), p1 = pk(acc[l].z, acc[l].w);
          xp[2 * l] = pk(lo16(xres[l].x) + lo16(p0), hi16(xres[l].x) + hi16(p0));
          xp[2 * l + 1] = pk(lo16(xres[l].y) + lo16(p1), hi16(xres[l].y) + hi16(p1));
          const float v0 = lo16(xp[2 * l]), v1 = hi16(xp[2 * l]), v2 = lo16(xp[2 * l + 1]), v3 = hi16(xp[2 * l + 1]);
          sm += (v0 + v1) + (v2 + v3);
          sq += (v0 * v0 + v1 * v1) + (v2 * v2 + v3 * v3);
        }
        const int dst = OFF_X1 + T * (18 * 1024) + lane * 16;
        ST16((v4u{xp[0], xp[1], xp[2], xp[3]}), dst, upair * 1024);
        ST8((v2u{xp[4], xp[5]}), dst + odd * 8, usingle * 1024);
        sm = qsum(sm); sq = qsum(sq);
        if (q == 0) ST8((v2u{__float_as_uint(sm), __float_as_uint(sq)}), OFF_STAT + t * 8, (T * 12 + ct) * 128);
        STAMP(7);
        DRAIN();
        lds_set(L_FREE + 4 * i, 3, lane);
        arrive(ws, FB, b, lane);
        STAMP(8);
      }
    }
    unsigned b0 = (4 * q + (t >> 2)) * 288 + (t & 3) * 8, b1 = b0 + 6 * SLOT, b2 = b0 + 12 * SLOT;
    asm volatile("" : "+v"(b0), "+v"(b1), "+v"(b2));
#define WADDR(z, c) ((((z) % 16) < 6 ? b0 : ((z) % 16) < 12 ? b1 : b2) + (((z) % 16) % 6) * SLOT + (c) * 32)
#define FN(i_) ((i_) < 108 ? (i_) / 9 : 12 + ((i_) - 108) % 6)
#define FC(i_) ((i_) < 108 ? (i_) % 9 : ((i_) - 108) / 6)
    const int T0 = 8 * g + 2 * i;
    const int hdst = OFF_HH + (T0 * 144 + j * 18) * 1024;
    bf16x8 own[4][2];
    s16x4 lo[PD], hi[PD];
    {
      wait_full(6);
      wait_word(L_RDY + 4 * (R_X0 + (i >> 1)), 1);
      STAMP(9);
      v2u st[2][3];
#pragma unroll
      for (int tt = 0; tt < 2; ++tt)
#pragma unroll
        for (int k = 0; k < 3; ++k) st[tt][k] = LD8(OFF_STAT + (3 * q + k) * 128 + t * 8, (T0 + tt) * (12 * 128));
      const int xsrc = OFF_X1 + T0 * (18 * 1024);
      bf16x8 xb[12][2];
#pragma unroll
      for (int n = 0; n < 12; ++n)
#pragma unroll
        for (int tt = 0; tt < 2; ++tt) xb[n][tt] = asfrag(LD16(lane * 16 + tt * (18 * 1024), xsrc + cstep(n, s) * 1024));
      SB();
#pragma unroll
      for (int k = 0; k < PD; ++k) { lo[k] = trd(WADDR(0, k)); hi[k] = trd(WADDR(0, k) + 4608); }
      float mean[2], ga[2], gb[2];
#pragma unroll
      for (int tt = 0; tt < 2; ++tt) {
        const float sm = qsum((__uint_as_float(st[tt][0].x) + __uint_as_float(st[tt][1].x)) + __uint_as_float(st[tt][2].x));
        const float sq = qsum((__uint_as_float(st[tt][0].y) + __uint_as_float(st[tt][1].y)) + __uint_as_float(st[tt][2].y));
        float rstd;
        ln_stats(sm, sq, mean[tt], rstd);
        mean[tt] = -mean[tt];
        ga[tt] = GA * rstd; gb[tt] = GB * rstd * rstd * rstd;
      }
      f32x4 acc[2][9];
#pragma unroll
      for (int c = 0; c < 9; ++c) { acc[0][c] = f32x4{0, 0, 0, 0}; acc[1][c] = f32x4{0, 0, 0, 0}; }
      unsigned hp[2][2][2];
      f32x4 csv;
      v4u fv;
      auto epi = [&](auto Cc) INL {
        constexpr int c = Cc.value;
        f32x2 z[4];
#pragma unroll
        for (int tt = 0; tt < 2; ++tt) {
          const f32x4 zz = csv * mean[tt] + acc[tt][c];
          z[2 * tt] = f32x2{zz.x, zz.y}; z[2 * tt + 1] = f32x2{zz.z, zz.w};
        }
        SB();
        const float ga4[4] = {ga[0], ga[0], ga[1], ga[1]}, gb4[4] = {gb[0], gb[0], gb[1], gb[1]};
        geluN<4>(z, ga4, gb4);
#pragma unroll
        for (int tt = 0; tt < 2; ++tt) { hp[tt][c & 1][0] = pk(z[2 * tt].x, z[2 * tt].y); hp[tt][c & 1][1] = pk(z[2 * tt + 1].x, z[2 * tt + 1].y); }
        if constexpr (c == 5) { STAMP(18); DRAIN(); arrive(ws, FC1, b, lane); STAMP(19); }
        if constexpr ((c & 1) && c < 8) {
#pragma unroll
          for (int tt = 0; tt < 2; ++tt) {
            const v4u v = {hp[tt][0][0], hp[tt][0][1], hp[tt][1][0], hp[tt][1][1]};
            own[c >> 1][tt] = asfrag(v);
            ST16(v, lane * 16 + tt * (144 * 1024), hdst + (pbase(s) + (c >> 1)) * 1024);
          }
        }
        if constexpr (c == 8) {
#pragma unroll
          for (int tt = 0; tt < 2; ++tt)
            ST8((v2u{hp[tt][0][0], hp[tt][0][1]}), lane * 16 + tt * (144 * 1024) + (s & 1) * 8, hdst + (s < 2 ? 4 : 13) * 1024);
          STAMP(22);
          VMW(4); arrive(ws, FC2, b, lane);
        }
        SB();
      };
      sfor<162>([&](auto I) INL {
        constexpr int ii = I.value, n = FN(ii), c = FC(ii), nx = ii - 1 + PD;
        const bf16x8 cur = cat(lo[ii % PD], hi[ii % PD]);
        acc[0][c] = mfma(cur, xb[n % 12][0], acc[0][c]); SB();
        if constexpr (ii >= 1 && nx < 162) lo[nx % PD] = trd(WADDR(FN(nx), FC(nx)));
        SB();
        acc[1][c] = mfma(cur, xb[n % 12][1], acc[1][c]); SB();
        if constexpr (ii >= 1 && nx < 162) hi[nx % PD] = trd(WADDR(FN(nx), FC(nx)) + 4608);
        SB();
        if constexpr (ii < 108 && c == 8) {
          if constexpr (n < 6) {
#pragma unroll
            for (int tt = 0; tt < 2; ++tt) xb[n][tt] = asfrag(LD16(lane * 16 + tt * (18 * 1024), xsrc + cstep(12 + n, s) * 1024));
          }
          if constexpr (n == 1 || n == 8 || n == 11) { CLOB(); lds_set(L_FREE + 4 * i, n == 1 ? 4 : n == 8 ? 5 : 6, lane); }
          if constexpr (n == 0) STAMP(23);
          if constexpr (n == 3) STAMP(24);
          if constexpr (n == 7) STAMP(25);
          SB();
        }
        if constexpr (ii == 96) { fv = lds4(L_FULL); SB(); }
        if constexpr (ii == 104) { if (min3(fv) < 9) wait_full(9); CLOB(); SB(); STAMP(10); }
        if constexpr (ii >= 108 && n == 14 && c >= 1) { csv = *(const LAS f32x4*)(L_CS3 + (c - 1) * 64 + q * 16); SB(); }
        if constexpr (ii >= 108 && n == 17 && c >= 1) epi(std::integral_constant<int, c - 1>{});
        if constexpr (ii == 150) { fv = lds4(L_FULL); SB(); }
      });
      CLOB();
      lds_set(L_FREE + 4 * i, 7, lane);
      csv = *(const LAS f32x4*)(L_CS3 + 8 * 64 + q * 16);
      if (min3(fv) < 10) wait_full(10);
      CLOB();
#pragma unroll
      for (int k = 0; k < PD; ++k) { lo[k] = trd(WADDR(18, k)); hi[k] = trd(WADDR(18, k) + 4608); }
      SB();
      epi(std::integral_constant<int, 8>{});
      STAMP(11);
    }
    {
      bf16x8 hb[18][2];
#pragma unroll
      for (int m = 0; m < 4; ++m) { hb[m][0] = own[m][0]; hb[m][1] = own[m][1]; }
      auto hload = [&](auto M) INL {
        constexpr int m = M.value;
#pragma unroll
        for (int tt = 0; tt < 2; ++tt) hb[m][tt] = asfrag(LD16(lane * 16 + tt * (144 * 1024), hdst + dstep<m>(s) * 1024));
      };
      wait_word(L_RDY + 4 * R_H1, 1);
      sfor<6>([&](auto M) INL { hload(std::integral_constant<int, 4 + M.value>{}); });
      SB();
      STAMP(12);
      f32x4 acc[2][9];
#pragma unroll
      for (int c = 0; c < 9; ++c) { acc[0][c] = f32x4{0, 0, 0, 0}; acc[1][c] = f32x4{0, 0, 0, 0}; }
      const int pdst = OFF_P + (((g * 4 + s) * 8 + 2 * i) * 8 + j) * (5 * 1024);
      v4u fv;
      unsigned rv;
      auto epi = [&](auto Cc) INL {
        constexpr int c = Cc.value;
        if constexpr ((c & 1) && c < 8) {
#pragma unroll
          for (int tt = 0; tt < 2; ++tt) {
            const f32x4 a0 = acc[tt][c - 1], a1 = acc[tt][c];
            ST16((v4u{pk(a0.x, a0.y), pk(a0.z, a0.w), pk(a1.x, a1.y), pk(a1.z, a1.w)}), lane * 16 + tt * (40 * 1024) + (c >> 1) * 1024, pdst);
          }
        }
        if constexpr (c == 8) {
#pragma unroll
          for (int tt = 0; tt < 2; ++tt) ST16(__builtin_bit_cast(v4u, acc[tt][8]), lane * 16 + tt * (40 * 1024) + 4096, pdst);
        }
        SB();
      };
      sfor<162>([&](auto I) INL {
        constexpr int ii = I.value, n = FN(ii), c = FC(ii), nx = ii - 1 + PD;
        const bf16x8 cur = cat(lo[ii % PD], hi[ii % PD]);
        acc[0][c] = mfma(cur, hb[n][0], acc[0][c]); SB();
        if constexpr (ii >= 1 && nx < 162) lo[nx % PD] = trd(WADDR(18 + FN(nx), FC(nx)));
        SB();
        acc[1][c] = mfma(cur, hb[n][1], acc[1][c]); SB();
        if constexpr (ii >= 1 && nx < 162) hi[nx % PD] = trd(WADDR(18 + FN(nx), FC(nx)) + 4608);
        SB();
        if constexpr (ii == 17) { CLOB(); lds_set(L_FREE + 4 * i, 8, lane); SB(); }
        if constexpr (ii == 35) { VMW(12); arrive(ws, FC3, b, lane); SB(); }
        if constexpr (ii == 50) { rv = ldsw(L_RDY + 4 * R_H2); SB(); }
        if constexpr (ii == 56) {
          if (rfl(rv) == 0) wait_word(L_RDY + 4 * R_H2, 1);
          CLOB();
          sfor<3>([&](auto M) INL { hload(std::integral_constant<int, 10 + M.value>{}); });
          SB();
          STAMP(13);
        }
        if constexpr (ii == 72) { fv = lds4(L_FULL); SB(); }
        if constexpr (ii == 80) { if (min3(fv) < 11) wait_full(11); CLOB(); SB(); }
        if constexpr (ii == 84) { rv = ldsw(L_RDY + 4 * R_H3); SB(); }
        if constexpr (ii == 90) {
          if (rfl(rv) == 0) wait_word(L_RDY + 4 * R_H3, 1);
          CLOB();
          sfor<5>([&](auto M) INL { hload(std::integral_constant<int, 13 + M.value>{}); });
          SB();
          STAMP(14);
        }
        if constexpr (ii == 96) { fv = lds4(L_FULL); SB(); }
        if constexpr (ii == 104) { if (min3(fv) < 12) wait_full(12); CLOB(); SB(); }
        if constexpr (ii >= 108 && n == 17 && c >= 1) epi(std::integral_constant<int, c - 1>{});
      });
      epi(std::integral_constant<int, 8>{});
      STAMP(15);
      DRAIN();
      arrive(ws, FD, b, lane);
      STAMP(16);
    }
  } else if (w < 7) {
    const int ld = w - 4;
    const unsigned fullw = L_FULL + 4 * ld;
    unsigned v3[3], v4[3];
#pragma unroll
    for (int e = 0; e < 3; ++e) {
      const int idx = (3 * ld + e) * 64 + lane, row = idx / 18, ch = idx % 18;
      const int sc = (ch + 2 * (s & 1)) % 18;
      v3[e] = row * (F_ * 2) + sc * 16;
      v4[e] = row * (D_ * 2) + sc * 16;
    }
    const char* w3b = w3 + (576 * j + 144 * s) * 2;
    const char* w4b = w4 + (size_t)(576 * j) * (D_ * 2) + 144 * s * 2;
    auto w3slice = [&](int n) INL {
#pragma unroll
      for (int e = 0; e < 3; ++e) dma(w3b + (size_t)cstep(n, s) * (32 * F_ * 2), v3[e], (n & 15) * SLOT + (3 * ld + e) * 1024);
    };
    auto w4slice = [&](auto M) INL {
      constexpr int m = M.value;
      const char* src = w4b + dstep<m>(s) * (32 * D_ * 2);
#pragma unroll
      for (int e = 0; e < 3; ++e) dma(src, v4[e], ((18 + m) & 15) * SLOT + (3 * ld + e) * 1024);
    };
    const bf16x8 ones = {0x3F80, 0x3F80, 0x3F80, 0x3F80, 0x3F80, 0x3F80, 0x3F80, 0x3F80};
    f32x4 cs[3] = {{0, 0, 0, 0}, {0, 0, 0, 0}, {0, 0, 0, 0}};
    const unsigned cb = (4 * q + (t >> 2)) * 288 + (t & 3) * 8 + ld * 96;
    auto colsum3 = [&](int n0, int n1) INL {
      _Pragma("nounroll") for (int n = n0; n < n1; ++n) {
        const unsigned a = cb + (n & 15) * SLOT;
#pragma unroll
        for (int c = 0; c < 3; ++c) cs[c] = mfma(cat(trd(a + c * 32), trd(a + 4608 + c * 32)), ones, cs[c]);
      }
      CLOB();
    };
    if (ab) {
      const int idx = ld * 64 + lane, row = idx / 6, ch = idx % 6;
      const int sc = (ch + 2 * (ct & 1)) % 6;
      const int krow = 8 * ((row >> 2) & 3) + 4 * (row >> 4) + (row & 3);
      const unsigned v1 = krow * (D_ * 2) + sc * 16, v2 = row * (D_ * 2) + sc * 16;
      _Pragma("nounroll") for (int u = 0; u < 18; ++u) dma(w1 + 96 * ct + u * (32 * D_ * 2), v1, 4 * SLOT + u * 3072 + ld * 1024);
      STAMP(1);
      VMW(9); lds_set(fullw, 1, lane);
      wait_free(1);
      _Pragma("nounroll") for (int u = 0; u < 6; ++u) dma(w2 + 96 * ct + u * (32 * D_ * 2), v2, 10 * SLOT + u * 3072 + ld * 1024);
      VMW(6); lds_set(fullw, 2, lane);
      _Pragma("nounroll") for (int u = 6; u < 18; ++u) dma(w2 + 96 * ct + u * (32 * D_ * 2), v2, 10 * SLOT + u * 3072 + ld * 1024);
      STAMP(2);
      wait_full(2);
      {
        f32x4 c1 = {0, 0, 0, 0};
        const unsigned a = 4 * SLOT + (4 * q + (t >> 2)) * 96 + (t & 3) * 8 + ld * 32;
        s16x4 l1[18], h1[18];
#pragma unroll
        for (int u = 0; u < 18; ++u) { l1[u] = trd(a + u * 3072); h1[u] = trd(a + u * 3072 + 1536); }
        SB();
#pragma unroll
        for (int u = 0; u < 18; ++u) c1 = mfma(cat(l1[u], h1[u]), ones, c1);
        if (t == 0) *(LAS f32x4*)(L_CS1 + ld * 64 + q * 16) = c1;
        CLOB();
      }
      lds_set(fullw, 3, lane);
      STAMP(3);
      DRAIN(); lds_set(fullw, 4, lane);
      wait_free(2); wait_full(3);
      _Pragma("nounroll") for (int n = 0; n < 10; ++n) w3slice(n);
      STAMP(4);
      DRAIN(); lds_set(fullw, 5, lane);
      wait_full(5);
      colsum3(0, 10);
      wait_free(3);
      _Pragma("nounroll") for (int n = 10; n < 16; ++n) w3slice(n);
      STAMP(5);
      DRAIN(); lds_set(fullw, 6, lane);
      STAMP(6);
      wait_full(6);
      colsum3(10, 16);
    } else {
      _Pragma("nounroll") for (int n = 0; n < 16; ++n) w3slice(n);
      DRAIN(); lds_set(fullw, 6, lane);
      wait_full(6);
      colsum3(0, 16);
    }
    lds_set(fullw, 7, lane);
    STAMP(7);
    wait_free(4); wait_full(7);
    STAMP(11);
    w3slice(16); w3slice(17);
    DRAIN(); lds_set(fullw, 8, lane);
    STAMP(12);
    wait_full(8);
    colsum3(16, 18);
    if (t == 0) {
#pragma unroll
      for (int c = 0; c < 3; ++c) *(LAS f32x4*)(L_CS3 + (3 * ld + c) * 64 + q * 16) = cs[c];
    }
    CLOB();
    lds_set(fullw, 9, lane);
    STAMP(8);
    wait_free(5);
    sfor<7>([&](auto M) INL { w4slice(M); });
    wait_free(6);
    sfor<3>([&](auto M) INL { w4slice(std::integral_constant<int, 7 + M.value>{}); });
    DRAIN(); lds_set(fullw, 10, lane);
    STAMP(9);
    wait_free(7); wait_full(9);
    sfor<6>([&](auto M) INL { w4slice(std::integral_constant<int, 10 + M.value>{}); });
    wait_free(8);
    sfor<2>([&](auto M) INL { w4slice(std::integral_constant<int, 16 + M.value>{}); });
    VMW(6); lds_set(fullw, 11, lane);
    DRAIN(); lds_set(fullw, 12, lane);
    STAMP(10);
  } else {
    const u64 tag = __hip_atomic_load((GAS u64*)(ws + OFF_EPOCH), __ATOMIC_RELAXED, __HIP_MEMORY_SCOPE_AGENT) + 1ull;
    if (lane == 0) {
      *(volatile LAS u64*)L_TAG = tag;
      *(volatile LAS unsigned*)(L_RDY + 4 * R_TAG) = 1u;
      __hip_atomic_store(flagp(ws, FS, b), tag, __ATOMIC_RELAXED, __HIP_MEMORY_SCOPE_AGENT);
    }
    STAMP(1);
    auto poll = [&](int kind, int wg, bool need) INL {
      GAS u64* p = flagp(ws, kind, wg);
      _Pragma("nounroll") for (unsigned n = 0; n < GLB_CAP; ++n) {
        const u64 v = __hip_atomic_load(p, __ATOMIC_RELAXED, __HIP_MEMORY_SCOPE_AGENT);
        if (__all(!need || v == tag)) break;
        __builtin_amdgcn_s_sleep(1);
      }
      CLOB();
    };
    if (ab) {
      poll(FA, j + 8 * ((r & 1) + 2 * (lane % 12)), true);
      lds_set(L_RDY + 4 * R_H, 1, lane);
    }
    STAMP(2);
    {
      GAS u64* p = flagp(ws, FB, g + 8 * (lane % 24));
      bool d0 = false, d1 = false;
      _Pragma("nounroll") for (unsigned n = 0; n < GLB_CAP; ++n) {
        const u64 v = __hip_atomic_load(p, __ATOMIC_RELAXED, __HIP_MEMORY_SCOPE_AGENT);
        const bool ok = v == tag;
        if (!d0 && __all(ok || (lane & 1))) { d0 = true; lds_set(L_RDY + 4 * R_X0, 1, lane); }
        if (!d1 && __all(ok || !(lane & 1))) { d1 = true; lds_set(L_RDY + 4 * R_X1, 1, lane); }
        if (d0 && d1) break;
        __builtin_amdgcn_s_sleep(1);
      }
      lds_set(L_RDY + 4 * R_X0, 1, lane); lds_set(L_RDY + 4 * R_X1, 1, lane);
    }
    STAMP(3);
    if (b == 0) {
#pragma unroll
      for (int k = 0; k < 4; ++k) poll(FS, k * 64 + lane, true);
      if (lane == 0) __hip_atomic_store((GAS u64*)(ws + OFF_EPOCH), tag, __ATOMIC_RELAXED, __HIP_MEMORY_SCOPE_AGENT);
    }
    const int peer = j + 8 * (4 * g + (lane & 3));
    poll(FC1, peer, true); lds_set(L_RDY + 4 * R_H1, 1, lane);
    STAMP(4);
    poll(FC2, peer, true); lds_set(L_RDY + 4 * R_H2, 1, lane);
    STAMP(5);
    poll(FC3, peer, true); lds_set(L_RDY + 4 * R_H3, 1, lane);
    STAMP(6);
  }
  if (w >= 3) {
    const int p = w - 3;
    const int T = 8 * g + j;
    if (w != 7) { wait_word(L_RDY + 4 * R_X0, 1); wait_word(L_RDY + 4 * R_X1, 1); }
    v2u st[3];
#pragma unroll
    for (int k = 0; k < 3; ++k) st[k] = LD8(OFF_STAT + (3 * q + k) * 128 + t * 8, T * (12 * 128));
    const int u = p < 4 ? pbase(s) + p : (s < 2 ? 4 : 13);
    const v4u xv = LD16(lane * 16, OFF_X1 + (T * 18 + u) * 1024);
    float mean, rstd;
    ln_stats(qsum((__uint_as_float(st[0].x) + __uint_as_float(st[1].x)) + __uint_as_float(st[2].x)),
             qsum((__uint_as_float(st[0].y) + __uint_as_float(st[1].y)) + __uint_as_float(st[2].y)), mean, rstd);
    if (w == 7) {
      const u64 tag = *(volatile LAS u64*)L_TAG;
      GAS u64* fp = flagp(ws, FD, (lane & 7) + 8 * (4 * g + s));
      _Pragma("nounroll") for (unsigned n = 0; n < GLB_CAP; ++n) {
        const u64 v = __hip_atomic_load(fp, __ATOMIC_RELAXED, __HIP_MEMORY_SCOPE_AGENT);
        if (__all(v == tag)) break;
        __builtin_amdgcn_s_sleep(1);
      }
      CLOB();
      lds_set(L_RDY + 4 * R_P, 1, lane);
    } else {
      wait_word(L_RDY + 4 * R_P, 1);
    }
    STAMP(20);
    v4u pv[8];
    const int psrc = OFF_P + (((g * 4 + s) * 8 + j) * 8 * 5 + p) * 1024;
#pragma unroll
    for (int k = 0; k < 8; ++k) pv[k] = LD16(lane * 16 + k * (5 * 1024), psrc);
    char* orow = out + (size_t)(16 * T + t) * (D_ * 2) + (32 * u + 4 * q) * 2;
    if (p < 4) {
      float a[8];
#pragma unroll
      for (int e = 0; e < 8; ++e) a[e] = 0.f;
#pragma unroll
      for (int k = 0; k < 8; ++k)
#pragma unroll
        for (int e = 0; e < 4; ++e) { const unsigned d = pv[k][e]; a[2 * e] += lo16(d); a[2 * e + 1] += hi16(d); }
      unsigned o[4];
#pragma unroll
      for (int e = 0; e < 4; ++e) {
        const unsigned h2 = pk(a[2 * e] * rstd, a[2 * e + 1] * rstd), xe = xv[e];
        o[e] = pk(lo16(xe) + lo16(h2), hi16(xe) + hi16(h2));
      }
      *(GAS v2u*)orow = v2u{o[0], o[1]};
      *(GAS v2u*)(orow + 32) = v2u{o[2], o[3]};
    } else {
      f32x4 a = {0, 0, 0, 0};
#pragma unroll
      for (int k = 0; k < 8; ++k) a += __builtin_bit_cast(f32x4, pv[k]);
      const unsigned x0 = (s & 1) ? xv.z : xv.x, x1v = (s & 1) ? xv.w : xv.y;
      const unsigned h0 = pk(a.x * rstd, a.y * rstd), h1 = pk(a.z * rstd, a.w * rstd);
      *(GAS v2u*)(orow + (s & 1) * 32) = v2u{pk(lo16(x0) + lo16(h0), hi16(x0) + hi16(h0)), pk(lo16(x1v) + lo16(h1), hi16(x1v) + hi16(h1))};
    }
    STAMP(21);
  }
#endif
}
}

extern "C" void kernel_launch(void* const* d_in, const int* in_sizes, int n_in, void* d_out, int out_size, void* d_ws, size_t ws_size,
                              hipStream_t stream) {
  mx::mixer_kernel<<<256, 512, 0, stream>>>((const char*)d_in[0], (const char*)d_in[1], (const char*)d_in[2], (const char*)d_in[3],
                                            (const char*)d_in[4], (char*)d_out, (char*)d_ws);
}
```

```cpp
#define PRE 7
#include <hip/hip_runtime.h>
#include <utility>

#define STAMPS 0

#define LAS __attribute__((address_space(3)))
#define GAS __attribute__((address_space(1)))
#define DI __device__ __forceinline__
typedef unsigned v4u __attribute__((ext_vector_type(4)));
typedef unsigned v2u __attribute__((ext_vector_type(2)));
typedef float f32x4 __attribute__((ext_vector_type(4)));
typedef float f32x2 __attribute__((ext_vector_type(2)));
typedef short bf16x8 __attribute__((ext_vector_type(8)));
typedef short s16x4 __attribute__((ext_vector_type(4)));
typedef __bf16 bf16x2_t __attribute__((ext_vector_type(2)));
typedef unsigned long long u64;

namespace mx {
constexpr int D_ = 576, F_ = 4608;
constexpr int OFF_EPOCH = 0;
constexpr int OFF_FLAGS = 4096;
constexpr int OFF_STAT = 262144;
constexpr int OFF_H1 = 524288;
constexpr int OFF_X1 = OFF_H1 + 64 * 18 * 1024;
constexpr int OFF_HH = OFF_X1 + 64 * 18 * 1024;
constexpr int OFF_P = OFF_HH + 64 * 144 * 1024;
constexpr int OFF_DBG = OFF_P + 8 * 4 * 8 * 8 * 5 * 1024;
constexpr int WS_BYTES = OFF_DBG + 2 * 256 * 8 * 32 * 8;
enum { FS = 0, FA = 1, FB = 2, FC1 = 3, FC2 = 4, FC3 = 5, FD = 6 };
constexpr int SLOT = 9216, MISC = 16 * SLOT;
constexpr int L_FULL = MISC, L_FREE = MISC + 16, L_RDY = MISC + 32  , L_CNT = MISC + 64  , L_TAG = MISC + 96;
constexpr int L_CS1 = MISC + 256, L_CS3 = MISC + 512, L_DBG = MISC + 2048;
enum { R_H = 0, R_X0 = 1, R_X1 = 2, R_H1 = 3, R_H2 = 4, R_H3 = 5, R_P = 6, R_TAG = 7 };
constexpr unsigned LDS_CAP = 1u << 21, GLB_CAP = 1u << 16;
constexpr int PD = 4;

template <class F, int... I> DI void sfor_impl(F&& f, std::integer_sequence<int, I...>) { (f(std::integral_constant<int, I>{}), ...); }
template <int N, class F> DI void sfor(F&& f) { sfor_impl(f, std::make_integer_sequence<int, N>{}); }
#define INL __attribute__((always_inline))
#define SB() __builtin_amdgcn_sched_barrier(0)
#define CLOB() asm volatile("" ::: "memory")
#define DRAIN() asm volatile("s_waitcnt vmcnt(0)" ::: "memory")
#define VMW(n) asm volatile("s_waitcnt vmcnt(%0)" ::"i"(n) : "memory")

DI unsigned rfl(unsigned v) { return __builtin_amdgcn_readfirstlane(v); }
DI f32x4 mfma(bf16x8 a, bf16x8 b, f32x4 c) { return __builtin_amdgcn_mfma_f32_16x16x32_bf16(a, b, c, 0, 0, 0); }
DI bf16x8 asfrag(v4u v) { return __builtin_bit_cast(bf16x8, v); }
DI s16x4 trd(unsigned addr) { return __builtin_amdgcn_ds_read_tr16_b64_v4i16((LAS s16x4*)addr); }
DI bf16x8 cat(s16x4 lo, s16x4 hi) { return __builtin_shufflevector(lo, hi, 0, 1, 2, 3, 4, 5, 6, 7); }
DI unsigned pk(float a, float b) { bf16x2_t r = __builtin_convertvector(f32x2{a, b}, bf16x2_t); return __builtin_bit_cast(unsigned, r); }
DI float lo16(unsigned p) { return __uint_as_float(p << 16); }
DI float hi16(unsigned p) { return __uint_as_float(p & 0xffff0000u); }
DI float bfr(float a) { return lo16(pk(a, a)); }
DI float qsum(float v) {
  auto r = __builtin_amdgcn_permlane16_swap(__float_as_uint(v), __float_as_uint(v), false, false);
  float a = __uint_as_float(r[0]) + __uint_as_float(r[1]);
  auto r2 = __builtin_amdgcn_permlane32_swap(__float_as_uint(a), __float_as_uint(a), false, false);
  return __uint_as_float(r2[0]) + __uint_as_float(r2[1]);
}
DI void ln_stats(float s, float ss, float& mean, float& rstd) {
  const float m = s * (1.0f / D_);
  float var = __builtin_fmaf(-m, m, ss * (1.0f / D_));
  var = var > 0.f ? var : 0.f;
  mean = bfr(m);
  const float sd = bfr(__builtin_sqrtf(bfr(bfr(var) + 1e-5f)));
  rstd = 1.0f / sd;
}
constexpr float GA = -2.302208198144325f;
constexpr float GB = -0.1029432397357593f;
template <int N> DI void geluN(f32x2 (&z)[N], const float (&a)[N], const float (&b)[N]) {
  f32x2 p[N];
#pragma unroll
  for (int k = 0; k < N; ++k) p[k] = z[k] * z[k];
  SB();
#pragma unroll
  for (int k = 0; k < N; ++k) p[k] = p[k] * b[k] + a[k];
  SB();
#pragma unroll
  for (int k = 0; k < N; ++k) p[k] = z[k] * p[k];
  SB();
#pragma unroll
  for (int k = 0; k < N; ++k) { p[k].x = __builtin_amdgcn_exp2f(p[k].x); p[k].y = __builtin_amdgcn_exp2f(p[k].y); }
  SB();
#pragma unroll
  for (int k = 0; k < N; ++k) p[k] = p[k] + 1.0f;
  SB();
#pragma unroll
  for (int k = 0; k < N; ++k) { p[k].x = __builtin_amdgcn_rcpf(p[k].x); p[k].y = __builtin_amdgcn_rcpf(p[k].y); }
  SB();
#pragma unroll
  for (int k = 0; k < N; ++k) z[k] = z[k] * p[k];
  SB();
}

DI unsigned ldsw(int addr) { return *(volatile LAS unsigned*)addr; }
DI v4u lds4(int addr) { return *(volatile LAS v4u*)addr; }
DI void lds_set(int addr, unsigned v, int lane) { if (lane == 0) *(volatile LAS unsigned*)addr = v; }
DI void wait_word(int addr, unsigned v) {
  _Pragma("nounroll") for (unsigned n = 0; n < LDS_CAP; ++n) { if (rfl(ldsw(addr)) >= v) break; __builtin_amdgcn_s_sleep(1); }
  CLOB();
}
DI unsigned min3(v4u x) { return rfl(min(min(x.x, x.y), x.z)); }
DI unsigned min4(v4u x) { return rfl(min(min(x.x, x.y), min(x.z, x.w))); }
DI void wait_full(unsigned v) {
  _Pragma("nounroll") for (unsigned n = 0; n < LDS_CAP; ++n) { if (min3(lds4(L_FULL)) >= v) break; __builtin_amdgcn_s_sleep(1); }
  CLOB();
}
DI void wait_free(unsigned v) {
  _Pragma("nounroll") for (unsigned n = 0; n < LDS_CAP; ++n) { if (min4(lds4(L_FREE)) >= v) break; __builtin_amdgcn_s_sleep(1); }
  CLOB();
}
DI GAS u64* flagp(char* ws, int kind, int b) { return (GAS u64*)(ws + OFF_FLAGS + (kind * 256 + b) * 128); }
DI void arrive(char* ws, int kind, int b, int lane) {
  if (lane == 0) {
    const unsigned old = __hip_atomic_fetch_add((LAS unsigned*)(L_CNT + 4 * kind), 1u, __ATOMIC_RELAXED, __HIP_MEMORY_SCOPE_WORKGROUP);
    if (old == 3u) {
      _Pragma("nounroll") for (unsigned n = 0; n < LDS_CAP; ++n) { if (ldsw(L_RDY + 4 * R_TAG) != 0u) break; __builtin_amdgcn_s_sleep(1); }
      const u64 tag = *(volatile LAS u64*)L_TAG;
      __hip_atomic_store(flagp(ws, kind, b), tag, __ATOMIC_RELAXED, __HIP_MEMORY_SCOPE_AGENT);
    }
  }
}
#define LD16(off, soff) __builtin_amdgcn_raw_buffer_load_b128(rs, (off), (soff), 16)
#define LD8(off, soff) __builtin_amdgcn_raw_buffer_load_b64(rs, (off), (soff), 16)
#define ST16(v, off, soff) __builtin_amdgcn_raw_buffer_store_b128((v), rs, (off), (soff), 16)
#define ST8(v, off, soff) __builtin_amdgcn_raw_buffer_store_b64((v), rs, (off), (soff), 16)
DI void dma(const char* sbase, unsigned voff, unsigned ldsaddr) {
  asm volatile("s_mov_b32 m0, %0\n\ts_nop 0\n\tglobal_load_lds_dwordx4 %1, %2" ::"s"(ldsaddr), "v"(voff), "s"(sbase) : "memory");
}
DI int cstep(int n, int s) { const int u = n + ((9 * s) >> 1); return u >= 18 ? u - 18 : u; }
DI int pbase(int s) { return (9 * s + (s & 1)) >> 1; }
template <int M> DI int dstep(int s) {
  if constexpr (M < 4) return pbase(s) + M;
  else if constexpr (M < 10) return pbase((s + 1 + (M - 4) / 2) & 3) + ((M - 4) & 1);
  else if constexpr (M < 13) return pbase((s + M - 9) & 3) + 2;
  else if constexpr (M < 16) return pbase((s + M - 12) & 3) + 3;
  else return M == 16 ? 4 : 13;
}
#define STAMP(k) do { } while (0)

__global__ __launch_bounds__(512, 2) void mixer_kernel(const char* __restrict__ x, const char* __restrict__ w1, const char* __restrict__ w2,
                                                       const char* __restrict__ w3, const char* __restrict__ w4, char* __restrict__ out, char* ws) {
#if defined(__HIP_DEVICE_COMPILE__)
  __shared__ __attribute__((aligned(16))) char smem[163840];
  const int tid = threadIdx.x, lane = tid & 63, w = rfl(tid >> 6);
  const int t = lane & 15, q = lane >> 4;
  const int b = blockIdx.x, j = b & 7, r = b >> 3;
  const bool ab = r < 24;
  const int rt = 2 * j + (r & 1), ct = r >> 1;
  const int g = r >> 2, s = r & 3;
  const __amdgpu_buffer_rsrc_t rs = __builtin_amdgcn_make_buffer_rsrc(ws, 0, WS_BYTES, 0x00020000);

  {
    const u64 tstart = __builtin_amdgcn_s_memrealtime();
    unsigned acc_ = 0;
    const int gt = b * 512 + tid;
    {
      const int xt = r * 512 + tid;
      const char* ptrs[5] = {x, w1, w2, w3, w4};
      const int lens[5] = {1024 * 576 * 2, 576 * 576 * 2, 576 * 576 * 2, 576 * 4608 * 2, 576 * 4608 * 2};
#pragma unroll
      for (int k = 0; k < 5; ++k)
        for (int o = xt * 128; o < lens[k]; o += 16384 * 128) acc_ ^= *(const GAS unsigned*)(ptrs[k] + o);
    }
    {
      const int xt = r * 512 + tid;
      if (xt * 4096 < OFF_DBG) acc_ ^= *(const GAS unsigned*)(ws + (size_t)xt * 4096 + 2048);
      if (xt * 4096 < 1024 * 576 * 2) acc_ ^= *(const GAS unsigned*)(out + (size_t)xt * 4096);
    }
    {
      const char* pc = (const char*)(__builtin_amdgcn_s_getpc() & ~255ull);
      if (tid * 64 < 39000 - 512) acc_ ^= *(const GAS unsigned*)(pc + tid * 64);
      if ((tid + 512) * 64 < 39000 - 512) acc_ ^= *(const GAS unsigned*)(pc + (tid + 512) * 64);
    }
    asm volatile("" ::"v"(acc_), "v"(gt));
    for (int n = 0; n < 100000; ++n) { if (__builtin_amdgcn_s_memrealtime() - tstart >= 2000ull) break; __builtin_amdgcn_s_sleep(2); }
  }
  if (tid < 32) *(volatile LAS unsigned*)(MISC + tid * 4) = 0u;
  asm volatile("" ::"v"(smem));
  STAMP(0);
  asm volatile("s_waitcnt lgkmcnt(0)\n\ts_barrier" ::: "memory");

  if (w < 4) {
    const int i = w;
    if (ab) {
      unsigned ba = 4 * SLOT + (4 * q + (t >> 2)) * 96 + (t & 3) * 8;
      const int T = 4 * rt + i, odd = ct & 1;
      const int upair = (3 * ct + odd) >> 1, usingle = odd ? (3 * ct - 1) >> 1 : (3 * ct + 2) >> 1;
      v2u xres[3];
      {
        const char* xsrc = x + (size_t)T * (16 * D_ * 2);
        const unsigned xl = (i == 3 ? 2 : 10 + 2 * i) * SLOT;
#pragma unroll
        for (int e = 0; e < 18; ++e) {
          const unsigned f = (((unsigned)(lane + 64 * e) * 911u) >> 17) & 7u;
          dma(xsrc + e * 1024, (lane * 16) ^ (f << 4), xl + e * 1024);
        }
        unsigned xa = xl + t * (D_ * 2), xs = ((t >> 1) & 7) << 4;
        unsigned xe0 = xa + ((q * 16) ^ xs), xe1 = xa + ((64 + q * 16) ^ xs);
        const int grp = ct / 3;
        const int sh = grp == 0 ? 1 : grp == 1 ? -1 : grp == 2 ? 2 : -2;
        const int ts = 16 * (i & 1) + t + sh;
        const bool keep = ts >= 0 && ts < 32;
        const int dst = OFF_H1 + ((T & ~1) + ((ts >> 4) & 1)) * (18 * 1024) + ((ts & 15) + 16 * q) * 16;
        asm volatile("" : "+v"(xe0), "+v"(xe1));
        DRAIN();
        STAMP(1);
        bf16x8 xf[18];
#pragma unroll
        for (int u = 0; u < 18; ++u) xf[u] = *(const LAS bf16x8*)(((u & 1) ? xe1 : xe0) + (u >> 1) * 128);
#pragma unroll
        for (int l = 0; l < 3; ++l) {
          const unsigned cb = (48 * ct + 16 * ((l + odd) % 3) + 4 * q) * 2;
          xres[l] = *(const LAS v2u*)(xa + (cb ^ xs));
        }
        CLOB();
        lds_set(L_FREE + 4 * i, 1, lane);
        const bf16x8 ones = {0x3F80, 0x3F80, 0x3F80, 0x3F80, 0x3F80, 0x3F80, 0x3F80, 0x3F80};
        f32x4 aS = {0, 0, 0, 0}, aG = {0, 0, 0, 0};
#pragma unroll
        for (int u = 0; u < 18; ++u) { aS = mfma(ones, xf[u], aS); aG = mfma(xf[u], xf[u], aG); }
        STAMP(17);
        f32x4 acc[3] = {{0, 0, 0, 0}, {0, 0, 0, 0}, {0, 0, 0, 0}};
        float nmean, rstd;
        sfor<2>([&](auto H) INL {
          constexpr int h = H.value;
          wait_full(h + 1);
          asm volatile("" : "+v"(ba));
#pragma unroll
          for (int u = 9 * h; u < 9 * h + 9; ++u)
#pragma unroll
            for (int l = 0; l < 3; ++l) acc[l] = mfma(cat(trd(ba + u * 3072 + l * 32), trd(ba + u * 3072 + 1536 + l * 32)), xf[u], acc[l]);
          if constexpr (h == 0) {
            const int e = t & 3;
            float gd = e == 0 ? aG[0] : e == 1 ? aG[1] : e == 2 ? aG[2] : aG[3];
            gd = (q == (t >> 2)) ? gd : 0.f;
            ln_stats(aS[0], qsum(gd), nmean, rstd);
            nmean = -nmean;
          }
        });
        STAMP(2);
        const float ga = GA * rstd, gb = GB * rstd * rstd * rstd;
        wait_full(3);
        f32x2 z[6];
#pragma unroll
        for (int l = 0; l < 3; ++l) {
          const f32x4 cs = *(const LAS f32x4*)(L_CS1 + l * 64 + q * 16);
          const f32x4 zz = cs * nmean + acc[l];
          z[2 * l] = f32x2{zz.x, zz.y}; z[2 * l + 1] = f32x2{zz.z, zz.w};
        }
        const float ga6[6] = {ga, ga, ga, ga, ga, ga}, gb6[6] = {gb, gb, gb, gb, gb, gb};
        geluN<6>(z, ga6, gb6);
        unsigned hp[6];
#pragma unroll
        for (int k = 0; k < 6; ++k) { const f32x2 v = z[k] * rstd; hp[k] = keep ? pk(v.x, v.y) : 0u; }
        ST16((v4u{hp[0], hp[1], hp[2], hp[3]}), dst, upair * 1024);
        ST8((v2u{hp[4], hp[5]}), dst + odd * 8, usingle * 1024);
        STAMP(3);
        DRAIN();
        lds_set(L_FREE + 4 * i, 2, lane);
        arrive(ws, FA, b, lane);
        STAMP(4);
      }
      {
        wait_full(4);
        wait_word(L_RDY + 4 * R_H, 1);
        STAMP(5);
        bf16x8 hf[18];
#pragma unroll
        for (int u = 0; u < 18; ++u) hf[u] = asfrag(LD16(lane * 16 + u * 1024, OFF_H1 + T * (18 * 1024)));
        SB();
        unsigned bb = ba + 6 * SLOT;
        asm volatile("" : "+v"(bb));
        f32x4 acc[3] = {{0, 0, 0, 0}, {0, 0, 0, 0}, {0, 0, 0, 0}};
#pragma unroll
        for (int u = 0; u < 18; ++u)
#pragma unroll
          for (int l = 0; l < 3; ++l) acc[l] = mfma(cat(trd(bb + u * 3072 + l * 32), trd(bb + u * 3072 + 1536 + l * 32)), hf[u], acc[l]);
        STAMP(6);
        unsigned xp[6];
        float sm = 0.f, sq = 0.f;
#pragma unroll
        for (int l = 0; l < 3; ++l) {
          const unsigned p0 = pk(acc[l].x, acc[l].y), p1 = pk(acc[l].z, acc[l].w);
          xp[2 * l] = pk(lo16(xres[l].x) + lo16(p0), hi16(xres[l].x) + hi16(p0));
          xp[2 * l + 1] = pk(lo16(xres[l].y) + lo16(p1), hi16(xres[l].y) + hi16(p1));
          const float v0 = lo16(xp[2 * l]), v1 = hi16(xp[2 * l]), v2 = lo16(xp[2 * l + 1]), v3 = hi16(xp[2 * l + 1]);
          sm += (v0 + v1) + (v2 + v3);
          sq += (v0 * v0 + v1 * v1) + (v2 * v2 + v3 * v3);
        }
        const int dst = OFF_X1 + T * (18 * 1024) + lane * 16;
        ST16((v4u{xp[0], xp[1], xp[2], xp[3]}), dst, upair * 1024);
        ST8((v2u{xp[4], xp[5]}), dst + odd * 8, usingle * 1024);
        sm = qsum(sm); sq = qsum(sq);
        if (q == 0) ST8((v2u{__float_as_uint(sm), __float_as_uint(sq)}), OFF_STAT + t * 8, (T * 12 + ct) * 128);
        STAMP(7);
        DRAIN();
        lds_set(L_FREE + 4 * i, 3, lane);
        arrive(ws, FB, b, lane);
        STAMP(8);
      }
    }
    unsigned b0 = (4 * q + (t >> 2)) * 288 + (t & 3) * 8, b1 = b0 + 6 * SLOT, b2 = b0 + 12 * SLOT;
    asm volatile("" : "+v"(b0), "+v"(b1), "+v"(b2));
#define WADDR(z, c) ((((z) % 16) < 6 ? b0 : ((z) % 16) < 12 ? b1 : b2) + (((z) % 16) % 6) * SLOT + (c) * 32)
#define FN(i_) ((i_) < 108 ? (i_) / 9 : 12 + ((i_) - 108) % 6)
#define FC(i_) ((i_) < 108 ? (i_) % 9 : ((i_) - 108) / 6)
    const int T0 = 8 * g + 2 * i;
    const int hdst = OFF_HH + (T0 * 144 + j * 18) * 1024;
    bf16x8 own[4][2];
    s16x4 lo[PD], hi[PD];
    {
      wait_full(6);
      wait_word(L_RDY + 4 * (R_X0 + (i >> 1)), 1);
      STAMP(9);
      v2u st[2][3];
#pragma unroll
      for (int tt = 0; tt < 2; ++tt)
#pragma unroll
        for (int k = 0; k < 3; ++k) st[tt][k] = LD8(OFF_STAT + (3 * q + k) * 128 + t * 8, (T0 + tt) * (12 * 128));
      const int xsrc = OFF_X1 + T0 * (18 * 1024);
      bf16x8 xb[12][2];
#pragma unroll
      for (int n = 0; n < 12; ++n)
#pragma unroll
        for (int tt = 0; tt < 2; ++tt) xb[n][tt] = asfrag(LD16(lane * 16 + tt * (18 * 1024), xsrc + cstep(n, s) * 1024));
      SB();
#pragma unroll
      for (int k = 0; k < PD; ++k) { lo[k] = trd(WADDR(0, k)); hi[k] = trd(WADDR(0, k) + 4608); }
      float mean[2], ga[2], gb[2];
#pragma unroll
      for (int tt = 0; tt < 2; ++tt) {
        const float sm = qsum((__uint_as_float(st[tt][0].x) + __uint_as_float(st[tt][1].x)) + __uint_as_float(st[tt][2].x));
        const float sq = qsum((__uint_as_float(st[tt][0].y) + __uint_as_float(st[tt][1].y)) + __uint_as_float(st[tt][2].y));
        float rstd;
        ln_stats(sm, sq, mean[tt], rstd);
        mean[tt] = -mean[tt];
        ga[tt] = GA * rstd; gb[tt] = GB * rstd * rstd * rstd;
      }
      f32x4 acc[2][9];
#pragma unroll
      for (int c = 0; c < 9; ++c) { acc[0][c] = f32x4{0, 0, 0, 0}; acc[1][c] = f32x4{0, 0, 0, 0}; }
      unsigned hp[2][2][2];
      f32x4 csv;
      v4u fv;
      auto epi = [&](auto Cc) INL {
        constexpr int c = Cc.value;
        f32x2 z[4];
#pragma unroll
        for (int tt = 0; tt < 2; ++tt) {
          const f32x4 zz = csv * mean[tt] + acc[tt][c];
          z[2 * tt] = f32x2{zz.x, zz.y}; z[2 * tt + 1] = f32x2{zz.z, zz.w};
        }
        SB();
        const float ga4[4] = {ga[0], ga[0], ga[1], ga[1]}, gb4[4] = {gb[0], gb[0], gb[1], gb[1]};
        geluN<4>(z, ga4, gb4);
#pragma unroll
        for (int tt = 0; tt < 2; ++tt) { hp[tt][c & 1][0] = pk(z[2 * tt].x, z[2 * tt].y); hp[tt][c & 1][1] = pk(z[2 * tt + 1].x, z[2 * tt + 1].y); }
        if constexpr (c == 5) { STAMP(18); DRAIN(); arrive(ws, FC1, b, lane); STAMP(19); }
        if constexpr ((c & 1) && c < 8) {
#pragma unroll
          for (int tt = 0; tt < 2; ++tt) {
            const v4u v = {hp[tt][0][0], hp[tt][0][1], hp[tt][1][0], hp[tt][1][1]};
            own[c >> 1][tt] = asfrag(v);
            ST16(v, lane * 16 + tt * (144 * 1024), hdst + (pbase(s) + (c >> 1)) * 1024);
          }
        }
        if constexpr (c == 8) {
#pragma unroll
          for (int tt = 0; tt < 2; ++tt)
            ST8((v2u{hp[tt][0][0], hp[tt][0][1]}), lane * 16 + tt * (144 * 1024) + (s & 1) * 8, hdst + (s < 2 ? 4 : 13) * 1024);
          STAMP(22);
          VMW(4); arrive(ws, FC2, b, lane);
        }
        SB();
      };
      sfor<162>([&](auto I) INL {
        constexpr int ii = I.value, n = FN(ii), c = FC(ii), nx = ii - 1 + PD;
        const bf16x8 cur = cat(lo[ii % PD], hi[ii % PD]);
        acc[0][c] = mfma(cur, xb[n % 12][0], acc[0][c]); SB();
        if constexpr (ii >= 1 && nx < 162) lo[nx % PD] = trd(WADDR(FN(nx), FC(nx)));
        SB();
        acc[1][c] = mfma(cur, xb[n % 12][1], acc[1][c]); SB();
        if constexpr (ii >= 1 && nx < 162) hi[nx % PD] = trd(WADDR(FN(nx), FC(nx)) + 4608);
        SB();
        if constexpr (ii < 108 && c == 8) {
          if constexpr (n < 6) {
#pragma unroll
            for (int tt = 0; tt < 2; ++tt) xb[n][tt] = asfrag(LD16(lane * 16 + tt * (18 * 1024), xsrc + cstep(12 + n, s) * 1024));
          }
          if constexpr (n == 1 || n == 8 || n == 11) { CLOB(); lds_set(L_FREE + 4 * i, n == 1 ? 4 : n == 8 ? 5 : 6, lane); }
          if constexpr (n == 0) STAMP(23);
          if constexpr (n == 3) STAMP(24);
          if constexpr (n == 7) STAMP(25);
          SB();
        }
        if constexpr (ii == 96) { fv = lds4(L_FULL); SB(); }
        if constexpr (ii == 104) { if (min3(fv) < 9) wait_full(9); CLOB(); SB(); STAMP(10); }
        if constexpr (ii >= 108 && n == 14 && c >= 1) { csv = *(const LAS f32x4*)(L_CS3 + (c - 1) * 64 + q * 16); SB(); }
        if constexpr (ii >= 108 && n == 17 && c >= 1) epi(std::integral_constant<int, c - 1>{});
        if constexpr (ii == 150) { fv = lds4(L_FULL); SB(); }
      });
      CLOB();
      lds_set(L_FREE + 4 * i, 7, lane);
      csv = *(const LAS f32x4*)(L_CS3 + 8 * 64 + q * 16);
      if (min3(fv) < 10) wait_full(10);
      CLOB();
#pragma unroll
      for (int k = 0; k < PD; ++k) { lo[k] = trd(WADDR(18, k)); hi[k] = trd(WADDR(18, k) + 4608); }
      SB();
      epi(std::integral_constant<int, 8>{});
      STAMP(11);
    }
    {
      bf16x8 hb[18][2];
#pragma unroll
      for (int m = 0; m < 4; ++m) { hb[m][0] = own[m][0]; hb[m][1] = own[m][1]; }
      auto hload = [&](auto M) INL {
        constexpr int m = M.value;
#pragma unroll
        for (int tt = 0; tt < 2; ++tt) hb[m][tt] = asfrag(LD16(lane * 16 + tt * (144 * 1024), hdst + dstep<m>(s) * 1024));
      };
      wait_word(L_RDY + 4 * R_H1, 1);
      sfor<6>([&](auto M) INL { hload(std::integral_constant<int, 4 + M.value>{}); });
      SB();
      STAMP(12);
      f32x4 acc[2][9];
#pragma unroll
      for (int c = 0; c < 9; ++c) { acc[0][c] = f32x4{0, 0, 0, 0}; acc[1][c] = f32x4{0, 0, 0, 0}; }
      const int pdst = OFF_P + (((g * 4 + s) * 8 + 2 * i) * 8 + j) * (5 * 1024);
      v4u fv;
      unsigned rv;
      auto epi = [&](auto Cc) INL {
        constexpr int c = Cc.value;
        if constexpr ((c & 1) && c < 8) {
#pragma unroll
          for (int tt = 0; tt < 2; ++tt) {
            const f32x4 a0 = acc[tt][c - 1], a1 = acc[tt][c];
            ST16((v4u{pk(a0.x, a0.y), pk(a0.z, a0.w), pk(a1.x, a1.y), pk(a1.z, a1.w)}), lane * 16 + tt * (40 * 1024) + (c >> 1) * 1024, pdst);
          }
        }
        if constexpr (c == 8) {
#pragma unroll
          for (int tt = 0; tt < 2; ++tt) ST16(__builtin_bit_cast(v4u, acc[tt][8]), lane * 16 + tt * (40 * 1024) + 4096, pdst);
        }
        SB();
      };
      sfor<162>([&](auto I) INL {
        constexpr int ii = I.value, n = FN(ii), c = FC(ii), nx = ii - 1 + PD;
        const bf16x8 cur = cat(lo[ii % PD], hi[ii % PD]);
        acc[0][c] = mfma(cur, hb[n][0], acc[0][c]); SB();
        if constexpr (ii >= 1 && nx < 162) lo[nx % PD] = trd(WADDR(18 + FN(nx), FC(nx)));
        SB();
        acc[1][c] = mfma(cur, hb[n][1], acc[1][c]); SB();
        if constexpr (ii >= 1 && nx < 162) hi[nx % PD] = trd(WADDR(18 + FN(nx), FC(nx)) + 4608);
        SB();
        if constexpr (ii == 17) { CLOB(); lds_set(L_FREE + 4 * i, 8, lane); SB(); }
        if constexpr (ii == 35) { VMW(12); arrive(ws, FC3, b, lane); SB(); }
        if constexpr (ii == 50) { rv = ldsw(L_RDY + 4 * R_H2); SB(); }
        if constexpr (ii == 56) {
          if (rfl(rv) == 0) wait_word(L_RDY + 4 * R_H2, 1);
          CLOB();
          sfor<3>([&](auto M) INL { hload(std::integral_constant<int, 10 + M.value>{}); });
          SB();
          STAMP(13);
        }
        if constexpr (ii == 72) { fv = lds4(L_FULL); SB(); }
        if constexpr (ii == 80) { if (min3(fv) < 11) wait_full(11); CLOB(); SB(); }
        if constexpr (ii == 84) { rv = ldsw(L_RDY + 4 * R_H3); SB(); }
        if constexpr (ii == 90) {
          if (rfl(rv) == 0) wait_word(L_RDY + 4 * R_H3, 1);
          CLOB();
          sfor<5>([&](auto M) INL { hload(std::integral_constant<int, 13 + M.value>{}); });
          SB();
          STAMP(14);
        }
        if constexpr (ii == 96) { fv = lds4(L_FULL); SB(); }
        if constexpr (ii == 104) { if (min3(fv) < 12) wait_full(12); CLOB(); SB(); }
        if constexpr (ii >= 108 && n == 17 && c >= 1) epi(std::integral_constant<int, c - 1>{});
      });
      epi(std::integral_constant<int, 8>{});
      STAMP(15);
      DRAIN();
      arrive(ws, FD, b, lane);
      STAMP(16);
    }
  } else if (w < 7) {
    const int ld = w - 4;
    const unsigned fullw = L_FULL + 4 * ld;
    unsigned v3[3], v4[3];
#pragma unroll
    for (int e = 0; e < 3; ++e) {
      const int idx = (3 * ld + e) * 64 + lane, row = idx / 18, ch = idx % 18;
      const int sc = (ch + 2 * (s & 1)) % 18;
      v3[e] = row * (F_ * 2) + sc * 16;
      v4[e] = row * (D_ * 2) + sc * 16;
    }
    const char* w3b = w3 + (576 * j + 144 * s) * 2;
    const char* w4b = w4 + (size_t)(576 * j) * (D_ * 2) + 144 * s * 2;
    auto w3slice = [&](int n) INL {
#pragma unroll
      for (int e = 0; e < 3; ++e) dma(w3b + (size_t)cstep(n, s) * (32 * F_ * 2), v3[e], (n & 15) * SLOT + (3 * ld + e) * 1024);
    };
    auto w4slice = [&](auto M) INL {
      constexpr int m = M.value;
      const char* src = w4b + dstep<m>(s) * (32 * D_ * 2);
#pragma unroll
      for (int e = 0; e < 3; ++e) dma(src, v4[e], ((18 + m) & 15) * SLOT + (3 * ld + e) * 1024);
    };
    const bf16x8 ones = {0x3F80, 0x3F80, 0x3F80, 0x3F80, 0x3F80, 0x3F80, 0x3F80, 0x3F80};
    f32x4 cs[3] = {{0, 0, 0, 0}, {0, 0, 0, 0}, {0, 0, 0, 0}};
    const unsigned cb = (4 * q + (t >> 2)) * 288 + (t & 3) * 8 + ld * 96;
    auto colsum3 = [&](int n0, int n1) INL {
      _Pragma("nounroll") for (int n = n0; n < n1; ++n) {
        const unsigned a = cb + (n & 15) * SLOT;
#pragma unroll
        for (int c = 0; c < 3; ++c) cs[c] = mfma(cat(trd(a + c * 32), trd(a + 4608 + c * 32)), ones, cs[c]);
      }
      CLOB();
    };
    if (ab) {
      const int idx = ld * 64 + lane, row = idx / 6, ch = idx % 6;
      const int sc = (ch + 2 * (ct & 1)) % 6;
      const int krow = 8 * ((row >> 2) & 3) + 4 * (row >> 4) + (row & 3);
      const unsigned v1 = krow * (D_ * 2) + sc * 16, v2 = row * (D_ * 2) + sc * 16;
      _Pragma("nounroll") for (int u = 0; u < 18; ++u) dma(w1 + 96 * ct + u * (32 * D_ * 2), v1, 4 * SLOT + u * 3072 + ld * 1024);
      STAMP(1);
      VMW(9); lds_set(fullw, 1, lane);
      wait_free(1);
      _Pragma("nounroll") for (int u = 0; u < 6; ++u) dma(w2 + 96 * ct + u * (32 * D_ * 2), v2, 10 * SLOT + u * 3072 + ld * 1024);
      VMW(6); lds_set(fullw, 2, lane);
      _Pragma("nounroll") for (int u = 6; u < 18; ++u) dma(w2 + 96 * ct + u * (32 * D_ * 2), v2, 10 * SLOT + u * 3072 + ld * 1024);
      STAMP(2);
      wait_full(2);
      {
        f32x4 c1 = {0, 0, 0, 0};
        const unsigned a = 4 * SLOT + (4 * q + (t >> 2)) * 96 + (t & 3) * 8 + ld * 32;
        s16x4 l1[18], h1[18];
#pragma unroll
        for (int u = 0; u < 18; ++u) { l1[u] = trd(a + u * 3072); h1[u] = trd(a + u * 3072 + 1536); }
        SB();
#pragma unroll
        for (int u = 0; u < 18; ++u) c1 = mfma(cat(l1[u], h1[u]), ones, c1);
        if (t == 0) *(LAS f32x4*)(L_CS1 + ld * 64 + q * 16) = c1;
        CLOB();
      }
      lds_set(fullw, 3, lane);
      STAMP(3);
      DRAIN(); lds_set(fullw, 4, lane);
      wait_free(2); wait_full(3);
      _Pragma("nounroll") for (int n = 0; n < 10; ++n) w3slice(n);
      STAMP(4);
      DRAIN(); lds_set(fullw, 5, lane);
      wait_full(5);
      colsum3(0, 10);
      wait_free(3);
      _Pragma("nounroll") for (int n = 10; n < 16; ++n) w3slice(n);
      STAMP(5);
      DRAIN(); lds_set(fullw, 6, lane);
      STAMP(6);
      wait_full(6);
      colsum3(10, 16);
    } else {
      _Pragma("nounroll") for (int n = 0; n < 16; ++n) w3slice(n);
      DRAIN(); lds_set(fullw, 6, lane);
      wait_full(6);
      colsum3(0, 16);
    }
    lds_set(fullw, 7, lane);
    STAMP(7);
    wait_free(4); wait_full(7);
    STAMP(11);
    w3slice(16); w3slice(17);
    DRAIN(); lds_set(fullw, 8, lane);
    STAMP(12);
    wait_full(8);
    colsum3(16, 18);
    if (t == 0) {
#pragma unroll
      for (int c = 0; c < 3; ++c) *(LAS f32x4*)(L_CS3 + (3 * ld + c) * 64 + q * 16) = cs[c];
    }
    CLOB();
    lds_set(fullw, 9, lane);
    STAMP(8);
    wait_free(5);
    sfor<7>([&](auto M) INL { w4slice(M); });
    wait_free(6);
    sfor<3>([&](auto M) INL { w4slice(std::integral_constant<int, 7 + M.value>{}); });
    DRAIN(); lds_set(fullw, 10, lane);
    STAMP(9);
    wait_free(7); wait_full(9);
    sfor<6>([&](auto M) INL { w4slice(std::integral_constant<int, 10 + M.value>{}); });
    wait_free(8);
    sfor<2>([&](auto M) INL { w4slice(std::integral_constant<int, 16 + M.value>{}); });
    VMW(6); lds_set(fullw, 11, lane);
    DRAIN(); lds_set(fullw, 12, lane);
    STAMP(10);
  } else {
    const u64 tag = __hip_atomic_load((GAS u64*)(ws + OFF_EPOCH), __ATOMIC_RELAXED, __HIP_MEMORY_SCOPE_AGENT) + 1ull;
    if (lane == 0) {
      *(volatile LAS u64*)L_TAG = tag;
      *(volatile LAS unsigned*)(L_RDY + 4 * R_TAG) = 1u;
      __hip_atomic_store(flagp(ws, FS, b), tag, __ATOMIC_RELAXED, __HIP_MEMORY_SCOPE_AGENT);
    }
    STAMP(1);
    auto poll = [&](int kind, int wg, bool need) INL {
      GAS u64* p = flagp(ws, kind, wg);
      _Pragma("nounroll") for (unsigned n = 0; n < GLB_CAP; ++n) {
        const u64 v = __hip_atomic_load(p, __ATOMIC_RELAXED, __HIP_MEMORY_SCOPE_AGENT);
        if (__all(!need || v == tag)) break;
        __builtin_amdgcn_s_sleep(1);
      }
      CLOB();
    };
    if (ab) {
      poll(FA, j + 8 * ((r & 1) + 2 * (lane % 12)), true);
      lds_set(L_RDY + 4 * R_H, 1, lane);
    }
    STAMP(2);
    {
      GAS u64* p = flagp(ws, FB, g + 8 * (lane % 24));
      bool d0 = false, d1 = false;
      _Pragma("nounroll") for (unsigned n = 0; n < GLB_CAP; ++n) {
        const u64 v = __hip_atomic_load(p, __ATOMIC_RELAXED, __HIP_MEMORY_SCOPE_AGENT);
        const bool ok = v == tag;
        if (!d0 && __all(ok || (lane & 1))) { d0 = true; lds_set(L_RDY + 4 * R_X0, 1, lane); }
        if (!d1 && __all(ok || !(lane & 1))) { d1 = true; lds_set(L_RDY + 4 * R_X1, 1, lane); }
        if (d0 && d1) break;
        __builtin_amdgcn_s_sleep(1);
      }
      lds_set(L_RDY + 4 * R_X0, 1, lane); lds_set(L_RDY + 4 * R_X1, 1, lane);
    }
    STAMP(3);
    if (b == 0) {
#pragma unroll
      for (int k = 0; k < 4; ++k) poll(FS, k * 64 + lane, true);
      if (lane == 0) __hip_atomic_store((GAS u64*)(ws + OFF_EPOCH), tag, __ATOMIC_RELAXED, __HIP_MEMORY_SCOPE_AGENT);
    }
    const int peer = j + 8 * (4 * g + (lane & 3));
    poll(FC1, peer, true); lds_set(L_RDY + 4 * R_H1, 1, lane);
    STAMP(4);
    poll(FC2, peer, true); lds_set(L_RDY + 4 * R_H2, 1, lane);
    STAMP(5);
    poll(FC3, peer, true); lds_set(L_RDY + 4 * R_H3, 1, lane);
    STAMP(6);
  }
  if (w >= 3) {
    const int p = w - 3;
    const int T = 8 * g + j;
    if (w != 7) { wait_word(L_RDY + 4 * R_X0, 1); wait_word(L_RDY + 4 * R_X1, 1); }
    v2u st[3];
#pragma unroll
    for (int k = 0; k < 3; ++k) st[k] = LD8(OFF_STAT + (3 * q + k) * 128 + t * 8, T * (12 * 128));
    const int u = p < 4 ? pbase(s) + p : (s < 2 ? 4 : 13);
    const v4u xv = LD16(lane * 16, OFF_X1 + (T * 18 + u) * 1024);
    float mean, rstd;
    ln_stats(qsum((__uint_as_float(st[0].x) + __uint_as_float(st[1].x)) + __uint_as_float(st[2].x)),
             qsum((__uint_as_float(st[0].y) + __uint_as_float(st[1].y)) + __uint_as_float(st[2].y)), mean, rstd);
    if (w == 7) {
      const u64 tag = *(volatile LAS u64*)L_TAG;
      GAS u64* fp = flagp(ws, FD, (lane & 7) + 8 * (4 * g + s));
      _Pragma("nounroll") for (unsigned n = 0; n < GLB_CAP; ++n) {
        const u64 v = __hip_atomic_load(fp, __ATOMIC_RELAXED, __HIP_MEMORY_SCOPE_AGENT);
        if (__all(v == tag)) break;
        __builtin_amdgcn_s_sleep(1);
      }
      CLOB();
      lds_set(L_RDY + 4 * R_P, 1, lane);
    } else {
      wait_word(L_RDY + 4 * R_P, 1);
    }
    STAMP(20);
    v4u pv[8];
    const int psrc = OFF_P + (((g * 4 + s) * 8 + j) * 8 * 5 + p) * 1024;
#pragma unroll
    for (int k = 0; k < 8; ++k) pv[k] = LD16(lane * 16 + k * (5 * 1024), psrc);
    char* orow = out + (size_t)(16 * T + t) * (D_ * 2) + (32 * u + 4 * q) * 2;
    if (p < 4) {
      float a[8];
#pragma unroll
      for (int e = 0; e < 8; ++e) a[e] = 0.f;
#pragma unroll
      for (int k = 0; k < 8; ++k)
#pragma unroll
        for (int e = 0; e < 4; ++e) { const unsigned d = pv[k][e]; a[2 * e] += lo16(d); a[2 * e + 1] += hi16(d); }
      unsigned o[4];
#pragma unroll
      for (int e = 0; e < 4; ++e) {
        const unsigned h2 = pk(a[2 * e] * rstd, a[2 * e + 1] * rstd), xe = xv[e];
        o[e] = pk(lo16(xe) + lo16(h2), hi16(xe) + hi16(h2));
      }
      *(GAS v2u*)orow = v2u{o[0], o[1]};
      *(GAS v2u*)(orow + 32) = v2u{o[2], o[3]};
    } else {
      f32x4 a = {0, 0, 0, 0};
#pragma unroll
      for (int k = 0; k < 8; ++k) a += __builtin_bit_cast(f32x4, pv[k]);
      const unsigned x0 = (s & 1) ? xv.z : xv.x, x1v = (s & 1) ? xv.w : xv.y;
      const unsigned h0 = pk(a.x * rstd, a.y * rstd), h1 = pk(a.z * rstd, a.w * rstd);
      *(GAS v2u*)(orow + (s & 1) * 32) = v2u{pk(lo16(x0) + lo16(h0), hi16(x0) + hi16(h0)), pk(lo16(x1v) + lo16(h1), hi16(x1v) + hi16(h1))};
    }
    STAMP(21);
  }
#endif
}
}

extern "C" void kernel_launch(void* const* d_in, const int* in_sizes, int n_in, void* d_out, int out_size, void* d_ws, size_t ws_size,
                              hipStream_t stream) {
  mx::mixer_kernel<<<256, 512, 0, stream>>>((const char*)d_in[0], (const char*)d_in[1], (const char*)d_in[2], (const char*)d_in[3],
                                            (const char*)d_in[4], (char*)d_out, (char*)d_ws);
}
```

```cpp
#include <hip/hip_runtime.h>
#include <utility>

#define STAMPS 0

#define LAS __attribute__((address_space(3)))
#define GAS __attribute__((address_space(1)))
#define DI __device__ __forceinline__
typedef unsigned v4u __attribute__((ext_vector_type(4)));
typedef unsigned v2u __attribute__((ext_vector_type(2)));
typedef float f32x4 __attribute__((ext_vector_type(4)));
typedef float f32x2 __attribute__((ext_vector_type(2)));
typedef short bf16x8 __attribute__((ext_vector_type(8)));
typedef short s16x4 __attribute__((ext_vector_type(4)));
typedef __bf16 bf16x2_t __attribute__((ext_vector_type(2)));
typedef unsigned long long u64;

namespace mx {
constexpr int D_ = 576, F_ = 4608;
constexpr int OFF_EPOCH = 0;
constexpr int OFF_FLAGS = 4096;
constexpr int OFF_STAT = 262144;
constexpr int OFF_H1 = 524288;
constexpr int OFF_X1 = OFF_H1 + 64 * 18 * 1024;
constexpr int OFF_HH = OFF_X1 + 64 * 18 * 1024;
constexpr int OFF_P = OFF_HH + 64 * 144 * 1024;
constexpr int OFF_DBG = OFF_P + 8 * 4 * 8 * 8 * 5 * 1024;
constexpr int WS_BYTES = OFF_DBG + 2 * 256 * 8 * 32 * 8;
enum { FS = 0, FA = 1, FB = 2, FC1 = 3, FC2 = 4, FC3 = 5, FD = 6 };
constexpr int SLOT = 9216, MISC = 16 * SLOT;
constexpr int L_FULL = MISC, L_FREE = MISC + 16, L_RDY = MISC + 32  , L_CNT = MISC + 64  , L_TAG = MISC + 96;
constexpr int L_CS1 = MISC + 256, L_CS3 = MISC + 512, L_DBG = MISC + 2048;
enum { R_H = 0, R_X0 = 1, R_X1 = 2, R_H1 = 3, R_H2 = 4, R_H3 = 5, R_P = 6, R_TAG = 7 };
constexpr unsigned LDS_CAP = 1u << 21, GLB_CAP = 1u << 16;
constexpr int PD = 4;

template <class F, int... I> DI void sfor_impl(F&& f, std::integer_sequence<int, I...>) { (f(std::integral_constant<int, I>{}), ...); }
template <int N, class F> DI void sfor(F&& f) { sfor_impl(f, std::make_integer_sequence<int, N>{}); }
#define INL __attribute__((always_inline))
#define SB() __builtin_amdgcn_sched_barrier(0)
#define CLOB() asm volatile("" ::: "memory")
#define DRAIN() asm volatile("s_waitcnt vmcnt(0)" ::: "memory")
#define VMW(n) asm volatile("s_waitcnt vmcnt(%0)" ::"i"(n) : "memory")

DI unsigned rfl(unsigned v) { return __builtin_amdgcn_readfirstlane(v); }
DI f32x4 mfma(bf16x8 a, bf16x8 b, f32x4 c) { return __builtin_amdgcn_mfma_f32_16x16x32_bf16(a, b, c, 0, 0, 0); }
DI bf16x8 asfrag(v4u v) { return __builtin_bit_cast(bf16x8, v); }
DI s16x4 trd(unsigned addr) { return __builtin_amdgcn_ds_read_tr16_b64_v4i16((LAS s16x4*)addr); }
DI bf16x8 cat(s16x4 lo, s16x4 hi) { return __builtin_shufflevector(lo, hi, 0, 1, 2, 3, 4, 5, 6, 7); }
DI unsigned pk(float a, float b) { bf16x2_t r = __builtin_convertvector(f32x2{a, b}, bf16x2_t); return __builtin_bit_cast(unsigned, r); }
DI float lo16(unsigned p) { return __uint_as_float(p << 16); }
DI float hi16(unsigned p) { return __uint_as_float(p & 0xffff0000u); }
DI float bfr(float a) { return lo16(pk(a, a)); }
DI float qsum(float v) {
  auto r = __builtin_amdgcn_permlane16_swap(__float_as_uint(v), __float_as_uint(v), false, false);
  float a = __uint_as_float(r[0]) + __uint_as_float(r[1]);
  auto r2 = __builtin_amdgcn_permlane32_swap(__float_as_uint(a), __float_as_uint(a), false, false);
  return __uint_as_float(r2[0]) + __uint_as_float(r2[1]);
}
DI void ln_stats(float s, float ss, float& mean, float& rstd) {
  const float m = s * (1.0f / D_);
  float var = __builtin_fmaf(-m, m, ss * (1.0f / D_));
  var = var > 0.f ? var : 0.f;
  mean = bfr(m);
  const float sd = bfr(__builtin_sqrtf(bfr(bfr(var) + 1e-5f)));
  rstd = 1.0f / sd;
}
constexpr float GA = -2.302208198144325f;
constexpr float GB = -0.1029432397357593f;
template <int N> DI void geluN(f32x2 (&z)[N], const float (&a)[N], const float (&b)[N]) {
  f32x2 p[N];
#pragma unroll
  for (int k = 0; k < N; ++k) p[k] = z[k] * z[k];
  SB();
#pragma unroll
  for (int k = 0; k < N; ++k) p[k] = p[k] * b[k] + a[k];
  SB();
#pragma unroll
  for (int k = 0; k < N; ++k) p[k] = z[k] * p[k];
  SB();
#pragma unroll
  for (int k = 0; k < N; ++k) { p[k].x = __builtin_amdgcn_exp2f(p[k].x); p[k].y = __builtin_amdgcn_exp2f(p[k].y); }
  SB();
#pragma unroll
  for (int k = 0; k < N; ++k) p[k] = p[k] + 1.0f;
  SB();
#pragma unroll
  for (int k = 0; k < N; ++k) { p[k].x = __builtin_amdgcn_rcpf(p[k].x); p[k].y = __builtin_amdgcn_rcpf(p[k].y); }
  SB();
#pragma unroll
  for (int k = 0; k < N; ++k) z[k] = z[k] * p[k];
  SB();
}

DI unsigned ldsw(int addr) { return *(volatile LAS unsigned*)addr; }
DI v4u lds4(int addr) { return *(volatile LAS v4u*)addr; }
DI void lds_set(int addr, unsigned v, int lane) { if (lane == 0) *(volatile LAS unsigned*)addr = v; }
DI void wait_word(int addr, unsigned v) {
  _Pragma("nounroll") for (unsigned n = 0; n < LDS_CAP; ++n) { if (rfl(ldsw(addr)) >= v) break; __builtin_amdgcn_s_sleep(1); }
  CLOB();
}
DI unsigned min3(v4u x) { return rfl(min(min(x.x, x.y), x.z)); }
DI unsigned min4(v4u x) { return rfl(min(min(x.x, x.y), min(x.z, x.w))); }
DI void wait_full(unsigned v) {
  _Pragma("nounroll") for (unsigned n = 0; n < LDS_CAP; ++n) { if (min3(lds4(L_FULL)) >= v) break; __builtin_amdgcn_s_sleep(1); }
  CLOB();
}
DI void wait_free(unsigned v) {
  _Pragma("nounroll") for (unsigned n = 0; n < LDS_CAP; ++n) { if (min4(lds4(L_FREE)) >= v) break; __builtin_amdgcn_s_sleep(1); }
  CLOB();
}
DI GAS u64* flagp(char* ws, int kind, int b) { return (GAS u64*)(ws + OFF_FLAGS + (kind * 256 + b) * 128); }
DI void arrive(char* ws, int kind, int b, int lane) {
  if (lane == 0) {
    const unsigned old = __hip_atomic_fetch_add((LAS unsigned*)(L_CNT + 4 * kind), 1u, __ATOMIC_RELAXED, __HIP_MEMORY_SCOPE_WORKGROUP);
    if (old == 3u) {
      _Pragma("nounroll") for (unsigned n = 0; n < LDS_CAP; ++n) { if (ldsw(L_RDY + 4 * R_TAG) != 0u) break; __builtin_amdgcn_s_sleep(1); }
      const u64 tag = *(volatile LAS u64*)L_TAG;
      __hip_atomic_store(flagp(ws, kind, b), tag, __ATOMIC_RELAXED, __HIP_MEMORY_SCOPE_AGENT);
    }
  }
}
#define LD16(off, soff) __builtin_amdgcn_raw_buffer_load_b128(rs, (off), (soff), 16)
#define LD8(off, soff) __builtin_amdgcn_raw_buffer_load_b64(rs, (off), (soff), 16)
#define ST16(v, off, soff) __builtin_amdgcn_raw_buffer_store_b128((v), rs, (off), (soff), 16)
#define ST8(v, off, soff) __builtin_amdgcn_raw_buffer_store_b64((v), rs, (off), (soff), 16)
DI void dma(const char* sbase, unsigned voff, unsigned ldsaddr) {
  asm volatile("s_mov_b32 m0, %0\n\ts_nop 0\n\tglobal_load_lds_dwordx4 %1, %2" ::"s"(ldsaddr), "v"(voff), "s"(sbase) : "memory");
}
DI int cstep(int n, int s) { const int u = n + ((9 * s) >> 1); return u >= 18 ? u - 18 : u; }
DI int pbase(int s) { return (9 * s + (s & 1)) >> 1; }
template <int M> DI int dstep(int s) {
  if constexpr (M < 4) return pbase(s) + M;
  else if constexpr (M < 10) return pbase((s + 1 + (M - 4) / 2) & 3) + ((M - 4) & 1);
  else if constexpr (M < 13) return pbase((s + M - 9) & 3) + 2;
  else if constexpr (M < 16) return pbase((s + M - 12) & 3) + 3;
  else return M == 16 ? 4 : 13;
}
#define STAMP(k) do { } while (0)

__global__ __launch_bounds__(512, 2) void mixer_kernel(const char* __restrict__ x, const char* __restrict__ w1, const char* __restrict__ w2,
                                                       const char* __restrict__ w3, const char* __restrict__ w4, char* __restrict__ out, char* ws) {
#if defined(__HIP_DEVICE_COMPILE__)
  __shared__ __attribute__((aligned(16))) char smem[163840];
  const int tid = threadIdx.x, lane = tid & 63, w = rfl(tid >> 6);
  const int t = lane & 15, q = lane >> 4;
  const int b = blockIdx.x, j = b & 7, r = b >> 3;
  const bool ab = r < 24;
  const int rt = 2 * j + (r & 1), ct = r >> 1;
  const int g = r >> 2, s = r & 3;
  const __amdgpu_buffer_rsrc_t rs = __builtin_amdgcn_make_buffer_rsrc(ws, 0, WS_BYTES, 0x00020000);
  if (tid < 32) *(volatile LAS unsigned*)(MISC + tid * 4) = 0u;
  asm volatile("" ::"v"(smem));
  STAMP(0);
  asm volatile("s_waitcnt lgkmcnt(0)\n\ts_barrier" ::: "memory");

  if (w < 4) {
    const int i = w;
    if (ab) {
      unsigned ba = 4 * SLOT + (4 * q + (t >> 2)) * 96 + (t & 3) * 8;
      const int T = 4 * rt + i, odd = ct & 1;
      const int upair = (3 * ct + odd) >> 1, usingle = odd ? (3 * ct - 1) >> 1 : (3 * ct + 2) >> 1;
      v2u xres[3];
      {
        const char* xsrc = x + (size_t)T * (16 * D_ * 2);
        const unsigned xl = (i == 3 ? 2 : 10 + 2 * i) * SLOT;
#pragma unroll
        for (int e = 0; e < 18; ++e) {
          const unsigned f = (((unsigned)(lane + 64 * e) * 911u) >> 17) & 7u;
          dma(xsrc + e * 1024, (lane * 16) ^ (f << 4), xl + e * 1024);
        }
        unsigned xa = xl + t * (D_ * 2), xs = ((t >> 1) & 7) << 4;
        unsigned xe0 = xa + ((q * 16) ^ xs), xe1 = xa + ((64 + q * 16) ^ xs);
        const int grp = ct / 3;
        const int sh = grp == 0 ? 1 : grp == 1 ? -1 : grp == 2 ? 2 : -2;
        const int ts = 16 * (i & 1) + t + sh;
        const bool keep = ts >= 0 && ts < 32;
        const int dst = OFF_H1 + ((T & ~1) + ((ts >> 4) & 1)) * (18 * 1024) + ((ts & 15) + 16 * q) * 16;
        asm volatile("" : "+v"(xe0), "+v"(xe1));
        DRAIN();
        STAMP(1);
        bf16x8 xf[18];
#pragma unroll
        for (int u = 0; u < 18; ++u) xf[u] = *(const LAS bf16x8*)(((u & 1) ? xe1 : xe0) + (u >> 1) * 128);
#pragma unroll
        for (int l = 0; l < 3; ++l) {
          const unsigned cb = (48 * ct + 16 * ((l + odd) % 3) + 4 * q) * 2;
          xres[l] = *(const LAS v2u*)(xa + (cb ^ xs));
        }
        CLOB();
        lds_set(L_FREE + 4 * i, 1, lane);
        const bf16x8 ones = {0x3F80, 0x3F80, 0x3F80, 0x3F80, 0x3F80, 0x3F80, 0x3F80, 0x3F80};
        f32x4 aS = {0, 0, 0, 0}, aG = {0, 0, 0, 0};
#pragma unroll
        for (int u = 0; u < 18; ++u) { aS = mfma(ones, xf[u], aS); aG = mfma(xf[u], xf[u], aG); }
        STAMP(17);
        f32x4 acc[3] = {{0, 0, 0, 0}, {0, 0, 0, 0}, {0, 0, 0, 0}};
        float nmean, rstd;
        sfor<2>([&](auto H) INL {
          constexpr int h = H.value;
          wait_full(h + 1);
          asm volatile("" : "+v"(ba));
#pragma unroll
          for (int u = 9 * h; u < 9 * h + 9; ++u)
#pragma unroll
            for (int l = 0; l < 3; ++l) acc[l] = mfma(cat(trd(ba + u * 3072 + l * 32), trd(ba + u * 3072 + 1536 + l * 32)), xf[u], acc[l]);
          if constexpr (h == 0) {
            const int e = t & 3;
            float gd = e == 0 ? aG[0] : e == 1 ? aG[1] : e == 2 ? aG[2] : aG[3];
            gd = (q == (t >> 2)) ? gd : 0.f;
            ln_stats(aS[0], qsum(gd), nmean, rstd);
            nmean = -nmean;
          }
        });
        STAMP(2);
        const float ga = GA * rstd, gb = GB * rstd * rstd * rstd;
        wait_full(3);
        f32x2 z[6];
#pragma unroll
        for (int l = 0; l < 3; ++l) {
          const f32x4 cs = *(const LAS f32x4*)(L_CS1 + l * 64 + q * 16);
          const f32x4 zz = cs * nmean + acc[l];
          z[2 * l] = f32x2{zz.x, zz.y}; z[2 * l + 1] = f32x2{zz.z, zz.w};
        }
        const float ga6[6] = {ga, ga, ga, ga, ga, ga}, gb6[6] = {gb, gb, gb, gb, gb, gb};
        geluN<6>(z, ga6, gb6);
        unsigned hp[6];
#pragma unroll
        for (int k = 0; k < 6; ++k) { const f32x2 v = z[k] * rstd; hp[k] = keep ? pk(v.x, v.y) : 0u; }
        ST16((v4u{hp[0], hp[1], hp[2], hp[3]}), dst, upair * 1024);
        ST8((v2u{hp[4], hp[5]}), dst + odd * 8, usingle * 1024);
        STAMP(3);
        DRAIN();
        lds_set(L_FREE + 4 * i, 2, lane);
        arrive(ws, FA, b, lane);
        STAMP(4);
      }
      {
        wait_full(4);
        wait_word(L_RDY + 4 * R_H, 1);
        STAMP(5);
        bf16x8 hf[18];
#pragma unroll
        for (int u = 0; u < 18; ++u) hf[u] = asfrag(LD16(lane * 16 + u * 1024, OFF_H1 + T * (18 * 1024)));
        SB();
        unsigned bb = ba + 6 * SLOT;
        asm volatile("" : "+v"(bb));
        f32x4 acc[3] = {{0, 0, 0, 0}, {0, 0, 0, 0}, {0, 0, 0, 0}};
#pragma unroll
        for (int u = 0; u < 18; ++u)
#pragma unroll
          for (int l = 0; l < 3; ++l) acc[l] = mfma(cat(trd(bb + u * 3072 + l * 32), trd(bb + u * 3072 + 1536 + l * 32)), hf[u], acc[l]);
        STAMP(6);
        unsigned xp[6];
        float sm = 0.f, sq = 0.f;
#pragma unroll
        for (int l = 0; l < 3; ++l) {
          const unsigned p0 = pk(acc[l].x, acc[l].y), p1 = pk(acc[l].z, acc[l].w);
          xp[2 * l] = pk(lo16(xres[l].x) + lo16(p0), hi16(xres[l].x) + hi16(p0));
          xp[2 * l + 1] = pk(lo16(xres[l].y) + lo16(p1), hi16(xres[l].y) + hi16(p1));
          const float v0 = lo16(xp[2 * l]), v1 = hi16(xp[2 * l]), v2 = lo16(xp[2 * l + 1]), v3 = hi16(xp[2 * l + 1]);
          sm += (v0 + v1) + (v2 + v3);
          sq += (v0 * v0 + v1 * v1) + (v2 * v2 + v3 * v3);
        }
        const int dst = OFF_X1 + T * (18 * 1024) + lane * 16;
        ST16((v4u{xp[0], xp[1], xp[2], xp[3]}), dst, upair * 1024);
        ST8((v2u{xp[4], xp[5]}), dst + odd * 8, usingle * 1024);
        sm = qsum(sm); sq = qsum(sq);
        if (q == 0) ST8((v2u{__float_as_uint(sm), __float_as_uint(sq)}), OFF_STAT + t * 8, (T * 12 + ct) * 128);
        STAMP(7);
        DRAIN();
        lds_set(L_FREE + 4 * i, 3, lane);
        arrive(ws, FB, b, lane);
        STAMP(8);
      }
    }
    unsigned b0 = (4 * q + (t >> 2)) * 288 + (t & 3) * 8, b1 = b0 + 6 * SLOT, b2 = b0 + 12 * SLOT;
    asm volatile("" : "+v"(b0), "+v"(b1), "+v"(b2));
#define WADDR(z, c) ((((z) % 16) < 6 ? b0 : ((z) % 16) < 12 ? b1 : b2) + (((z) % 16) % 6) * SLOT + (c) * 32)
#define FN(i_) ((i_) < 108 ? (i_) / 9 : 12 + ((i_) - 108) % 6)
#define FC(i_) ((i_) < 108 ? (i_) % 9 : ((i_) - 108) / 6)
    const int T0 = 8 * g + 2 * i;
    const int hdst = OFF_HH + (T0 * 144 + j * 18) * 1024;
    bf16x8 own[4][2];
    s16x4 lo[PD], hi[PD];
    {
      wait_full(6);
      wait_word(L_RDY + 4 * (R_X0 + (i >> 1)), 1);
      STAMP(9);
      v2u st[2][3];
#pragma unroll
      for (int tt = 0; tt < 2; ++tt)
#pragma unroll
        for (int k = 0; k < 3; ++k) st[tt][k] = LD8(OFF_STAT + (3 * q + k) * 128 + t * 8, (T0 + tt) * (12 * 128));
      const int xsrc = OFF_X1 + T0 * (18 * 1024);
      bf16x8 xb[12][2];
#pragma unroll
      for (int n = 0; n < 12; ++n)
#pragma unroll
        for (int tt = 0; tt < 2; ++tt) xb[n][tt] = asfrag(LD16(lane * 16 + tt * (18 * 1024), xsrc + cstep(n, s) * 1024));
      SB();
#pragma unroll
      for (int k = 0; k < PD; ++k) { lo[k] = trd(WADDR(0, k)); hi[k] = trd(WADDR(0, k) + 4608); }
      float mean[2], ga[2], gb[2];
#pragma unroll
      for (int tt = 0; tt < 2; ++tt) {
        const float sm = qsum((__uint_as_float(st[tt][0].x) + __uint_as_float(st[tt][1].x)) + __uint_as_float(st[tt][2].x));
        const float sq = qsum((__uint_as_float(st[tt][0].y) + __uint_as_float(st[tt][1].y)) + __uint_as_float(st[tt][2].y));
        float rstd;
        ln_stats(sm, sq, mean[tt], rstd);
        mean[tt] = -mean[tt];
        ga[tt] = GA * rstd; gb[tt] = GB * rstd * rstd * rstd;
      }
      f32x4 acc[2][9];
#pragma unroll
      for (int c = 0; c < 9; ++c) { acc[0][c] = f32x4{0, 0, 0, 0}; acc[1][c] = f32x4{0, 0, 0, 0}; }
      unsigned hp[2][2][2];
      f32x4 csv;
      v4u fv;
      auto epi = [&](auto Cc) INL {
        constexpr int c = Cc.value;
        f32x2 z[4];
#pragma unroll
        for (int tt = 0; tt < 2; ++tt) {
          const f32x4 zz = csv * mean[tt] + acc[tt][c];
          z[2 * tt] = f32x2{zz.x, zz.y}; z[2 * tt + 1] = f32x2{zz.z, zz.w};
        }
        SB();
        const float ga4[4] = {ga[0], ga[0], ga[1], ga[1]}, gb4[4] = {gb[0], gb[0], gb[1], gb[1]};
        geluN<4>(z, ga4, gb4);
#pragma unroll
        for (int tt = 0; tt < 2; ++tt) { hp[tt][c & 1][0] = pk(z[2 * tt].x, z[2 * tt].y); hp[tt][c & 1][1] = pk(z[2 * tt + 1].x, z[2 * tt + 1].y); }
        if constexpr (c == 5) { STAMP(18); DRAIN(); arrive(ws, FC1, b, lane); STAMP(19); }
        if constexpr ((c & 1) && c < 8) {
#pragma unroll
          for (int tt = 0; tt < 2; ++tt) {
            const v4u v = {hp[tt][0][0], hp[tt][0][1], hp[tt][1][0], hp[tt][1][1]};
            own[c >> 1][tt] = asfrag(v);
            ST16(v, lane * 16 + tt * (144 * 1024), hdst + (pbase(s) + (c >> 1)) * 1024);
          }
        }
        if constexpr (c == 8) {
#pragma unroll
          for (int tt = 0; tt < 2; ++tt)
            ST8((v2u{hp[tt][0][0], hp[tt][0][1]}), lane * 16 + tt * (144 * 1024) + (s & 1) * 8, hdst + (s < 2 ? 4 : 13) * 1024);
          STAMP(22);
          VMW(4); arrive(ws, FC2, b, lane);
        }
        SB();
      };
      sfor<162>([&](auto I) INL {
        constexpr int ii = I.value, n = FN(ii), c = FC(ii), nx = ii - 1 + PD;
        const bf16x8 cur = cat(lo[ii % PD], hi[ii % PD]);
        acc[0][c] = mfma(cur, xb[n % 12][0], acc[0][c]); SB();
        if constexpr (ii >= 1 && nx < 162) lo[nx % PD] = trd(WADDR(FN(nx), FC(nx)));
        SB();
        acc[1][c] = mfma(cur, xb[n % 12][1], acc[1][c]); SB();
        if constexpr (ii >= 1 && nx < 162) hi[nx % PD] = trd(WADDR(FN(nx), FC(nx)) + 4608);
        SB();
        if constexpr (ii < 108 && c == 8) {
          if constexpr (n < 6) {
#pragma unroll
            for (int tt = 0; tt < 2; ++tt) xb[n][tt] = asfrag(LD16(lane * 16 + tt * (18 * 1024), xsrc + cstep(12 + n, s) * 1024));
          }
          if constexpr (n == 1 || n == 8 || n == 11) { CLOB(); lds_set(L_FREE + 4 * i, n == 1 ? 4 : n == 8 ? 5 : 6, lane); }
          if constexpr (n == 0) STAMP(23);
          if constexpr (n == 3) STAMP(24);
          if constexpr (n == 7) STAMP(25);
          SB();
        }
        if constexpr (ii == 96) { fv = lds4(L_FULL); SB(); }
        if constexpr (ii == 104) { if (min3(fv) < 9) wait_full(9); CLOB(); SB(); STAMP(10); }
        if constexpr (ii >= 108 && n == 14 && c >= 1) { csv = *(const LAS f32x4*)(L_CS3 + (c - 1) * 64 + q * 16); SB(); }
        if constexpr (ii >= 108 && n == 17 && c >= 1) epi(std::integral_constant<int, c - 1>{});
        if constexpr (ii == 150) { fv = lds4(L_FULL); SB(); }
      });
      CLOB();
      lds_set(L_FREE + 4 * i, 7, lane);
      csv = *(const LAS f32x4*)(L_CS3 + 8 * 64 + q * 16);
      if (min3(fv) < 10) wait_full(10);
      CLOB();
#pragma unroll
      for (int k = 0; k < PD; ++k) { lo[k] = trd(WADDR(18, k)); hi[k] = trd(WADDR(18, k) + 4608); }
      SB();
      epi(std::integral_constant<int, 8>{});
      STAMP(11);
    }
    {
      bf16x8 hb[18][2];
#pragma unroll
      for (int m = 0; m < 4; ++m) { hb[m][0] = own[m][0]; hb[m][1] = own[m][1]; }
      auto hload = [&](auto M) INL {
        constexpr int m = M.value;
#pragma unroll
        for (int tt = 0; tt < 2; ++tt) hb[m][tt] = asfrag(LD16(lane * 16 + tt * (144 * 1024), hdst + dstep<m>(s) * 1024));
      };
      wait_word(L_RDY + 4 * R_H1, 1);
      sfor<6>([&](auto M) INL { hload(std::integral_constant<int, 4 + M.value>{}); });
      SB();
      STAMP(12);
      f32x4 acc[2][9];
#pragma unroll
      for (int c = 0; c < 9; ++c) { acc[0][c] = f32x4{0, 0, 0, 0}; acc[1][c] = f32x4{0, 0, 0, 0}; }
      const int pdst = OFF_P + (((g * 4 + s) * 8 + 2 * i) * 8 + j) * (5 * 1024);
      v4u fv;
      unsigned rv;
      auto epi = [&](auto Cc) INL {
        constexpr int c = Cc.value;
        if constexpr ((c & 1) && c < 8) {
#pragma unroll
          for (int tt = 0; tt < 2; ++tt) {
            const f32x4 a0 = acc[tt][c - 1], a1 = acc[tt][c];
            ST16((v4u{pk(a0.x, a0.y), pk(a0.z, a0.w), pk(a1.x, a1.y), pk(a1.z, a1.w)}), lane * 16 + tt * (40 * 1024) + (c >> 1) * 1024, pdst);
          }
        }
        if constexpr (c == 8) {
#pragma unroll
          for (int tt = 0; tt < 2; ++tt) ST16(__builtin_bit_cast(v4u, acc[tt][8]), lane * 16 + tt * (40 * 1024) + 4096, pdst);
        }
        SB();
      };
      sfor<162>([&](auto I) INL {
        constexpr int ii = I.value, n = FN(ii), c = FC(ii), nx = ii - 1 + PD;
        const bf16x8 cur = cat(lo[ii % PD], hi[ii % PD]);
        acc[0][c] = mfma(cur, hb[n][0], acc[0][c]); SB();
        if constexpr (ii >= 1 && nx < 162) lo[nx % PD] = trd(WADDR(18 + FN(nx), FC(nx)));
        SB();
        acc[1][c] = mfma(cur, hb[n][1], acc[1][c]); SB();
        if constexpr (ii >= 1 && nx < 162) hi[nx % PD] = trd(WADDR(18 + FN(nx), FC(nx)) + 4608);
        SB();
        if constexpr (ii == 17) { CLOB(); lds_set(L_FREE + 4 * i, 8, lane); SB(); }
        if constexpr (ii == 35) { VMW(12); arrive(ws, FC3, b, lane); SB(); }
        if constexpr (ii == 50) { rv = ldsw(L_RDY + 4 * R_H2); SB(); }
        if constexpr (ii == 56) {
          if (rfl(rv) == 0) wait_word(L_RDY + 4 * R_H2, 1);
          CLOB();
          sfor<3>([&](auto M) INL { hload(std::integral_constant<int, 10 + M.value>{}); });
          SB();
          STAMP(13);
        }
        if constexpr (ii == 72) { fv = lds4(L_FULL); SB(); }
        if constexpr (ii == 80) { if (min3(fv) < 11) wait_full(11); CLOB(); SB(); }
        if constexpr (ii == 84) { rv = ldsw(L_RDY + 4 * R_H3); SB(); }
        if constexpr (ii == 90) {
          if (rfl(rv) == 0) wait_word(L_RDY + 4 * R_H3, 1);
          CLOB();
          sfor<5>([&](auto M) INL { hload(std::integral_constant<int, 13 + M.value>{}); });
          SB();
          STAMP(14);
        }
        if constexpr (ii == 96) { fv = lds4(L_FULL); SB(); }
        if constexpr (ii == 104) { if (min3(fv) < 12) wait_full(12); CLOB(); SB(); }
        if constexpr (ii >= 108 && n == 17 && c >= 1) epi(std::integral_constant<int, c - 1>{});
      });
      epi(std::integral_constant<int, 8>{});
      STAMP(15);
      DRAIN();
      arrive(ws, FD, b, lane);
      STAMP(16);
    }
  } else if (w < 7) {
    const int ld = w - 4;
    const unsigned fullw = L_FULL + 4 * ld;
    unsigned v3[3], v4[3];
#pragma unroll
    for (int e = 0; e < 3; ++e) {
      const int idx = (3 * ld + e) * 64 + lane, row = idx / 18, ch = idx % 18;
      const int sc = (ch + 2 * (s & 1)) % 18;
      v3[e] = row * (F_ * 2) + sc * 16;
      v4[e] = row * (D_ * 2) + sc * 16;
    }
    const char* w3b = w3 + (576 * j + 144 * s) * 2;
    const char* w4b = w4 + (size_t)(576 * j) * (D_ * 2) + 144 * s * 2;
    auto w3slice = [&](int n) INL {
#pragma unroll
      for (int e = 0; e < 3; ++e) dma(w3b + (size_t)cstep(n, s) * (32 * F_ * 2), v3[e], (n & 15) * SLOT + (3 * ld + e) * 1024);
    };
    auto w4slice = [&](auto M) INL {
      constexpr int m = M.value;
      const char* src = w4b + dstep<m>(s) * (32 * D_ * 2);
#pragma unroll
      for (int e = 0; e < 3; ++e) dma(src, v4[e], ((18 + m) & 15) * SLOT + (3 * ld + e) * 1024);
    };
    const bf16x8 ones = {0x3F80, 0x3F80, 0x3F80, 0x3F80, 0x3F80, 0x3F80, 0x3F80, 0x3F80};
    f32x4 cs[3] = {{0, 0, 0, 0}, {0, 0, 0, 0}, {0, 0, 0, 0}};
    const unsigned cb = (4 * q + (t >> 2)) * 288 + (t & 3) * 8 + ld * 96;
    auto colsum3 = [&](int n0, int n1) INL {
      _Pragma("nounroll") for (int n = n0; n < n1; ++n) {
        const unsigned a = cb + (n & 15) * SLOT;
#pragma unroll
        for (int c = 0; c < 3; ++c) cs[c] = mfma(cat(trd(a + c * 32), trd(a + 4608 + c * 32)), ones, cs[c]);
      }
      CLOB();
    };
    if (ab) {
      const int idx = ld * 64 + lane, row = idx / 6, ch = idx % 6;
      const int sc = (ch + 2 * (ct & 1)) % 6;
      const int krow = 8 * ((row >> 2) & 3) + 4 * (row >> 4) + (row & 3);
      const unsigned v1 = krow * (D_ * 2) + sc * 16, v2 = row * (D_ * 2) + sc * 16;
      _Pragma("nounroll") for (int u = 0; u < 18; ++u) dma(w1 + 96 * ct + u * (32 * D_ * 2), v1, 4 * SLOT + u * 3072 + ld * 1024);
      STAMP(1);
      VMW(9); lds_set(fullw, 1, lane);
      wait_free(1);
      _Pragma("nounroll") for (int u = 0; u < 6; ++u) dma(w2 + 96 * ct + u * (32 * D_ * 2), v2, 10 * SLOT + u * 3072 + ld * 1024);
      VMW(6); lds_set(fullw, 2, lane);
      _Pragma("nounroll") for (int u = 6; u < 18; ++u) dma(w2 + 96 * ct + u * (32 * D_ * 2), v2, 10 * SLOT + u * 3072 + ld * 1024);
      STAMP(2);
      wait_full(2);
      {
        f32x4 c1 = {0, 0, 0, 0};
        const unsigned a = 4 * SLOT + (4 * q + (t >> 2)) * 96 + (t & 3) * 8 + ld * 32;
        s16x4 l1[18], h1[18];
#pragma unroll
        for (int u = 0; u < 18; ++u) { l1[u] = trd(a + u * 3072); h1[u] = trd(a + u * 3072 + 1536); }
        SB();
#pragma unroll
        for (int u = 0; u < 18; ++u) c1 = mfma(cat(l1[u], h1[u]), ones, c1);
        if (t == 0) *(LAS f32x4*)(L_CS1 + ld * 64 + q * 16) = c1;
        CLOB();
      }
      lds_set(fullw, 3, lane);
      STAMP(3);
      DRAIN(); lds_set(fullw, 4, lane);
      wait_free(2); wait_full(3);
      _Pragma("nounroll") for (int n = 0; n < 10; ++n) w3slice(n);
      STAMP(4);
      DRAIN(); lds_set(fullw, 5, lane);
      wait_full(5);
      colsum3(0, 10);
      wait_free(3);
      _Pragma("nounroll") for (int n = 10; n < 16; ++n) w3slice(n);
      STAMP(5);
      DRAIN(); lds_set(fullw, 6, lane);
      STAMP(6);
      wait_full(6);
      colsum3(10, 16);
    } else {
      _Pragma("nounroll") for (int n = 0; n < 16; ++n) w3slice(n);
      DRAIN(); lds_set(fullw, 6, lane);
      wait_full(6);
      colsum3(0, 16);
    }
    lds_set(fullw, 7, lane);
    STAMP(7);
    wait_free(4); wait_full(7);
    STAMP(11);
    w3slice(16); w3slice(17);
    DRAIN(); lds_set(fullw, 8, lane);
    STAMP(12);
    wait_full(8);
    colsum3(16, 18);
    if (t == 0) {
#pragma unroll
      for (int c = 0; c < 3; ++c) *(LAS f32x4*)(L_CS3 + (3 * ld + c) * 64 + q * 16) = cs[c];
    }
    CLOB();
    lds_set(fullw, 9, lane);
    STAMP(8);
    wait_free(5);
    sfor<7>([&](auto M) INL { w4slice(M); });
    wait_free(6);
    sfor<3>([&](auto M) INL { w4slice(std::integral_constant<int, 7 + M.value>{}); });
    DRAIN(); lds_set(fullw, 10, lane);
    STAMP(9);
    wait_free(7); wait_full(9);
    sfor<6>([&](auto M) INL { w4slice(std::integral_constant<int, 10 + M.value>{}); });
    wait_free(8);
    sfor<2>([&](auto M) INL { w4slice(std::integral_constant<int, 16 + M.value>{}); });
    VMW(6); lds_set(fullw, 11, lane);
    DRAIN(); lds_set(fullw, 12, lane);
    STAMP(10);
  } else {
    const u64 tag = __hip_atomic_load((GAS u64*)(ws + OFF_EPOCH), __ATOMIC_RELAXED, __HIP_MEMORY_SCOPE_AGENT) + 1ull;
    if (lane == 0) {
      *(volatile LAS u64*)L_TAG = tag;
      *(volatile LAS unsigned*)(L_RDY + 4 * R_TAG) = 1u;
      __hip_atomic_store(flagp(ws, FS, b), tag, __ATOMIC_RELAXED, __HIP_MEMORY_SCOPE_AGENT);
    }
    STAMP(1);
    auto poll = [&](int kind, int wg, bool need) INL {
      GAS u64* p = flagp(ws, kind, wg);
      _Pragma("nounroll") for (unsigned n = 0; n < GLB_CAP; ++n) {
        const u64 v = __hip_atomic_load(p, __ATOMIC_RELAXED, __HIP_MEMORY_SCOPE_AGENT);
        if (__all(!need || v == tag)) break;
        __builtin_amdgcn_s_sleep(1);
      }
      CLOB();
    };
    if (ab) {
      poll(FA, j + 8 * ((r & 1) + 2 * (lane % 12)), true);
      lds_set(L_RDY + 4 * R_H, 1, lane);
    }
    STAMP(2);
    {
      GAS u64* p = flagp(ws, FB, g + 8 * (lane % 24));
      bool d0 = false, d1 = false;
      _Pragma("nounroll") for (unsigned n = 0; n < GLB_CAP; ++n) {
        const u64 v = __hip_atomic_load(p, __ATOMIC_RELAXED, __HIP_MEMORY_SCOPE_AGENT);
        const bool ok = v == tag;
        if (!d0 && __all(ok || (lane & 1))) { d0 = true; lds_set(L_RDY + 4 * R_X0, 1, lane); }
        if (!d1 && __all(ok || !(lane & 1))) { d1 = true; lds_set(L_RDY + 4 * R_X1, 1, lane); }
        if (d0 && d1) break;
        __builtin_amdgcn_s_sleep(1);
      }
      lds_set(L_RDY + 4 * R_X0, 1, lane); lds_set(L_RDY + 4 * R_X1, 1, lane);
    }
    STAMP(3);
    if (b == 0) {
#pragma unroll
      for (int k = 0; k < 4; ++k) poll(FS, k * 64 + lane, true);
      if (lane == 0) __hip_atomic_store((GAS u64*)(ws + OFF_EPOCH), tag, __ATOMIC_RELAXED, __HIP_MEMORY_SCOPE_AGENT);
    }
    const int peer = j + 8 * (4 * g + (lane & 3));
    poll(FC1, peer, true); lds_set(L_RDY + 4 * R_H1, 1, lane);
    STAMP(4);
    poll(FC2, peer, true); lds_set(L_RDY + 4 * R_H2, 1, lane);
    STAMP(5);
    poll(FC3, peer, true); lds_set(L_RDY + 4 * R_H3, 1, lane);
    STAMP(6);
  }
  if (w >= 3) {
    const int p = w - 3;
    const int T = 8 * g + j;
    if (w != 7) { wait_word(L_RDY + 4 * R_X0, 1); wait_word(L_RDY + 4 * R_X1, 1); }
    v2u st[3];
#pragma unroll
    for (int k = 0; k < 3; ++k) st[k] = LD8(OFF_STAT + (3 * q + k) * 128 + t * 8, T * (12 * 128));
    const int u = p < 4 ? pbase(s) + p : (s < 2 ? 4 : 13);
    const v4u xv = LD16(lane * 16, OFF_X1 + (T * 18 + u) * 1024);
    float mean, rstd;
    ln_stats(qsum((__uint_as_float(st[0].x) + __uint_as_float(st[1].x)) + __uint_as_float(st[2].x)),
             qsum((__uint_as_float(st[0].y) + __uint_as_float(st[1].y)) + __uint_as_float(st[2].y)), mean, rstd);
    if (w == 7) {
      const u64 tag = *(volatile LAS u64*)L_TAG;
      GAS u64* fp = flagp(ws, FD, (lane & 7) + 8 * (4 * g + s));
      _Pragma("nounroll") for (unsigned n = 0; n < GLB_CAP; ++n) {
        const u64 v = __hip_atomic_load(fp, __ATOMIC_RELAXED, __HIP_MEMORY_SCOPE_AGENT);
        if (__all(v == tag)) break;
        __builtin_amdgcn_s_sleep(1);
      }
      CLOB();
      lds_set(L_RDY + 4 * R_P, 1, lane);
    } else {
      wait_word(L_RDY + 4 * R_P, 1);
    }
    STAMP(20);
    v4u pv[8];
    const int psrc = OFF_P + (((g * 4 + s) * 8 + j) * 8 * 5 + p) * 1024;
#pragma unroll
    for (int k = 0; k < 8; ++k) pv[k] = LD16(lane * 16 + k * (5 * 1024), psrc);
    char* orow = out + (size_t)(16 * T + t) * (D_ * 2) + (32 * u + 4 * q) * 2;
    if (p < 4) {
      float a[8];
#pragma unroll
      for (int e = 0; e < 8; ++e) a[e] = 0.f;
#pragma unroll
      for (int k = 0; k < 8; ++k)
#pragma unroll
        for (int e = 0; e < 4; ++e) { const unsigned d = pv[k][e]; a[2 * e] += lo16(d); a[2 * e + 1] += hi16(d); }
      unsigned o[4];
#pragma unroll
      for (int e = 0; e < 4; ++e) {
        const unsigned h2 = pk(a[2 * e] * rstd, a[2 * e + 1] * rstd), xe = xv[e];
        o[e] = pk(lo16(xe) + lo16(h2), hi16(xe) + hi16(h2));
      }
      *(GAS v2u*)orow = v2u{o[0], o[1]};
      *(GAS v2u*)(orow + 32) = v2u{o[2], o[3]};
    } else {
      f32x4 a = {0, 0, 0, 0};
#pragma unroll
      for (int k = 0; k < 8; ++k) a += __builtin_bit_cast(f32x4, pv[k]);
      const unsigned x0 = (s & 1) ? xv.z : xv.x, x1v = (s & 1) ? xv.w : xv.y;
      const unsigned h0 = pk(a.x * rstd, a.y * rstd), h1 = pk(a.z * rstd, a.w * rstd);
      *(GAS v2u*)(orow + (s & 1) * 32) = v2u{pk(lo16(x0) + lo16(h0), hi16(x0) + hi16(h0)), pk(lo16(x1v) + lo16(h1), hi16(x1v) + hi16(h1))};
    }
    STAMP(21);
  }
#endif
}
}

extern "C" void kernel_launch(void* const* d_in, const int* in_sizes, int n_in, void* d_out, int out_size, void* d_ws, size_t ws_size,
                              hipStream_t stream) {
  mx::mixer_kernel<<<256, 512, 0, stream>>>((const char*)d_in[0], (const char*)d_in[1], (const char*)d_in[2], (const char*)d_in[3],
                                            (const char*)d_in[4], (char*)d_out, (char*)d_ws + ((ws_size / 2) & ~(size_t)0x1fffff));
}
```

```cpp
#define PRE 23
#include <hip/hip_runtime.h>
#include <utility>

#define STAMPS 0

#define LAS __attribute__((address_space(3)))
#define GAS __attribute__((address_space(1)))
#define DI __device__ __forceinline__
typedef unsigned v4u __attribute__((ext_vector_type(4)));
typedef unsigned v2u __attribute__((ext_vector_type(2)));
typedef float f32x4 __attribute__((ext_vector_type(4)));
typedef float f32x2 __attribute__((ext_vector_type(2)));
typedef short bf16x8 __attribute__((ext_vector_type(8)));
typedef short s16x4 __attribute__((ext_vector_type(4)));
typedef __bf16 bf16x2_t __attribute__((ext_vector_type(2)));
typedef unsigned long long u64;

namespace mx {
constexpr int D_ = 576, F_ = 4608;
constexpr int OFF_EPOCH = 0;
constexpr int OFF_FLAGS = 4096;
constexpr int OFF_STAT = 262144;
constexpr int OFF_H1 = 524288;
constexpr int OFF_X1 = OFF_H1 + 64 * 18 * 1024;
constexpr int OFF_HH = OFF_X1 + 64 * 18 * 1024;
constexpr int OFF_P = OFF_HH + 64 * 144 * 1024;
constexpr int OFF_DBG = OFF_P + 8 * 4 * 8 * 8 * 5 * 1024;
constexpr int WS_BYTES = OFF_DBG + 2 * 256 * 8 * 32 * 8;
enum { FS = 0, FA = 1, FB = 2, FC1 = 3, FC2 = 4, FC3 = 5, FD = 6 };
constexpr int SLOT = 9216, MISC = 16 * SLOT;
constexpr int L_FULL = MISC, L_FREE = MISC + 16, L_RDY = MISC + 32  , L_CNT = MISC + 64  , L_TAG = MISC + 96;
constexpr int L_CS1 = MISC + 256, L_CS3 = MISC + 512, L_DBG = MISC + 2048;
enum { R_H = 0, R_X0 = 1, R_X1 = 2, R_H1 = 3, R_H2 = 4, R_H3 = 5, R_P = 6, R_TAG = 7 };
constexpr unsigned LDS_CAP = 1u << 21, GLB_CAP = 1u << 16;
constexpr int PD = 4;

template <class F, int... I> DI void sfor_impl(F&& f, std::integer_sequence<int, I...>) { (f(std::integral_constant<int, I>{}), ...); }
template <int N, class F> DI void sfor(F&& f) { sfor_impl(f, std::make_integer_sequence<int, N>{}); }
#define INL __attribute__((always_inline))
#define SB() __builtin_amdgcn_sched_barrier(0)
#define CLOB() asm volatile("" ::: "memory")
#define DRAIN() asm volatile("s_waitcnt vmcnt(0)" ::: "memory")
#define VMW(n) asm volatile("s_waitcnt vmcnt(%0)" ::"i"(n) : "memory")

DI unsigned rfl(unsigned v) { return __builtin_amdgcn_readfirstlane(v); }
DI f32x4 mfma(bf16x8 a, bf16x8 b, f32x4 c) { return __builtin_amdgcn_mfma_f32_16x16x32_bf16(a, b, c, 0, 0, 0); }
DI bf16x8 asfrag(v4u v) { return __builtin_bit_cast(bf16x8, v); }
DI s16x4 trd(unsigned addr) { return __builtin_amdgcn_ds_read_tr16_b64_v4i16((LAS s16x4*)addr); }
DI bf16x8 cat(s16x4 lo, s16x4 hi) { return __builtin_shufflevector(lo, hi, 0, 1, 2, 3, 4, 5, 6, 7); }
DI unsigned pk(float a, float b) { bf16x2_t r = __builtin_convertvector(f32x2{a, b}, bf16x2_t); return __builtin_bit_cast(unsigned, r); }
DI float lo16(unsigned p) { return __uint_as_float(p << 16); }
DI float hi16(unsigned p) { return __uint_as_float(p & 0xffff0000u); }
DI float bfr(float a) { return lo16(pk(a, a)); }
DI float qsum(float v) {
  auto r = __builtin_amdgcn_permlane16_swap(__float_as_uint(v), __float_as_uint(v), false, false);
  float a = __uint_as_float(r[0]) + __uint_as_float(r[1]);
  auto r2 = __builtin_amdgcn_permlane32_swap(__float_as_uint(a), __float_as_uint(a), false, false);
  return __uint_as_float(r2[0]) + __uint_as_float(r2[1]);
}
DI void ln_stats(float s, float ss, float& mean, float& rstd) {
  const float m = s * (1.0f / D_);
  float var = __builtin_fmaf(-m, m, ss * (1.0f / D_));
  var = var > 0.f ? var : 0.f;
  mean = bfr(m);
  const float sd = bfr(__builtin_sqrtf(bfr(bfr(var) + 1e-5f)));
  rstd = 1.0f / sd;
}
constexpr float GA = -2.302208198144325f;
constexpr float GB = -0.1029432397357593f;
template <int N> DI void geluN(f32x2 (&z)[N], const float (&a)[N], const float (&b)[N]) {
  f32x2 p[N];
#pragma unroll
  for (int k = 0; k < N; ++k) p[k] = z[k] * z[k];
  SB();
#pragma unroll
  for (int k = 0; k < N; ++k) p[k] = p[k] * b[k] + a[k];
  SB();
#pragma unroll
  for (int k = 0; k < N; ++k) p[k] = z[k] * p[k];
  SB();
#pragma unroll
  for (int k = 0; k < N; ++k) { p[k].x = __builtin_amdgcn_exp2f(p[k].x); p[k].y = __builtin_amdgcn_exp2f(p[k].y); }
  SB();
#pragma unroll
  for (int k = 0; k < N; ++k) p[k] = p[k] + 1.0f;
  SB();
#pragma unroll
  for (int k = 0; k < N; ++k) { p[k].x = __builtin_amdgcn_rcpf(p[k].x); p[k].y = __builtin_amdgcn_rcpf(p[k].y); }
  SB();
#pragma unroll
  for (int k = 0; k < N; ++k) z[k] = z[k] * p[k];
  SB();
}

DI unsigned ldsw(int addr) { return *(volatile LAS unsigned*)addr; }
DI v4u lds4(int addr) { return *(volatile LAS v4u*)addr; }
DI void lds_set(int addr, unsigned v, int lane) { if (lane == 0) *(volatile LAS unsigned*)addr = v; }
DI void wait_word(int addr, unsigned v) {
  _Pragma("nounroll") for (unsigned n = 0; n < LDS_CAP; ++n) { if (rfl(ldsw(addr)) >= v) break; __builtin_amdgcn_s_sleep(1); }
  CLOB();
}
DI unsigned min3(v4u x) { return rfl(min(min(x.x, x.y), x.z)); }
DI unsigned min4(v4u x) { return rfl(min(min(x.x, x.y), min(x.z, x.w))); }
DI void wait_full(unsigned v) {
  _Pragma("nounroll") for (unsigned n = 0; n < LDS_CAP; ++n) { if (min3(lds4(L_FULL)) >= v) break; __builtin_amdgcn_s_sleep(1); }
  CLOB();
}
DI void wait_free(unsigned v) {
  _Pragma("nounroll") for (unsigned n = 0; n < LDS_CAP; ++n) { if (min4(lds4(L_FREE)) >= v) break; __builtin_amdgcn_s_sleep(1); }
  CLOB();
}
DI GAS u64* flagp(char* ws, int kind, int b) { return (GAS u64*)(ws + OFF_FLAGS + (kind * 256 + b) * 128); }
DI void arrive(char* ws, int kind, int b, int lane) {
  if (lane == 0) {
    const unsigned old = __hip_atomic_fetch_add((LAS unsigned*)(L_CNT + 4 * kind), 1u, __ATOMIC_RELAXED, __HIP_MEMORY_SCOPE_WORKGROUP);
    if (old == 3u) {
      _Pragma("nounroll") for (unsigned n = 0; n < LDS_CAP; ++n) { if (ldsw(L_RDY + 4 * R_TAG) != 0u) break; __builtin_amdgcn_s_sleep(1); }
      const u64 tag = *(volatile LAS u64*)L_TAG;
      __hip_atomic_store(flagp(ws, kind, b), tag, __ATOMIC_RELAXED, __HIP_MEMORY_SCOPE_AGENT);
    }
  }
}
#define LD16(off, soff) __builtin_amdgcn_raw_buffer_load_b128(rs, (off), (soff), 16)
#define LD8(off, soff) __builtin_amdgcn_raw_buffer_load_b64(rs, (off), (soff), 16)
#define ST16(v, off, soff) __builtin_amdgcn_raw_buffer_store_b128((v), rs, (off), (soff), 16)
#define ST8(v, off, soff) __builtin_amdgcn_raw_buffer_store_b64((v), rs, (off), (soff), 16)
DI void dma(const char* sbase, unsigned voff, unsigned ldsaddr) {
  asm volatile("s_mov_b32 m0, %0\n\ts_nop 0\n\tglobal_load_lds_dwordx4 %1, %2" ::"s"(ldsaddr), "v"(voff), "s"(sbase) : "memory");
}
DI int cstep(int n, int s) { const int u = n + ((9 * s) >> 1); return u >= 18 ? u - 18 : u; }
DI int pbase(int s) { return (9 * s + (s & 1)) >> 1; }
template <int M> DI int dstep(int s) {
  if constexpr (M < 4) return pbase(s) + M;
  else if constexpr (M < 10) return pbase((s + 1 + (M - 4) / 2) & 3) + ((M - 4) & 1);
  else if constexpr (M < 13) return pbase((s + M - 9) & 3) + 2;
  else if constexpr (M < 16) return pbase((s + M - 12) & 3) + 3;
  else return M == 16 ? 4 : 13;
}
#define STAMP(k) do { } while (0)

__global__ __launch_bounds__(512, 2) void mixer_kernel(const char* __restrict__ x, const char* __restrict__ w1, const char* __restrict__ w2,
                                                       const char* __restrict__ w3, const char* __restrict__ w4, char* __restrict__ out, char* ws) {
#if defined(__HIP_DEVICE_COMPILE__)
  __shared__ __attribute__((aligned(16))) char smem[163840];
  const int tid = threadIdx.x, lane = tid & 63, w = rfl(tid >> 6);
  const int t = lane & 15, q = lane >> 4;
  const int b = blockIdx.x, j = b & 7, r = b >> 3;
  const bool ab = r < 24;
  const int rt = 2 * j + (r & 1), ct = r >> 1;
  const int g = r >> 2, s = r & 3;
  const __amdgpu_buffer_rsrc_t rs = __builtin_amdgcn_make_buffer_rsrc(ws, 0, WS_BYTES, 0x00020000);

  {
    const u64 tstart = __builtin_amdgcn_s_memrealtime();
    unsigned acc_ = 0;
    const int gt = b * 512 + tid;
    {
      const int xt = r * 512 + tid;
      const char* ptrs[5] = {x, w1, w2, w3, w4};
      const int lens[5] = {1024 * 576 * 2, 576 * 576 * 2, 576 * 576 * 2, 576 * 4608 * 2, 576 * 4608 * 2};
#pragma unroll
      for (int k = 0; k < 5; ++k)
        for (int o = xt * 128; o < lens[k]; o += 16384 * 128) acc_ ^= *(const GAS unsigned*)(ptrs[k] + o);
    }
    {
      const int xt = r * 512 + tid;
      if (xt * 4096 < OFF_DBG) acc_ ^= *(const GAS unsigned*)(ws + (size_t)xt * 4096 + 2048);
      if (xt * 4096 < 1024 * 576 * 2) acc_ ^= *(const GAS unsigned*)(out + (size_t)xt * 4096);
    }
    {
      const char* pc = (const char*)(__builtin_amdgcn_s_getpc() & ~255ull);
      if (tid * 64 < 39000 - 512) acc_ ^= *(const GAS unsigned*)(pc + tid * 64);
      if ((tid + 512) * 64 < 39000 - 512) acc_ ^= *(const GAS unsigned*)(pc + (tid + 512) * 64);
    }

    for (int o = gt * 128; o < OFF_DBG; o += 131072 * 128) if (o >= OFF_STAT) acc_ ^= *(const GAS unsigned*)(ws + o);
    asm volatile("" ::"v"(acc_), "v"(gt));
    for (int n = 0; n < 100000; ++n) { if (__builtin_amdgcn_s_memrealtime() - tstart >= 2000ull) break; __builtin_amdgcn_s_sleep(2); }
  }
  if (tid < 32) *(volatile LAS unsigned*)(MISC + tid * 4) = 0u;
  asm volatile("" ::"v"(smem));
  STAMP(0);
  asm volatile("s_waitcnt lgkmcnt(0)\n\ts_barrier" ::: "memory");

  if (w < 4) {
    const int i = w;
    if (ab) {
      unsigned ba = 4 * SLOT + (4 * q + (t >> 2)) * 96 + (t & 3) * 8;
      const int T = 4 * rt + i, odd = ct & 1;
      const int upair = (3 * ct + odd) >> 1, usingle = odd ? (3 * ct - 1) >> 1 : (3 * ct + 2) >> 1;
      v2u xres[3];
      {
        const char* xsrc = x + (size_t)T * (16 * D_ * 2);
        const unsigned xl = (i == 3 ? 2 : 10 + 2 * i) * SLOT;
#pragma unroll
        for (int e = 0; e < 18; ++e) {
          const unsigned f = (((unsigned)(lane + 64 * e) * 911u) >> 17) & 7u;
          dma(xsrc + e * 1024, (lane * 16) ^ (f << 4), xl + e * 1024);
        }
        unsigned xa = xl + t * (D_ * 2), xs = ((t >> 1) & 7) << 4;
        unsigned xe0 = xa + ((q * 16) ^ xs), xe1 = xa + ((64 + q * 16) ^ xs);
        const int grp = ct / 3;
        const int sh = grp == 0 ? 1 : grp == 1 ? -1 : grp == 2 ? 2 : -2;
        const int ts = 16 * (i & 1) + t + sh;
        const bool keep = ts >= 0 && ts < 32;
        const int dst = OFF_H1 + ((T & ~1) + ((ts >> 4) & 1)) * (18 * 1024) + ((ts & 15) + 16 * q) * 16;
        asm volatile("" : "+v"(xe0), "+v"(xe1));
        DRAIN();
        STAMP(1);
        bf16x8 xf[18];
#pragma unroll
        for (int u = 0; u < 18; ++u) xf[u] = *(const LAS bf16x8*)(((u & 1) ? xe1 : xe0) + (u >> 1) * 128);
#pragma unroll
        for (int l = 0; l < 3; ++l) {
          const unsigned cb = (48 * ct + 16 * ((l + odd) % 3) + 4 * q) * 2;
          xres[l] = *(const LAS v2u*)(xa + (cb ^ xs));
        }
        CLOB();
        lds_set(L_FREE + 4 * i, 1, lane);
        const bf16x8 ones = {0x3F80, 0x3F80, 0x3F80, 0x3F80, 0x3F80, 0x3F80, 0x3F80, 0x3F80};
        f32x4 aS = {0, 0, 0, 0}, aG = {0, 0, 0, 0};
#pragma unroll
        for (int u = 0; u < 18; ++u) { aS = mfma(ones, xf[u], aS); aG = mfma(xf[u], xf[u], aG); }
        STAMP(17);
        f32x4 acc[3] = {{0, 0, 0, 0}, {0, 0, 0, 0}, {0, 0, 0, 0}};
        float nmean, rstd;
        sfor<2>([&](auto H) INL {
          constexpr int h = H.value;
          wait_full(h + 1);
          asm volatile("" : "+v"(ba));
#pragma unroll
          for (int u = 9 * h; u < 9 * h + 9; ++u)
#pragma unroll
            for (int l = 0; l < 3; ++l) acc[l] = mfma(cat(trd(ba + u * 3072 + l * 32), trd(ba + u * 3072 + 1536 + l * 32)), xf[u], acc[l]);
          if constexpr (h == 0) {
            const int e = t & 3;
            float gd = e == 0 ? aG[0] : e == 1 ? aG[1] : e == 2 ? aG[2] : aG[3];
            gd = (q == (t >> 2)) ? gd : 0.f;
            ln_stats(aS[0], qsum(gd), nmean, rstd);
            nmean = -nmean;
          }
        });
        STAMP(2);
        const float ga = GA * rstd, gb = GB * rstd * rstd * rstd;
        wait_full(3);
        f32x2 z[6];
#pragma unroll
        for (int l = 0; l < 3; ++l) {
          const f32x4 cs = *(const LAS f32x4*)(L_CS1 + l * 64 + q * 16);
          const f32x4 zz = cs * nmean + acc[l];
          z[2 * l] = f32x2{zz.x, zz.y}; z[2 * l + 1] = f32x2{zz.z, zz.w};
        }
        const float ga6[6] = {ga, ga, ga, ga, ga, ga}, gb6[6] = {gb, gb, gb, gb, gb, gb};
        geluN<6>(z, ga6, gb6);
        unsigned hp[6];
#pragma unroll
        for (int k = 0; k < 6; ++k) { const f32x2 v = z[k] * rstd; hp[k] = keep ? pk(v.x, v.y) : 0u; }
        ST16((v4u{hp[0], hp[1], hp[2], hp[3]}), dst, upair * 1024);
        ST8((v2u{hp[4], hp[5]}), dst + odd * 8, usingle * 1024);
        STAMP(3);
        DRAIN();
        lds_set(L_FREE + 4 * i, 2, lane);
        arrive(ws, FA, b, lane);
        STAMP(4);
      }
      {
        wait_full(4);
        wait_word(L_RDY + 4 * R_H, 1);
        STAMP(5);
        bf16x8 hf[18];
#pragma unroll
        for (int u = 0; u < 18; ++u) hf[u] = asfrag(LD16(lane * 16 + u * 1024, OFF_H1 + T * (18 * 1024)));
        SB();
        unsigned bb = ba + 6 * SLOT;
        asm volatile("" : "+v"(bb));
        f32x4 acc[3] = {{0, 0, 0, 0}, {0, 0, 0, 0}, {0, 0, 0, 0}};
#pragma unroll
        for (int u = 0; u < 18; ++u)
#pragma unroll
          for (int l = 0; l < 3; ++l) acc[l] = mfma(cat(trd(bb + u * 3072 + l * 32), trd(bb + u * 3072 + 1536 + l * 32)), hf[u], acc[l]);
        STAMP(6);
        unsigned xp[6];
        float sm = 0.f, sq = 0.f;
#pragma unroll
        for (int l = 0; l < 3; ++l) {
          const unsigned p0 = pk(acc[l].x, acc[l].y), p1 = pk(acc[l].z, acc[l].w);
          xp[2 * l] = pk(lo16(xres[l].x) + lo16(p0), hi16(xres[l].x) + hi16(p0));
          xp[2 * l + 1] = pk(lo16(xres[l].y) + lo16(p1), hi16(xres[l].y) + hi16(p1));
          const float v0 = lo16(xp[2 * l]), v1 = hi16(xp[2 * l]), v2 = lo16(xp[2 * l + 1]), v3 = hi16(xp[2 * l + 1]);
          sm += (v0 + v1) + (v2 + v3);
          sq += (v0 * v0 + v1 * v1) + (v2 * v2 + v3 * v3);
        }
        const int dst = OFF_X1 + T * (18 * 1024) + lane * 16;
        ST16((v4u{xp[0], xp[1], xp[2], xp[3]}), dst, upair * 1024);
        ST8((v2u{xp[4], xp[5]}), dst + odd * 8, usingle * 1024);
        sm = qsum(sm); sq = qsum(sq);
        if (q == 0) ST8((v2u{__float_as_uint(sm), __float_as_uint(sq)}), OFF_STAT + t * 8, (T * 12 + ct) * 128);
        STAMP(7);
        DRAIN();
        lds_set(L_FREE + 4 * i, 3, lane);
        arrive(ws, FB, b, lane);
        STAMP(8);
      }
    }
    unsigned b0 = (4 * q + (t >> 2)) * 288 + (t & 3) * 8, b1 = b0 + 6 * SLOT, b2 = b0 + 12 * SLOT;
    asm volatile("" : "+v"(b0), "+v"(b1), "+v"(b2));
#define WADDR(z, c) ((((z) % 16) < 6 ? b0 : ((z) % 16) < 12 ? b1 : b2) + (((z) % 16) % 6) * SLOT + (c) * 32)
#define FN(i_) ((i_) < 108 ? (i_) / 9 : 12 + ((i_) - 108) % 6)
#define FC(i_) ((i_) < 108 ? (i_) % 9 : ((i_) - 108) / 6)
    const int T0 = 8 * g + 2 * i;
    const int hdst = OFF_HH + (T0 * 144 + j * 18) * 1024;
    bf16x8 own[4][2];
    s16x4 lo[PD], hi[PD];
    {
      wait_full(6);
      wait_word(L_RDY + 4 * (R_X0 + (i >> 1)), 1);
      STAMP(9);
      v2u st[2][3];
#pragma unroll
      for (int tt = 0; tt < 2; ++tt)
#pragma unroll
        for (int k = 0; k < 3; ++k) st[tt][k] = LD8(OFF_STAT + (3 * q + k) * 128 + t * 8, (T0 + tt) * (12 * 128));
      const int xsrc = OFF_X1 + T0 * (18 * 1024);
      bf16x8 xb[12][2];
#pragma unroll
      for (int n = 0; n < 12; ++n)
#pragma unroll
        for (int tt = 0; tt < 2; ++tt) xb[n][tt] = asfrag(LD16(lane * 16 + tt * (18 * 1024), xsrc + cstep(n, s) * 1024));
      SB();
#pragma unroll
      for (int k = 0; k < PD; ++k) { lo[k] = trd(WADDR(0, k)); hi[k] = trd(WADDR(0, k) + 4608); }
      float mean[2], ga[2], gb[2];
#pragma unroll
      for (int tt = 0; tt < 2; ++tt) {
        const float sm = qsum((__uint_as_float(st[tt][0].x) + __uint_as_float(st[tt][1].x)) + __uint_as_float(st[tt][2].x));
        const float sq = qsum((__uint_as_float(st[tt][0].y) + __uint_as_float(st[tt][1].y)) + __uint_as_float(st[tt][2].y));
        float rstd;
        ln_stats(sm, sq, mean[tt], rstd);
        mean[tt] = -mean[tt];
        ga[tt] = GA * rstd; gb[tt] = GB * rstd * rstd * rstd;
      }
      f32x4 acc[2][9];
#pragma unroll
      for (int c = 0; c < 9; ++c) { acc[0][c] = f32x4{0, 0, 0, 0}; acc[1][c] = f32x4{0, 0, 0, 0}; }
      unsigned hp[2][2][2];
      f32x4 csv;
      v4u fv;
      auto epi = [&](auto Cc) INL {
        constexpr int c = Cc.value;
        f32x2 z[4];
#pragma unroll
        for (int tt = 0; tt < 2; ++tt) {
          const f32x4 zz = csv * mean[tt] + acc[tt][c];
          z[2 * tt] = f32x2{zz.x, zz.y}; z[2 * tt + 1] = f32x2{zz.z, zz.w};
        }
        SB();
        const float ga4[4] = {ga[0], ga[0], ga[1], ga[1]}, gb4[4] = {gb[0], gb[0], gb[1], gb[1]};
        geluN<4>(z, ga4, gb4);
#pragma unroll
        for (int tt = 0; tt < 2; ++tt) { hp[tt][c & 1][0] = pk(z[2 * tt].x, z[2 * tt].y); hp[tt][c & 1][1] = pk(z[2 * tt + 1].x, z[2 * tt + 1].y); }
        if constexpr (c == 5) { STAMP(18); DRAIN(); arrive(ws, FC1, b, lane); STAMP(19); }
        if constexpr ((c & 1) && c < 8) {
#pragma unroll
          for (int tt = 0; tt < 2; ++tt) {
            const v4u v = {hp[tt][0][0], hp[tt][0][1], hp[tt][1][0], hp[tt][1][1]};
            own[c >> 1][tt] = asfrag(v);
            ST16(v, lane * 16 + tt * (144 * 1024), hdst + (pbase(s) + (c >> 1)) * 1024);
          }
        }
        if constexpr (c == 8) {
#pragma unroll
          for (int tt = 0; tt < 2; ++tt)
            ST8((v2u{hp[tt][0][0], hp[tt][0][1]}), lane * 16 + tt * (144 * 1024) + (s & 1) * 8, hdst + (s < 2 ? 4 : 13) * 1024);
          STAMP(22);
          VMW(4); arrive(ws, FC2, b, lane);
        }
        SB();
      };
      sfor<162>([&](auto I) INL {
        constexpr int ii = I.value, n = FN(ii), c = FC(ii), nx = ii - 1 + PD;
        const bf16x8 cur = cat(lo[ii % PD], hi[ii % PD]);
        acc[0][c] = mfma(cur, xb[n % 12][0], acc[0][c]); SB();
        if constexpr (ii >= 1 && nx < 162) lo[nx % PD] = trd(WADDR(FN(nx), FC(nx)));
        SB();
        acc[1][c] = mfma(cur, xb[n % 12][1], acc[1][c]); SB();
        if constexpr (ii >= 1 && nx < 162) hi[nx % PD] = trd(WADDR(FN(nx), FC(nx)) + 4608);
        SB();
        if constexpr (ii < 108 && c == 8) {
          if constexpr (n < 6) {
#pragma unroll
            for (int tt = 0; tt < 2; ++tt) xb[n][tt] = asfrag(LD16(lane * 16 + tt * (18 * 1024), xsrc + cstep(12 + n, s) * 1024));
          }
          if constexpr (n == 1 || n == 8 || n == 11) { CLOB(); lds_set(L_FREE + 4 * i, n == 1 ? 4 : n == 8 ? 5 : 6, lane); }
          if constexpr (n == 0) STAMP(23);
          if constexpr (n == 3) STAMP(24);
          if constexpr (n == 7) STAMP(25);
          SB();
        }
        if constexpr (ii == 96) { fv = lds4(L_FULL); SB(); }
        if constexpr (ii == 104) { if (min3(fv) < 9) wait_full(9); CLOB(); SB(); STAMP(10); }
        if constexpr (ii >= 108 && n == 14 && c >= 1) { csv = *(const LAS f32x4*)(L_CS3 + (c - 1) * 64 + q * 16); SB(); }
        if constexpr (ii >= 108 && n == 17 && c >= 1) epi(std::integral_constant<int, c - 1>{});
        if constexpr (ii == 150) { fv = lds4(L_FULL); SB(); }
      });
      CLOB();
      lds_set(L_FREE + 4 * i, 7, lane);
      csv = *(const LAS f32x4*)(L_CS3 + 8 * 64 + q * 16);
      if (min3(fv) < 10) wait_full(10);
      CLOB();
#pragma unroll
      for (int k = 0; k < PD; ++k) { lo[k] = trd(WADDR(18, k)); hi[k] = trd(WADDR(18, k) + 4608); }
      SB();
      epi(std::integral_constant<int, 8>{});
      STAMP(11);
    }
    {
      bf16x8 hb[18][2];
#pragma unroll
      for (int m = 0; m < 4; ++m) { hb[m][0] = own[m][0]; hb[m][1] = own[m][1]; }
      auto hload = [&](auto M) INL {
        constexpr int m = M.value;
#pragma unroll
        for (int tt = 0; tt < 2; ++tt) hb[m][tt] = asfrag(LD16(lane * 16 + tt * (144 * 1024), hdst + dstep<m>(s) * 1024));
      };
      wait_word(L_RDY + 4 * R_H1, 1);
      sfor<6>([&](auto M) INL { hload(std::integral_constant<int, 4 + M.value>{}); });
      SB();
      STAMP(12);
      f32x4 acc[2][9];
#pragma unroll
      for (int c = 0; c < 9; ++c) { acc[0][c] = f32x4{0, 0, 0, 0}; acc[1][c] = f32x4{0, 0, 0, 0}; }
      const int pdst = OFF_P + (((g * 4 + s) * 8 + 2 * i) * 8 + j) * (5 * 1024);
      v4u fv;
      unsigned rv;
      auto epi = [&](auto Cc) INL {
        constexpr int c = Cc.value;
        if constexpr ((c & 1) && c < 8) {
#pragma unroll
          for (int tt = 0; tt < 2; ++tt) {
            const f32x4 a0 = acc[tt][c - 1], a1 = acc[tt][c];
            ST16((v4u{pk(a0.x, a0.y), pk(a0.z, a0.w), pk(a1.x, a1.y), pk(a1.z, a1.w)}), lane * 16 + tt * (40 * 1024) + (c >> 1) * 1024, pdst);
          }
        }
        if constexpr (c == 8) {
#pragma unroll
          for (int tt = 0; tt < 2; ++tt) ST16(__builtin_bit_cast(v4u, acc[tt][8]), lane * 16 + tt * (40 * 1024) + 4096, pdst);
        }
        SB();
      };
      sfor<162>([&](auto I) INL {
        constexpr int ii = I.value, n = FN(ii), c = FC(ii), nx = ii - 1 + PD;
        const bf16x8 cur = cat(lo[ii % PD], hi[ii % PD]);
        acc[0][c] = mfma(cur, hb[n][0], acc[0][c]); SB();
        if constexpr (ii >= 1 && nx < 162) lo[nx % PD] = trd(WADDR(18 + FN(nx), FC(nx)));
        SB();
        acc[1][c] = mfma(cur, hb[n][1], acc[1][c]); SB();
        if constexpr (ii >= 1 && nx < 162) hi[nx % PD] = trd(WADDR(18 + FN(nx), FC(nx)) + 4608);
        SB();
        if constexpr (ii == 17) { CLOB(); lds_set(L_FREE + 4 * i, 8, lane); SB(); }
        if constexpr (ii == 35) { VMW(12); arrive(ws, FC3, b, lane); SB(); }
        if constexpr (ii == 50) { rv = ldsw(L_RDY + 4 * R_H2); SB(); }
        if constexpr (ii == 56) {
          if (rfl(rv) == 0) wait_word(L_RDY + 4 * R_H2, 1);
          CLOB();
          sfor<3>([&](auto M) INL { hload(std::integral_constant<int, 10 + M.value>{}); });
          SB();
          STAMP(13);
        }
        if constexpr (ii == 72) { fv = lds4(L_FULL); SB(); }
        if constexpr (ii == 80) { if (min3(fv) < 11) wait_full(11); CLOB(); SB(); }
        if constexpr (ii == 84) { rv = ldsw(L_RDY + 4 * R_H3); SB(); }
        if constexpr (ii == 90) {
          if (rfl(rv) == 0) wait_word(L_RDY + 4 * R_H3, 1);
          CLOB();
          sfor<5>([&](auto M) INL { hload(std::integral_constant<int, 13 + M.value>{}); });
          SB();
          STAMP(14);
        }
        if constexpr (ii == 96) { fv = lds4(L_FULL); SB(); }
        if constexpr (ii == 104) { if (min3(fv) < 12) wait_full(12); CLOB(); SB(); }
        if constexpr (ii >= 108 && n == 17 && c >= 1) epi(std::integral_constant<int, c - 1>{});
      });
      epi(std::integral_constant<int, 8>{});
      STAMP(15);
      DRAIN();
      arrive(ws, FD, b, lane);
      STAMP(16);
    }
  } else if (w < 7) {
    const int ld = w - 4;
    const unsigned fullw = L_FULL + 4 * ld;
    unsigned v3[3], v4[3];
#pragma unroll
    for (int e = 0; e < 3; ++e) {
      const int idx = (3 * ld + e) * 64 + lane, row = idx / 18, ch = idx % 18;
      const int sc = (ch + 2 * (s & 1)) % 18;
      v3[e] = row * (F_ * 2) + sc * 16;
      v4[e] = row * (D_ * 2) + sc * 16;
    }
    const char* w3b = w3 + (576 * j + 144 * s) * 2;
    const char* w4b = w4 + (size_t)(576 * j) * (D_ * 2) + 144 * s * 2;
    auto w3slice = [&](int n) INL {
#pragma unroll
      for (int e = 0; e < 3; ++e) dma(w3b + (size_t)cstep(n, s) * (32 * F_ * 2), v3[e], (n & 15) * SLOT + (3 * ld + e) * 1024);
    };
    auto w4slice = [&](auto M) INL {
      constexpr int m = M.value;
      const char* src = w4b + dstep<m>(s) * (32 * D_ * 2);
#pragma unroll
      for (int e = 0; e < 3; ++e) dma(src, v4[e], ((18 + m) & 15) * SLOT + (3 * ld + e) * 1024);
    };
    const bf16x8 ones = {0x3F80, 0x3F80, 0x3F80, 0x3F80, 0x3F80, 0x3F80, 0x3F80, 0x3F80};
    f32x4 cs[3] = {{0, 0, 0, 0}, {0, 0, 0, 0}, {0, 0, 0, 0}};
    const unsigned cb = (4 * q + (t >> 2)) * 288 + (t & 3) * 8 + ld * 96;
    auto colsum3 = [&](int n0, int n1) INL {
      _Pragma("nounroll") for (int n = n0; n < n1; ++n) {
        const unsigned a = cb + (n & 15) * SLOT;
#pragma unroll
        for (int c = 0; c < 3; ++c) cs[c] = mfma(cat(trd(a + c * 32), trd(a + 4608 + c * 32)), ones, cs[c]);
      }
      CLOB();
    };
    if (ab) {
      const int idx = ld * 64 + lane, row = idx / 6, ch = idx % 6;
      const int sc = (ch + 2 * (ct & 1)) % 6;
      const int krow = 8 * ((row >> 2) & 3) + 4 * (row >> 4) + (row & 3);
      const unsigned v1 = krow * (D_ * 2) + sc * 16, v2 = row * (D_ * 2) + sc * 16;
      _Pragma("nounroll") for (int u = 0; u < 18; ++u) dma(w1 + 96 * ct + u * (32 * D_ * 2), v1, 4 * SLOT + u * 3072 + ld * 1024);
      STAMP(1);
      VMW(9); lds_set(fullw, 1, lane);
      wait_free(1);
      _Pragma("nounroll") for (int u = 0; u < 6; ++u) dma(w2 + 96 * ct + u * (32 * D_ * 2), v2, 10 * SLOT + u * 3072 + ld * 1024);
      VMW(6); lds_set(fullw, 2, lane);
      _Pragma("nounroll") for (int u = 6; u < 18; ++u) dma(w2 + 96 * ct + u * (32 * D_ * 2), v2, 10 * SLOT + u * 3072 + ld * 1024);
      STAMP(2);
      wait_full(2);
      {
        f32x4 c1 = {0, 0, 0, 0};
        const unsigned a = 4 * SLOT + (4 * q + (t >> 2)) * 96 + (t & 3) * 8 + ld * 32;
        s16x4 l1[18], h1[18];
#pragma unroll
        for (int u = 0; u < 18; ++u) { l1[u] = trd(a + u * 3072); h1[u] = trd(a + u * 3072 + 1536); }
        SB();
#pragma unroll
        for (int u = 0; u < 18; ++u) c1 = mfma(cat(l1[u], h1[u]), ones, c1);
        if (t == 0) *(LAS f32x4*)(L_CS1 + ld * 64 + q * 16) = c1;
        CLOB();
      }
      lds_set(fullw, 3, lane);
      STAMP(3);
      DRAIN(); lds_set(fullw, 4, lane);
      wait_free(2); wait_full(3);
      _Pragma("nounroll") for (int n = 0; n < 10; ++n) w3slice(n);
      STAMP(4);
      DRAIN(); lds_set(fullw, 5, lane);
      wait_full(5);
      colsum3(0, 10);
      wait_free(3);
      _Pragma("nounroll") for (int n = 10; n < 16; ++n) w3slice(n);
      STAMP(5);
      DRAIN(); lds_set(fullw, 6, lane);
      STAMP(6);
      wait_full(6);
      colsum3(10, 16);
    } else {
      _Pragma("nounroll") for (int n = 0; n < 16; ++n) w3slice(n);
      DRAIN(); lds_set(fullw, 6, lane);
      wait_full(6);
      colsum3(0, 16);
    }
    lds_set(fullw, 7, lane);
    STAMP(7);
    wait_free(4); wait_full(7);
    STAMP(11);
    w3slice(16); w3slice(17);
    DRAIN(); lds_set(fullw, 8, lane);
    STAMP(12);
    wait_full(8);
    colsum3(16, 18);
    if (t == 0) {
#pragma unroll
      for (int c = 0; c < 3; ++c) *(LAS f32x4*)(L_CS3 + (3 * ld + c) * 64 + q * 16) = cs[c];
    }
    CLOB();
    lds_set(fullw, 9, lane);
    STAMP(8);
    wait_free(5);
    sfor<7>([&](auto M) INL { w4slice(M); });
    wait_free(6);
    sfor<3>([&](auto M) INL { w4slice(std::integral_constant<int, 7 + M.value>{}); });
    DRAIN(); lds_set(fullw, 10, lane);
    STAMP(9);
    wait_free(7); wait_full(9);
    sfor<6>([&](auto M) INL { w4slice(std::integral_constant<int, 10 + M.value>{}); });
    wait_free(8);
    sfor<2>([&](auto M) INL { w4slice(std::integral_constant<int, 16 + M.value>{}); });
    VMW(6); lds_set(fullw, 11, lane);
    DRAIN(); lds_set(fullw, 12, lane);
    STAMP(10);
  } else {
    const u64 tag = __hip_atomic_load((GAS u64*)(ws + OFF_EPOCH), __ATOMIC_RELAXED, __HIP_MEMORY_SCOPE_AGENT) + 1ull;
    if (lane == 0) {
      *(volatile LAS u64*)L_TAG = tag;
      *(volatile LAS unsigned*)(L_RDY + 4 * R_TAG) = 1u;
      __hip_atomic_store(flagp(ws, FS, b), tag, __ATOMIC_RELAXED, __HIP_MEMORY_SCOPE_AGENT);
    }
    STAMP(1);
    auto poll = [&](int kind, int wg, bool need) INL {
      GAS u64* p = flagp(ws, kind, wg);
      _Pragma("nounroll") for (unsigned n = 0; n < GLB_CAP; ++n) {
        const u64 v = __hip_atomic_load(p, __ATOMIC_RELAXED, __HIP_MEMORY_SCOPE_AGENT);
        if (__all(!need || v == tag)) break;
        __builtin_amdgcn_s_sleep(1);
      }
      CLOB();
    };
    if (ab) {
      poll(FA, j + 8 * ((r & 1) + 2 * (lane % 12)), true);
      lds_set(L_RDY + 4 * R_H, 1, lane);
    }
    STAMP(2);
    {
      GAS u64* p = flagp(ws, FB, g + 8 * (lane % 24));
      bool d0 = false, d1 = false;
      _Pragma("nounroll") for (unsigned n = 0; n < GLB_CAP; ++n) {
        const u64 v = __hip_atomic_load(p, __ATOMIC_RELAXED, __HIP_MEMORY_SCOPE_AGENT);
        const bool ok = v == tag;
        if (!d0 && __all(ok || (lane & 1))) { d0 = true; lds_set(L_RDY + 4 * R_X0, 1, lane); }
        if (!d1 && __all(ok || !(lane & 1))) { d1 = true; lds_set(L_RDY + 4 * R_X1, 1, lane); }
        if (d0 && d1) break;
        __builtin_amdgcn_s_sleep(1);
      }
      lds_set(L_RDY + 4 * R_X0, 1, lane); lds_set(L_RDY + 4 * R_X1, 1, lane);
    }
    STAMP(3);
    if (b == 0) {
#pragma unroll
      for (int k = 0; k < 4; ++k) poll(FS, k * 64 + lane, true);
      if (lane == 0) __hip_atomic_store((GAS u64*)(ws + OFF_EPOCH), tag, __ATOMIC_RELAXED, __HIP_MEMORY_SCOPE_AGENT);
    }
    const int peer = j + 8 * (4 * g + (lane & 3));
    poll(FC1, peer, true); lds_set(L_RDY + 4 * R_H1, 1, lane);
    STAMP(4);
    poll(FC2, peer, true); lds_set(L_RDY + 4 * R_H2, 1, lane);
    STAMP(5);
    poll(FC3, peer, true); lds_set(L_RDY + 4 * R_H3, 1, lane);
    STAMP(6);
  }
  if (w >= 3) {
    const int p = w - 3;
    const int T = 8 * g + j;
    if (w != 7) { wait_word(L_RDY + 4 * R_X0, 1); wait_word(L_RDY + 4 * R_X1, 1); }
    v2u st[3];
#pragma unroll
    for (int k = 0; k < 3; ++k) st[k] = LD8(OFF_STAT + (3 * q + k) * 128 + t * 8, T * (12 * 128));
    const int u = p < 4 ? pbase(s) + p : (s < 2 ? 4 : 13);
    const v4u xv = LD16(lane * 16, OFF_X1 + (T * 18 + u) * 1024);
    float mean, rstd;
    ln_stats(qsum((__uint_as_float(st[0].x) + __uint_as_float(st[1].x)) + __uint_as_float(st[2].x)),
             qsum((__uint_as_float(st[0].y) + __uint_as_float(st[1].y)) + __uint_as_float(st[2].y)), mean, rstd);
    if (w == 7) {
      const u64 tag = *(volatile LAS u64*)L_TAG;
      GAS u64* fp = flagp(ws, FD, (lane & 7) + 8 * (4 * g + s));
      _Pragma("nounroll") for (unsigned n = 0; n < GLB_CAP; ++n) {
        const u64 v = __hip_atomic_load(fp, __ATOMIC_RELAXED, __HIP_MEMORY_SCOPE_AGENT);
        if (__all(v == tag)) break;
        __builtin_amdgcn_s_sleep(1);
      }
      CLOB();
      lds_set(L_RDY + 4 * R_P, 1, lane);
    } else {
      wait_word(L_RDY + 4 * R_P, 1);
    }
    STAMP(20);
    v4u pv[8];
    const int psrc = OFF_P + (((g * 4 + s) * 8 + j) * 8 * 5 + p) * 1024;
#pragma unroll
    for (int k = 0; k < 8; ++k) pv[k] = LD16(lane * 16 + k * (5 * 1024), psrc);
    char* orow = out + (size_t)(16 * T + t) * (D_ * 2) + (32 * u + 4 * q) * 2;
    if (p < 4) {
      float a[8];
#pragma unroll
      for (int e = 0; e < 8; ++e) a[e] = 0.f;
#pragma unroll
      for (int k = 0; k < 8; ++k)
#pragma unroll
        for (int e = 0; e < 4; ++e) { const unsigned d = pv[k][e]; a[2 * e] += lo16(d); a[2 * e + 1] += hi16(d); }
      unsigned o[4];
#pragma unroll
      for (int e = 0; e < 4; ++e) {
        const unsigned h2 = pk(a[2 * e] * rstd, a[2 * e + 1] * rstd), xe = xv[e];
        o[e] = pk(lo16(xe) + lo16(h2), hi16(xe) + hi16(h2));
      }
      *(GAS v2u*)orow = v2u{o[0], o[1]};
      *(GAS v2u*)(orow + 32) = v2u{o[2], o[3]};
    } else {
      f32x4 a = {0, 0, 0, 0};
#pragma unroll
      for (int k = 0; k < 8; ++k) a += __builtin_bit_cast(f32x4, pv[k]);
      const unsigned x0 = (s & 1) ? xv.z : xv.x, x1v = (s & 1) ? xv.w : xv.y;
      const unsigned h0 = pk(a.x * rstd, a.y * rstd), h1 = pk(a.z * rstd, a.w * rstd);
      *(GAS v2u*)(orow + (s & 1) * 32) = v2u{pk(lo16(x0) + lo16(h0), hi16(x0) + hi16(h0)), pk(lo16(x1v) + lo16(h1), hi16(x1v) + hi16(h1))};
    }
    STAMP(21);
  }
#endif
}
}

extern "C" void kernel_launch(void* const* d_in, const int* in_sizes, int n_in, void* d_out, int out_size, void* d_ws, size_t ws_size,
                              hipStream_t stream) {
  mx::mixer_kernel<<<256, 512, 0, stream>>>((const char*)d_in[0], (const char*)d_in[1], (const char*)d_in[2], (const char*)d_in[3],
                                            (const char*)d_in[4], (char*)d_out, (char*)d_ws);
}
```

```cpp
#include <hip/hip_runtime.h>
#include <utility>

#define STAMPS 0

#define LAS __attribute__((address_space(3)))
#define GAS __attribute__((address_space(1)))
#define DI __device__ __forceinline__
typedef unsigned v4u __attribute__((ext_vector_type(4)));
typedef unsigned v2u __attribute__((ext_vector_type(2)));
typedef float f32x4 __attribute__((ext_vector_type(4)));
typedef float f32x2 __attribute__((ext_vector_type(2)));
typedef short bf16x8 __attribute__((ext_vector_type(8)));
typedef short s16x4 __attribute__((ext_vector_type(4)));
typedef __bf16 bf16x2_t __attribute__((ext_vector_type(2)));
typedef unsigned long long u64;

namespace mx {
constexpr int D_ = 576, F_ = 4608;
constexpr int OFF_EPOCH = 0;
constexpr int OFF_FLAGS = 4096;
constexpr int OFF_STAT = 327680;
constexpr int OFF_H1 = 524288;
constexpr int OFF_X1 = OFF_H1 + 64 * 18 * 1024;
constexpr int OFF_HH = OFF_X1 + 64 * 18 * 1024;
constexpr int PUNIT = 4608;
constexpr int OFF_P = OFF_HH + 64 * 144 * 1024;
constexpr int OFF_DBG = OFF_P + 8 * 4 * 8 * 8 * PUNIT;
constexpr int WS_BYTES = OFF_DBG + 2 * 256 * 8 * 32 * 8;
enum { FS = 0, FA = 1, FB = 2, FC1 = 3, FC2 = 4, FC3 = 5, FC4 = 6, FD1 = 7, FD2 = 8 };
constexpr int SLOT = 9216, MISC = 16 * SLOT;
constexpr int L_FULL = MISC, L_FREE = MISC + 16, L_RDY = MISC + 32  , L_CNT = MISC + 80  , L_TAG = MISC + 120;
constexpr int L_CS1 = MISC + 256  , L_CS3 = MISC + 1024  , L_LN = MISC + 2048  , L_OWN = MISC + 4096  ,
              L_DBG = MISC + 12288;
enum { R_H = 0, R_X0 = 1, R_X1 = 2, R_H1 = 3, R_H2 = 4, R_H3 = 5, R_H4 = 6, R_P1 = 7, R_P2 = 8, R_TAG = 9, R_LN = 10 };
constexpr unsigned LDS_CAP = 1u << 21, GLB_CAP = 1u << 16;
constexpr int PD = 4;

template <class F, int... I> DI void sfor_impl(F&& f, std::integer_sequence<int, I...>) { (f(std::integral_constant<int, I>{}), ...); }
template <int N, class F> DI void sfor(F&& f) { sfor_impl(f, std::make_integer_sequence<int, N>{}); }
template <int V> using IC = std::integral_constant<int, V>;
#define INL __attribute__((always_inline))
#define NOUNROLL _Pragma("nounroll")
#define SB() __builtin_amdgcn_sched_barrier(0)
#define CLOB() asm volatile("" ::: "memory")
#define DRAIN() asm volatile("s_waitcnt vmcnt(0)" ::: "memory")
#define VMW(n) asm volatile("s_waitcnt vmcnt(%0)" ::"i"(n) : "memory")

DI unsigned rfl(unsigned v) { return __builtin_amdgcn_readfirstlane(v); }
DI f32x4 mfma(bf16x8 a, bf16x8 b, f32x4 c) { return __builtin_amdgcn_mfma_f32_16x16x32_bf16(a, b, c, 0, 0, 0); }
DI bf16x8 asfrag(v4u v) { return __builtin_bit_cast(bf16x8, v); }
DI s16x4 trd(unsigned addr) { return __builtin_amdgcn_ds_read_tr16_b64_v4i16((LAS s16x4*)addr); }
DI bf16x8 cat(s16x4 lo, s16x4 hi) { return __builtin_shufflevector(lo, hi, 0, 1, 2, 3, 4, 5, 6, 7); }
DI unsigned pk(float a, float b) { bf16x2_t r = __builtin_convertvector(f32x2{a, b}, bf16x2_t); return __builtin_bit_cast(unsigned, r); }
DI float lo16(unsigned p) { return __uint_as_float(p << 16); }
DI float hi16(unsigned p) { return __uint_as_float(p & 0xffff0000u); }
DI float bfr(float a) { return lo16(pk(a, a)); }
DI float qsum(float v) {
  auto r = __builtin_amdgcn_permlane16_swap(__float_as_uint(v), __float_as_uint(v), false, false);
  float a = __uint_as_float(r[0]) + __uint_as_float(r[1]);
  auto r2 = __builtin_amdgcn_permlane32_swap(__float_as_uint(a), __float_as_uint(a), false, false);
  return __uint_as_float(r2[0]) + __uint_as_float(r2[1]);
}
constexpr float GA = -2.302208198144325f;
constexpr float GB = -0.1029432397357593f;
DI f32x4 ln_consts(float s, float ss) {
  const float m = s * (1.0f / D_);
  float var = __builtin_fmaf(-m, m, ss * (1.0f / D_));
  var = var > 0.f ? var : 0.f;
  const float sd = bfr(__builtin_sqrtf(bfr(bfr(var) + 1e-5f)));
  const float r = 1.0f / sd;
  return f32x4{-bfr(m), GA * r, GB * r * r * r, r};
}
template <int N> DI void geluN(f32x2 (&z)[N], const float (&a)[N], const float (&b)[N]) {
  f32x2 p[N];
#pragma unroll
  for (int k = 0; k < N; ++k) p[k] = z[k] * z[k];
  SB();
#pragma unroll
  for (int k = 0; k < N; ++k) p[k] = p[k] * b[k] + a[k];
  SB();
#pragma unroll
  for (int k = 0; k < N; ++k) p[k] = z[k] * p[k];
  SB();
#pragma unroll
  for (int k = 0; k < N; ++k) { p[k].x = __builtin_amdgcn_exp2f(p[k].x); p[k].y = __builtin_amdgcn_exp2f(p[k].y); }
  SB();
#pragma unroll
  for (int k = 0; k < N; ++k) p[k] = p[k] + 1.0f;
  SB();
#pragma unroll
  for (int k = 0; k < N; ++k) { p[k].x = __builtin_amdgcn_rcpf(p[k].x); p[k].y = __builtin_amdgcn_rcpf(p[k].y); }
  SB();
#pragma unroll
  for (int k = 0; k < N; ++k) z[k] = z[k] * p[k];
  SB();
}

DI unsigned ldsw(int addr) { return *(volatile LAS unsigned*)addr; }
DI v4u lds4(int addr) { return *(volatile LAS v4u*)addr; }
DI void lds_set(int addr, unsigned v, int lane) { if (lane == 0) *(volatile LAS unsigned*)addr = v; }
DI void wait_word(int addr, unsigned v) {
  NOUNROLL for (unsigned n = 0; n < LDS_CAP; ++n) { if (rfl(ldsw(addr)) >= v) break; __builtin_amdgcn_s_sleep(1); }
  CLOB();
}
DI unsigned min3(v4u x) { return rfl(min(min(x.x, x.y), x.z)); }
DI unsigned min4(v4u x) { return rfl(min(min(x.x, x.y), min(x.z, x.w))); }
DI void wait_full(unsigned v) {
  NOUNROLL for (unsigned n = 0; n < LDS_CAP; ++n) { if (min3(lds4(L_FULL)) >= v) break; __builtin_amdgcn_s_sleep(1); }
  CLOB();
}
DI void wait_free(unsigned v) {
  NOUNROLL for (unsigned n = 0; n < LDS_CAP; ++n) { if (min4(lds4(L_FREE)) >= v) break; __builtin_amdgcn_s_sleep(1); }
  CLOB();
}
DI GAS u64* flagp(char* ws, int kind, int b) { return (GAS u64*)(ws + OFF_FLAGS + (kind * 256 + b) * 128); }
DI void arrive(char* ws, int kind, int b, int lane) {
  if (lane == 0) {
    const unsigned old = __hip_atomic_fetch_add((LAS unsigned*)(L_CNT + 4 * kind), 1u, __ATOMIC_RELAXED, __HIP_MEMORY_SCOPE_WORKGROUP);
    if (old == 3u) {
      NOUNROLL for (unsigned n = 0; n < LDS_CAP; ++n) { if (ldsw(L_RDY + 4 * R_TAG) != 0u) break; __builtin_amdgcn_s_sleep(1); }
      const u64 tag = *(volatile LAS u64*)L_TAG;
      __hip_atomic_store(flagp(ws, kind, b), tag, __ATOMIC_RELAXED, __HIP_MEMORY_SCOPE_AGENT);
    }
  }
}
#define LD16(off, soff) __builtin_amdgcn_raw_buffer_load_b128(rs, (off), (soff), 16)
#define LD8(off, soff) __builtin_amdgcn_raw_buffer_load_b64(rs, (off), (soff), 16)
#define ST16(v, off, soff) __builtin_amdgcn_raw_buffer_store_b128((v), rs, (off), (soff), 16)
#define ST8(v, off, soff) __builtin_amdgcn_raw_buffer_store_b64((v), rs, (off), (soff), 16)
DI void dma(const char* sbase, unsigned voff, unsigned ldsaddr) {
  asm volatile("s_mov_b32 m0, %0\n\ts_nop 0\n\tglobal_load_lds_dwordx4 %1, %2" ::"s"(ldsaddr), "v"(voff), "s"(sbase) : "memory");
}
DI void dmac(const char* sbase, unsigned voff, unsigned ldsaddr) { dma(sbase, voff, ldsaddr); VMW(6); }
DI int cstep(int n, int s) { const int u = n + ((9 * s) >> 1); return u >= 18 ? u - 18 : u; }
DI int pbase(int s) { return (9 * s + (s & 1)) >> 1; }
template <int M> DI int dstep(int s) {
  if constexpr (M < 4) return pbase(s) + M;
  else if constexpr (M < 16) return pbase((s + 1 + (M - 4) % 3) & 3) + (M - 4) / 3;
  else return M == 16 ? 4 : 13;
}
#define STAMP(k) do { } while (0)

__global__ __launch_bounds__(512, 2) void mixer_kernel(const char* __restrict__ x, const char* __restrict__ w1, const char* __restrict__ w2,
                                                       const char* __restrict__ w3, const char* __restrict__ w4, char* __restrict__ out, char* ws) {
#if defined(__HIP_DEVICE_COMPILE__)
  __shared__ __attribute__((aligned(16))) char smem[163840];
  const int tid = threadIdx.x, lane = tid & 63, w = rfl(tid >> 6);
  const int t = lane & 15, q = lane >> 4;
  const int b = blockIdx.x, j = b & 7, r = b >> 3;
  const bool ab = r < 18;
  const int rt = 2 * j + (r & 1), ct = r >> 1;
  const int g = 4 * (j & 1) + ((r >> 2) & 3), fr = (j & 6) + (r >> 4), s = r & 3;
  const __amdgpu_buffer_rsrc_t rs = __builtin_amdgcn_make_buffer_rsrc(ws, 0, WS_BYTES, 0x00020000);
  const bf16x8 ones = {0x3F80, 0x3F80, 0x3F80, 0x3F80, 0x3F80, 0x3F80, 0x3F80, 0x3F80};
  asm volatile("" ::"v"(smem));
  STAMP(0);
  if (tid < 32) *(volatile LAS unsigned*)(MISC + tid * 4) = 0u;
  asm volatile("s_waitcnt lgkmcnt(0)\n\ts_barrier" ::: "memory");
  if (ab) {
    if (w < 4) {
      const char* xsrc = x + (size_t)(4 * rt + w) * (16 * D_ * 2);
#pragma unroll
      for (int e = 0; e < 18; ++e) {
        const unsigned f = (((unsigned)(lane + 64 * e) * 911u) >> 17) & 7u;
        dma(xsrc + e * 1024, (lane * 16) ^ (f << 4), (8 + 2 * w) * SLOT + e * 1024);
      }
    }
  }

  if (w < 4) {
    const int i = w;
    if (ab) {
      unsigned ab4[4];
#pragma unroll
      for (int c = 0; c < 4; ++c) ab4[c] = (4 * q + (t >> 2)) * 128 + ((c ^ ((2 * q + (t >> 3)) & 3)) << 5) + (t & 3) * 8;
      const int T = 4 * rt + i;
      v2u xres[4];
      {
        const unsigned xa = (8 + 2 * i) * SLOT + t * (D_ * 2), xs = ((t >> 1) & 7) << 4;
        unsigned xe0 = xa + ((q * 16) ^ xs), xe1 = xa + ((64 + q * 16) ^ xs);
        int dst[4];
        unsigned keep[4];
#pragma unroll
        for (int l = 0; l < 4; ++l) {
          const int gt = 4 * ct + l, grp = gt / 9;
          const int sh = grp == 0 ? 1 : grp == 1 ? -1 : grp == 2 ? 2 : -2;
          const int ts = 16 * (i & 1) + t + sh;
          keep[l] = (ts >= 0 && ts < 32) ? 0xffffffffu : 0u;
          dst[l] = OFF_H1 + ((T & ~1) + ((ts >> 4) & 1)) * (18 * 1024) + ((ts & 15) + 16 * q) * 16 + (l & 1) * 8;
        }
        asm volatile("" : "+v"(xe0), "+v"(xe1), "+v"(ab4[0]), "+v"(ab4[1]), "+v"(ab4[2]), "+v"(ab4[3]));
        asm volatile("" : "+v"(dst[0]), "+v"(dst[1]), "+v"(dst[2]), "+v"(dst[3]));
        asm volatile("" : "+v"(keep[0]), "+v"(keep[1]), "+v"(keep[2]), "+v"(keep[3]));
        DRAIN();
        STAMP(1);
        bf16x8 xf[18];
        f32x4 aS = {0, 0, 0, 0}, aG = {0, 0, 0, 0};
#pragma unroll
        for (int u = 0; u < 8; ++u) xf[u] = *(const LAS bf16x8*)(((u & 1) ? xe1 : xe0) + (u >> 1) * 128);
        SB();
        sfor<18>([&](auto U) INL {
          constexpr int u = U.value;
          aS = mfma(ones, xf[u], aS); SB();
          if constexpr (u + 8 < 18) xf[u + 8] = *(const LAS bf16x8*)((((u + 8) & 1) ? xe1 : xe0) + ((u + 8) >> 1) * 128);
          else if constexpr (u + 8 < 22) xres[u - 10] = *(const LAS v2u*)(xa + 128 * ct + ((32 * (u - 10) + 8 * q) ^ xs));
          SB();
          aG = mfma(xf[u], xf[u], aG); SB();
        });
        f32x4 lc;
        {
          const int e = t & 3;
          float gd = e == 0 ? aG[0] : e == 1 ? aG[1] : e == 2 ? aG[2] : aG[3];
          gd = (q == (t >> 2)) ? gd : 0.f;
          lc = ln_consts(aS[0], qsum(gd));
          asm volatile("" : "+v"(lc));
        }
        SB();
        STAMP(17);
        wait_full(1);
        constexpr int PA = 6;
        s16x4 wl[PA], wh[PA];
#pragma unroll
        for (int k = 0; k < PA; ++k) { wl[k] = trd(ab4[k & 3] + (k >> 2) * 4096); wh[k] = trd(ab4[k & 3] + (k >> 2) * 4096 + 2048); }
        SB();
        f32x4 acc[4] = {{0, 0, 0, 0}, {0, 0, 0, 0}, {0, 0, 0, 0}, {0, 0, 0, 0}};
        v4u fv;
        sfor<72>([&](auto K) INL {
          constexpr int k = K.value, nx = k - 1 + PA;
          acc[k & 3] = mfma(cat(wl[k % PA], wh[k % PA]), xf[k >> 2], acc[k & 3]); SB();
          if constexpr (nx == 24 || nx == 48) { if (min3(fv) < nx / 24 + 1) wait_full(nx / 24 + 1); CLOB(); SB(); }
          if constexpr (k >= 1 && nx < 72) { wl[nx % PA] = trd(ab4[nx & 3] + (nx >> 2) * 4096); wh[nx % PA] = trd(ab4[nx & 3] + (nx >> 2) * 4096 + 2048); }
          if constexpr (nx == 20 || nx == 44 || k == 66) fv = lds4(L_FULL);
          SB();
        });
        CLOB();
        lds_set(L_FREE + 4 * i, 1, lane);
        STAMP(2);
        if (min3(fv) < 4) wait_full(4);
        CLOB();
        f32x2 z[8];
        f32x4 cp[12];
        unsigned ca = L_CS1 + q * 16;
        asm volatile("" : "+v"(ca));
#pragma unroll
        for (int k = 0; k < 12; ++k) cp[k] = *(const LAS f32x4*)(ca + k * 64);
        SB();
#pragma unroll
        for (int l = 0; l < 4; ++l) {
          const f32x4 zz = ((cp[l] + cp[4 + l]) + cp[8 + l]) * lc.x + acc[l];
          z[2 * l] = f32x2{zz.x, zz.y}; z[2 * l + 1] = f32x2{zz.z, zz.w};
        }
        SB();
        const float ga8[8] = {lc.y, lc.y, lc.y, lc.y, lc.y, lc.y, lc.y, lc.y}, gb8[8] = {lc.z, lc.z, lc.z, lc.z, lc.z, lc.z, lc.z, lc.z};
        geluN<8>(z, ga8, gb8);
        unsigned hp[8];
#pragma unroll
        for (int k = 0; k < 8; ++k) { const f32x2 v = z[k] * lc.w; hp[k] = pk(v.x, v.y) & keep[k >> 1]; }
#pragma unroll
        for (int p = 0; p < 2; ++p) {
          if (2 * ct + p == 4 || 2 * ct + p == 13) {
            ST8((v2u{hp[4 * p], hp[4 * p + 1]}), dst[2 * p], (2 * ct + p) * 1024);
            ST8((v2u{hp[4 * p + 2], hp[4 * p + 3]}), dst[2 * p + 1], (2 * ct + p) * 1024);
          } else {
            ST16((v4u{hp[4 * p], hp[4 * p + 1], hp[4 * p + 2], hp[4 * p + 3]}), dst[2 * p], (2 * ct + p) * 1024);
          }
        }
        CLOB();
        lds_set(L_FREE + 4 * i, 2, lane);
        STAMP(3);
        DRAIN();
        arrive(ws, FA, b, lane);
        STAMP(4);
      }
      {
        wait_full(5);
#pragma unroll
        for (int c = 0; c < 4; ++c) { ab4[c] += 8 * SLOT; asm volatile("" : "+v"(ab4[c])); }
        constexpr int PB = 40;
        s16x4 wl[PB], wh[PB];
#pragma unroll
        for (int k = 0; k < PB; ++k) { wl[k] = trd(ab4[k & 3] + (k >> 2) * 4096); wh[k] = trd(ab4[k & 3] + (k >> 2) * 4096 + 2048); }
        SB();
        wait_word(L_RDY + 4 * R_H, 1);
        STAMP(5);
        bf16x8 hf[18];
        const int hsrc = OFF_H1 + T * (18 * 1024);
#pragma unroll
        for (int u = 0; u < 9; ++u) hf[u] = asfrag(LD16(lane * 16 + u * 1024, hsrc));
        SB();
        f32x4 acc[4] = {{0, 0, 0, 0}, {0, 0, 0, 0}, {0, 0, 0, 0}, {0, 0, 0, 0}};
        sfor<72>([&](auto K) INL {
          constexpr int k = K.value, u = k >> 2;
          acc[k & 3] = mfma(cat(wl[k % PB], wh[k % PB]), hf[u], acc[k & 3]); SB();
          if constexpr ((k & 1) == 0 && k / 2 + 9 < 18) hf[k / 2 + 9] = asfrag(LD16(lane * 16 + (k / 2 + 9) * 1024, hsrc));
          if constexpr (k + PB < 72) { wl[k % PB] = trd(ab4[k & 3] + ((k + PB) >> 2) * 4096); wh[k % PB] = trd(ab4[k & 3] + ((k + PB) >> 2) * 4096 + 2048); }
          SB();
        });
        STAMP(6);
        unsigned xp[8];
        float sm = 0.f, sq = 0.f;
#pragma unroll
        for (int l = 0; l < 4; ++l) {
          const unsigned p0 = pk(acc[l].x, acc[l].y), p1 = pk(acc[l].z, acc[l].w);
          xp[2 * l] = pk(lo16(xres[l].x) + lo16(p0), hi16(xres[l].x) + hi16(p0));
          xp[2 * l + 1] = pk(lo16(xres[l].y) + lo16(p1), hi16(xres[l].y) + hi16(p1));
        }
        const int dst = OFF_X1 + (T * 18 + 2 * ct) * 1024;
        ST16((v4u{xp[0], xp[1], xp[2], xp[3]}), lane * 16, dst);
        ST16((v4u{xp[4], xp[5], xp[6], xp[7]}), lane * 16 + 1024, dst);
        SB();
#pragma unroll
        for (int k = 0; k < 8; ++k) { const float v0 = lo16(xp[k]), v1 = hi16(xp[k]); sm += v0 + v1; sq += v0 * v0 + v1 * v1; }
        sm = qsum(sm); sq = qsum(sq);
        if (q == 0) ST8((v2u{__float_as_uint(sm), __float_as_uint(sq)}), OFF_STAT + t * 8, (T * 9 + ct) * 128);
        CLOB();
        lds_set(L_FREE + 4 * i, 3, lane);
        STAMP(7);
        DRAIN();
        arrive(ws, FB, b, lane);
        STAMP(8);
      }
    } else {
      const int lt = ((r - 18) * 4 + i) * 64 + lane;
      unsigned sink = 0;
      auto touch = [&](const char* base, unsigned off) INL { asm volatile("global_load_dword %0, %1, %2" : "+v"(sink) : "v"(off), "s"(base) : "memory"); };
      __builtin_amdgcn_s_sleep(40);
#pragma unroll
      for (int k = 0; k < 2; ++k) touch(w2, min(lt + 3584 * k, 5183) * 128);
#pragma unroll
      for (int k = 0; k < 2; ++k) { const int l = min(lt + 3584 * k, 5183); touch(w3 + (j & 6) * (576 * 2), (l / 9) * (F_ * 2) + (l % 9) * 128); }
#pragma unroll
      for (int k = 0; k < 3; ++k) touch(w4 + (size_t)(j & 6) * (576 * D_ * 2), min(lt + 3584 * k, 10367) * 128);
      DRAIN();
      asm volatile("" ::"v"(sink));
    }
    unsigned b0 = (4 * q + (t >> 2)) * 288 + (t & 3) * 8, b1 = b0 + 6 * SLOT, b2 = b0 + 12 * SLOT;
    asm volatile("" : "+v"(b0), "+v"(b1), "+v"(b2));
#define WADDR(z, c) ((((z) % 16) < 6 ? b0 : ((z) % 16) < 12 ? b1 : b2) + (((z) % 16) % 6) * SLOT + (c) * 32)
#define FN(i_) ((i_) < 108 ? (i_) / 9 : 12 + ((i_) - 108) % 6)
#define FC(i_) ((i_) < 108 ? (i_) % 9 : ((i_) - 108) / 6)
    const int T0 = 8 * g + 2 * i;
    const int hdst = OFF_HH + (T0 * 144 + fr * 18) * 1024;
    bf16x8 hb[18][2];
    auto hload = [&](auto K) INL {
      constexpr int m = K.value >> 1, tt = K.value & 1;
      hb[m][tt] = asfrag(LD16(lane * 16 + tt * (144 * 1024), hdst + dstep<m>(s) * 1024));
    };
    s16x4 lo[PD], hi[PD];
    unsigned rv;
    {
      wait_full(7);
#pragma unroll
      for (int k = 0; k < PD; ++k) { lo[k] = trd(WADDR(0, k)); hi[k] = trd(WADDR(0, k) + 4608); }
      f32x4 acc[2][9];
#pragma unroll
      for (int c = 0; c < 9; ++c) { acc[0][c] = f32x4{0, 0, 0, 0}; acc[1][c] = f32x4{0, 0, 0, 0}; }
      const int xsrc = OFF_X1 + T0 * (18 * 1024);
      bf16x8 xb[12][2];
      auto xload = [&](auto K) INL {
        constexpr int n = K.value >> 1, tt = K.value & 1;
        xb[n % 12][tt] = asfrag(LD16(lane * 16 + tt * (18 * 1024), xsrc + cstep(n, s) * 1024));
      };
      SB();
      wait_word(L_RDY + 4 * (R_X0 + (i >> 1)), 1);
      STAMP(9);
      sfor<10>([&](auto K) INL { xload(K); });
      SB();
      f32x4 lc[2], csv;
      unsigned hp[2][2][2];
      v4u fv;
      auto epi = [&](auto Cc) INL {
        constexpr int c = Cc.value;
        f32x2 z[4];
#pragma unroll
        for (int tt = 0; tt < 2; ++tt) {
          const f32x4 zz = csv * lc[tt].x + acc[tt][c];
          z[2 * tt] = f32x2{zz.x, zz.y}; z[2 * tt + 1] = f32x2{zz.z, zz.w};
        }
        SB();
        const float ga4[4] = {lc[0].y, lc[0].y, lc[1].y, lc[1].y}, gb4[4] = {lc[0].z, lc[0].z, lc[1].z, lc[1].z};
        geluN<4>(z, ga4, gb4);
#pragma unroll
        for (int tt = 0; tt < 2; ++tt) { hp[tt][c & 1][0] = pk(z[2 * tt].x, z[2 * tt].y); hp[tt][c & 1][1] = pk(z[2 * tt + 1].x, z[2 * tt + 1].y); }
        if constexpr (c == 5) { STAMP(18); VMW(2); arrive(ws, FC1, b, lane); STAMP(19); }
        if constexpr ((c & 1) && c < 8) {
#pragma unroll
          for (int tt = 0; tt < 2; ++tt) {
            const v4u v = {hp[tt][0][0], hp[tt][0][1], hp[tt][1][0], hp[tt][1][1]};
            hb[c >> 1][tt] = asfrag(v);
            ST16(v, lane * 16 + tt * (144 * 1024), hdst + (pbase(s) + (c >> 1)) * 1024);
          }
        }
        if constexpr (c == 8) {
#pragma unroll
          for (int tt = 0; tt < 2; ++tt)
            ST8((v2u{hp[tt][0][0], hp[tt][0][1]}), lane * 16 + tt * (144 * 1024) + (s & 1) * 8, hdst + (s < 2 ? 4 : 13) * 1024);
          STAMP(22);
          VMW(6); arrive(ws, FC2, b, lane);
        }
        SB();
      };
      sfor<162>([&](auto I) INL {
        constexpr int ii = I.value, n = FN(ii), c = FC(ii), nx = ii - 1 + PD;
        const bf16x8 cur = cat(lo[ii % PD], hi[ii % PD]);
        acc[0][c] = mfma(cur, xb[n % 12][0], acc[0][c]); SB();
        if constexpr (ii >= 1 && nx < 162) lo[nx % PD] = trd(WADDR(FN(nx), FC(nx)));
        SB();
        acc[1][c] = mfma(cur, xb[n % 12][1], acc[1][c]); SB();
        if constexpr (ii >= 1 && nx < 162) hi[nx % PD] = trd(WADDR(FN(nx), FC(nx)) + 4608);
        SB();
        if constexpr (ii < 28 && ii % 2 == 0) { xload(IC<10 + ii / 2>{}); SB(); }
        if constexpr (ii >= 30 && ii < 66 && ii % 3 == 0) { xload(IC<14 + ii / 3>{}); SB(); }
        if constexpr (ii < 108 && c == 8) {
          if constexpr (n == 1 || n == 8 || n == 11) { CLOB(); lds_set(L_FREE + 4 * i, n == 1 ? 4 : n == 8 ? 5 : 6, lane); }
          if constexpr (n == 0) STAMP(23);
          if constexpr (n == 3) STAMP(24);
          if constexpr (n == 7) STAMP(25);
          SB();
        }
        if constexpr (ii == 92) { fv = lds4(L_FULL); rv = ldsw(L_RDY + 4 * R_LN); SB(); }
        if constexpr (ii == 100) {
          if (min3(fv) < 10) wait_full(10);
          if (rfl(rv) == 0) wait_word(L_RDY + 4 * R_LN, 1);
          CLOB();
          lc[0] = *(const LAS f32x4*)(L_LN + (32 * i + t) * 16); lc[1] = *(const LAS f32x4*)(L_LN + (32 * i + 16 + t) * 16);
          SB(); STAMP(10);
        }
        if constexpr (ii >= 108 && n == 14 && c >= 1) { csv = *(const LAS f32x4*)(L_CS3 + (c - 1) * 64 + q * 16); SB(); }
        if constexpr (ii >= 108 && n == 17 && c >= 1) epi(IC<c - 1>{});
        if constexpr (ii == 150) { fv = lds4(L_FULL); SB(); }
      });
      CLOB();
      lds_set(L_FREE + 4 * i, 7, lane);
      csv = *(const LAS f32x4*)(L_CS3 + 8 * 64 + q * 16);
      if (min3(fv) < 11) wait_full(11);
      CLOB();
#pragma unroll
      for (int k = 0; k < PD; ++k) { lo[k] = trd(WADDR(18, k)); hi[k] = trd(WADDR(18, k) + 4608); }
      SB();
      epi(IC<8>{});
      STAMP(11);
    }
    {
      STAMP(12);
      f32x4 acc[2][9];
#pragma unroll
      for (int c = 0; c < 9; ++c) { acc[0][c] = f32x4{0, 0, 0, 0}; acc[1][c] = f32x4{0, 0, 0, 0}; }
      const int pdst = OFF_P + (((g * 4 + s) * 8 + 2 * i) * 8 + fr) * PUNIT;
      v4u fv;
      auto epi = [&](auto Cc) INL {
        constexpr int c = Cc.value;
        if constexpr (c == 7) { if (i == (fr >> 1)) VMW(1); else VMW(2); arrive(ws, FD1, b, lane); }
        if constexpr ((c & 1) && c < 8) {
#pragma unroll
          for (int tt = 0; tt < 2; ++tt) {
            const f32x4 a0 = acc[tt][c - 1], a1 = acc[tt][c];
            const v4u v = {pk(a0.x, a0.y), pk(a0.z, a0.w), pk(a1.x, a1.y), pk(a1.z, a1.w)};
            if (2 * i + tt == fr) *(LAS v4u*)(L_OWN + (c >> 1) * 1024 + lane * 16) = v;
            else ST16(v, lane * 16 + tt * (8 * PUNIT) + (c >> 1) * 1024, pdst);
          }
        }
        if constexpr (c == 8) {
#pragma unroll
          for (int tt = 0; tt < 2; ++tt) {
            const f32x4 a0 = acc[tt][8];
            const v2u v = {pk(a0.x, a0.y), pk(a0.z, a0.w)};
            if (2 * i + tt == fr) *(LAS v2u*)(L_OWN + 4096 + lane * 8) = v;
            else ST8(v, lane * 8 + tt * (8 * PUNIT) + 4096, pdst);
          }
        }
        SB();
      };
      sfor<162>([&](auto I) INL {
        constexpr int ii = I.value, n = FN(ii), c = FC(ii), nx = ii - 1 + PD;
        const bf16x8 cur = cat(lo[ii % PD], hi[ii % PD]);
        acc[0][c] = mfma(cur, hb[n][0], acc[0][c]); SB();
        if constexpr (ii >= 1 && nx < 162) lo[nx % PD] = trd(WADDR(18 + FN(nx), FC(nx)));
        SB();
        acc[1][c] = mfma(cur, hb[n][1], acc[1][c]); SB();
        if constexpr (ii >= 1 && nx < 162) hi[nx % PD] = trd(WADDR(18 + FN(nx), FC(nx)) + 4608);
        SB();
        if constexpr (ii == 2) { rv = ldsw(L_RDY + 4 * R_H1); SB(); }
        if constexpr (ii == 6) { if (rfl(rv) == 0) wait_word(L_RDY + 4 * R_H1, 1); CLOB(); SB(); STAMP(27); }
        if constexpr (ii >= 6 && ii < 12) { hload(IC<8 + ii - 6>{}); SB(); }
        if constexpr (ii == 17) { CLOB(); lds_set(L_FREE + 4 * i, 8, lane); SB(); }
        if constexpr (ii == 18) { VMW(10); arrive(ws, FC3, b, lane); SB(); }
        if constexpr (ii == 28) { rv = ldsw(L_RDY + 4 * R_H2); SB(); }
        if constexpr (ii == 32) { if (rfl(rv) == 0) wait_word(L_RDY + 4 * R_H2, 1); CLOB(); SB(); STAMP(28); }
        if constexpr (ii >= 32 && ii < 38) { hload(IC<14 + ii - 32>{}); SB(); }
        if constexpr (ii == 40) { VMW(6); arrive(ws, FC4, b, lane); SB(); }
        if constexpr (ii == 50) { rv = ldsw(L_RDY + 4 * R_H3); SB(); }
        if constexpr (ii == 54) { if (rfl(rv) == 0) wait_word(L_RDY + 4 * R_H3, 1); CLOB(); SB(); STAMP(13); }
        if constexpr (ii >= 54 && ii < 60) { hload(IC<20 + ii - 54>{}); SB(); }
        if constexpr (ii == 72) { rv = ldsw(L_RDY + 4 * R_H4); SB(); }
        if constexpr (ii == 76) { if (rfl(rv) == 0) wait_word(L_RDY + 4 * R_H4, 1); CLOB(); SB(); STAMP(14); }
        if constexpr (ii >= 76 && ii < 86) { hload(IC<26 + ii - 76>{}); SB(); }
        if constexpr (ii == 73) { fv = lds4(L_FULL); SB(); }
        if constexpr (ii == 81) { if (min3(fv) < 12) wait_full(12); CLOB(); SB(); }
        if constexpr (ii == 96) { fv = lds4(L_FULL); SB(); }
        if constexpr (ii == 104) { if (min3(fv) < 13) wait_full(13); CLOB(); SB(); STAMP(26); }
        if constexpr (ii >= 108 && n == 17 && c >= 1) epi(IC<c - 1>{});
      });
      epi(IC<8>{});
      STAMP(15);
      asm volatile("s_waitcnt vmcnt(0) lgkmcnt(0)" ::: "memory");
      arrive(ws, FD2, b, lane);
      STAMP(16);
    }
  } else if (w < 7) {
    const int ld = w - 4;
    const unsigned fullw = L_FULL + 4 * ld;
    unsigned v3[3], v4[3];
#pragma unroll
    for (int e = 0; e < 3; ++e) {
      const int idx = (3 * ld + e) * 64 + lane, row = idx / 18, ch = idx % 18;
      const int sc = (ch + 2 * (s & 1)) % 18;
      v3[e] = row * (F_ * 2) + sc * 16;
      v4[e] = row * (D_ * 2) + sc * 16;
    }
    const char* w3b = w3 + (576 * fr + 144 * s) * 2;
    const char* w4b = w4 + (size_t)(576 * fr) * (D_ * 2) + 144 * s * 2;
    auto w3slice = [&](int n) INL {
#pragma unroll
      for (int e = 0; e < 3; ++e) dmac(w3b + (size_t)cstep(n, s) * (32 * F_ * 2), v3[e], (n & 15) * SLOT + (3 * ld + e) * 1024);
    };
    auto w4slice = [&](auto M) INL {
      constexpr int m = M.value;
      const char* src = w4b + dstep<m>(s) * (32 * D_ * 2);
#pragma unroll
      for (int e = 0; e < 3; ++e) dmac(src, v4[e], ((18 + m) & 15) * SLOT + (3 * ld + e) * 1024);
    };
    f32x4 cs[3] = {{0, 0, 0, 0}, {0, 0, 0, 0}, {0, 0, 0, 0}};
    const unsigned cb = (4 * q + (t >> 2)) * 288 + (t & 3) * 8 + ld * 96;
    auto colsum3 = [&](int n0, int n1) INL {
      NOUNROLL for (int n = n0; n < n1; n += 2) {
        const unsigned a0 = cb + (n & 15) * SLOT;
        s16x4 l[6], h[6];
#pragma unroll
        for (int k = 0; k < 6; ++k) { l[k] = trd(a0 + (k / 3) * SLOT + (k % 3) * 32); h[k] = trd(a0 + (k / 3) * SLOT + 4608 + (k % 3) * 32); }
        SB();
#pragma unroll
        for (int k = 0; k < 6; ++k) cs[k % 3] = mfma(cat(l[k], h[k]), ones, cs[k % 3]);
      }
      CLOB();
    };
    if (ab) {
      unsigned a4[4];
#pragma unroll
      for (int c = 0; c < 4; ++c) a4[c] = (4 * q + (t >> 2)) * 128 + ((c ^ ((2 * q + (t >> 3)) & 3)) << 5) + (t & 3) * 8 + ld * 4096;
      f32x4 c1[4] = {{0, 0, 0, 0}, {0, 0, 0, 0}, {0, 0, 0, 0}, {0, 0, 0, 0}};
      const unsigned v1 = (8 * (lane >> 5) + ((lane >> 3) & 3)) * (D_ * 2) + (((lane & 7) ^ ((q & 3) << 1)) << 4);
      const unsigned v2 = (lane >> 3) * (D_ * 2) + (((lane & 7) ^ ((q & 3) << 1)) << 4);
      auto w1units = [&](int u0) INL {
#pragma unroll
        for (int e = 0; e < 8; ++e) {
          const int u = u0 + 3 * (e >> 2);
          dma(w1 + 128 * ct + (32 * u + 16 * (e & 1) + 4 * ((e >> 1) & 1)) * (D_ * 2), v1, u * 4096 + (e & 3) * 1024);
        }
      };
      auto w2units = [&](int u0) INL {
        NOUNROLL for (int u = u0; u < u0 + 9; u += 3) {
#pragma unroll
          for (int e = 0; e < 4; ++e) dmac(w2 + 128 * ct + (32 * u + 8 * e) * (D_ * 2), v2, 8 * SLOT + u * 4096 + e * 1024);
        }
      };
      w1units(ld); w1units(ld + 6);
      VMW(8); lds_set(fullw, 1, lane);
      w1units(ld + 12);
      VMW(8); lds_set(fullw, 2, lane);
      STAMP(1);
      DRAIN(); lds_set(fullw, 3, lane);
      __builtin_amdgcn_s_setprio(1);
      NOUNROLL for (int h = 0; h < 3; ++h) {
        s16x4 l[8], hh[8];
#pragma unroll
        for (int k = 0; k < 8; ++k) { l[k] = trd(a4[k & 3] + (6 * h + 3 * (k >> 2)) * 4096); hh[k] = trd(a4[k & 3] + (6 * h + 3 * (k >> 2)) * 4096 + 2048); }
        SB();
#pragma unroll
        for (int k = 0; k < 8; ++k) c1[k & 3] = mfma(cat(l[k], hh[k]), ones, c1[k & 3]);
      }
      if (t == 0) {
#pragma unroll
        for (int c = 0; c < 4; ++c) *(LAS f32x4*)(L_CS1 + ld * 256 + c * 64 + q * 16) = c1[c];
      }
      CLOB();
      lds_set(fullw, 4, lane);
      __builtin_amdgcn_s_setprio(0);
      STAMP(2);
      wait_free(1);
      w2units(ld);
      wait_free(2);
      w2units(ld + 9);
      STAMP(3);
      DRAIN(); lds_set(fullw, 5, lane);
      wait_full(4);
      NOUNROLL for (int n = 0; n < 4; ++n) w3slice(n);
      STAMP(4);
      DRAIN(); lds_set(fullw, 6, lane);
      wait_full(6);
      colsum3(0, 4);
      wait_free(3);
      NOUNROLL for (int n = 4; n < 8; ++n) w3slice(n);
      NOUNROLL for (int n = 8; n < 16; ++n) w3slice(n);
      STAMP(5);
      DRAIN(); lds_set(fullw, 7, lane);
      STAMP(6);
      wait_full(7);
      colsum3(4, 16);
    } else {
      NOUNROLL for (int n = 0; n < 16; ++n) {
#pragma unroll
        for (int e = 0; e < 3; ++e) dma(w3b + (size_t)cstep(n, s) * (32 * F_ * 2), v3[e], n * SLOT + (3 * ld + e) * 1024);
      }
      DRAIN(); lds_set(fullw, 7, lane);
      wait_full(7);
      colsum3(0, 16);
    }
    lds_set(fullw, 8, lane);
    STAMP(7);
    wait_free(4); wait_full(8);
    STAMP(11);
    w3slice(16); w3slice(17);
    DRAIN(); lds_set(fullw, 9, lane);
    STAMP(12);
    wait_full(9);
    __builtin_amdgcn_s_setprio(1);
    colsum3(16, 18);
    if (t == 0) {
#pragma unroll
      for (int c = 0; c < 3; ++c) *(LAS f32x4*)(L_CS3 + (3 * ld + c) * 64 + q * 16) = cs[c];
    }
    CLOB();
    lds_set(fullw, 10, lane);
    __builtin_amdgcn_s_setprio(0);
    STAMP(8);
    wait_free(5);
    sfor<7>([&](auto M) INL { w4slice(M); });
    wait_free(6);
    sfor<3>([&](auto M) INL { w4slice(IC<7 + M.value>{}); });
    DRAIN(); lds_set(fullw, 11, lane);
    STAMP(9);
    wait_free(7); wait_full(10);
    sfor<6>([&](auto M) INL { w4slice(IC<10 + M.value>{}); });
    wait_free(8);
    sfor<2>([&](auto M) INL { w4slice(IC<16 + M.value>{}); });
    VMW(6); lds_set(fullw, 12, lane);
    DRAIN(); lds_set(fullw, 13, lane);
    STAMP(10);
  } else {
    const u64 tag = __hip_atomic_load((GAS u64*)(ws + OFF_EPOCH), __ATOMIC_RELAXED, __HIP_MEMORY_SCOPE_AGENT) + 1ull;
    if (lane == 0) {
      *(volatile LAS u64*)L_TAG = tag;
      *(volatile LAS unsigned*)(L_RDY + 4 * R_TAG) = 1u;
      __hip_atomic_store(flagp(ws, FS, b), tag, __ATOMIC_RELAXED, __HIP_MEMORY_SCOPE_AGENT);
    }
    STAMP(1);
    auto poll = [&](int kind, int wg) INL {
      GAS u64* p = flagp(ws, kind, wg);
      NOUNROLL for (unsigned n = 0; n < GLB_CAP; ++n) {
        const u64 v = __hip_atomic_load(p, __ATOMIC_RELAXED, __HIP_MEMORY_SCOPE_AGENT);
        if (__all(v == tag)) break;
        __builtin_amdgcn_s_sleep(1);
      }
      CLOB();
    };
    if (ab) {
      poll(FA, j + 8 * ((r & 1) + 2 * (lane % 9)));
      lds_set(L_RDY + 4 * R_H, 1, lane);
    }
    STAMP(2);
    {
      GAS u64* p = flagp(ws, FB, g + 8 * (lane % 18));
      bool d0 = false, d1 = false;
      NOUNROLL for (unsigned n = 0; n < GLB_CAP; ++n) {
        const u64 v = __hip_atomic_load(p, __ATOMIC_RELAXED, __HIP_MEMORY_SCOPE_AGENT);
        const bool ok = v == tag;
        if (!d0 && __all(ok || (lane & 1))) { d0 = true; lds_set(L_RDY + 4 * R_X0, 1, lane); }
        if (!d1 && __all(ok || !(lane & 1))) { d1 = true; lds_set(L_RDY + 4 * R_X1, 1, lane); }
        if (d0 && d1) break;
        __builtin_amdgcn_s_sleep(1);
      }
      lds_set(L_RDY + 4 * R_X0, 1, lane); lds_set(L_RDY + 4 * R_X1, 1, lane);
      CLOB();
    }
    STAMP(3);
    {
      v4u st[9];
#pragma unroll
      for (int k = 0; k < 9; ++k) st[k] = LD16(OFF_STAT + ((lane >> 3) * 9 + k) * 128 + (lane & 7) * 16, g * (8 * 9 * 128));
      f32x4 a = __builtin_bit_cast(f32x4, st[0]);
#pragma unroll
      for (int k = 1; k < 9; ++k) a += __builtin_bit_cast(f32x4, st[k]);
      *(LAS f32x4*)(L_LN + lane * 32) = ln_consts(a.x, a.y);
      *(LAS f32x4*)(L_LN + lane * 32 + 16) = ln_consts(a.z, a.w);
      CLOB();
      lds_set(L_RDY + 4 * R_LN, 1, lane);
    }
    if (b == 0) {
      NOUNROLL for (int k = 0; k < 4; ++k) poll(FS, k * 64 + lane);
      if (lane == 0) __hip_atomic_store((GAS u64*)(ws + OFF_EPOCH), tag, __ATOMIC_RELAXED, __HIP_MEMORY_SCOPE_AGENT);
    }
    const int peer = j + 8 * ((r & ~3) + (lane & 3));
    NOUNROLL for (int k = 0; k < 4; ++k) { poll(FC1 + k, peer); lds_set(L_RDY + 4 * (R_H1 + k), 1, lane); STAMP(4 + k); }
  }
  if (w >= 3) {
    const int p = w == 3 ? 3 : w == 7 ? 4 : w - 4;
    const int T = 8 * g + fr;
    wait_word(L_RDY + 4 * R_LN, 1);
    const int u = p < 4 ? pbase(s) + p : (s < 2 ? 4 : 13);
    const v4u xv = LD16(lane * 16, OFF_X1 + (T * 18 + u) * 1024);
    const float rstd = *(const LAS float*)(L_LN + (16 * fr + t) * 16 + 12);
    if (w == 7) {
      const u64 tag = *(volatile LAS u64*)L_TAG;
      NOUNROLL for (int k = 0; k < 2; ++k) {
        GAS u64* fp = flagp(ws, FD1 + k, (j & 1) + (lane & 6) + 8 * (((lane & 1) << 4) + (r & 15)));
        NOUNROLL for (unsigned n = 0; n < GLB_CAP; ++n) {
          const u64 v = __hip_atomic_load(fp, __ATOMIC_RELAXED, __HIP_MEMORY_SCOPE_AGENT);
          if (__all(v == tag)) break;
          __builtin_amdgcn_s_sleep(1);
        }
        CLOB();
        lds_set(L_RDY + 4 * (R_P1 + k), 1, lane);
      }
    } else {
      wait_word(L_RDY + 4 * (p < 2 ? R_P1 : R_P2), 1);
    }
    STAMP(20);
    const int psrc = OFF_P + ((g * 4 + s) * 8 + fr) * (8 * PUNIT) + p * 1024;
    char* orow = out + (size_t)(16 * T + t) * (D_ * 2) + (32 * u + 4 * q) * 2;
    if (p < 4) {
      v4u pv[8];
#pragma unroll
      for (int k = 1; k < 8; ++k) pv[k] = LD16(lane * 16, psrc + ((fr + k) & 7) * PUNIT);
      pv[0] = *(const LAS v4u*)(L_OWN + p * 1024 + lane * 16);
      float a[8];
#pragma unroll
      for (int e = 0; e < 8; ++e) a[e] = 0.f;
#pragma unroll
      for (int k = 0; k < 8; ++k)
#pragma unroll
        for (int e = 0; e < 4; ++e) { const unsigned d = pv[k][e]; a[2 * e] += lo16(d); a[2 * e + 1] += hi16(d); }
      unsigned o[4];
#pragma unroll
      for (int e = 0; e < 4; ++e) {
        const unsigned h2 = pk(a[2 * e] * rstd, a[2 * e + 1] * rstd), xe = xv[e];
        o[e] = pk(lo16(xe) + lo16(h2), hi16(xe) + hi16(h2));
      }
      *(GAS v2u*)orow = v2u{o[0], o[1]};
      *(GAS v2u*)(orow + 32) = v2u{o[2], o[3]};
    } else {
      v2u pv[8];
#pragma unroll
      for (int k = 1; k < 8; ++k) pv[k] = LD8(lane * 8, psrc + ((fr + k) & 7) * PUNIT);
      pv[0] = *(const LAS v2u*)(L_OWN + 4096 + lane * 8);
      float a[4] = {0.f, 0.f, 0.f, 0.f};
#pragma unroll
      for (int k = 0; k < 8; ++k) { a[0] += lo16(pv[k].x); a[1] += hi16(pv[k].x); a[2] += lo16(pv[k].y); a[3] += hi16(pv[k].y); }
      const unsigned x0 = (s & 1) ? xv.z : xv.x, x1v = (s & 1) ? xv.w : xv.y;
      const unsigned h0 = pk(a[0] * rstd, a[1] * rstd), h1 = pk(a[2] * rstd, a[3] * rstd);
      *(GAS v2u*)(orow + (s & 1) * 32) = v2u{pk(lo16(x0) + lo16(h0), hi16(x0) + hi16(h0)), pk(lo16(x1v) + lo16(h1), hi16(x1v) + hi16(h1))};
    }
    STAMP(21);
  }
#endif
}
}

extern "C" void kernel_launch(void* const* d_in, const int* in_sizes, int n_in, void* d_out, int out_size, void* d_ws, size_t ws_size,
                              hipStream_t stream) {
  mx::mixer_kernel<<<256, 512, 0, stream>>>((const char*)d_in[0], (const char*)d_in[1], (const char*)d_in[2], (const char*)d_in[3],
                                            (const char*)d_in[4], (char*)d_out, (char*)d_ws);
}
```

```cpp
#include <hip/hip_runtime.h>
#include <utility>

#define STAMPS 0
#define MAP 1
#define SL1 32
#define SL2 64

#define LAS __attribute__((address_space(3)))
#define GAS __attribute__((address_space(1)))
#define DI __device__ __forceinline__
typedef unsigned v4u __attribute__((ext_vector_type(4)));
typedef unsigned v2u __attribute__((ext_vector_type(2)));
typedef float f32x4 __attribute__((ext_vector_type(4)));
typedef float f32x2 __attribute__((ext_vector_type(2)));
typedef short bf16x8 __attribute__((ext_vector_type(8)));
typedef short s16x4 __attribute__((ext_vector_type(4)));
typedef __bf16 bf16x2_t __attribute__((ext_vector_type(2)));
typedef unsigned long long u64;

namespace mx {
constexpr int D_ = 576, F_ = 4608;
constexpr int OFF_EPOCH = 0;
constexpr int OFF_FLAGS = 4096;
constexpr int OFF_STAT = 344064;
constexpr int OFF_H1 = 524288;
constexpr int OFF_X1 = OFF_H1 + 64 * 18 * 1024;
constexpr int OFF_HH = OFF_X1 + 64 * 18 * 1024;
constexpr int PUNIT = 4608;
constexpr int OFF_P = OFF_HH + 64 * 144 * 1024;
constexpr int OFF_DBG = OFF_P + 8 * 4 * 8 * 8 * PUNIT;
constexpr int WS_BYTES = OFF_DBG + 2 * 256 * 8 * 32 * 8 + 256;
enum { FS = 0, FA = 1, FB = 2, FC = 3, FD = 4 };
constexpr int SLOT = 9216, MISC = 16 * SLOT;
constexpr int L_FULL = MISC, L_FREE = MISC + 16, L_RQ = MISC + 32, L_RDY = MISC + 48  , L_SY = MISC + 96, L_AGR = MISC + 112, L_TAG = MISC + 120;
constexpr int L_DUM = MISC + 128  , L_CS1 = MISC + 512  , L_CS3 = MISC + 1024  , L_LN = MISC + 2048  ,
              L_HS = MISC + 4096  , L_LN1 = L_HS  , L_DBG = MISC + 12288, L_PS = 4 * SLOT  ;
enum { R_H = 0, R_X = 1, R_HH = 2, R_P = 3, R_TAG = 4, R_LN = 5, R_Z = 6 };
constexpr unsigned EMPTY = 0xffffffffu;
constexpr unsigned TRY_CAP = 1u << 12;
constexpr unsigned LDS_CAP = 1u << 21, GLB_CAP = 1u << 16;
#define DCAPV 6
constexpr int PD = 4, DCAP = DCAPV;

template <class F, int... I> DI void sfor_impl(F&& f, std::integer_sequence<int, I...>) { (f(std::integral_constant<int, I>{}), ...); }
template <int N, class F> DI void sfor(F&& f) { sfor_impl(f, std::make_integer_sequence<int, N>{}); }
template <int V> using IC = std::integral_constant<int, V>;
#define INL __attribute__((always_inline))
#define NOUNROLL _Pragma("nounroll")
#define RARE(x) __builtin_expect(!!(x), 0)
#define SB() __builtin_amdgcn_sched_barrier(0)
#define CLOB() asm volatile("" ::: "memory")
#define DRAIN() asm volatile("s_waitcnt vmcnt(0)" ::: "memory")
#define VMW(n) asm volatile("s_waitcnt vmcnt(%0)" ::"i"(n) : "memory")

DI unsigned rfl(unsigned v) { return __builtin_amdgcn_readfirstlane(v); }
DI f32x4 mfma(bf16x8 a, bf16x8 b, f32x4 c) { return __builtin_amdgcn_mfma_f32_16x16x32_bf16(a, b, c, 0, 0, 0); }
template <bool FIRST> DI void mfa(f32x4& c, bf16x8 a, bf16x8 b) {
  if constexpr (FIRST) asm volatile("v_mfma_f32_16x16x32_bf16 %0, %1, %2, 0" : "=&v"(c) : "v"(a), "v"(b));
  else asm volatile("v_mfma_f32_16x16x32_bf16 %0, %1, %2, %0" : "+v"(c) : "v"(a), "v"(b));
}
#define MFMA_DONE() asm volatile("s_nop 7\n\ts_nop 3" ::: "memory")
DI bf16x8 asfrag(v4u v) { return __builtin_bit_cast(bf16x8, v); }
DI s16x4 trd(unsigned addr) { return __builtin_amdgcn_ds_read_tr16_b64_v4i16((LAS s16x4*)addr); }
DI bf16x8 cat(s16x4 lo, s16x4 hi) { return __builtin_shufflevector(lo, hi, 0, 1, 2, 3, 4, 5, 6, 7); }
DI unsigned pk(float a, float b) { bf16x2_t r = __builtin_convertvector(f32x2{a, b}, bf16x2_t); return __builtin_bit_cast(unsigned, r); }
DI unsigned pkc(float a, float b) { return min(pk(a, b), EMPTY - 1u); }
DI unsigned mx2(unsigned m, unsigned a, unsigned b) { return max(max(m, a), b); }
DI unsigned mx4(unsigned m, v4u d) { return mx2(mx2(m, d.x, d.y), d.z, d.w); }
DI bool missing(unsigned m) { return __any(m == EMPTY); }
DI v4u asv4(bf16x8 f) { return __builtin_bit_cast(v4u, f); }
DI float lo16(unsigned p) { return __uint_as_float(p << 16); }
DI float hi16(unsigned p) { return __uint_as_float(p & 0xffff0000u); }
DI float bfr(float a) { return lo16(pk(a, a)); }
DI float qsum(float v) {
  auto r = __builtin_amdgcn_permlane16_swap(__float_as_uint(v), __float_as_uint(v), false, false);
  float a = __uint_as_float(r[0]) + __uint_as_float(r[1]);
  auto r2 = __builtin_amdgcn_permlane32_swap(__float_as_uint(a), __float_as_uint(a), false, false);
  return __uint_as_float(r2[0]) + __uint_as_float(r2[1]);
}
constexpr float GA = -2.302208198144325f;
constexpr float GB = -0.1029432397357593f;
DI f32x4 ln_consts(float s, float ss) {
  const float m = s * (1.0f / D_);
  float var = __builtin_fmaf(-m, m, ss * (1.0f / D_));
  var = var > 0.f ? var : 0.f;
  const float sd = bfr(__builtin_amdgcn_sqrtf(bfr(bfr(var) + 1e-5f)));
  const float r = __builtin_amdgcn_rcpf(sd);
  return f32x4{-bfr(m), GA * r, GB * r * r * r, r};
}
template <int N> DI void geluN(f32x2 (&z)[N], const float (&a)[N], const float (&b)[N]) {
  f32x2 p[N];
#pragma unroll
  for (int k = 0; k < N; ++k) p[k] = z[k] * z[k];
  SB();
#pragma unroll
  for (int k = 0; k < N; ++k) p[k] = p[k] * b[k] + a[k];
  SB();
#pragma unroll
  for (int k = 0; k < N; ++k) p[k] = z[k] * p[k];
  SB();
#pragma unroll
  for (int k = 0; k < N; ++k) { p[k].x = __builtin_amdgcn_exp2f(p[k].x); p[k].y = __builtin_amdgcn_exp2f(p[k].y); }
  SB();
#pragma unroll
  for (int k = 0; k < N; ++k) p[k] = p[k] + 1.0f;
  SB();
#pragma unroll
  for (int k = 0; k < N; ++k) { p[k].x = __builtin_amdgcn_rcpf(p[k].x); p[k].y = __builtin_amdgcn_rcpf(p[k].y); }
  SB();
#pragma unroll
  for (int k = 0; k < N; ++k) z[k] = z[k] * p[k];
  SB();
}

DI unsigned ldsw(int addr) { return *(volatile LAS unsigned*)addr; }
DI v4u lds4(int addr) { return *(volatile LAS v4u*)addr; }
DI void lds_set(int addr, unsigned v, int lane) { if (lane == 0) *(volatile LAS unsigned*)addr = v; }
DI v4u peek4(int addr) { CLOB(); return *(const LAS v4u*)addr; }
DI unsigned peek(int addr) { CLOB(); return *(const LAS unsigned*)addr; }
DI void post(unsigned addr, unsigned v) { CLOB(); *(LAS unsigned*)addr = v; CLOB(); }
DI void wait_word(int addr, unsigned v) {
  NOUNROLL for (unsigned n = 0; n < LDS_CAP; ++n) { if (rfl(ldsw(addr)) >= v) break; __builtin_amdgcn_s_sleep(1); }
  CLOB();
}
DI unsigned min3(v4u x) { return rfl(min(min(x.x, x.y), x.z)); }
DI unsigned min4(v4u x) { return rfl(min(min(x.x, x.y), min(x.z, x.w))); }
DI void wait_full(unsigned v) {
  NOUNROLL for (unsigned n = 0; n < LDS_CAP; ++n) { if (min3(lds4(L_FULL)) >= v) break; __builtin_amdgcn_s_sleep(1); }
  CLOB();
}
DI void wait_min4(int addr, unsigned v) {
  NOUNROLL for (unsigned n = 0; n < LDS_CAP; ++n) { if (min4(lds4(addr)) >= v) break; __builtin_amdgcn_s_sleep(1); }
  CLOB();
}
DI void wait_free(unsigned v) { wait_min4(L_FREE, v); }
DI void wait_bit(int addr, unsigned bit) {
  NOUNROLL for (unsigned n = 0; n < LDS_CAP; ++n) { if ((rfl(ldsw(addr)) & bit) == bit) break; __builtin_amdgcn_s_sleep(1); }
  CLOB();
}
DI GAS u64* flagp(char* ws, int kind, int b, int wv) { return (GAS u64*)(ws + OFF_FLAGS + (kind * 256 + b) * 128 + wv * 8); }
DI u64 get_tag() {
  wait_word(L_RDY + 4 * R_TAG, 1);
  return ((u64)rfl(ldsw(L_TAG + 4)) << 32) | rfl(ldsw(L_TAG));
}
#define LD16(off, soff) __builtin_amdgcn_raw_buffer_load_b128(rs, (off), (soff), 16)
#define LD8(off, soff) __builtin_amdgcn_raw_buffer_load_b64(rs, (off), (soff), 16)
#define ST16(v, off, soff) __builtin_amdgcn_raw_buffer_store_b128((v), rs, (off), (soff), 16)
#define ST8(v, off, soff) __builtin_amdgcn_raw_buffer_store_b64((v), rs, (off), (soff), 16)
#define SIGNAL(kind, wv) do { CLOB(); ST8((v2u{(unsigned)tag, (unsigned)(tag >> 32)}), lane ? 0x7ffffff0 : 0, OFF_FLAGS + ((kind) * 256 + b) * 128 + (wv) * 8); CLOB(); } while (0)
DI void dma(const char* sbase, unsigned voff, unsigned ldsaddr) {
  asm volatile("s_mov_b32 m0, %0\n\ts_nop 0\n\tglobal_load_lds_dwordx4 %1, %2" ::"s"(ldsaddr), "v"(voff), "s"(sbase) : "memory");
}
DI void dmac(const char* sbase, unsigned voff, unsigned ldsaddr) { dma(sbase, voff, ldsaddr); VMW(DCAP); }
DI int cstep(int n, int s) { const int u = n + ((9 * s) >> 1); return u >= 18 ? u - 18 : u; }
DI int pbase(int s) { return (9 * s + (s & 1)) >> 1; }
DI int dstep(int M, int s) {
  if (M < 4) return pbase(s) + M;
  else if (M < 16) return pbase((s + 1 + (M - 4) % 3) & 3) + (M - 4) / 3;
  else return M == 16 ? 4 : 13;
}
#define STAMP(k) do { } while (0)
#define COUNT(k) do { } while (0)

__global__ __launch_bounds__(512, 2) void mixer_kernel(const char* __restrict__ x, const char* __restrict__ w1, const char* __restrict__ w2,
                                                       const char* __restrict__ w3, const char* __restrict__ w4, char* __restrict__ out, char* ws) {
#if defined(__HIP_DEVICE_COMPILE__)
  __shared__ __attribute__((aligned(16))) char smem[163840];
  const int tid = threadIdx.x, lane = tid & 63, w = rfl(tid >> 6);
  const int t = lane & 15, q = lane >> 4;
  const int b = blockIdx.x, j = b & 7, r = b >> 3;
  const bool ab = r < 18;
  const int rt = 2 * j + (r & 1), ct = r >> 1;
  const int g = MAP ? 4 * (j & 1) + ((r >> 2) & 3) : r >> 2, fr = MAP ? (j & 6) + (r >> 4) : j, s = r & 3;
  const __amdgpu_buffer_rsrc_t rs = __builtin_amdgcn_make_buffer_rsrc(ws, 0, WS_BYTES, 0x00020000);
  const bf16x8 ones = {0x3F80, 0x3F80, 0x3F80, 0x3F80, 0x3F80, 0x3F80, 0x3F80, 0x3F80};
  asm volatile("" ::"v"(smem));
  STAMP(0);
  if (tid < 32) *(volatile LAS unsigned*)(MISC + tid * 4) = 0u;
  asm volatile("s_waitcnt lgkmcnt(0)\n\ts_barrier" ::: "memory");
  if (ab) {
    if (w < 4) {
      const char* xsrc = x + (size_t)(4 * rt + w) * (16 * D_ * 2);
#pragma unroll
      for (int e = 0; e < 18; ++e) {
        const unsigned f = (((unsigned)(lane + 64 * e) * 911u) >> 17) & 7u;
        dma(xsrc + e * 1024, (lane * 16) ^ (f << 4), (8 + 2 * w) * SLOT + e * 1024);
      }
    }
  }

  if (w < 4) {
    const int i = w;
    unsigned pa = lane ? L_DUM + 4 * lane : L_FREE + 4 * i;
    asm volatile("" : "+v"(pa));
    const u64 tag = get_tag();
    constexpr int P_FREE = 0, P_RQ = L_RQ - L_FREE, P_SY = L_SY - L_FREE;
    if (ab) {
      unsigned ab4[4];
#pragma unroll
      for (int c = 0; c < 4; ++c) ab4[c] = (4 * q + (t >> 2)) * 128 + ((c ^ ((2 * q + (t >> 3)) & 3)) << 5) + (t & 3) * 8;
      const int T = 4 * rt + i;
      v2u xres[4];
      {
        const int tp = i >> 1, ch = i & 1;
        const unsigned xa = (8 + 2 * i) * SLOT + t * (D_ * 2), xs = ((t >> 1) & 7) << 4;
        unsigned xe[2][2], wb[2][2];
#pragma unroll
        for (int e = 0; e < 2; ++e) {
          xe[0][e] = xa + ((64 * (e ^ tp) + q * 16) ^ xs);
          xe[1][e] = xe[0][e] + (ch ? -2 * SLOT : 2 * SLOT);
          wb[e][0] = (ch ? ab4[2] : ab4[0]) + (e ^ tp) * 4096;
          wb[e][1] = (ch ? ab4[3] : ab4[1]) + (e ^ tp) * 4096;
          asm volatile("" : "+v"(xe[0][e]), "+v"(xe[1][e]), "+v"(wb[e][0]), "+v"(wb[e][1]));
        }
        int dst[4];
        unsigned keep[4], dm[4];
#pragma unroll
        for (int k = 0; k < 4; ++k) {
          const int gt = 4 * ct + 2 * ch + (k & 1), grp = gt / 9;
          const int sh = grp == 0 ? 1 : grp == 1 ? -1 : grp == 2 ? 2 : -2;
          const int ts = 16 * (ch ^ (k >> 1)) + t + sh;
          keep[k] = (ts >= 0 && ts < 32) ? 0xffffffffu : 0u;
          dst[k] = OFF_H1 + ((T & ~1) + ((ts >> 4) & 1)) * (18 * 1024) + ((ts & 15) + 16 * q) * 16 + (k & 1) * 8;
          dm[k] = (q == (t >> 2) && k == (t & 3)) ? 0xffffffffu : 0u;
          asm volatile("" : "+v"(dst[k]), "+v"(keep[k]), "+v"(dm[k]));
        }
        DRAIN();
        post(pa + P_FREE, 1);
        wait_word(L_FREE + 4 * (i ^ 1), 1);
        STAMP(1);
        bf16x8 xf[2][18];
        f32x4 aS = {0, 0, 0, 0}, aG = {0, 0, 0, 0};
#pragma unroll
        for (int p = 0; p < 6; ++p) xf[0][p] = *(const LAS bf16x8*)(xe[0][p & 1] + (p >> 1) * 128);
        SB();
        sfor<18>([&](auto P) INL {
          constexpr int p = P.value;
          aS = mfma(ones, xf[0][p], aS); SB();
          if constexpr (p + 6 < 18) xf[0][p + 6] = *(const LAS bf16x8*)(xe[0][p & 1] + ((p + 6) >> 1) * 128);
          else if constexpr (p + 6 < 22) xres[p - 12] = *(const LAS v2u*)(xa + 128 * ct + ((32 * (p - 12) + 8 * q) ^ xs));
          SB();
          aG = mfma(xf[0][p], xf[0][p], aG); SB();
          xf[1][p] = *(const LAS bf16x8*)(xe[1][p & 1] + (p >> 1) * 128);
          SB();
        });
        CLOB();
        post(pa + P_FREE, 2);
        f32x4 lc[2];
        {
          const float gd = __uint_as_float(((__float_as_uint(aG.x) & dm[0]) | (__float_as_uint(aG.y) & dm[1])) | ((__float_as_uint(aG.z) & dm[2]) | (__float_as_uint(aG.w) & dm[3])));
          lc[0] = ln_consts(aS[0], qsum(gd));
          if (q == 0) *(LAS f32x4*)(L_LN1 + i * 256 + t * 16) = lc[0];
          CLOB();
          post(pa + P_SY, 1);
        }
        SB();
        STAMP(17);
        wait_full(1);
        constexpr int PA = 4;
        s16x4 wl[PA], wh[PA];
#define AADDR(k) (wb[((k) >> 1) & 1][(k) & 1] + ((k) >> 2) * 8192)
#pragma unroll
        for (int k = 0; k < PA; ++k) { wl[k] = trd(AADDR(k)); wh[k] = trd(AADDR(k) + 2048); }
        bf16x8 one2 = ones;
        asm volatile("" : "+v"(one2));
        SB();
        f32x4 acc[2][2], cs[2];
        v4u fv;
        sfor<36>([&](auto K) INL {
          constexpr int k = K.value, p = k >> 1, l = k & 1, nx = k - 1 + PA;
          const bf16x8 cur = cat(wl[k % PA], wh[k % PA]);
          mfa<(p == 0)>(acc[0][l], cur, xf[0][p]); SB();
          if constexpr (nx == 12 || nx == 24) { if (RARE(min3(fv) < nx / 12 + 1)) wait_full(nx / 12 + 1); CLOB(); SB(); }
          if constexpr (k >= 1 && nx < 36) wl[nx % PA] = trd(AADDR(nx));
          SB();
          mfa<(p == 0)>(acc[1][l], cur, xf[1][p]); SB();
          if constexpr (k >= 1 && nx < 36) wh[nx % PA] = trd(AADDR(nx) + 2048);
          if constexpr (nx == 9 || nx == 21) fv = peek4(L_FULL);
          SB();
          if constexpr ((p & 1) == 0) { mfa<(p == 0)>(cs[l], cur, one2); SB(); }
        });
        MFMA_DONE();
        if (t == 0) { *(LAS f32x4*)(L_CS1 + i * 128 + q * 16) = cs[0]; *(LAS f32x4*)(L_CS1 + i * 128 + 64 + q * 16) = cs[1]; }
        CLOB();
        post(pa + P_SY, 2);
        STAMP(2);
        wait_word(L_SY + 4 * (i ^ 1), 1);
        lc[1] = *(const LAS f32x4*)(L_LN1 + (i ^ 1) * 256 + t * 16);
        wait_word(L_SY + 4 * (i ^ 2), 2);
        const f32x4 o0 = *(const LAS f32x4*)(L_CS1 + (i ^ 2) * 128 + q * 16), o1 = *(const LAS f32x4*)(L_CS1 + (i ^ 2) * 128 + 64 + q * 16);
        cs[0] += o0; cs[1] += o1;
        f32x2 z[8];
        float ga8[8], gb8[8];
#pragma unroll
        for (int k = 0; k < 4; ++k) {
          const f32x4 zz = cs[k & 1] * lc[k >> 1].x + acc[k >> 1][k & 1];
          z[2 * k] = f32x2{zz.x, zz.y}; z[2 * k + 1] = f32x2{zz.z, zz.w};
          ga8[2 * k] = ga8[2 * k + 1] = lc[k >> 1].y; gb8[2 * k] = gb8[2 * k + 1] = lc[k >> 1].z;
        }
        SB();
        geluN<8>(z, ga8, gb8);
        unsigned hp[8];
#pragma unroll
        for (int k = 0; k < 8; ++k) { const f32x2 v = z[k] * lc[k >> 2].w; hp[k] = pk(v.x, v.y) & keep[k >> 1]; }
        const int up = 2 * ct + ch;
        if (RARE(up == 4 || up == 13)) {
#pragma unroll
          for (int k = 0; k < 4; ++k) ST8((v2u{hp[2 * k], hp[2 * k + 1]}), dst[k], up * 1024);
        } else {
#pragma unroll
          for (int tt = 0; tt < 2; ++tt) ST16((v4u{hp[4 * tt], hp[4 * tt + 1], hp[4 * tt + 2], hp[4 * tt + 3]}), dst[2 * tt], up * 1024);
        }
        STAMP(3);
        DRAIN();
        SIGNAL(FA, i);
        post(pa + P_FREE, 3);
        STAMP(4);
      }
      {
        wait_full(4);
#pragma unroll
        for (int c = 0; c < 4; ++c) { ab4[c] += 8 * SLOT; asm volatile("" : "+v"(ab4[c])); }
        constexpr int PB = 32;
        s16x4 wl[PB], wh[PB];
#pragma unroll
        for (int k = 0; k < PB; ++k) { wl[k] = trd(ab4[k & 3] + (k >> 2) * 4096); wh[k] = trd(ab4[k & 3] + (k >> 2) * 4096 + 2048); }
        SB();
        f32x2 xr[8];
#pragma unroll
        for (int l = 0; l < 4; ++l) { xr[2 * l] = f32x2{lo16(xres[l].x), hi16(xres[l].x)}; xr[2 * l + 1] = f32x2{lo16(xres[l].y), hi16(xres[l].y)}; }
#pragma unroll
        for (int k = 0; k < 8; ++k) asm volatile("" : "+v"(xr[k]));
        wait_bit(L_RDY + 4 * R_H, 1u << (i >> 1));
        STAMP(5);
        bf16x8 hf[18];
        const int hsrc = OFF_H1 + T * (18 * 1024);
#pragma unroll
        for (int u = 0; u < 9; ++u) hf[u] = asfrag(LD16(lane * 16 + u * 1024, hsrc));
        SB();
        f32x4 acc[4];
        unsigned zv;
        sfor<72>([&](auto K) INL {
          constexpr int k = K.value, u = k >> 2;
          mfa<(k < 4)>(acc[k & 3], cat(wl[k % PB], wh[k % PB]), hf[u]); SB();
          if constexpr (k == 17) post(pa + P_FREE, 4);
          if constexpr (k == 60) zv = peek(L_RDY + 4 * R_Z);
          if constexpr ((k & 1) == 0 && k / 2 + 9 < 18) hf[k / 2 + 9] = asfrag(LD16(lane * 16 + (k / 2 + 9) * 1024, hsrc));
          if constexpr (k + PB < 72) { wl[k % PB] = trd(ab4[k & 3] + ((k + PB) >> 2) * 4096); wh[k % PB] = trd(ab4[k & 3] + ((k + PB) >> 2) * 4096 + 2048); }
          SB();
        });
        MFMA_DONE();
        STAMP(6);
        unsigned xp[8];
#pragma unroll
        for (int l = 0; l < 4; ++l) {
          const f32x2 v0 = xr[2 * l] + f32x2{acc[l].x, acc[l].y}, v1 = xr[2 * l + 1] + f32x2{acc[l].z, acc[l].w};
          xp[2 * l] = pkc(v0.x, v0.y); xp[2 * l + 1] = pkc(v1.x, v1.y);
        }
        if (RARE(rfl(zv) == 0)) wait_word(L_RDY + 4 * R_Z, 1);
        CLOB();
        const int dst = OFF_X1 + (T * 18 + 2 * ct) * 1024;
        ST16((v4u{xp[0], xp[1], xp[2], xp[3]}), lane * 16, dst);
        ST16((v4u{xp[4], xp[5], xp[6], xp[7]}), lane * 16 + 1024, dst);
        SB();
        float sm = 0.f, sq = 0.f;
#pragma unroll
        for (int k = 0; k < 8; ++k) {
          const bf16x2_t v = __builtin_bit_cast(bf16x2_t, xp[k]);
          sm = __builtin_amdgcn_fdot2_f32_bf16(v, bf16x2_t{(__bf16)1.0f, (__bf16)1.0f}, sm, false);
          sq = __builtin_amdgcn_fdot2_f32_bf16(v, v, sq, false);
        }
        sm = qsum(sm); sq = qsum(sq);
        ST8((v2u{min(__float_as_uint(sm), EMPTY - 1u), min(__float_as_uint(sq), EMPTY - 1u)}), q ? 0x7ffffff0 : t * 8, OFF_STAT + (T * 9 + ct) * 128);
        SIGNAL(FB, i);
        post(pa + P_FREE, 5);
        STAMP(7);
      }
    } else {
      const int lt = ((r - 18) * 4 + i) * 64 + lane;
      unsigned sink = 0;
      auto touch = [&](const char* base, unsigned off) INL { asm volatile("global_load_dword %0, %1, %2\n\tglobal_load_dword %0, %1, %2 offset:64" : "+v"(sink) : "v"(off), "s"(base) : "memory"); };
      __builtin_amdgcn_s_sleep(SL1);
#pragma unroll
      for (int k = 0; k < 2; ++k) touch(w2, min(lt + 3584 * k, 5183) * 128);
      DRAIN();
      if (MAP) {
#pragma unroll
        for (int k = 0; k < 2; ++k) { const int l = min(lt + 3584 * k, 5183); touch(w3 + (j & 6) * (576 * 2), (l / 9) * (F_ * 2) + (l % 9) * 128); }
        DRAIN();
      }
      __builtin_amdgcn_s_sleep(SL2);
#pragma unroll
      for (int k = 0; k < (MAP ? 3 : 2); ++k) touch(w4 + (size_t)(MAP ? (j & 6) : j) * (576 * D_ * 2), min(lt + 3584 * k, MAP ? 10367 : 5183) * 128);
      DRAIN();
      asm volatile("" ::"v"(sink));
    }
    unsigned b0 = (4 * q + (t >> 2)) * 288 + (t & 3) * 8, b1 = b0 + 6 * SLOT, b2 = b0 + 12 * SLOT;
    asm volatile("" : "+v"(b0), "+v"(b1), "+v"(b2));
#define WADDR(z, c) ((((z) % 16) < 6 ? b0 : ((z) % 16) < 12 ? b1 : b2) + (((z) % 16) % 6) * SLOT + (c) * 32)
#define FN(i_) ((i_) < 108 ? (i_) / 9 : 12 + ((i_) - 108) % 6)
#define FC(i_) ((i_) < 108 ? (i_) % 9 : ((i_) - 108) / 6)
    const int T0 = 8 * g + 2 * i;
    const int hdst = OFF_HH + (T0 * 144 + fr * 18) * 1024;
    bf16x8 hb[18][2];
    auto hload = [&](auto K) INL {
      constexpr int m = K.value >> 1, tt = K.value & 1;
      hb[m][tt] = asfrag(LD16(lane * 16 + tt * (144 * 1024), hdst + dstep(m, s) * 1024));
    };
    s16x4 lo[PD], hi[PD];
    unsigned rv, hw = L_HS + 2 * i * 1024 + lane * 16;
    asm volatile("" : "+v"(hw));
    v2u h8[2];
    {
      wait_full(6);
#pragma unroll
      for (int k = 0; k < PD; ++k) { lo[k] = trd(WADDR(0, k)); hi[k] = trd(WADDR(0, k) + 4608); }
      f32x4 acc[2][9];
      const int xsrc = OFF_X1 + T0 * (18 * 1024);
      bf16x8 xb[12][2];
      auto xload = [&](auto K) INL {
        constexpr int n = K.value >> 1, tt = K.value & 1;
        xb[n % 12][tt] = asfrag(LD16(lane * 16 + tt * (18 * 1024), xsrc + cstep(n, s) * 1024));
      };
      SB();
      auto xfix = [&](auto K0, auto K1) INL {
        NOUNROLL for (unsigned n = 0; n < TRY_CAP; ++n) { CLOB();
          unsigned m = 0;
          COUNT(1);
          sfor<K1.value - K0.value>([&](auto K) INL { xload(IC<K0.value + K.value>{}); });
          sfor<K1.value - K0.value>([&](auto K) INL { constexpr int kk = K0.value + K.value; m = mx4(m, asv4(xb[(kk >> 1) % 12][kk & 1])); });
          if (!missing(m)) break;
        }
      };
      wait_bit(L_RDY + 4 * R_X, 1u << i);
      STAMP(9);
      sfor<10>([&](auto K) INL { xload(K); });
      SB();
      unsigned chk = mx4(mx4(0u, asv4(xb[0][0])), asv4(xb[0][1])), chk2 = 0;
      if (RARE(missing(chk))) xfix(IC<0>{}, IC<2>{});
      SB();
      f32x4 lc[2], csv;
      unsigned hp[2][2][2];
      unsigned la = L_LN + (32 * i + t) * 16, ca = L_CS3 + q * 16;
      asm volatile("" : "+v"(la), "+v"(ca));
      v4u fv;
      u64 bad;
      unsigned sv;
      auto epi = [&](auto Cc) INL {
        constexpr int c = Cc.value;
        f32x2 z[4];
#pragma unroll
        for (int tt = 0; tt < 2; ++tt) {
          const f32x4 zz = csv * lc[tt].x + acc[tt][c];
          z[2 * tt] = f32x2{zz.x, zz.y}; z[2 * tt + 1] = f32x2{zz.z, zz.w};
        }
        if constexpr (c >= 3 && (c & 1)) sv = rfl(rv);
        SB();
        const float ga4[4] = {lc[0].y, lc[0].y, lc[1].y, lc[1].y}, gb4[4] = {lc[0].z, lc[0].z, lc[1].z, lc[1].z};
        geluN<4>(z, ga4, gb4);
#pragma unroll
        for (int tt = 0; tt < 2; ++tt) { hp[tt][c & 1][0] = pk(z[2 * tt].x, z[2 * tt].y); hp[tt][c & 1][1] = pk(z[2 * tt + 1].x, z[2 * tt + 1].y); }
        asm volatile("" : "+v"(hp[0][c & 1][0]), "+v"(hp[0][c & 1][1]), "+v"(hp[1][c & 1][0]), "+v"(hp[1][c & 1][1]));
        if constexpr (c >= 3 && (c & 1)) { if (RARE(sv < c / 2)) wait_word(L_AGR, c / 2); CLOB(); }
        if constexpr ((c & 1) && c < 8) {
#pragma unroll
          for (int tt = 0; tt < 2; ++tt) {
            const v4u v = {hp[tt][0][0], hp[tt][0][1], hp[tt][1][0], hp[tt][1][1]};
            hb[c >> 1][tt] = asfrag(v);
            *(LAS v4u*)(hw + tt * 1024) = v;
          }
        }
        if constexpr (c == 8) { h8[0] = v2u{hp[0][0][0], hp[0][0][1]}; h8[1] = v2u{hp[1][0][0], hp[1][0][1]}; }
        if constexpr (c & 1) post(pa + P_RQ, c / 2 + 1);
        SB();
      };
      sfor<162>([&](auto I) INL {
        constexpr int ii = I.value, n = FN(ii), c = FC(ii), nx = ii - 1 + PD;
        const bf16x8 cur = cat(lo[ii % PD], hi[ii % PD]);
        mfa<(n == 0)>(acc[0][c], cur, xb[n % 12][0]); SB();
        constexpr bool ck1 = ii < 99 && (c == 5 || c == 6);
        constexpr bool ck2 = ii >= 72 && ii < 99 && c >= 1 && c <= 4;
        constexpr int k2 = 24 + 4 * (n - 8) + c - 1;
        if constexpr (ii >= 1 && nx < 162) lo[nx % PD] = trd(WADDR(FN(nx), FC(nx)));
        if constexpr (ck1) { chk = mx2(c == 5 ? 0u : chk, asv4(xb[n + 1][c - 5]).x, asv4(xb[n + 1][c - 5]).y); asm volatile("" : "+v"(chk)); }
        if constexpr (ck2) { chk2 = mx2(chk2, asv4(xb[(k2 >> 1) % 12][k2 & 1]).x, asv4(xb[(k2 >> 1) % 12][k2 & 1]).y); asm volatile("" : "+v"(chk2)); }
        if constexpr (ii < 99 && c == 7) { bad = __ballot(chk == EMPTY); asm volatile("" : "+s"(bad)); }
        if constexpr (ii == 99) { bad = __ballot(chk2 == EMPTY); asm volatile("" : "+s"(bad)); }
        if constexpr (ii == 63 || ii == 101) { sv = min3(fv); asm volatile("" : "+s"(sv)); }
        if constexpr (ii == 103) { sv = rfl(rv); asm volatile("" : "+s"(sv)); }
        SB();
        mfa<(n == 0)>(acc[1][c], cur, xb[n % 12][1]); SB();
        if constexpr (ii >= 1 && nx < 162) hi[nx % PD] = trd(WADDR(FN(nx), FC(nx)) + 4608);
        if constexpr (ck1) { chk = mx2(chk, asv4(xb[n + 1][c - 5]).z, asv4(xb[n + 1][c - 5]).w); asm volatile("" : "+v"(chk)); }
        if constexpr (ck2) { chk2 = mx2(chk2, asv4(xb[(k2 >> 1) % 12][k2 & 1]).z, asv4(xb[(k2 >> 1) % 12][k2 & 1]).w); asm volatile("" : "+v"(chk2)); }
        SB();
        if constexpr (ii < 99 && c == 8) { if (RARE(bad != 0)) xfix(IC<2 * n + 2>{}, IC<2 * n + 4>{}); SB(); }
        if constexpr (ii == 100) { if (RARE(bad != 0)) xfix(IC<24>{}, IC<36>{}); SB(); }
        if constexpr (ii < 28 && ii % 2 == 0) { xload(IC<10 + ii / 2>{}); SB(); }
        if constexpr (ii >= 30 && ii < 66 && ii % 3 == 0) { xload(IC<14 + ii / 3>{}); SB(); }
        if constexpr (ii == 18 || ii == 108) { post(pa + P_FREE, ii == 18 ? 6 : 8); SB(); }
        if constexpr (ii == 8) STAMP(23);
        if constexpr (ii == 35) STAMP(24);
        if constexpr (ii == 71) STAMP(25);
        if constexpr (ii == 58 || ii == 96) { fv = peek4(L_FULL); SB(); }
        if constexpr (ii == 64) { if (RARE(sv < 7)) wait_full(7); CLOB(); SB(); }
        if constexpr (ii == 97) { rv = peek(L_RDY + 4 * R_LN); SB(); }
        if constexpr (ii == 102) { if (RARE(sv < 10)) wait_full(10); CLOB(); SB(); }
        if constexpr (ii == 104) {
          if (RARE(sv == 0)) wait_word(L_RDY + 4 * R_LN, 1);
          CLOB();
          lc[0] = *(const LAS f32x4*)la; lc[1] = *(const LAS f32x4*)(la + 256);
          SB(); STAMP(10);
        }
        if constexpr (ii >= 108 && n == 14 && c >= 1) { csv = *(const LAS f32x4*)(ca + (c - 1) * 64); SB(); }
        if constexpr (ii >= 108 && n == 15 && c >= 4 && !(c & 1)) { rv = peek(L_AGR); SB(); }
        if constexpr (ii >= 108 && n == 17 && c >= 1) epi(IC<c - 1>{});
        if constexpr (ii == 150) { fv = peek4(L_FULL); SB(); }
      });
      post(pa + P_FREE, 9);
      csv = *(const LAS f32x4*)(ca + 8 * 64);
      if (RARE(min3(fv) < 11)) wait_full(11);
      CLOB();
#pragma unroll
      for (int k = 0; k < PD; ++k) { lo[k] = trd(WADDR(18, k)); hi[k] = trd(WADDR(18, k) + 4608); }
      SB();
      epi(IC<8>{});
      STAMP(11);
    }
    {
      f32x4 acc[2][9];
      unsigned pw = L_PS + 2 * i * 1024 + lane * 16, pw8 = L_PS + 32768 + 2 * i * 512 + lane * 8;
      asm volatile("" : "+v"(pw), "+v"(pw8));
      int po[2];
#pragma unroll
      for (int tt = 0; tt < 2; ++tt) { po[tt] = (2 * i + tt == fr) ? 0x7ff00000 : lane * 8; asm volatile("" : "+v"(po[tt])); }
      const int pdst = OFF_P + (((g * 4 + s) * 8 + 2 * i) * 8 + fr) * PUNIT;
      v4u fv;
      unsigned sv;
      auto epi = [&](auto Cc) INL {
        constexpr int c = Cc.value;
        if constexpr ((c & 1) && c < 8) {
#pragma unroll
          for (int tt = 0; tt < 2; ++tt) {
            const f32x4 a0 = acc[tt][c - 1], a1 = acc[tt][c];
            const v4u v = {pk(a0.x, a0.y), pk(a0.z, a0.w), pk(a1.x, a1.y), pk(a1.z, a1.w)};
            if constexpr (c == 7) ST16(v, 2 * po[tt] , pdst + tt * (8 * PUNIT) + 3 * 1024);
            *(LAS v4u*)(pw + tt * 1024 + (c >> 1) * 8192) = v;
          }
        }
        if constexpr (c == 8) {
#pragma unroll
          for (int tt = 0; tt < 2; ++tt) {
            const f32x4 a0 = acc[tt][8];
            const v2u v = {pk(a0.x, a0.y), pk(a0.z, a0.w)};
            ST8(v, po[tt], pdst + tt * (8 * PUNIT) + 4096);
            *(LAS v2u*)(pw8 + tt * 512) = v;
          }
        }
        if constexpr ((c & 1) && c < 7) post(pa + P_RQ, 5 + c / 2 + 1);
        SB();
      };
      constexpr int HS0 = 8, HS1 = 24, HS2 = 44, HS3 = 66;
      rv = peek(L_AGR);
      sfor<162>([&](auto I) INL {
        constexpr int ii = I.value, n = FN(ii), c = FC(ii), nx = ii - 1 + PD;
        constexpr int G = ii == HS0 ? 0 : ii == HS1 ? 1 : ii == HS2 ? 2 : ii == HS3 ? 3 : -1;
        const bf16x8 cur = cat(lo[ii % PD], hi[ii % PD]);
        mfa<(n == 0)>(acc[0][c], cur, hb[n][0]); SB();
        if constexpr (ii >= 1 && nx < 162) lo[nx % PD] = trd(WADDR(18 + FN(nx), FC(nx)));
        if constexpr (G >= 0 || ii == 3) { sv = rfl(rv); asm volatile("" : "+s"(sv)); }
        if constexpr (ii == 31 || ii == 84 || ii == 102) { sv = min3(fv); asm volatile("" : "+s"(sv)); }
        if constexpr (ii == 106) { sv = min4(fv); asm volatile("" : "+s"(sv)); }
        SB();
        mfa<(n == 0)>(acc[1][c], cur, hb[n][1]); SB();
        if constexpr (ii >= 1 && nx < 162) hi[nx % PD] = trd(WADDR(18 + FN(nx), FC(nx)) + 4608);
        SB();
        if constexpr (ii == 3) {
          if (RARE(sv < 4)) wait_word(L_AGR, 4);
          CLOB();
          *(LAS v2u*)hw = h8[0]; *(LAS v2u*)(hw + 1024) = h8[1];
          post(pa + P_RQ, 5);
          SB();
        }
        if constexpr (ii + 4 == HS0 || ii + 4 == HS1 || ii + 4 == HS2 || ii + 4 == HS3) { rv = peek(L_RDY + 4 * R_HH); SB(); }
        if constexpr (G >= 0) {
          constexpr unsigned need = G < 3 ? 1u << G : 24u;
          if (RARE((sv & need) != need)) wait_bit(L_RDY + 4 * R_HH, need);
          CLOB(); SB(); STAMP(G == 0 ? 27 : G == 1 ? 28 : G == 2 ? 13 : 14);
        }
        if constexpr (ii >= HS0 && ii < HS0 + 6) { hload(IC<8 + ii - HS0>{}); SB(); }
        if constexpr (ii >= HS1 && ii < HS1 + 6) { hload(IC<14 + ii - HS1>{}); SB(); }
        if constexpr (ii >= HS2 && ii < HS2 + 6) { hload(IC<20 + ii - HS2>{}); SB(); }
        if constexpr (ii >= HS3 && ii < HS3 + 10) { hload(IC<26 + ii - HS3>{}); SB(); }
        if constexpr (ii == 18 || ii == 54) { post(pa + P_FREE, ii == 18 ? 10 : 11); SB(); }
        if constexpr (ii == 27 || ii == 79 || ii == 97) { fv = peek4(L_FULL); SB(); }
        if constexpr (ii == 31) { if (RARE(sv < 12)) wait_full(12); CLOB(); SB(); }
        if constexpr (ii == 84) { if (RARE(sv < 13)) wait_full(13); CLOB(); SB(); }
        if constexpr (ii == 102) { if (RARE(sv < 14)) wait_full(14); fv = peek4(L_FREE); SB(); STAMP(26); }
        if constexpr (ii == 106) { if (RARE(sv < 11)) wait_free(11); CLOB(); SB(); }
        if constexpr (ii >= 108 && n == 17 && c >= 1) epi(IC<c - 1>{});
      });
      MFMA_DONE();
      epi(IC<8>{});
      STAMP(15);
      asm volatile("s_waitcnt vmcnt(0) lgkmcnt(0)" ::: "memory");
      SIGNAL(FD, 3 + i);
      STAMP(16);
    }
  } else if (w < 7) {
    const int ld = w - 4;
    const unsigned fullw = L_FULL + 4 * ld;
    unsigned v3[3], v4a[9];
#pragma unroll
    for (int e = 0; e < 3; ++e) {
      const int idx = (3 * ld + e) * 64 + lane, row = idx / 18, ch = idx % 18;
      const int sc = (ch + 2 * (s & 1)) % 18;
      v3[e] = row * (F_ * 2) + sc * 16;
    }
#pragma unroll
    for (int e = 0; e < 9; ++e) { const int idx = e * 64 + lane; v4a[e] = (idx / 18) * (D_ * 2) + ((idx % 18 + 2 * (s & 1)) % 18) * 16; }
    const char* w3b = w3 + (576 * fr + 144 * s) * 2;
    const char* w4b = w4 + (size_t)(576 * fr) * (D_ * 2) + 144 * s * 2;
    auto w3slice = [&](int n) INL {
#pragma unroll
      for (int e = 0; e < 3; ++e) dma(w3b + (size_t)cstep(n, s) * (32 * F_ * 2), v3[e], (n & 15) * SLOT + (3 * ld + e) * 1024);
    };
    auto w4whole = [&](int m, int u, auto CAP) INL {
      const char* src = w4b + u * (32 * D_ * 2);
#pragma unroll
      for (int e = 0; e < 9; ++e) { dma(src, v4a[e], ((18 + m) & 15) * SLOT + e * 1024); if constexpr (CAP.value) VMW(DCAP); }
    };
    f32x4 cs[3] = {{0, 0, 0, 0}, {0, 0, 0, 0}, {0, 0, 0, 0}};
    const unsigned cb = (4 * q + (t >> 2)) * 288 + (t & 3) * 8 + ld * 96;
    auto colsum3 = [&](int n0, int n1) INL {
      NOUNROLL for (int n = n0; n < n1; n += 2) {
        const unsigned a0 = cb + (n & 15) * SLOT;
        s16x4 l[6], h[6];
#pragma unroll
        for (int k = 0; k < 6; ++k) { l[k] = trd(a0 + (k / 3) * SLOT + (k % 3) * 32); h[k] = trd(a0 + (k / 3) * SLOT + 4608 + (k % 3) * 32); }
        SB();
#pragma unroll
        for (int k = 0; k < 6; ++k) cs[k % 3] = mfma(cat(l[k], h[k]), ones, cs[k % 3]);
      }
      CLOB();
    };
    if (ab) {
      const unsigned v1 = (8 * (lane >> 5) + ((lane >> 3) & 3)) * (D_ * 2) + (((lane & 7) ^ ((q & 3) << 1)) << 4);
      auto w1units = [&](int u0) INL {
#pragma unroll
        for (int e = 0; e < 8; ++e) {
          const int u = u0 + 3 * (e >> 2);
          dma(w1 + 128 * ct + (32 * u + 16 * (e & 1) + 4 * ((e >> 1) & 1)) * (D_ * 2), v1, u * 4096 + (e & 3) * 1024);
        }
      };
      w1units(ld); w1units(ld + 6);
      VMW(8); lds_set(fullw, 1, lane);
      w1units(ld + 12);
      VMW(8); lds_set(fullw, 2, lane);
      STAMP(1);
      DRAIN(); lds_set(fullw, 3, lane);
      wait_free(2);
      const unsigned v2 = (lane >> 3) * (D_ * 2) + (((lane & 7) ^ ((q & 3) << 1)) << 4);
      NOUNROLL for (int u = ld; u < 18; u += 3) {
#pragma unroll
        for (int e = 0; e < 4; ++e) dma(w2 + 128 * ct + (32 * u + 8 * e) * (D_ * 2), v2, 8 * SLOT + u * 4096 + e * 1024);
      }
      STAMP(2);
      DRAIN(); lds_set(fullw, 4, lane);
      STAMP(3);
      wait_free(3);
      NOUNROLL for (int n = 0; n < 8; ++n) w3slice(n);
      STAMP(4);
      wait_free(4);
      NOUNROLL for (int n = 8; n < 11; ++n) w3slice(n);
      wait_free(5);
      NOUNROLL for (int n = 11; n < 16; ++n) w3slice(n);
      VMW(15); lds_set(fullw, 6, lane);
      wait_full(6);
      colsum3(0, 10);
      STAMP(5);
      DRAIN(); lds_set(fullw, 7, lane);
      STAMP(6);
      wait_full(7);
      colsum3(10, 16);
    } else {
      __builtin_amdgcn_s_sleep(SL2);
      NOUNROLL for (int n = 0; n < 16; ++n) w3slice(n);
      DRAIN(); lds_set(fullw, 7, lane);
      wait_full(7);
      colsum3(0, 16);
    }
    lds_set(fullw, 8, lane);
    STAMP(7);
    wait_free(6); wait_full(8);
    STAMP(11);
    w3slice(16); w3slice(17);
    DRAIN(); lds_set(fullw, 9, lane);
    STAMP(12);
    wait_full(9);
    __builtin_amdgcn_s_setprio(1);
    colsum3(16, 18);
    if (t == 0) {
#pragma unroll
      for (int c = 0; c < 3; ++c) *(LAS f32x4*)(L_CS3 + (3 * ld + c) * 64 + q * 16) = cs[c];
    }
    CLOB();
    lds_set(fullw, 10, lane);
    __builtin_amdgcn_s_setprio(0);
    STAMP(8);
    const u64 tag = get_tag();
    auto hround = [&](int p) INL {
      wait_min4(L_RQ, p + 1);
      const int so = OFF_HH + (8 * g * 144 + fr * 18) * 1024;
      if (p < 4) {
        v4u d[8];
#pragma unroll
        for (int k = 0; k < 8; ++k) d[k] = *(const LAS v4u*)(L_HS + k * 1024 + lane * 16);
        CLOB();
        lds_set(L_AGR, p + 1, lane);
#pragma unroll
        for (int k = 0; k < 8; ++k) ST16(d[k], lane * 16 + k * (144 * 1024), so + (pbase(s) + p) * 1024);
      } else {
        v2u d[8];
#pragma unroll
        for (int k = 0; k < 8; ++k) d[k] = *(const LAS v2u*)(L_HS + k * 1024 + lane * 16);
#pragma unroll
        for (int k = 0; k < 8; ++k) ST8(d[k], lane * 16 + (s & 1) * 8 + k * (144 * 1024), so + (s < 2 ? 4 : 13) * 1024);
        CLOB();
        lds_set(L_AGR, 5, lane);
      }
      DRAIN();
      SIGNAL(FC, p);
    };
    auto pround = [&](int p) INL {
      wait_min4(L_RQ, 5 + p + 1);
      const int so = OFF_P + ((g * 4 + s) * 64 + fr) * PUNIT + p * 1024;
      v4u d[7];
#pragma unroll
      for (int k = 0; k < 7; ++k) d[k] = *(const LAS v4u*)(L_PS + p * 8192 + ((fr + 1 + k) & 7) * 1024 + lane * 16);
#pragma unroll
      for (int k = 0; k < 7; ++k) ST16(d[k], lane * 16, so + ((fr + 1 + k) & 7) * (8 * PUNIT));
      DRAIN();
      SIGNAL(FD, p);
    };
    if (ld == 0) {
      wait_free(8);
      NOUNROLL for (int m = 0; m < 6; ++m) w4whole(m, dstep(m, s), IC<0>{});
      VMW(18); lds_set(fullw, 11, lane);
      NOUNROLL for (int m = 6; m < 10; ++m) w4whole(m, dstep(m, s), IC<0>{});
      DRAIN(); lds_set(fullw, 12, lane);
      STAMP(9);
    } else {
      lds_set(fullw, 13, lane);
      NOUNROLL for (int p = ld - 1; p < 5; p += 2) { hround(p); STAMP(13 + p); }
    }
    wait_word(L_AGR, 5); wait_full(10);
    if (ld == 1) wait_free(10);
    NOUNROLL for (int m = ld == 0 ? 10 : ld == 1 ? 16 : 13; m < (ld == 0 ? 13 : ld == 1 ? 18 : 16); ++m) w4whole(m, dstep(m, s), IC<1>{});
    DRAIN(); lds_set(fullw, 14, lane);
    STAMP(10);
    pround(ld);
    STAMP(23);
  } else {
    const u64 tag = __hip_atomic_load((GAS u64*)(ws + OFF_EPOCH), __ATOMIC_RELAXED, __HIP_MEMORY_SCOPE_AGENT) + 1ull;
    if (lane == 0) {
      *(volatile LAS u64*)L_TAG = tag;
      *(volatile LAS unsigned*)(L_RDY + 4 * R_TAG) = 1u;
      __hip_atomic_store(flagp(ws, FS, b, 0), tag, __ATOMIC_RELAXED, __HIP_MEMORY_SCOPE_AGENT);
    }
    if (ab) {
      const v4u e4 = {EMPTY, EMPTY, EMPTY, EMPTY};
      wait_full(3);
#pragma unroll
      for (int k = 0; k < 8; ++k) ST16(e4, lane * 16 + (k & 1) * 1024 + (k >> 1) * (18 * 1024), OFF_X1 + (4 * rt * 18 + 2 * ct) * 1024);
      if (lane < 32) ST16(e4, OFF_STAT + (lane >> 3) * (9 * 128) + (lane & 7) * 16, (4 * rt * 9 + ct) * 128);
      DRAIN();
      lds_set(L_RDY + 4 * R_Z, 1, lane);
    }
    STAMP(1);
    auto poll = [&](GAS u64* p) INL {
      NOUNROLL for (unsigned n = 0; n < GLB_CAP; ++n) {
        const u64 v = __hip_atomic_load(p, __ATOMIC_RELAXED, __HIP_MEMORY_SCOPE_AGENT);
        if (__all(v == tag)) break;
        __builtin_amdgcn_s_sleep(1);
      }
      CLOB();
    };
    auto poll_bits = [&](auto TWO, GAS u64* p0, GAS u64* p1, int word, u64 ma, u64 mb, u64 mc, u64 md) INL {
      unsigned done = 0;
      NOUNROLL for (unsigned n = 0; n < GLB_CAP; ++n) {
        const u64 v0 = __hip_atomic_load(p0, __ATOMIC_RELAXED, __HIP_MEMORY_SCOPE_AGENT);
        u64 v1 = v0;
        if constexpr (TWO.value) v1 = __hip_atomic_load(p1, __ATOMIC_RELAXED, __HIP_MEMORY_SCOPE_AGENT);
        const u64 bad0 = __ballot(v0 != tag), bad1 = __ballot(v1 != tag);
        const unsigned now = ((bad0 & ma) ? 0u : 1u) | ((bad0 & mb) ? 0u : 2u) | ((bad1 & mc) ? 0u : 4u) | ((bad1 & md) ? 0u : 8u);
        if (now != done) { done = now; lds_set(L_RDY + 4 * word, now, lane); }
        if (now == 15u) break;
        __builtin_amdgcn_s_sleep(1);
      }
      lds_set(L_RDY + 4 * word, 15u, lane);
      CLOB();
    };
    constexpr u64 M01 = 0x3333333333333333ull, M23 = 0xccccccccccccccccull;
    const int pl = (lane % 36) >> 2;
    if (ab) poll_bits(IC<0>{}, flagp(ws, FA, j + 8 * ((r & 1) + 2 * pl), lane & 3), nullptr, R_H, M01, M23, M01, M23);
    STAMP(2);
    poll_bits(IC<1>{}, flagp(ws, FB, g + 16 * pl, lane & 3), flagp(ws, FB, g + 8 + 16 * pl, lane & 3), R_X, M01, M23, M01, M23);
    STAMP(3);
    {
      v4u st[9];
      NOUNROLL for (unsigned n = 0; n < TRY_CAP; ++n) { CLOB();
        unsigned m = 0;
#pragma unroll
        for (int k = 0; k < 9; ++k) st[k] = LD16(OFF_STAT + ((lane >> 3) * 9 + k) * 128 + (lane & 7) * 16, g * (8 * 9 * 128));
#pragma unroll
        for (int k = 0; k < 9; ++k) m = mx4(m, st[k]);
        if (!missing(m)) break;
      }
      f32x4 a = __builtin_bit_cast(f32x4, st[0]);
#pragma unroll
      for (int k = 1; k < 9; ++k) a += __builtin_bit_cast(f32x4, st[k]);
      *(LAS f32x4*)(L_LN + lane * 32) = ln_consts(a.x, a.y);
      *(LAS f32x4*)(L_LN + lane * 32 + 16) = ln_consts(a.z, a.w);
      CLOB();
      lds_set(L_RDY + 4 * R_LN, 1, lane);
    }
    if (b == 0) {
      NOUNROLL for (int k = 0; k < 4; ++k) poll(flagp(ws, FS, k * 64 + lane, 0));
      if (lane == 0) __hip_atomic_store((GAS u64*)(ws + OFF_EPOCH), tag, __ATOMIC_RELAXED, __HIP_MEMORY_SCOPE_AGENT);
    }
    auto poll5 = [&](GAS u64* p0, int word) INL {
      unsigned done = 0;
      NOUNROLL for (unsigned n = 0; n < GLB_CAP; ++n) {
        const u64 bad = __ballot(__hip_atomic_load(p0, __ATOMIC_RELAXED, __HIP_MEMORY_SCOPE_AGENT) != tag);
        unsigned now = 0;
#pragma unroll
        for (int k = 0; k < 5; ++k) now |= (bad & (0x0101010101010101ull << k)) ? 0u : 1u << k;
        if (now != done) { done = now; lds_set(L_RDY + 4 * word, now, lane); }
        if (now == 31u) break;
        __builtin_amdgcn_s_sleep(1);
      }
      lds_set(L_RDY + 4 * word, 31u, lane);
    };
    poll5(flagp(ws, FC, j + 8 * ((r & ~3) + ((lane >> 3) & 3)), lane & 7), R_HH);
    STAMP(4);
    const int wr = lane >> 3;
    poll5(flagp(ws, FD, MAP ? (j & 1) + (wr & 6) + 8 * (((wr & 1) << 4) + (r & 15)) : wr + 8 * r, (lane & 7) < 3 ? (lane & 7) : 3 + (fr >> 1)), R_P);
    STAMP(5);
  }
  if (w < 2 || (w >= 4 && w < 7)) {
    const int p = w < 2 ? w + 3 : w - 4;
    const int T = 8 * g + fr;
    const int u = p < 4 ? pbase(s) + p : (s < 2 ? 4 : 13);
    v4u xv;
    NOUNROLL for (unsigned n = 0; n < TRY_CAP; ++n) { CLOB(); xv = LD16(lane * 16, OFF_X1 + (T * 18 + u) * 1024); if (!missing(mx4(0u, xv))) break; }
    const float rstd = *(const LAS float*)(L_LN + (16 * fr + t) * 16 + 12);
    wait_bit(L_RDY + 4 * R_P, 1u << p);
    STAMP(20);
    const int psrc = OFF_P + ((g * 4 + s) * 8 + fr) * (8 * PUNIT) + p * 1024;
    char* orow = out + (size_t)(16 * T + t) * (D_ * 2) + (32 * u + 4 * q) * 2;
    if (p < 4) {
      v4u pv[8];
#pragma unroll
      for (int k = 1; k < 8; ++k) pv[k] = LD16(lane * 16, psrc + ((fr + k) & 7) * PUNIT);
      pv[0] = *(const LAS v4u*)(L_PS + p * 8192 + fr * 1024 + lane * 16);
      float a[8];
#pragma unroll
      for (int e = 0; e < 8; ++e) a[e] = 0.f;
#pragma unroll
      for (int k = 0; k < 8; ++k)
#pragma unroll
        for (int e = 0; e < 4; ++e) { const unsigned d = pv[k][e]; a[2 * e] += lo16(d); a[2 * e + 1] += hi16(d); }
      unsigned o[4];
#pragma unroll
      for (int e = 0; e < 4; ++e) {
        const unsigned h2 = pk(a[2 * e] * rstd, a[2 * e + 1] * rstd), xe = xv[e];
        o[e] = pk(lo16(xe) + lo16(h2), hi16(xe) + hi16(h2));
      }
      *(GAS v2u*)orow = v2u{o[0], o[1]};
      *(GAS v2u*)(orow + 32) = v2u{o[2], o[3]};
    } else {
      v2u pv[8];
#pragma unroll
      for (int k = 1; k < 8; ++k) pv[k] = LD8(lane * 8, psrc + ((fr + k) & 7) * PUNIT);
      pv[0] = *(const LAS v2u*)(L_PS + 32768 + fr * 512 + lane * 8);
      float a[4] = {0.f, 0.f, 0.f, 0.f};
#pragma unroll
      for (int k = 0; k < 8; ++k) { a[0] += lo16(pv[k].x); a[1] += hi16(pv[k].x); a[2] += lo16(pv[k].y); a[3] += hi16(pv[k].y); }
      const unsigned x0 = (s & 1) ? xv.z : xv.x, x1v = (s & 1) ? xv.w : xv.y;
      const unsigned h0 = pk(a[0] * rstd, a[1] * rstd), h1 = pk(a[2] * rstd, a[3] * rstd);
      *(GAS v2u*)(orow + (s & 1) * 32) = v2u{pk(lo16(x0) + lo16(h0), hi16(x0) + hi16(h0)), pk(lo16(x1v) + lo16(h1), hi16(x1v) + hi16(h1))};
    }
    STAMP(21);
  }
#endif
}
}

extern "C" void kernel_launch(void* const* d_in, const int* in_sizes, int n_in, void* d_out, int out_size, void* d_ws, size_t ws_size,
                              hipStream_t stream) {
  mx::mixer_kernel<<<256, 512, 0, stream>>>((const char*)d_in[0], (const char*)d_in[1], (const char*)d_in[2], (const char*)d_in[3],
                                            (const char*)d_in[4], (char*)d_out, (char*)d_ws);
}
```

```cpp
#include <hip/hip_runtime.h>
#include <utility>

#define STAMPS 0
#define MAP 1
#define SL1 32
#define SL2 64

#define LAS __attribute__((address_space(3)))
#define GAS __attribute__((address_space(1)))
#define DI __device__ __forceinline__
typedef unsigned v4u __attribute__((ext_vector_type(4)));
typedef unsigned v2u __attribute__((ext_vector_type(2)));
typedef float f32x4 __attribute__((ext_vector_type(4)));
typedef float f32x2 __attribute__((ext_vector_type(2)));
typedef short bf16x8 __attribute__((ext_vector_type(8)));
typedef short s16x4 __attribute__((ext_vector_type(4)));
typedef __bf16 bf16x2_t __attribute__((ext_vector_type(2)));
typedef unsigned long long u64;

namespace mx {
constexpr int D_ = 576, F_ = 4608;
constexpr int OFF_EPOCH = 0;
constexpr int OFF_FLAGS = 4096;
constexpr int OFF_STAT = 344064;
constexpr int OFF_H1 = 524288;
constexpr int OFF_X1 = OFF_H1 + 64 * 18 * 1024;
constexpr int OFF_HH = OFF_X1 + 64 * 18 * 1024;
constexpr int PUNIT = 4608;
constexpr int OFF_P = OFF_HH + 64 * 144 * 1024;
constexpr int OFF_DBG = OFF_P + 8 * 4 * 8 * 8 * PUNIT;
constexpr int WS_BYTES = OFF_DBG + 2 * 256 * 8 * 32 * 8 + 256;
enum { FS = 0, FA = 1, FB = 2, FC = 3, FD = 4 };
constexpr int SLOT = 9216, MISC = 16 * SLOT;
constexpr int L_FULL = MISC, L_FREE = MISC + 16, L_RQ = MISC + 32, L_RDY = MISC + 48  , L_SY = MISC + 96, L_AGR = MISC + 112, L_TAG = MISC + 120;
constexpr int L_DUM = MISC + 128  , L_CS1 = MISC + 512  , L_CS3 = MISC + 1024  , L_LN = MISC + 2048  ,
              L_HS = MISC + 4096  , L_LN1 = L_HS  , L_DBG = MISC + 12288, L_PS = 4 * SLOT  ;
enum { R_H = 0, R_X = 1, R_HH = 2, R_P = 3, R_TAG = 4, R_LN = 5, R_Z = 6 };
constexpr unsigned EMPTY = 0xffffffffu;
constexpr unsigned TRY_CAP = 1u << 12;
constexpr unsigned LDS_CAP = 1u << 21, GLB_CAP = 1u << 16;
#define DCAPV 6
constexpr int PD = 4, DCAP = DCAPV;

template <class F, int... I> DI void sfor_impl(F&& f, std::integer_sequence<int, I...>) { (f(std::integral_constant<int, I>{}), ...); }
template <int N, class F> DI void sfor(F&& f) { sfor_impl(f, std::make_integer_sequence<int, N>{}); }
template <int V> using IC = std::integral_constant<int, V>;
#define INL __attribute__((always_inline))
#define NOUNROLL _Pragma("nounroll")
#define RARE(x) __builtin_expect(!!(x), 0)
#define SB() __builtin_amdgcn_sched_barrier(0)
#define CLOB() asm volatile("" ::: "memory")
#define DRAIN() asm volatile("s_waitcnt vmcnt(0)" ::: "memory")
#define VMW(n) asm volatile("s_waitcnt vmcnt(%0)" ::"i"(n) : "memory")

DI unsigned rfl(unsigned v) { return __builtin_amdgcn_readfirstlane(v); }
DI f32x4 mfma(bf16x8 a, bf16x8 b, f32x4 c) { return __builtin_amdgcn_mfma_f32_16x16x32_bf16(a, b, c, 0, 0, 0); }
template <bool FIRST> DI void mfa(f32x4& c, bf16x8 a, bf16x8 b) {
  if constexpr (FIRST) asm volatile("v_mfma_f32_16x16x32_bf16 %0, %1, %2, 0" : "=&v"(c) : "v"(a), "v"(b));
  else asm volatile("v_mfma_f32_16x16x32_bf16 %0, %1, %2, %0" : "+v"(c) : "v"(a), "v"(b));
}
#define MFMA_DONE() asm volatile("s_nop 7\n\ts_nop 3" ::: "memory")
DI bf16x8 asfrag(v4u v) { return __builtin_bit_cast(bf16x8, v); }
DI s16x4 trd(unsigned addr) { return __builtin_amdgcn_ds_read_tr16_b64_v4i16((LAS s16x4*)addr); }
DI bf16x8 cat(s16x4 lo, s16x4 hi) { return __builtin_shufflevector(lo, hi, 0, 1, 2, 3, 4, 5, 6, 7); }
DI unsigned pk(float a, float b) { bf16x2_t r = __builtin_convertvector(f32x2{a, b}, bf16x2_t); return __builtin_bit_cast(unsigned, r); }
DI unsigned pkc(float a, float b) { return min(pk(a, b), EMPTY - 1u); }
DI unsigned mx2(unsigned m, unsigned a, unsigned b) { return max(max(m, a), b); }
DI unsigned mx4(unsigned m, v4u d) { return mx2(mx2(m, d.x, d.y), d.z, d.w); }
DI bool missing(unsigned m) { return __any(m == EMPTY); }
DI v4u asv4(bf16x8 f) { return __builtin_bit_cast(v4u, f); }
DI float lo16(unsigned p) { return __uint_as_float(p << 16); }
DI float hi16(unsigned p) { return __uint_as_float(p & 0xffff0000u); }
DI float bfr(float a) { return lo16(pk(a, a)); }
DI float qsum(float v) {
  auto r = __builtin_amdgcn_permlane16_swap(__float_as_uint(v), __float_as_uint(v), false, false);
  float a = __uint_as_float(r[0]) + __uint_as_float(r[1]);
  auto r2 = __builtin_amdgcn_permlane32_swap(__float_as_uint(a), __float_as_uint(a), false, false);
  return __uint_as_float(r2[0]) + __uint_as_float(r2[1]);
}
constexpr float GA = -2.302208198144325f;
constexpr float GB = -0.1029432397357593f;
DI f32x4 ln_consts(float s, float ss) {
  const float m = s * (1.0f / D_);
  float var = __builtin_fmaf(-m, m, ss * (1.0f / D_));
  var = var > 0.f ? var : 0.f;
  const float sd = bfr(__builtin_amdgcn_sqrtf(bfr(bfr(var) + 1e-5f)));
  const float r = __builtin_amdgcn_rcpf(sd);
  return f32x4{-bfr(m), GA * r, GB * r * r * r, r};
}
template <int N> DI void geluN(f32x2 (&z)[N], const float (&a)[N], const float (&b)[N]) {
  f32x2 p[N];
#pragma unroll
  for (int k = 0; k < N; ++k) p[k] = z[k] * z[k];
  SB();
#pragma unroll
  for (int k = 0; k < N; ++k) p[k] = p[k] * b[k] + a[k];
  SB();
#pragma unroll
  for (int k = 0; k < N; ++k) p[k] = z[k] * p[k];
  SB();
#pragma unroll
  for (int k = 0; k < N; ++k) { p[k].x = __builtin_amdgcn_exp2f(p[k].x); p[k].y = __builtin_amdgcn_exp2f(p[k].y); }
  SB();
#pragma unroll
  for (int k = 0; k < N; ++k) p[k] = p[k] + 1.0f;
  SB();
#pragma unroll
  for (int k = 0; k < N; ++k) { p[k].x = __builtin_amdgcn_rcpf(p[k].x); p[k].y = __builtin_amdgcn_rcpf(p[k].y); }
  SB();
#pragma unroll
  for (int k = 0; k < N; ++k) z[k] = z[k] * p[k];
  SB();
}

DI unsigned ldsw(int addr) { return *(volatile LAS unsigned*)addr; }
DI v4u lds4(int addr) { return *(volatile LAS v4u*)addr; }
DI void lds_set(int addr, unsigned v, int lane) { if (lane == 0) *(volatile LAS unsigned*)addr = v; }
DI v4u peek4(int addr) { CLOB(); return *(const LAS v4u*)addr; }
DI unsigned peek(int addr) { CLOB(); return *(const LAS unsigned*)addr; }
DI void post(unsigned addr, unsigned v) { CLOB(); *(LAS unsigned*)addr = v; CLOB(); }
DI void wait_word(int addr, unsigned v) {
  NOUNROLL for (unsigned n = 0; n < LDS_CAP; ++n) { if (rfl(ldsw(addr)) >= v) break; __builtin_amdgcn_s_sleep(1); }
  CLOB();
}
DI unsigned min3(v4u x) { return rfl(min(min(x.x, x.y), x.z)); }
DI unsigned min4(v4u x) { return rfl(min(min(x.x, x.y), min(x.z, x.w))); }
DI void wait_full(unsigned v) {
  NOUNROLL for (unsigned n = 0; n < LDS_CAP; ++n) { if (min3(lds4(L_FULL)) >= v) break; __builtin_amdgcn_s_sleep(1); }
  CLOB();
}
DI void wait_min4(int addr, unsigned v) {
  NOUNROLL for (unsigned n = 0; n < LDS_CAP; ++n) { if (min4(lds4(addr)) >= v) break; __builtin_amdgcn_s_sleep(1); }
  CLOB();
}
DI void wait_free(unsigned v) { wait_min4(L_FREE, v); }
DI void wait_bit(int addr, unsigned bit) {
  NOUNROLL for (unsigned n = 0; n < LDS_CAP; ++n) { if ((rfl(ldsw(addr)) & bit) == bit) break; __builtin_amdgcn_s_sleep(1); }
  CLOB();
}
DI GAS u64* flagp(char* ws, int kind, int b, int wv) { return (GAS u64*)(ws + OFF_FLAGS + (kind * 256 + b) * 128 + wv * 8); }
DI u64 get_tag() {
  wait_word(L_RDY + 4 * R_TAG, 1);
  return ((u64)rfl(ldsw(L_TAG + 4)) << 32) | rfl(ldsw(L_TAG));
}
#define LD16(off, soff) __builtin_amdgcn_raw_buffer_load_b128(rs, (off), (soff), 16)
#define LD8(off, soff) __builtin_amdgcn_raw_buffer_load_b64(rs, (off), (soff), 16)
#define ST16(v, off, soff) __builtin_amdgcn_raw_buffer_store_b128((v), rs, (off), (soff), 16)
#define ST8(v, off, soff) __builtin_amdgcn_raw_buffer_store_b64((v), rs, (off), (soff), 16)
#define SIGNAL(kind, wv) do { CLOB(); ST8((v2u{(unsigned)tag, (unsigned)(tag >> 32)}), lane ? 0x7ffffff0 : 0, OFF_FLAGS + ((kind) * 256 + b) * 128 + (wv) * 8); CLOB(); } while (0)
DI void dma(const char* sbase, unsigned voff, unsigned ldsaddr) {
  asm volatile("s_mov_b32 m0, %0\n\ts_nop 0\n\tglobal_load_lds_dwordx4 %1, %2" ::"s"(ldsaddr), "v"(voff), "s"(sbase) : "memory");
}
DI void dmac(const char* sbase, unsigned voff, unsigned ldsaddr) { dma(sbase, voff, ldsaddr); VMW(DCAP); }
DI int pbase(int s) { return (9 * s + (s & 1)) >> 1; }
DI int dstep(int M, int s) {
  if (M < 4) return pbase(s) + M;
  else if (M < 16) return pbase((s + 1 + (M - 4) % 3) & 3) + (M - 4) / 3;
  else return M == 16 ? 4 : 13;
}
#define STAMP(k) do { } while (0)
#define COUNT(k) do { } while (0)

__global__ __launch_bounds__(512, 2) void mixer_kernel(const char* __restrict__ x, const char* __restrict__ w1, const char* __restrict__ w2,
                                                       const char* __restrict__ w3, const char* __restrict__ w4, char* __restrict__ out, char* ws) {
#if defined(__HIP_DEVICE_COMPILE__)
  __shared__ __attribute__((aligned(16))) char smem[163840];
  const int tid = threadIdx.x, lane = tid & 63, w = rfl(tid >> 6);
  const int t = lane & 15, q = lane >> 4;
  const int b = blockIdx.x, j = b & 7, r = b >> 3;
  const bool ab = r < 18;
  const int rt = 2 * j + (r & 1), ct = r >> 1;
  const int g = MAP ? 4 * (j & 1) + ((r >> 2) & 3) : r >> 2, fr = MAP ? (j & 6) + (r >> 4) : j, s = r & 3;
  const __amdgpu_buffer_rsrc_t rs = __builtin_amdgcn_make_buffer_rsrc(ws, 0, WS_BYTES, 0x00020000);
  const bf16x8 ones = {0x3F80, 0x3F80, 0x3F80, 0x3F80, 0x3F80, 0x3F80, 0x3F80, 0x3F80};
  asm volatile("" ::"v"(smem));
  STAMP(0);
  if (tid < 32) *(volatile LAS unsigned*)(MISC + tid * 4) = 0u;
  asm volatile("s_waitcnt lgkmcnt(0)\n\ts_barrier" ::: "memory");
  if (ab) {
    if (w < 4) {
      const char* xsrc = x + (size_t)(4 * rt + w) * (16 * D_ * 2);
#pragma unroll
      for (int e = 0; e < 18; ++e) {
        const unsigned f = (((unsigned)(lane + 64 * e) * 911u) >> 17) & 7u;
        dma(xsrc + e * 1024, (lane * 16) ^ (f << 4), (8 + 2 * w) * SLOT + e * 1024);
      }
    }
  }

  if (w < 4) {
    const int i = w;
    unsigned pa = lane ? L_DUM + 4 * lane : L_FREE + 4 * i;
    asm volatile("" : "+v"(pa));
    const u64 tag = get_tag();
    constexpr int P_FREE = 0, P_RQ = L_RQ - L_FREE, P_SY = L_SY - L_FREE;
    if (ab) {
      unsigned ab4[4];
#pragma unroll
      for (int c = 0; c < 4; ++c) ab4[c] = (4 * q + (t >> 2)) * 128 + ((c ^ ((2 * q + (t >> 3)) & 3)) << 5) + (t & 3) * 8;
      const int T = 4 * rt + i;
      v2u xres[4];
      {
        const int tp = i >> 1, ch = i & 1;
        const unsigned xa = (8 + 2 * i) * SLOT + t * (D_ * 2), xs = ((t >> 1) & 7) << 4;
        unsigned xe[2][2], wb[2][2];
#pragma unroll
        for (int e = 0; e < 2; ++e) {
          xe[0][e] = xa + ((64 * (e ^ tp) + q * 16) ^ xs);
          xe[1][e] = xe[0][e] + (ch ? -2 * SLOT : 2 * SLOT);
          wb[e][0] = (ch ? ab4[2] : ab4[0]) + (e ^ tp) * 4096;
          wb[e][1] = (ch ? ab4[3] : ab4[1]) + (e ^ tp) * 4096;
          asm volatile("" : "+v"(xe[0][e]), "+v"(xe[1][e]), "+v"(wb[e][0]), "+v"(wb[e][1]));
        }
        int dst[4];
        unsigned keep[4], dm[4];
#pragma unroll
        for (int k = 0; k < 4; ++k) {
          const int gt = 4 * ct + 2 * ch + (k & 1), grp = gt / 9;
          const int sh = grp == 0 ? 1 : grp == 1 ? -1 : grp == 2 ? 2 : -2;
          const int ts = 16 * (ch ^ (k >> 1)) + t + sh;
          keep[k] = (ts >= 0 && ts < 32) ? 0xffffffffu : 0u;
          dst[k] = OFF_H1 + ((T & ~1) + ((ts >> 4) & 1)) * (18 * 1024) + ((ts & 15) + 16 * q) * 16 + (k & 1) * 8;
          dm[k] = (q == (t >> 2) && k == (t & 3)) ? 0xffffffffu : 0u;
          asm volatile("" : "+v"(dst[k]), "+v"(keep[k]), "+v"(dm[k]));
        }
        DRAIN();
        post(pa + P_FREE, 1);
        wait_word(L_FREE + 4 * (i ^ 1), 1);
        STAMP(1);
        bf16x8 xf[2][18];
        f32x4 aS = {0, 0, 0, 0}, aG = {0, 0, 0, 0};
#pragma unroll
        for (int p = 0; p < 6; ++p) xf[0][p] = *(const LAS bf16x8*)(xe[0][p & 1] + (p >> 1) * 128);
        SB();
        sfor<18>([&](auto P) INL {
          constexpr int p = P.value;
          aS = mfma(ones, xf[0][p], aS); SB();
          if constexpr (p + 6 < 18) xf[0][p + 6] = *(const LAS bf16x8*)(xe[0][p & 1] + ((p + 6) >> 1) * 128);
          else if constexpr (p + 6 < 22) xres[p - 12] = *(const LAS v2u*)(xa + 128 * ct + ((32 * (p - 12) + 8 * q) ^ xs));
          SB();
          aG = mfma(xf[0][p], xf[0][p], aG); SB();
          xf[1][p] = *(const LAS bf16x8*)(xe[1][p & 1] + (p >> 1) * 128);
          SB();
        });
        CLOB();
        post(pa + P_FREE, 2);
        f32x4 lc[2];
        {
          const float gd = __uint_as_float(((__float_as_uint(aG.x) & dm[0]) | (__float_as_uint(aG.y) & dm[1])) | ((__float_as_uint(aG.z) & dm[2]) | (__float_as_uint(aG.w) & dm[3])));
          lc[0] = ln_consts(aS[0], qsum(gd));
          if (q == 0) *(LAS f32x4*)(L_LN1 + i * 256 + t * 16) = lc[0];
          CLOB();
          post(pa + P_SY, 1);
        }
        SB();
        STAMP(17);
        wait_full(1);
        constexpr int PA = 4;
        s16x4 wl[PA], wh[PA];
#define AADDR(k) (wb[((k) >> 1) & 1][(k) & 1] + ((k) >> 2) * 8192)
#pragma unroll
        for (int k = 0; k < PA; ++k) { wl[k] = trd(AADDR(k)); wh[k] = trd(AADDR(k) + 2048); }
        bf16x8 one2 = ones;
        asm volatile("" : "+v"(one2));
        SB();
        f32x4 acc[2][2], cs[2];
        v4u fv;
        sfor<36>([&](auto K) INL {
          constexpr int k = K.value, p = k >> 1, l = k & 1, nx = k - 1 + PA;
          const bf16x8 cur = cat(wl[k % PA], wh[k % PA]);
          mfa<(p == 0)>(acc[0][l], cur, xf[0][p]); SB();
          if constexpr (nx == 12 || nx == 24) { if (RARE(min3(fv) < nx / 12 + 1)) wait_full(nx / 12 + 1); CLOB(); SB(); }
          if constexpr (k >= 1 && nx < 36) wl[nx % PA] = trd(AADDR(nx));
          SB();
          mfa<(p == 0)>(acc[1][l], cur, xf[1][p]); SB();
          if constexpr (k >= 1 && nx < 36) wh[nx % PA] = trd(AADDR(nx) + 2048);
          if constexpr (nx == 9 || nx == 21) fv = peek4(L_FULL);
          SB();
          if constexpr ((p & 1) == 0) { mfa<(p == 0)>(cs[l], cur, one2); SB(); }
        });
        MFMA_DONE();
        if (t == 0) { *(LAS f32x4*)(L_CS1 + i * 128 + q * 16) = cs[0]; *(LAS f32x4*)(L_CS1 + i * 128 + 64 + q * 16) = cs[1]; }
        CLOB();
        post(pa + P_SY, 2);
        STAMP(2);
        wait_word(L_SY + 4 * (i ^ 1), 1);
        lc[1] = *(const LAS f32x4*)(L_LN1 + (i ^ 1) * 256 + t * 16);
        wait_word(L_SY + 4 * (i ^ 2), 2);
        const f32x4 o0 = *(const LAS f32x4*)(L_CS1 + (i ^ 2) * 128 + q * 16), o1 = *(const LAS f32x4*)(L_CS1 + (i ^ 2) * 128 + 64 + q * 16);
        cs[0] += o0; cs[1] += o1;
        f32x2 z[8];
        float ga8[8], gb8[8];
#pragma unroll
        for (int k = 0; k < 4; ++k) {
          const f32x4 zz = cs[k & 1] * lc[k >> 1].x + acc[k >> 1][k & 1];
          z[2 * k] = f32x2{zz.x, zz.y}; z[2 * k + 1] = f32x2{zz.z, zz.w};
          ga8[2 * k] = ga8[2 * k + 1] = lc[k >> 1].y; gb8[2 * k] = gb8[2 * k + 1] = lc[k >> 1].z;
        }
        SB();
        geluN<8>(z, ga8, gb8);
        unsigned hp[8];
#pragma unroll
        for (int k = 0; k < 8; ++k) { const f32x2 v = z[k] * lc[k >> 2].w; hp[k] = pk(v.x, v.y) & keep[k >> 1]; }
        const int up = 2 * ct + ch;
        if (RARE(up == 4 || up == 13)) {
#pragma unroll
          for (int k = 0; k < 4; ++k) ST8((v2u{hp[2 * k], hp[2 * k + 1]}), dst[k], up * 1024);
        } else {
#pragma unroll
          for (int tt = 0; tt < 2; ++tt) ST16((v4u{hp[4 * tt], hp[4 * tt + 1], hp[4 * tt + 2], hp[4 * tt + 3]}), dst[2 * tt], up * 1024);
        }
        STAMP(3);
        DRAIN();
        SIGNAL(FA, i);
        post(pa + P_FREE, 3);
        STAMP(4);
      }
      {
        wait_full(4);
#pragma unroll
        for (int c = 0; c < 4; ++c) { ab4[c] += 8 * SLOT; asm volatile("" : "+v"(ab4[c])); }
        constexpr int PB = 32;
        s16x4 wl[PB], wh[PB];
#pragma unroll
        for (int k = 0; k < PB; ++k) { wl[k] = trd(ab4[k & 3] + (k >> 2) * 4096); wh[k] = trd(ab4[k & 3] + (k >> 2) * 4096 + 2048); }
        SB();
        f32x2 xr[8];
#pragma unroll
        for (int l = 0; l < 4; ++l) { xr[2 * l] = f32x2{lo16(xres[l].x), hi16(xres[l].x)}; xr[2 * l + 1] = f32x2{lo16(xres[l].y), hi16(xres[l].y)}; }
#pragma unroll
        for (int k = 0; k < 8; ++k) asm volatile("" : "+v"(xr[k]));
        wait_bit(L_RDY + 4 * R_H, 1u << (i >> 1));
        STAMP(5);
        bf16x8 hf[18];
        const int hsrc = OFF_H1 + T * (18 * 1024);
#pragma unroll
        for (int u = 0; u < 9; ++u) hf[u] = asfrag(LD16(lane * 16 + u * 1024, hsrc));
        SB();
        f32x4 acc[4];
        unsigned zv;
        sfor<72>([&](auto K) INL {
          constexpr int k = K.value, u = k >> 2;
          mfa<(k < 4)>(acc[k & 3], cat(wl[k % PB], wh[k % PB]), hf[u]); SB();
          if constexpr (k == 17) post(pa + P_FREE, 4);
          if constexpr (k == 60) zv = peek(L_RDY + 4 * R_Z);
          if constexpr ((k & 1) == 0 && k / 2 + 9 < 18) hf[k / 2 + 9] = asfrag(LD16(lane * 16 + (k / 2 + 9) * 1024, hsrc));
          if constexpr (k + PB < 72) { wl[k % PB] = trd(ab4[k & 3] + ((k + PB) >> 2) * 4096); wh[k % PB] = trd(ab4[k & 3] + ((k + PB) >> 2) * 4096 + 2048); }
          SB();
        });
        MFMA_DONE();
        STAMP(6);
        unsigned xp[8];
#pragma unroll
        for (int l = 0; l < 4; ++l) {
          const f32x2 v0 = xr[2 * l] + f32x2{acc[l].x, acc[l].y}, v1 = xr[2 * l + 1] + f32x2{acc[l].z, acc[l].w};
          xp[2 * l] = pkc(v0.x, v0.y); xp[2 * l + 1] = pkc(v1.x, v1.y);
        }
        if (RARE(rfl(zv) == 0)) wait_word(L_RDY + 4 * R_Z, 1);
        CLOB();
        const int dst = OFF_X1 + (T * 18 + 2 * ct) * 1024;
        ST16((v4u{xp[0], xp[1], xp[2], xp[3]}), lane * 16, dst);
        ST16((v4u{xp[4], xp[5], xp[6], xp[7]}), lane * 16 + 1024, dst);
        SB();
        float sm = 0.f, sq = 0.f;
#pragma unroll
        for (int k = 0; k < 8; ++k) {
          const bf16x2_t v = __builtin_bit_cast(bf16x2_t, xp[k]);
          sm = __builtin_amdgcn_fdot2_f32_bf16(v, bf16x2_t{(__bf16)1.0f, (__bf16)1.0f}, sm, false);
          sq = __builtin_amdgcn_fdot2_f32_bf16(v, v, sq, false);
        }
        sm = qsum(sm); sq = qsum(sq);
        ST8((v2u{min(__float_as_uint(sm), EMPTY - 1u), min(__float_as_uint(sq), EMPTY - 1u)}), q ? 0x7ffffff0 : t * 8, OFF_STAT + (T * 9 + ct) * 128);
        SIGNAL(FB, i);
        post(pa + P_FREE, 5);
        STAMP(7);
      }
    } else {
      const int lt = ((r - 18) * 4 + i) * 64 + lane;
      unsigned sink = 0;
      auto touch = [&](const char* base, unsigned off) INL { asm volatile("global_load_dword %0, %1, %2\n\tglobal_load_dword %0, %1, %2 offset:64" : "+v"(sink) : "v"(off), "s"(base) : "memory"); };
      __builtin_amdgcn_s_sleep(SL1);
#pragma unroll
      for (int k = 0; k < 2; ++k) touch(w2, min(lt + 3584 * k, 5183) * 128);
      DRAIN();
      if (MAP) {
#pragma unroll
        for (int k = 0; k < 2; ++k) { const int l = min(lt + 3584 * k, 5183); touch(w3 + (j & 6) * (576 * 2), (l / 9) * (F_ * 2) + (l % 9) * 128); }
        DRAIN();
      }
      __builtin_amdgcn_s_sleep(SL2);
#pragma unroll
      for (int k = 0; k < (MAP ? 3 : 2); ++k) touch(w4 + (size_t)(MAP ? (j & 6) : j) * (576 * D_ * 2), min(lt + 3584 * k, MAP ? 10367 : 5183) * 128);
      DRAIN();
      asm volatile("" ::"v"(sink));
    }
    unsigned b0 = (4 * q + (t >> 2)) * 288 + (t & 3) * 8, b1 = b0 + 6 * SLOT, b2 = b0 + 12 * SLOT;
    asm volatile("" : "+v"(b0), "+v"(b1), "+v"(b2));
#define WADDR(z, c) ((((z) % 16) < 6 ? b0 : ((z) % 16) < 12 ? b1 : b2) + (((z) % 16) % 6) * SLOT + (c) * 32)
#define FN(i_) ((i_) < 108 ? (i_) / 9 : 12 + ((i_) - 108) % 6)
#define FC(i_) ((i_) < 108 ? (i_) % 9 : ((i_) - 108) / 6)
    const int T0 = 8 * g + 2 * i;
    const int hdst = OFF_HH + (T0 * 144 + fr * 18) * 1024;
    bf16x8 hb[18][2];
    auto hload = [&](auto K) INL {
      constexpr int m = K.value >> 1, tt = K.value & 1;
      hb[m][tt] = asfrag(LD16(lane * 16 + tt * (144 * 1024), hdst + dstep(m, s) * 1024));
    };
    s16x4 lo[PD], hi[PD];
    unsigned rv, hw = L_HS + 2 * i * 1024 + lane * 16;
    asm volatile("" : "+v"(hw));
    v2u h8[2];
    {
      wait_full(6);
#pragma unroll
      for (int k = 0; k < PD; ++k) { lo[k] = trd(WADDR(0, k)); hi[k] = trd(WADDR(0, k) + 4608); }
      f32x4 acc[2][9];
      const int xsrc = OFF_X1 + T0 * (18 * 1024);
      int xo[2] = {lane * 16, lane * 16 + 18 * 1024};
      asm volatile("" : "+v"(xo[0]), "+v"(xo[1]));
      bf16x8 xb[12][2];
      auto xload = [&](auto K) INL {
        constexpr int n = K.value >> 1, tt = K.value & 1;
        xb[n % 12][tt] = asfrag(LD16(xo[tt] + (n & 3) * 1024, xsrc + (n >> 2) * 4096));
      };
      SB();
      auto xfix = [&](auto K0, auto K1) INL {
        NOUNROLL for (unsigned n = 0; n < TRY_CAP; ++n) { CLOB();
          unsigned m = 0;
          COUNT(1);
          sfor<K1.value - K0.value>([&](auto K) INL { xload(IC<K0.value + K.value>{}); });
          sfor<K1.value - K0.value>([&](auto K) INL { constexpr int kk = K0.value + K.value; m = mx4(m, asv4(xb[(kk >> 1) % 12][kk & 1])); });
          if (!missing(m)) break;
        }
      };
      wait_bit(L_RDY + 4 * R_X, 1u << i);
      STAMP(9);
      sfor<10>([&](auto K) INL { xload(K); });
      SB();
      unsigned chk = mx4(mx4(0u, asv4(xb[0][0])), asv4(xb[0][1])), chk2 = 0;
      if (RARE(missing(chk))) xfix(IC<0>{}, IC<2>{});
      SB();
      f32x4 lc[2], csv;
      unsigned hp[2][2][2];
      unsigned la = L_LN + (32 * i + t) * 16, ca = L_CS3 + q * 16;
      asm volatile("" : "+v"(la), "+v"(ca));
      v4u fv;
      u64 bad;
      unsigned sv, hv;
      auto epi = [&](auto Cc) INL {
        constexpr int c = Cc.value;
        f32x2 z[4];
#pragma unroll
        for (int tt = 0; tt < 2; ++tt) {
          const f32x4 zz = csv * lc[tt].x + acc[tt][c];
          z[2 * tt] = f32x2{zz.x, zz.y}; z[2 * tt + 1] = f32x2{zz.z, zz.w};
        }
        if constexpr (c >= 3 && (c & 1)) sv = rfl(rv);
        SB();
        const float ga4[4] = {lc[0].y, lc[0].y, lc[1].y, lc[1].y}, gb4[4] = {lc[0].z, lc[0].z, lc[1].z, lc[1].z};
        geluN<4>(z, ga4, gb4);
#pragma unroll
        for (int tt = 0; tt < 2; ++tt) { hp[tt][c & 1][0] = pk(z[2 * tt].x, z[2 * tt].y); hp[tt][c & 1][1] = pk(z[2 * tt + 1].x, z[2 * tt + 1].y); }
        asm volatile("" : "+v"(hp[0][c & 1][0]), "+v"(hp[0][c & 1][1]), "+v"(hp[1][c & 1][0]), "+v"(hp[1][c & 1][1]));
        if constexpr (c >= 3 && (c & 1)) { if (RARE(sv < c / 2)) wait_word(L_AGR, c / 2); CLOB(); }
        if constexpr ((c & 1) && c < 8) {
#pragma unroll
          for (int tt = 0; tt < 2; ++tt) {
            const v4u v = {hp[tt][0][0], hp[tt][0][1], hp[tt][1][0], hp[tt][1][1]};
            hb[c >> 1][tt] = asfrag(v);
            *(LAS v4u*)(hw + tt * 1024) = v;
          }
        }
        if constexpr (c == 8) { h8[0] = v2u{hp[0][0][0], hp[0][0][1]}; h8[1] = v2u{hp[1][0][0], hp[1][0][1]}; }
        if constexpr (c & 1) post(pa + P_RQ, c / 2 + 1);
        SB();
      };
      sfor<162>([&](auto I) INL {
        constexpr int ii = I.value, n = FN(ii), c = FC(ii), nx = ii - 1 + PD;
        const bf16x8 cur = cat(lo[ii % PD], hi[ii % PD]);
        mfa<(n == 0)>(acc[0][c], cur, xb[n % 12][0]); SB();
        constexpr bool ck1 = ii < 99 && (c == 5 || c == 6);
        constexpr bool ck2 = ii >= 72 && ii < 99 && c >= 1 && c <= 4;
        constexpr int k2 = 24 + 4 * (n - 8) + c - 1;
        if constexpr (ii >= 1 && nx < 162) lo[nx % PD] = trd(WADDR(FN(nx), FC(nx)));
        if constexpr (ck1) { chk = mx2(c == 5 ? 0u : chk, asv4(xb[n + 1][c - 5]).x, asv4(xb[n + 1][c - 5]).y); asm volatile("" : "+v"(chk)); }
        if constexpr (ck2) { chk2 = mx2(chk2, asv4(xb[(k2 >> 1) % 12][k2 & 1]).x, asv4(xb[(k2 >> 1) % 12][k2 & 1]).y); asm volatile("" : "+v"(chk2)); }
        if constexpr (ii < 99 && c == 7) { bad = __ballot(chk == EMPTY); asm volatile("" : "+s"(bad)); }
        if constexpr (ii == 99) { bad = __ballot(chk2 == EMPTY); asm volatile("" : "+s"(bad)); }
        if constexpr (ii == 63 || ii == 101) { sv = min3(fv); asm volatile("" : "+s"(sv)); }
        if constexpr (ii == 103) { sv = rfl(rv); asm volatile("" : "+s"(sv)); }
        SB();
        mfa<(n == 0)>(acc[1][c], cur, xb[n % 12][1]); SB();
        if constexpr (ii >= 1 && nx < 162) hi[nx % PD] = trd(WADDR(FN(nx), FC(nx)) + 4608);
        if constexpr (ck1) { chk = mx2(chk, asv4(xb[n + 1][c - 5]).z, asv4(xb[n + 1][c - 5]).w); asm volatile("" : "+v"(chk)); }
        if constexpr (ck2) { chk2 = mx2(chk2, asv4(xb[(k2 >> 1) % 12][k2 & 1]).z, asv4(xb[(k2 >> 1) % 12][k2 & 1]).w); asm volatile("" : "+v"(chk2)); }
        SB();
        if constexpr (ii < 99 && c == 8) { if (RARE(bad != 0)) xfix(IC<2 * n + 2>{}, IC<2 * n + 4>{}); SB(); }
        if constexpr (ii == 100) { if (RARE(bad != 0)) xfix(IC<24>{}, IC<36>{}); SB(); }
        if constexpr (ii < 28 && ii % 2 == 0) { xload(IC<10 + ii / 2>{}); SB(); }
        if constexpr (ii >= 30 && ii < 66 && ii % 3 == 0) { xload(IC<14 + ii / 3>{}); SB(); }
        if constexpr (ii == 18 || ii == 81 || ii == 108 || ii == 159) { post(pa + P_FREE, ii == 18 ? 6 : ii == 81 ? 7 : ii == 108 ? 8 : 9); SB(); }
        if constexpr (ii == 8) STAMP(23);
        if constexpr (ii == 35) STAMP(24);
        if constexpr (ii == 71) STAMP(25);
        if constexpr (ii == 58 || ii == 96) { fv = peek4(L_FULL); SB(); }
        if constexpr (ii == 64) { if (RARE(sv < 7)) wait_full(7); CLOB(); SB(); }
        if constexpr (ii == 97) { rv = peek(L_RDY + 4 * R_LN); SB(); }
        if constexpr (ii == 102) { if (RARE(sv < 10)) wait_full(10); CLOB(); SB(); }
        if constexpr (ii == 104) {
          if (RARE(sv == 0)) wait_word(L_RDY + 4 * R_LN, 1);
          CLOB();
          lc[0] = *(const LAS f32x4*)la; lc[1] = *(const LAS f32x4*)(la + 256);
          SB(); STAMP(10);
        }
        if constexpr (ii >= 108 && n == 14 && c >= 1) { csv = *(const LAS f32x4*)(ca + (c - 1) * 64); SB(); }
        if constexpr (ii >= 108 && n == 15 && c >= 4 && !(c & 1)) { rv = peek(L_AGR); SB(); }
        if constexpr (ii >= 108 && n == 17 && c >= 1) epi(IC<c - 1>{});
        if constexpr (ii == 150) { fv = peek4(L_FULL); hv = peek(L_RDY + 4 * R_HH); SB(); }
        if constexpr (ii == 155) { if (RARE(!(rfl(hv) & 1u))) wait_bit(L_RDY + 4 * R_HH, 1u); CLOB(); SB(); STAMP(27); }
        if constexpr (ii >= 155 && ii < 161) { hload(IC<8 + ii - 155>{}); SB(); }
      });
      csv = *(const LAS f32x4*)(ca + 8 * 64);
      if (RARE(min3(fv) < 11)) wait_full(11);
      CLOB();
#pragma unroll
      for (int k = 0; k < PD; ++k) { lo[k] = trd(WADDR(18, k)); hi[k] = trd(WADDR(18, k) + 4608); }
      SB();
      epi(IC<8>{});
      STAMP(11);
    }
    {
      f32x4 acc[2][9];
      unsigned pw = L_PS + 2 * i * 1024 + lane * 16, pw8 = L_PS + 32768 + 2 * i * 512 + lane * 8;
      asm volatile("" : "+v"(pw), "+v"(pw8));
      int po[2];
#pragma unroll
      for (int tt = 0; tt < 2; ++tt) { po[tt] = (2 * i + tt == fr) ? 0x7ff00000 : lane * 8; asm volatile("" : "+v"(po[tt])); }
      const int pdst = OFF_P + (((g * 4 + s) * 8 + 2 * i) * 8 + fr) * PUNIT;
      v4u fv;
      unsigned sv;
      auto epi = [&](auto Cc) INL {
        constexpr int c = Cc.value;
        if constexpr ((c & 1) && c < 8) {
#pragma unroll
          for (int tt = 0; tt < 2; ++tt) {
            const f32x4 a0 = acc[tt][c - 1], a1 = acc[tt][c];
            const v4u v = {pk(a0.x, a0.y), pk(a0.z, a0.w), pk(a1.x, a1.y), pk(a1.z, a1.w)};
            if constexpr (c == 7) ST16(v, 2 * po[tt] , pdst + tt * (8 * PUNIT) + 3 * 1024);
            *(LAS v4u*)(pw + tt * 1024 + (c >> 1) * 8192) = v;
          }
        }
        if constexpr (c == 8) {
#pragma unroll
          for (int tt = 0; tt < 2; ++tt) {
            const f32x4 a0 = acc[tt][8];
            const v2u v = {pk(a0.x, a0.y), pk(a0.z, a0.w)};
            ST8(v, po[tt], pdst + tt * (8 * PUNIT) + 4096);
            *(LAS v2u*)(pw8 + tt * 512) = v;
          }
        }
        if constexpr ((c & 1) && c < 7) post(pa + P_RQ, 5 + c / 2 + 1);
        SB();
      };
      constexpr int HS1 = 8, HS2 = 28, HS3 = 52;
      rv = peek(L_AGR);
      sfor<162>([&](auto I) INL {
        constexpr int ii = I.value, n = FN(ii), c = FC(ii), nx = ii - 1 + PD;
        constexpr int G = ii == HS1 ? 1 : ii == HS2 ? 2 : ii == HS3 ? 3 : -1;
        const bf16x8 cur = cat(lo[ii % PD], hi[ii % PD]);
        mfa<(n == 0)>(acc[0][c], cur, hb[n][0]); SB();
        if constexpr (ii >= 1 && nx < 162) lo[nx % PD] = trd(WADDR(18 + FN(nx), FC(nx)));
        if constexpr (G >= 0 || ii == 3) { sv = rfl(rv); asm volatile("" : "+s"(sv)); }
        if constexpr (ii == 84 || ii == 102) { sv = min3(fv); asm volatile("" : "+s"(sv)); }
        if constexpr (ii == 106) { sv = min4(fv); asm volatile("" : "+s"(sv)); }
        SB();
        mfa<(n == 0)>(acc[1][c], cur, hb[n][1]); SB();
        if constexpr (ii >= 1 && nx < 162) hi[nx % PD] = trd(WADDR(18 + FN(nx), FC(nx)) + 4608);
        SB();
        if constexpr (ii == 3) {
          if (RARE(sv < 4)) wait_word(L_AGR, 4);
          CLOB();
          *(LAS v2u*)hw = h8[0]; *(LAS v2u*)(hw + 1024) = h8[1];
          post(pa + P_RQ, 5);
          SB();
        }
        if constexpr (ii + 4 == HS1 || ii + 4 == HS2 || ii + 4 == HS3) { rv = peek(L_RDY + 4 * R_HH); SB(); }
        if constexpr (G >= 0) {
          constexpr unsigned need = G < 3 ? 1u << G : 24u;
          if (RARE((sv & need) != need)) wait_bit(L_RDY + 4 * R_HH, need);
          CLOB(); SB(); STAMP(G == 1 ? 28 : G == 2 ? 13 : 14);
        }
        if constexpr (ii >= HS1 && ii < HS1 + 6) { hload(IC<14 + ii - HS1>{}); SB(); }
        if constexpr (ii >= HS2 && ii < HS2 + 6) { hload(IC<20 + ii - HS2>{}); SB(); }
        if constexpr (ii >= HS3 && ii < HS3 + 10) { hload(IC<26 + ii - HS3>{}); SB(); }
        if constexpr (ii == 18 || ii == 54) { post(pa + P_FREE, ii == 18 ? 10 : 11); SB(); }
        if constexpr (ii == 79 || ii == 97) { fv = peek4(L_FULL); SB(); }
        if constexpr (ii == 84) { if (RARE(sv < 12)) wait_full(12); CLOB(); SB(); }
        if constexpr (ii == 102) { if (RARE(sv < 13)) wait_full(13); fv = peek4(L_FREE); SB(); STAMP(26); }
        if constexpr (ii == 106) { if (RARE(sv < 11)) wait_free(11); CLOB(); SB(); }
        if constexpr (ii >= 108 && n == 17 && c >= 1) epi(IC<c - 1>{});
      });
      MFMA_DONE();
      epi(IC<8>{});
      STAMP(15);
      asm volatile("s_waitcnt vmcnt(0) lgkmcnt(0)" ::: "memory");
      SIGNAL(FD, 3 + i);
      STAMP(16);
    }
  } else if (w < 7) {
    const int ld = w - 4;
    const unsigned fullw = L_FULL + 4 * ld;
    unsigned v3[3], v4a[9];
#pragma unroll
    for (int e = 0; e < 3; ++e) {
      const int idx = (3 * ld + e) * 64 + lane, row = idx / 18, ch = idx % 18;
      const int sc = (ch + 2 * (s & 1)) % 18;
      v3[e] = row * (F_ * 2) + sc * 16;
    }
#pragma unroll
    for (int e = 0; e < 9; ++e) { const int idx = e * 64 + lane; v4a[e] = (idx / 18) * (D_ * 2) + ((idx % 18 + 2 * (s & 1)) % 18) * 16; }
    const char* w3b = w3 + (576 * fr + 144 * s) * 2;
    const char* w4b = w4 + (size_t)(576 * fr) * (D_ * 2) + 144 * s * 2;
    auto w3slice = [&](int n) INL {
#pragma unroll
      for (int e = 0; e < 3; ++e) dma(w3b + (size_t)n * (32 * F_ * 2), v3[e], (n & 15) * SLOT + (3 * ld + e) * 1024);
    };
    auto w4whole = [&](int m, int u, auto CAP) INL {
      const char* src = w4b + u * (32 * D_ * 2);
#pragma unroll
      for (int e = 0; e < 9; ++e) { dma(src, v4a[e], ((18 + m) & 15) * SLOT + e * 1024); if constexpr (CAP.value) VMW(DCAP); }
    };
    const __amdgpu_buffer_rsrc_t r4 = __builtin_amdgcn_make_buffer_rsrc((void*)w4b, 0, 576 * D_ * 2 - 144 * s * 2, 0x00020000);
    v4u w4r[4][9];
    auto w4get = [&](auto K) INL {
      const int so = dstep(6 + ld + 3 * K.value, s) * (32 * D_ * 2);
#pragma unroll
      for (int e = 0; e < 9; ++e) { constexpr int eo[9] = {0, 2, 4, 6, 8, 1, 3, 5, 7}; w4r[K.value][eo[e]] = __builtin_amdgcn_raw_buffer_load_b128(r4, v4a[eo[e]], so, 0); }
      CLOB(); SB();
    };
    auto w4put = [&](auto K) INL {
      const unsigned a = ((24 + ld + 3 * K.value) & 15) * SLOT + lane * 16;
#pragma unroll
      for (int e = 0; e < 9; ++e) *(LAS v4u*)(a + e * 1024) = w4r[K.value][e];
      CLOB(); SB();
    };
    f32x4 cs[3] = {{0, 0, 0, 0}, {0, 0, 0, 0}, {0, 0, 0, 0}};
    const unsigned cb = (4 * q + (t >> 2)) * 288 + (t & 3) * 8 + ld * 96;
    auto colsum3 = [&](int n0, int n1) INL {
      NOUNROLL for (int n = n0; n < n1; n += 2) {
        const unsigned a0 = cb + (n & 15) * SLOT;
        s16x4 l[6], h[6];
#pragma unroll
        for (int k = 0; k < 6; ++k) { l[k] = trd(a0 + (k / 3) * SLOT + (k % 3) * 32); h[k] = trd(a0 + (k / 3) * SLOT + 4608 + (k % 3) * 32); }
        SB();
#pragma unroll
        for (int k = 0; k < 6; ++k) cs[k % 3] = mfma(cat(l[k], h[k]), ones, cs[k % 3]);
      }
      CLOB();
    };
    if (ab) {
      const unsigned v1 = (8 * (lane >> 5) + ((lane >> 3) & 3)) * (D_ * 2) + (((lane & 7) ^ ((q & 3) << 1)) << 4);
      auto w1units = [&](int u0) INL {
#pragma unroll
        for (int e = 0; e < 8; ++e) {
          const int u = u0 + 3 * (e >> 2);
          dma(w1 + 128 * ct + (32 * u + 16 * (e & 1) + 4 * ((e >> 1) & 1)) * (D_ * 2), v1, u * 4096 + (e & 3) * 1024);
        }
      };
      w1units(ld); w1units(ld + 6);
      VMW(8); lds_set(fullw, 1, lane);
      w1units(ld + 12);
      VMW(8); lds_set(fullw, 2, lane);
      STAMP(1);
      DRAIN(); lds_set(fullw, 3, lane);
      wait_free(2);
      const unsigned v2 = (lane >> 3) * (D_ * 2) + (((lane & 7) ^ ((q & 3) << 1)) << 4);
      NOUNROLL for (int u = ld; u < 18; u += 3) {
#pragma unroll
        for (int e = 0; e < 4; ++e) dma(w2 + 128 * ct + (32 * u + 8 * e) * (D_ * 2), v2, 8 * SLOT + u * 4096 + e * 1024);
      }
      w4get(IC<0>{});
      STAMP(2);
      VMW(9); lds_set(fullw, 4, lane);
      STAMP(3);
      wait_free(3);
      NOUNROLL for (int n = 0; n < 8; ++n) w3slice(n);
      STAMP(4);
      wait_free(4);
      NOUNROLL for (int n = 8; n < 11; ++n) w3slice(n);
      w4get(IC<1>{});
      wait_free(5);
      NOUNROLL for (int n = 11; n < 16; ++n) w3slice(n);
      VMW(15); lds_set(fullw, 6, lane);
      w4get(IC<2>{});
      wait_full(6);
      colsum3(0, 10);
      STAMP(5);
      VMW(9); lds_set(fullw, 7, lane);
      STAMP(6);
      wait_full(7);
      colsum3(10, 16);
    } else {
      __builtin_amdgcn_s_sleep(SL2);
      NOUNROLL for (int n = 0; n < 16; ++n) w3slice(n);
      DRAIN(); lds_set(fullw, 7, lane);
      w4get(IC<0>{}); w4get(IC<1>{}); w4get(IC<2>{});
      wait_full(7);
      colsum3(0, 16);
    }
    lds_set(fullw, 8, lane);
    STAMP(7);
    wait_free(6); wait_full(8);
    STAMP(11);
    w3slice(16); w3slice(17);
    DRAIN(); lds_set(fullw, 9, lane);
    STAMP(12);
    wait_full(9);
    __builtin_amdgcn_s_setprio(1);
    colsum3(16, 18);
    if (t == 0) {
#pragma unroll
      for (int c = 0; c < 3; ++c) *(LAS f32x4*)(L_CS3 + (3 * ld + c) * 64 + q * 16) = cs[c];
    }
    CLOB();
    lds_set(fullw, 10, lane);
    __builtin_amdgcn_s_setprio(0);
    STAMP(8);
    const u64 tag = get_tag();
    auto hround = [&](int p) INL {
      wait_min4(L_RQ, p + 1);
      const int so = OFF_HH + (8 * g * 144 + fr * 18) * 1024;
      if (p < 4) {
        v4u d[8];
#pragma unroll
        for (int k = 0; k < 8; ++k) d[k] = *(const LAS v4u*)(L_HS + k * 1024 + lane * 16);
        CLOB();
        lds_set(L_AGR, p + 1, lane);
#pragma unroll
        for (int k = 0; k < 8; ++k) ST16(d[k], lane * 16 + k * (144 * 1024), so + (pbase(s) + p) * 1024);
      } else {
        v2u d[8];
#pragma unroll
        for (int k = 0; k < 8; ++k) d[k] = *(const LAS v2u*)(L_HS + k * 1024 + lane * 16);
#pragma unroll
        for (int k = 0; k < 8; ++k) ST8(d[k], lane * 16 + (s & 1) * 8 + k * (144 * 1024), so + (s < 2 ? 4 : 13) * 1024);
      }
      DRAIN();
      SIGNAL(FC, p);
    };
    auto pround = [&](int p) INL {
      wait_min4(L_RQ, 5 + p + 1);
      const int so = OFF_P + ((g * 4 + s) * 64 + fr) * PUNIT + p * 1024;
      v4u d[7];
#pragma unroll
      for (int k = 0; k < 7; ++k) d[k] = *(const LAS v4u*)(L_PS + p * 8192 + ((fr + 1 + k) & 7) * 1024 + lane * 16);
#pragma unroll
      for (int k = 0; k < 7; ++k) ST16(d[k], lane * 16, so + ((fr + 1 + k) & 7) * (8 * PUNIT));
      DRAIN();
      SIGNAL(FD, p);
    };
    wait_free(7);
    NOUNROLL for (int m = ld; m < 6; m += 3) w4whole(m, dstep(m, s), IC<1>{});
    w4get(IC<3>{});
    wait_free(8);
    w4put(IC<0>{});
    if (ld == 0) w4put(IC<1>{});
    asm volatile("s_waitcnt vmcnt(0) lgkmcnt(0)" ::: "memory");
    lds_set(fullw, 11, lane);
    STAMP(9);
    if (ld > 0) NOUNROLL for (int p = ld - 1; p < 5; p += 2) { hround(p); STAMP(13 + p); }
    wait_free(9); wait_full(10);
    if (ld > 0) w4put(IC<1>{});
    w4put(IC<2>{});
    if (ld == 0) w4put(IC<3>{});
    lds_set(fullw, 12, lane);
    if (ld > 0) { wait_free(10); w4put(IC<3>{}); }
    lds_set(fullw, 13, lane);
    STAMP(10);
    pround(ld);
    STAMP(23);
  } else {
    const u64 tag = __hip_atomic_load((GAS u64*)(ws + OFF_EPOCH), __ATOMIC_RELAXED, __HIP_MEMORY_SCOPE_AGENT) + 1ull;
    if (lane == 0) {
      *(volatile LAS u64*)L_TAG = tag;
      *(volatile LAS unsigned*)(L_RDY + 4 * R_TAG) = 1u;
      __hip_atomic_store(flagp(ws, FS, b, 0), tag, __ATOMIC_RELAXED, __HIP_MEMORY_SCOPE_AGENT);
    }
    if (ab) {
      const v4u e4 = {EMPTY, EMPTY, EMPTY, EMPTY};
      wait_free(3);
#pragma unroll
      for (int k = 0; k < 8; ++k) ST16(e4, lane * 16 + (k & 1) * 1024 + (k >> 1) * (18 * 1024), OFF_X1 + (4 * rt * 18 + 2 * ct) * 1024);
      if (lane < 32) ST16(e4, OFF_STAT + (lane >> 3) * (9 * 128) + (lane & 7) * 16, (4 * rt * 9 + ct) * 128);
      DRAIN();
      lds_set(L_RDY + 4 * R_Z, 1, lane);
    }
    STAMP(1);
    auto poll = [&](GAS u64* p) INL {
      NOUNROLL for (unsigned n = 0; n < GLB_CAP; ++n) {
        const u64 v = __hip_atomic_load(p, __ATOMIC_RELAXED, __HIP_MEMORY_SCOPE_AGENT);
        if (__all(v == tag)) break;
        __builtin_amdgcn_s_sleep(1);
      }
      CLOB();
    };
    auto poll_bits = [&](auto TWO, GAS u64* p0, GAS u64* p1, int word, u64 ma, u64 mb, u64 mc, u64 md) INL {
      unsigned done = 0;
      NOUNROLL for (unsigned n = 0; n < GLB_CAP; ++n) {
        const u64 v0 = __hip_atomic_load(p0, __ATOMIC_RELAXED, __HIP_MEMORY_SCOPE_AGENT);
        u64 v1 = v0;
        if constexpr (TWO.value) v1 = __hip_atomic_load(p1, __ATOMIC_RELAXED, __HIP_MEMORY_SCOPE_AGENT);
        const u64 bad0 = __ballot(v0 != tag), bad1 = __ballot(v1 != tag);
        const unsigned now = ((bad0 & ma) ? 0u : 1u) | ((bad0 & mb) ? 0u : 2u) | ((bad1 & mc) ? 0u : 4u) | ((bad1 & md) ? 0u : 8u);
        if (now != done) { done = now; lds_set(L_RDY + 4 * word, now, lane); }
        if (now == 15u) break;
        __builtin_amdgcn_s_sleep(1);
      }
      lds_set(L_RDY + 4 * word, 15u, lane);
      CLOB();
    };
    constexpr u64 M01 = 0x3333333333333333ull, M23 = 0xccccccccccccccccull;
    const int pl = (lane % 36) >> 2;
    if (ab) poll_bits(IC<0>{}, flagp(ws, FA, j + 8 * ((r & 1) + 2 * pl), lane & 3), nullptr, R_H, M01, M23, M01, M23);
    STAMP(2);
    poll_bits(IC<1>{}, flagp(ws, FB, g + 16 * pl, lane & 3), flagp(ws, FB, g + 8 + 16 * pl, lane & 3), R_X, M01, M23, M01, M23);
    STAMP(3);
    {
      v4u st[9];
      NOUNROLL for (unsigned n = 0; n < TRY_CAP; ++n) { CLOB();
        unsigned m = 0;
#pragma unroll
        for (int k = 0; k < 9; ++k) st[k] = LD16(OFF_STAT + ((lane >> 3) * 9 + k) * 128 + (lane & 7) * 16, g * (8 * 9 * 128));
#pragma unroll
        for (int k = 0; k < 9; ++k) m = mx4(m, st[k]);
        if (!missing(m)) break;
      }
      f32x4 a = __builtin_bit_cast(f32x4, st[0]);
#pragma unroll
      for (int k = 1; k < 9; ++k) a += __builtin_bit_cast(f32x4, st[k]);
      *(LAS f32x4*)(L_LN + lane * 32) = ln_consts(a.x, a.y);
      *(LAS f32x4*)(L_LN + lane * 32 + 16) = ln_consts(a.z, a.w);
      CLOB();
      lds_set(L_RDY + 4 * R_LN, 1, lane);
    }
    if (b == 0) {
      NOUNROLL for (int k = 0; k < 4; ++k) poll(flagp(ws, FS, k * 64 + lane, 0));
      if (lane == 0) __hip_atomic_store((GAS u64*)(ws + OFF_EPOCH), tag, __ATOMIC_RELAXED, __HIP_MEMORY_SCOPE_AGENT);
    }
    auto poll5 = [&](GAS u64* p0, int word) INL {
      unsigned done = 0;
      NOUNROLL for (unsigned n = 0; n < GLB_CAP; ++n) {
        const u64 bad = __ballot(__hip_atomic_load(p0, __ATOMIC_RELAXED, __HIP_MEMORY_SCOPE_AGENT) != tag);
        unsigned now = 0;
#pragma unroll
        for (int k = 0; k < 5; ++k) now |= (bad & (0x0101010101010101ull << k)) ? 0u : 1u << k;
        if (now != done) { done = now; lds_set(L_RDY + 4 * word, now, lane); }
        if (now == 31u) break;
        __builtin_amdgcn_s_sleep(1);
      }
      lds_set(L_RDY + 4 * word, 31u, lane);
    };
    poll5(flagp(ws, FC, j + 8 * ((r & ~3) + ((lane >> 3) & 3)), lane & 7), R_HH);
    STAMP(4);
    const int wr = lane >> 3;
    poll5(flagp(ws, FD, MAP ? (j & 1) + (wr & 6) + 8 * (((wr & 1) << 4) + (r & 15)) : wr + 8 * r, (lane & 7) < 3 ? (lane & 7) : 3 + (fr >> 1)), R_P);
    STAMP(5);
  }
  if (w < 2 || (w >= 4 && w < 7)) {
    const int p = w < 2 ? w + 3 : w - 4;
    const int T = 8 * g + fr;
    const int u = p < 4 ? pbase(s) + p : (s < 2 ? 4 : 13);
    v4u xv;
    NOUNROLL for (unsigned n = 0; n < TRY_CAP; ++n) { CLOB(); xv = LD16(lane * 16, OFF_X1 + (T * 18 + u) * 1024); if (!missing(mx4(0u, xv))) break; }
    const float rstd = *(const LAS float*)(L_LN + (16 * fr + t) * 16 + 12);
    wait_bit(L_RDY + 4 * R_P, 1u << p);
    STAMP(20);
    const int psrc = OFF_P + ((g * 4 + s) * 8 + fr) * (8 * PUNIT) + p * 1024;
    char* orow = out + (size_t)(16 * T + t) * (D_ * 2) + (32 * u + 4 * q) * 2;
    if (p < 4) {
      v4u pv[8];
#pragma unroll
      for (int k = 1; k < 8; ++k) pv[k] = LD16(lane * 16, psrc + ((fr + k) & 7) * PUNIT);
      pv[0] = *(const LAS v4u*)(L_PS + p * 8192 + fr * 1024 + lane * 16);
      float a[8];
#pragma unroll
      for (int e = 0; e < 8; ++e) a[e] = 0.f;
#pragma unroll
      for (int k = 0; k < 8; ++k)
#pragma unroll
        for (int e = 0; e < 4; ++e) { const unsigned d = pv[k][e]; a[2 * e] += lo16(d); a[2 * e + 1] += hi16(d); }
      unsigned o[4];
#pragma unroll
      for (int e = 0; e < 4; ++e) {
        const unsigned h2 = pk(a[2 * e] * rstd, a[2 * e + 1] * rstd), xe = xv[e];
        o[e] = pk(lo16(xe) + lo16(h2), hi16(xe) + hi16(h2));
      }
      *(GAS v2u*)orow = v2u{o[0], o[1]};
      *(GAS v2u*)(orow + 32) = v2u{o[2], o[3]};
    } else {
      v2u pv[8];
#pragma unroll
      for (int k = 1; k < 8; ++k) pv[k] = LD8(lane * 8, psrc + ((fr + k) & 7) * PUNIT);
      pv[0] = *(const LAS v2u*)(L_PS + 32768 + fr * 512 + lane * 8);
      float a[4] = {0.f, 0.f, 0.f, 0.f};
#pragma unroll
      for (int k = 0; k < 8; ++k) { a[0] += lo16(pv[k].x); a[1] += hi16(pv[k].x); a[2] += lo16(pv[k].y); a[3] += hi16(pv[k].y); }
      const unsigned x0 = (s & 1) ? xv.z : xv.x, x1v = (s & 1) ? xv.w : xv.y;
      const unsigned h0 = pk(a[0] * rstd, a[1] * rstd), h1 = pk(a[2] * rstd, a[3] * rstd);
      *(GAS v2u*)(orow + (s & 1) * 32) = v2u{pk(lo16(x0) + lo16(h0), hi16(x0) + hi16(h0)), pk(lo16(x1v) + lo16(h1), hi16(x1v) + hi16(h1))};
    }
    STAMP(21);
  }
#endif
}
}

extern "C" void kernel_launch(void* const* d_in, const int* in_sizes, int n_in, void* d_out, int out_size, void* d_ws, size_t ws_size,
                              hipStream_t stream) {
  mx::mixer_kernel<<<256, 512, 0, stream>>>((const char*)d_in[0], (const char*)d_in[1], (const char*)d_in[2], (const char*)d_in[3],
                                            (const char*)d_in[4], (char*)d_out, (char*)d_ws);
}
```

```cpp
#include <hip/hip_runtime.h>
#include <utility>

#define STAMPS 0
#define SIGSV 27
#define HS3V 66
#define LATE 0
#define MAP 1
#define SL1 32
#define SL2 64

#define LAS __attribute__((address_space(3)))
#define GAS __attribute__((address_space(1)))
#define DI __device__ __forceinline__
typedef unsigned v4u __attribute__((ext_vector_type(4)));
typedef unsigned v2u __attribute__((ext_vector_type(2)));
typedef float f32x4 __attribute__((ext_vector_type(4)));
typedef float f32x2 __attribute__((ext_vector_type(2)));
typedef short bf16x8 __attribute__((ext_vector_type(8)));
typedef short s16x4 __attribute__((ext_vector_type(4)));
typedef __bf16 bf16x2_t __attribute__((ext_vector_type(2)));
typedef unsigned long long u64;

namespace mx {
constexpr int D_ = 576, F_ = 4608;
constexpr int OFF_EPOCH = 0;
constexpr int OFF_FLAGS = 4096;
constexpr int OFF_STAT = 344064;
constexpr int OFF_H1 = 524288;
constexpr int OFF_X1 = OFF_H1 + 64 * 18 * 1024;
constexpr int OFF_HH = OFF_X1 + 64 * 18 * 1024;
constexpr int PUNIT = 4608;
constexpr int OFF_P = OFF_HH + 64 * 144 * 1024;
constexpr int OFF_DBG = OFF_P + 8 * 4 * 8 * 8 * PUNIT;
constexpr int WS_BYTES = OFF_DBG + 2 * 256 * 8 * 32 * 8 + 256;
enum { FS = 0, FA = 1, FB = 2, FC = 3, FD = 4 };
constexpr int SLOT = 9216, MISC = 16 * SLOT;
constexpr int L_FULL = MISC, L_FREE = MISC + 16, L_RQ = MISC + 32, L_RDY = MISC + 48  , L_SY = MISC + 96, L_AGR = MISC + 112, L_TAG = MISC + 120;
constexpr int L_DUM = MISC + 128  , L_CS1 = MISC + 512  , L_CS3 = MISC + 1024  , L_LN = MISC + 2048  ,
              L_HS = MISC + 4096  , L_LN1 = L_HS  , L_DBG = MISC + 12288, L_PS = 4 * SLOT  ;
enum { R_H = 0, R_X = 1, R_HH = 2, R_P = 3, R_TAG = 4, R_LN = 5, R_Z = 6 };
constexpr unsigned EMPTY = 0xffffffffu;
constexpr unsigned TRY_CAP = 1u << 12;
constexpr unsigned LDS_CAP = 1u << 21, GLB_CAP = 1u << 16;
#define DCAPV 6
constexpr int PD = 4, DCAP = DCAPV;

template <class F, int... I> DI void sfor_impl(F&& f, std::integer_sequence<int, I...>) { (f(std::integral_constant<int, I>{}), ...); }
template <int N, class F> DI void sfor(F&& f) { sfor_impl(f, std::make_integer_sequence<int, N>{}); }
template <int V> using IC = std::integral_constant<int, V>;
#define INL __attribute__((always_inline))
#define NOUNROLL _Pragma("nounroll")
#define RARE(x) __builtin_expect(!!(x), 0)
#define SB() __builtin_amdgcn_sched_barrier(0)
#define CLOB() asm volatile("" ::: "memory")
#define DRAIN() asm volatile("s_waitcnt vmcnt(0)" ::: "memory")
#define VMW(n) asm volatile("s_waitcnt vmcnt(%0)" ::"i"(n) : "memory")

DI unsigned rfl(unsigned v) { return __builtin_amdgcn_readfirstlane(v); }
DI f32x4 mfma(bf16x8 a, bf16x8 b, f32x4 c) { return __builtin_amdgcn_mfma_f32_16x16x32_bf16(a, b, c, 0, 0, 0); }
template <bool FIRST> DI void mfa(f32x4& c, bf16x8 a, bf16x8 b) {
  if constexpr (FIRST) asm volatile("v_mfma_f32_16x16x32_bf16 %0, %1, %2, 0" : "=&v"(c) : "v"(a), "v"(b));
  else asm volatile("v_mfma_f32_16x16x32_bf16 %0, %1, %2, %0" : "+v"(c) : "v"(a), "v"(b));
}
#define MFMA_DONE() asm volatile("s_nop 7\n\ts_nop 3" ::: "memory")
DI bf16x8 asfrag(v4u v) { return __builtin_bit_cast(bf16x8, v); }
DI s16x4 trd(unsigned addr) { return __builtin_amdgcn_ds_read_tr16_b64_v4i16((LAS s16x4*)addr); }
DI bf16x8 cat(s16x4 lo, s16x4 hi) { return __builtin_shufflevector(lo, hi, 0, 1, 2, 3, 4, 5, 6, 7); }
DI unsigned pk(float a, float b) { bf16x2_t r = __builtin_convertvector(f32x2{a, b}, bf16x2_t); return __builtin_bit_cast(unsigned, r); }
DI unsigned pkc(float a, float b) { return min(pk(a, b), EMPTY - 1u); }
DI unsigned mx2(unsigned m, unsigned a, unsigned b) { return max(max(m, a), b); }
DI unsigned mx4(unsigned m, v4u d) { return mx2(mx2(m, d.x, d.y), d.z, d.w); }
DI bool missing(unsigned m) { return __any(m == EMPTY); }
DI v4u asv4(bf16x8 f) { return __builtin_bit_cast(v4u, f); }
DI float lo16(unsigned p) { return __uint_as_float(p << 16); }
DI float hi16(unsigned p) { return __uint_as_float(p & 0xffff0000u); }
DI float bfr(float a) { return lo16(pk(a, a)); }
DI float qsum(float v) {
  auto r = __builtin_amdgcn_permlane16_swap(__float_as_uint(v), __float_as_uint(v), false, false);
  float a = __uint_as_float(r[0]) + __uint_as_float(r[1]);
  auto r2 = __builtin_amdgcn_permlane32_swap(__float_as_uint(a), __float_as_uint(a), false, false);
  return __uint_as_float(r2[0]) + __uint_as_float(r2[1]);
}
constexpr float GA = -2.302208198144325f;
constexpr float GB = -0.1029432397357593f;
DI f32x4 ln_consts(float s, float ss) {
  const float m = s * (1.0f / D_);
  float var = __builtin_fmaf(-m, m, ss * (1.0f / D_));
  var = var > 0.f ? var : 0.f;
  const float sd = bfr(__builtin_amdgcn_sqrtf(bfr(bfr(var) + 1e-5f)));
  const float r = __builtin_amdgcn_rcpf(sd);
  return f32x4{-bfr(m), GA * r, GB * r * r * r, r};
}
template <int N> DI void geluN(f32x2 (&z)[N], const float (&a)[N], const float (&b)[N]) {
  f32x2 p[N];
#pragma unroll
  for (int k = 0; k < N; ++k) p[k] = z[k] * z[k];
  SB();
#pragma unroll
  for (int k = 0; k < N; ++k) p[k] = p[k] * b[k] + a[k];
  SB();
#pragma unroll
  for (int k = 0; k < N; ++k) p[k] = z[k] * p[k];
  SB();
#pragma unroll
  for (int k = 0; k < N; ++k) { p[k].x = __builtin_amdgcn_exp2f(p[k].x); p[k].y = __builtin_amdgcn_exp2f(p[k].y); }
  SB();
#pragma unroll
  for (int k = 0; k < N; ++k) p[k] = p[k] + 1.0f;
  SB();
#pragma unroll
  for (int k = 0; k < N; ++k) { p[k].x = __builtin_amdgcn_rcpf(p[k].x); p[k].y = __builtin_amdgcn_rcpf(p[k].y); }
  SB();
#pragma unroll
  for (int k = 0; k < N; ++k) z[k] = z[k] * p[k];
  SB();
}

DI unsigned ldsw(int addr) { return *(volatile LAS unsigned*)addr; }
DI v4u lds4(int addr) { return *(volatile LAS v4u*)addr; }
DI void lds_set(int addr, unsigned v, int lane) { if (lane == 0) *(volatile LAS unsigned*)addr = v; }
DI v4u peek4(int addr) { CLOB(); return *(const LAS v4u*)addr; }
DI unsigned peek(int addr) { CLOB(); return *(const LAS unsigned*)addr; }
DI void post(unsigned addr, unsigned v) { CLOB(); *(LAS unsigned*)addr = v; CLOB(); }
DI void wait_word(int addr, unsigned v) {
  NOUNROLL for (unsigned n = 0; n < LDS_CAP; ++n) { if (rfl(ldsw(addr)) >= v) break; __builtin_amdgcn_s_sleep(1); }
  CLOB();
}
DI unsigned min3(v4u x) { return rfl(min(min(x.x, x.y), x.z)); }
DI unsigned min4(v4u x) { return rfl(min(min(x.x, x.y), min(x.z, x.w))); }
DI void wait_full(unsigned v) {
  NOUNROLL for (unsigned n = 0; n < LDS_CAP; ++n) { if (min3(lds4(L_FULL)) >= v) break; __builtin_amdgcn_s_sleep(1); }
  CLOB();
}
DI void wait_min4(int addr, unsigned v) {
  NOUNROLL for (unsigned n = 0; n < LDS_CAP; ++n) { if (min4(lds4(addr)) >= v) break; __builtin_amdgcn_s_sleep(1); }
  CLOB();
}
DI void wait_free(unsigned v) { wait_min4(L_FREE, v); }
DI void wait_bit(int addr, unsigned bit) {
  NOUNROLL for (unsigned n = 0; n < LDS_CAP; ++n) { if ((rfl(ldsw(addr)) & bit) == bit) break; __builtin_amdgcn_s_sleep(1); }
  CLOB();
}
DI GAS u64* flagp(char* ws, int kind, int b, int wv) { return (GAS u64*)(ws + OFF_FLAGS + (kind * 256 + b) * 128 + wv * 8); }
DI u64 get_tag() {
  wait_word(L_RDY + 4 * R_TAG, 1);
  return ((u64)rfl(ldsw(L_TAG + 4)) << 32) | rfl(ldsw(L_TAG));
}
#define LD16(off, soff) __builtin_amdgcn_raw_buffer_load_b128(rs, (off), (soff), 16)
#define LD8(off, soff) __builtin_amdgcn_raw_buffer_load_b64(rs, (off), (soff), 16)
#define ST16(v, off, soff) do { v4u d_ = (v); asm volatile("" : "+v"(d_)); __builtin_amdgcn_raw_buffer_store_b128(d_, rs, (off), (soff), 16); asm volatile("s_nop 1" ::"v"(d_) : "memory"); } while (0)
#define ST8(v, off, soff) __builtin_amdgcn_raw_buffer_store_b64((v), rs, (off), (soff), 16)
#define SIGNAL(kind, wv) do { CLOB(); ST8((v2u{(unsigned)tag, (unsigned)(tag >> 32)}), lane ? 0x7ffffff0 : 0, OFF_FLAGS + ((kind) * 256 + b) * 128 + (wv) * 8); CLOB(); } while (0)
DI void dma(const char* sbase, unsigned voff, unsigned ldsaddr) {
  asm volatile("s_mov_b32 m0, %0\n\ts_nop 0\n\tglobal_load_lds_dwordx4 %1, %2" ::"s"(ldsaddr), "v"(voff), "s"(sbase) : "memory");
}
DI void dmac(const char* sbase, unsigned voff, unsigned ldsaddr) { dma(sbase, voff, ldsaddr); VMW(DCAP); }
DI int pbase(int s) { return (9 * s + (s & 1)) >> 1; }
DI int dstep(int M, int s) {
  if (M < 4) return pbase(s) + M;
  else if (M < 16) return pbase((s + 1 + (M - 4) % 3) & 3) + (M - 4) / 3;
  else return M == 16 ? 4 : 13;
}
#define STAMP(k) do { } while (0)
#define COUNT(k) do { } while (0)

__global__ __launch_bounds__(512, 2) void mixer_kernel(const char* __restrict__ x, const char* __restrict__ w1, const char* __restrict__ w2,
                                                       const char* __restrict__ w3, const char* __restrict__ w4, char* __restrict__ out, char* ws) {
#if defined(__HIP_DEVICE_COMPILE__)
  __shared__ __attribute__((aligned(16))) char smem[163840];
  const int tid = threadIdx.x, lane = tid & 63, w = rfl(tid >> 6);
  const int t = lane & 15, q = lane >> 4;
  const int b = blockIdx.x, j = b & 7, r = b >> 3;
  const bool ab = r < 18;
  const int rt = 2 * j + (r & 1), ct = r >> 1;
  const int g = MAP ? 4 * (j & 1) + ((r >> 2) & 3) : r >> 2, fr = MAP ? (j & 6) + (r >> 4) : j, s = r & 3;
  const __amdgpu_buffer_rsrc_t rs = __builtin_amdgcn_make_buffer_rsrc(ws, 0, WS_BYTES, 0x00020000);
  const bf16x8 ones = {0x3F80, 0x3F80, 0x3F80, 0x3F80, 0x3F80, 0x3F80, 0x3F80, 0x3F80};
  asm volatile("" ::"v"(smem));
  STAMP(0);
  if (tid < 32) *(volatile LAS unsigned*)(MISC + tid * 4) = 0u;
  asm volatile("s_waitcnt lgkmcnt(0)\n\ts_barrier" ::: "memory");
  if (ab) {
    if (w < 4) {
      const char* xsrc = x + (size_t)(4 * rt + w) * (16 * D_ * 2);
#pragma unroll
      for (int e = 0; e < 18; ++e) {
        const unsigned f = (((unsigned)(lane + 64 * e) * 911u) >> 17) & 7u;
        dma(xsrc + e * 1024, (lane * 16) ^ (f << 4), (8 + 2 * w) * SLOT + e * 1024);
      }
    }
  }

  if (w < 4) {
    const int i = w;
    unsigned pa = lane ? L_DUM + 4 * lane : L_FREE + 4 * i;
    asm volatile("" : "+v"(pa));
    const u64 tag = get_tag();
    constexpr int P_FREE = 0, P_RQ = L_RQ - L_FREE, P_SY = L_SY - L_FREE;
    if (ab) {
      unsigned ab4[4];
#pragma unroll
      for (int c = 0; c < 4; ++c) ab4[c] = (4 * q + (t >> 2)) * 128 + ((c ^ ((2 * q + (t >> 3)) & 3)) << 5) + (t & 3) * 8;
      const int T = 4 * rt + i;
      v2u xres[4];
      {
        const int tp = i >> 1, ch = i & 1;
        const unsigned xa = (8 + 2 * i) * SLOT + t * (D_ * 2), xs = ((t >> 1) & 7) << 4;
        unsigned xe[2][2], wb[2][2];
#pragma unroll
        for (int e = 0; e < 2; ++e) {
          xe[0][e] = xa + ((64 * (e ^ tp) + q * 16) ^ xs);
          xe[1][e] = xe[0][e] + (ch ? -2 * SLOT : 2 * SLOT);
          wb[e][0] = (ch ? ab4[2] : ab4[0]) + (e ^ tp) * 4096;
          wb[e][1] = (ch ? ab4[3] : ab4[1]) + (e ^ tp) * 4096;
          asm volatile("" : "+v"(xe[0][e]), "+v"(xe[1][e]), "+v"(wb[e][0]), "+v"(wb[e][1]));
        }
        int dst[4];
        unsigned keep[4], dm[4];
#pragma unroll
        for (int k = 0; k < 4; ++k) {
          const int gt = 4 * ct + 2 * ch + (k & 1), grp = gt / 9;
          const int sh = grp == 0 ? 1 : grp == 1 ? -1 : grp == 2 ? 2 : -2;
          const int ts = 16 * (ch ^ (k >> 1)) + t + sh;
          keep[k] = (ts >= 0 && ts < 32) ? 0xffffffffu : 0u;
          dst[k] = OFF_H1 + ((T & ~1) + ((ts >> 4) & 1)) * (18 * 1024) + ((ts & 15) + 16 * q) * 16 + (k & 1) * 8;
          dm[k] = (q == (t >> 2) && k == (t & 3)) ? 0xffffffffu : 0u;
          asm volatile("" : "+v"(dst[k]), "+v"(keep[k]), "+v"(dm[k]));
        }
        DRAIN();
        post(pa + P_FREE, 1);
        wait_word(L_FREE + 4 * (i ^ 1), 1);
        STAMP(1);
        bf16x8 xf[2][18];
        f32x4 aS = {0, 0, 0, 0}, aG = {0, 0, 0, 0};
#pragma unroll
        for (int p = 0; p < 6; ++p) xf[0][p] = *(const LAS bf16x8*)(xe[0][p & 1] + (p >> 1) * 128);
        SB();
        sfor<18>([&](auto P) INL {
          constexpr int p = P.value;
          aS = mfma(ones, xf[0][p], aS); SB();
          if constexpr (p + 6 < 18) xf[0][p + 6] = *(const LAS bf16x8*)(xe[0][p & 1] + ((p + 6) >> 1) * 128);
          else if constexpr (p + 6 < 22) xres[p - 12] = *(const LAS v2u*)(xa + 128 * ct + ((32 * (p - 12) + 8 * q) ^ xs));
          SB();
          aG = mfma(xf[0][p], xf[0][p], aG); SB();
          xf[1][p] = *(const LAS bf16x8*)(xe[1][p & 1] + (p >> 1) * 128);
          SB();
        });
        CLOB();
        post(pa + P_FREE, 2);
        f32x4 lc[2];
        {
          const float gd = __uint_as_float(((__float_as_uint(aG.x) & dm[0]) | (__float_as_uint(aG.y) & dm[1])) | ((__float_as_uint(aG.z) & dm[2]) | (__float_as_uint(aG.w) & dm[3])));
          lc[0] = ln_consts(aS[0], qsum(gd));
          if (q == 0) *(LAS f32x4*)(L_LN1 + i * 256 + t * 16) = lc[0];
          CLOB();
          post(pa + P_SY, 1);
        }
        SB();
        STAMP(17);
        wait_full(1);
        constexpr int PA = 4;
        s16x4 wl[PA], wh[PA];
#define AADDR(k) (wb[((k) >> 1) & 1][(k) & 1] + ((k) >> 2) * 8192)
#pragma unroll
        for (int k = 0; k < PA; ++k) { wl[k] = trd(AADDR(k)); wh[k] = trd(AADDR(k) + 2048); }
        bf16x8 one2 = ones;
        asm volatile("" : "+v"(one2));
        SB();
        f32x4 acc[2][2], cs[2];
        v4u fv;
        sfor<36>([&](auto K) INL {
          constexpr int k = K.value, p = k >> 1, l = k & 1, nx = k - 1 + PA;
          const bf16x8 cur = cat(wl[k % PA], wh[k % PA]);
          mfa<(p == 0)>(acc[0][l], cur, xf[0][p]); SB();
          if constexpr (nx == 12 || nx == 24) { if (RARE(min3(fv) < nx / 12 + 1)) wait_full(nx / 12 + 1); CLOB(); SB(); }
          if constexpr (k >= 1 && nx < 36) wl[nx % PA] = trd(AADDR(nx));
          SB();
          mfa<(p == 0)>(acc[1][l], cur, xf[1][p]); SB();
          if constexpr (k >= 1 && nx < 36) wh[nx % PA] = trd(AADDR(nx) + 2048);
          if constexpr (nx == 9 || nx == 21) fv = peek4(L_FULL);
          SB();
          if constexpr ((p & 1) == 0) { mfa<(p == 0)>(cs[l], cur, one2); SB(); }
        });
        MFMA_DONE();
        if (t == 0) { *(LAS f32x4*)(L_CS1 + i * 128 + q * 16) = cs[0]; *(LAS f32x4*)(L_CS1 + i * 128 + 64 + q * 16) = cs[1]; }
        CLOB();
        post(pa + P_SY, 2);
        STAMP(2);
        wait_word(L_SY + 4 * (i ^ 1), 1);
        lc[1] = *(const LAS f32x4*)(L_LN1 + (i ^ 1) * 256 + t * 16);
        wait_word(L_SY + 4 * (i ^ 2), 2);
        const f32x4 o0 = *(const LAS f32x4*)(L_CS1 + (i ^ 2) * 128 + q * 16), o1 = *(const LAS f32x4*)(L_CS1 + (i ^ 2) * 128 + 64 + q * 16);
        cs[0] += o0; cs[1] += o1;
        f32x2 z[8];
        float ga8[8], gb8[8];
#pragma unroll
        for (int k = 0; k < 4; ++k) {
          const f32x4 zz = cs[k & 1] * lc[k >> 1].x + acc[k >> 1][k & 1];
          z[2 * k] = f32x2{zz.x, zz.y}; z[2 * k + 1] = f32x2{zz.z, zz.w};
          ga8[2 * k] = ga8[2 * k + 1] = lc[k >> 1].y; gb8[2 * k] = gb8[2 * k + 1] = lc[k >> 1].z;
        }
        SB();
        geluN<8>(z, ga8, gb8);
        unsigned hp[8];
#pragma unroll
        for (int k = 0; k < 8; ++k) { const f32x2 v = z[k] * lc[k >> 2].w; hp[k] = pk(v.x, v.y) & keep[k >> 1]; }
        const int up = 2 * ct + ch;
        if (RARE(up == 4 || up == 13)) {
#pragma unroll
          for (int k = 0; k < 4; ++k) ST8((v2u{hp[2 * k], hp[2 * k + 1]}), dst[k], up * 1024);
        } else {
#pragma unroll
          for (int tt = 0; tt < 2; ++tt) ST16((v4u{hp[4 * tt], hp[4 * tt + 1], hp[4 * tt + 2], hp[4 * tt + 3]}), dst[2 * tt], up * 1024);
        }
        STAMP(3);
        DRAIN();
        SIGNAL(FA, i);
        post(pa + P_FREE, 3);
        STAMP(4);
      }
      {
        wait_full(4);
#pragma unroll
        for (int c = 0; c < 4; ++c) { ab4[c] += 8 * SLOT; asm volatile("" : "+v"(ab4[c])); }
        constexpr int PB = 32;
        s16x4 wl[PB], wh[PB];
#pragma unroll
        for (int k = 0; k < PB; ++k) { wl[k] = trd(ab4[k & 3] + (k >> 2) * 4096); wh[k] = trd(ab4[k & 3] + (k >> 2) * 4096 + 2048); }
        SB();
        f32x2 xr[8];
#pragma unroll
        for (int l = 0; l < 4; ++l) { xr[2 * l] = f32x2{lo16(xres[l].x), hi16(xres[l].x)}; xr[2 * l + 1] = f32x2{lo16(xres[l].y), hi16(xres[l].y)}; }
#pragma unroll
        for (int k = 0; k < 8; ++k) asm volatile("" : "+v"(xr[k]));
        wait_bit(L_RDY + 4 * R_H, 1u << (i >> 1));
        STAMP(5);
        bf16x8 hf[18];
        const int hsrc = OFF_H1 + T * (18 * 1024);
#pragma unroll
        for (int u = 0; u < 9; ++u) hf[u] = asfrag(LD16(lane * 16 + u * 1024, hsrc));
        SB();
        f32x4 acc[4];
        unsigned zv;
        sfor<72>([&](auto K) INL {
          constexpr int k = K.value, u = k >> 2;
          mfa<(k < 4)>(acc[k & 3], cat(wl[k % PB], wh[k % PB]), hf[u]); SB();
          if constexpr (k == 17) post(pa + P_FREE, 4);
          if constexpr (k == 60) zv = peek(L_RDY + 4 * R_Z);
          if constexpr ((k & 1) == 0 && k / 2 + 9 < 18) hf[k / 2 + 9] = asfrag(LD16(lane * 16 + (k / 2 + 9) * 1024, hsrc));
          if constexpr (k + PB < 72) { wl[k % PB] = trd(ab4[k & 3] + ((k + PB) >> 2) * 4096); wh[k % PB] = trd(ab4[k & 3] + ((k + PB) >> 2) * 4096 + 2048); }
          SB();
        });
        MFMA_DONE();
        STAMP(6);
        unsigned xp[8];
#pragma unroll
        for (int l = 0; l < 4; ++l) {
          const f32x2 v0 = xr[2 * l] + f32x2{acc[l].x, acc[l].y}, v1 = xr[2 * l + 1] + f32x2{acc[l].z, acc[l].w};
          xp[2 * l] = pkc(v0.x, v0.y); xp[2 * l + 1] = pkc(v1.x, v1.y);
        }
        if (RARE(rfl(zv) == 0)) wait_word(L_RDY + 4 * R_Z, 1);
        CLOB();
        const int dst = OFF_X1 + (T * 18 + 2 * ct) * 1024;
        ST16((v4u{xp[0], xp[1], xp[2], xp[3]}), lane * 16, dst);
        ST16((v4u{xp[4], xp[5], xp[6], xp[7]}), lane * 16 + 1024, dst);
        SB();
        float sm = 0.f, sq = 0.f;
#pragma unroll
        for (int k = 0; k < 8; ++k) {
          const bf16x2_t v = __builtin_bit_cast(bf16x2_t, xp[k]);
          sm = __builtin_amdgcn_fdot2_f32_bf16(v, bf16x2_t{(__bf16)1.0f, (__bf16)1.0f}, sm, false);
          sq = __builtin_amdgcn_fdot2_f32_bf16(v, v, sq, false);
        }
        sm = qsum(sm); sq = qsum(sq);
        ST8((v2u{min(__float_as_uint(sm), EMPTY - 1u), min(__float_as_uint(sq), EMPTY - 1u)}), q ? 0x7ffffff0 : t * 8, OFF_STAT + (T * 9 + ct) * 128);
        SIGNAL(FB, i);
        post(pa + P_FREE, 5);
        STAMP(7);
      }
    } else {
      const int lt = ((r - 18) * 4 + i) * 64 + lane;
      unsigned sink = 0;
      auto touch = [&](const char* base, unsigned off) INL { asm volatile("global_load_dword %0, %1, %2\n\tglobal_load_dword %0, %1, %2 offset:64" : "+v"(sink) : "v"(off), "s"(base) : "memory"); };
      __builtin_amdgcn_s_sleep(SL1);
#pragma unroll
      for (int k = 0; k < 2; ++k) touch(w2, min(lt + 3584 * k, 5183) * 128);
      DRAIN();
      if (MAP) {
#pragma unroll
        for (int k = 0; k < 2; ++k) { const int l = min(lt + 3584 * k, 5183); touch(w3 + (j & 6) * (576 * 2), (l / 9) * (F_ * 2) + (l % 9) * 128); }
        DRAIN();
      }
      __builtin_amdgcn_s_sleep(SL2);
#pragma unroll
      for (int k = 0; k < (MAP ? 3 : 2); ++k) touch(w4 + (size_t)(MAP ? (j & 6) : j) * (576 * D_ * 2), min(lt + 3584 * k, MAP ? 10367 : 5183) * 128);
      DRAIN();
      asm volatile("" ::"v"(sink));
    }
    unsigned b0 = (4 * q + (t >> 2)) * 288 + (t & 3) * 8, b1 = b0 + 6 * SLOT, b2 = b0 + 12 * SLOT;
    asm volatile("" : "+v"(b0), "+v"(b1), "+v"(b2));
#define WADDR(z, c) ((((z) % 16) < 6 ? b0 : ((z) % 16) < 12 ? b1 : b2) + (((z) % 16) % 6) * SLOT + (c) * 32)
#define FN(i_) ((i_) < 108 ? (i_) / 9 : 12 + ((i_) - 108) % 6)
#define FC(i_) ((i_) < 108 ? (i_) % 9 : ((i_) - 108) / 6)
    const int T0 = 8 * g + 2 * i;
    const int hdst = OFF_HH + (T0 * 144 + fr * 18) * 1024;
    bf16x8 hb[18][2];
    auto hload = [&](auto K) INL {
      constexpr int m = K.value >> 1, tt = K.value & 1;
      hb[m][tt] = asfrag(LD16(lane * 16 + tt * (144 * 1024), hdst + dstep(m, s) * 1024));
    };
    s16x4 lo[PD], hi[PD];
    unsigned rv, hw = L_HS + 2 * i * 1024 + lane * 16;
    asm volatile("" : "+v"(hw));
    {
      wait_full(6);
#pragma unroll
      for (int k = 0; k < PD; ++k) { lo[k] = trd(WADDR(0, k)); hi[k] = trd(WADDR(0, k) + 4608); }
      f32x4 acc[2][9];
      const int xsrc = OFF_X1 + T0 * (18 * 1024);
      int xo[2] = {lane * 16, lane * 16 + 18 * 1024};
      asm volatile("" : "+v"(xo[0]), "+v"(xo[1]));
      bf16x8 xb[12][2];
      auto xload = [&](auto K) INL {
        constexpr int n = K.value >> 1, tt = K.value & 1;
        xb[n % 12][tt] = asfrag(LD16(xo[tt] + (n & 3) * 1024, xsrc + (n >> 2) * 4096));
      };
      SB();
      auto xfix = [&](auto K0, auto K1) INL {
        NOUNROLL for (unsigned n = 0; n < TRY_CAP; ++n) { CLOB();
          unsigned m = 0;
          COUNT(1);
          sfor<K1.value - K0.value>([&](auto K) INL { xload(IC<K0.value + K.value>{}); });
          sfor<K1.value - K0.value>([&](auto K) INL { constexpr int kk = K0.value + K.value; m = mx4(m, asv4(xb[(kk >> 1) % 12][kk & 1])); });
          if (!missing(m)) break;
        }
      };
      wait_bit(L_RDY + 4 * R_X, 1u << i);
      STAMP(9);
      sfor<10>([&](auto K) INL { xload(K); });
      SB();
      unsigned chk = mx4(mx4(0u, asv4(xb[0][0])), asv4(xb[0][1])), chk2 = 0;
      if (RARE(missing(chk))) xfix(IC<0>{}, IC<2>{});
      SB();
      f32x4 lc[2], csv;
      unsigned hp[2][2][2];
      unsigned la = L_LN + (32 * i + t) * 16, ca = L_CS3 + q * 16;
      asm volatile("" : "+v"(la), "+v"(ca));
      v4u fv;
      u64 bad;
      unsigned sv, hv;
      auto epi = [&](auto Cc) INL {
        constexpr int c = Cc.value;
        f32x2 z[4];
#pragma unroll
        for (int tt = 0; tt < 2; ++tt) {
          const f32x4 zz = csv * lc[tt].x + acc[tt][c];
          z[2 * tt] = f32x2{zz.x, zz.y}; z[2 * tt + 1] = f32x2{zz.z, zz.w};
        }
        if constexpr (c == 3 || c == 5) sv = rfl(rv);
        SB();
        const float ga4[4] = {lc[0].y, lc[0].y, lc[1].y, lc[1].y}, gb4[4] = {lc[0].z, lc[0].z, lc[1].z, lc[1].z};
        geluN<4>(z, ga4, gb4);
#pragma unroll
        for (int tt = 0; tt < 2; ++tt) {
          { hp[tt][c & 1][0] = pk(z[2 * tt].x, z[2 * tt].y); hp[tt][c & 1][1] = pk(z[2 * tt + 1].x, z[2 * tt + 1].y); }
        }
        asm volatile("" : "+v"(hp[0][c & 1][0]), "+v"(hp[0][c & 1][1]), "+v"(hp[1][c & 1][0]), "+v"(hp[1][c & 1][1]));
        if constexpr (c == 3 || c == 5) { if (RARE(sv < c / 2)) wait_word(L_AGR, c / 2); CLOB(); }
        if constexpr ((c & 1) && c < 8) {
#pragma unroll
          for (int tt = 0; tt < 2; ++tt) {
            const v4u v = {hp[tt][0][0], hp[tt][0][1], hp[tt][1][0], hp[tt][1][1]};
            hb[c >> 1][tt] = asfrag(v);
            if constexpr (c < 7) *(LAS v4u*)(hw + tt * 1024) = v;
            else ST16(v, lane * 16 + tt * (144 * 1024), hdst + (pbase(s) + 3) * 1024);
          }
        }
        if constexpr (c == 8) {
#pragma unroll
          for (int tt = 0; tt < 2; ++tt)
            ST8((v2u{hp[tt][0][0], hp[tt][0][1]}), lane * 16 + tt * (144 * 1024) + (s & 1) * 8, hdst + (s < 2 ? 4 : 13) * 1024);
        }
        if constexpr ((c & 1) && c < 7) post(pa + P_RQ, c / 2 + 1);
        SB();
      };
      sfor<162>([&](auto I) INL {
        constexpr int ii = I.value, n = FN(ii), c = FC(ii), nx = ii - 1 + PD;
        const bf16x8 cur = cat(lo[ii % PD], hi[ii % PD]);
        mfa<(n == 0)>(acc[0][c], cur, xb[n % 12][0]); SB();
        constexpr bool ck1 = ii < 99 && (c == 5 || c == 6);
        constexpr bool ck2 = ii >= 72 && ii < 99 && c >= 1 && c <= 4;
        constexpr int k2 = 24 + 4 * (n - 8) + c - 1;
        if constexpr (ii >= 1 && nx < 162) lo[nx % PD] = trd(WADDR(FN(nx), FC(nx)));
        if constexpr (ck1) { chk = mx2(c == 5 ? 0u : chk, asv4(xb[n + 1][c - 5]).x, asv4(xb[n + 1][c - 5]).y); asm volatile("" : "+v"(chk)); }
        if constexpr (ck2) { chk2 = mx2(chk2, asv4(xb[(k2 >> 1) % 12][k2 & 1]).x, asv4(xb[(k2 >> 1) % 12][k2 & 1]).y); asm volatile("" : "+v"(chk2)); }
        if constexpr (ii < 99 && c == 7) { bad = __ballot(chk == EMPTY); asm volatile("" : "+s"(bad)); }
        if constexpr (ii == 99) { bad = __ballot(chk2 == EMPTY); asm volatile("" : "+s"(bad)); }
        if constexpr (ii == 63 || ii == 101) { sv = min3(fv); asm volatile("" : "+s"(sv)); }
        if constexpr (ii == 103) { sv = rfl(rv); asm volatile("" : "+s"(sv)); }
        SB();
        mfa<(n == 0)>(acc[1][c], cur, xb[n % 12][1]); SB();
        if constexpr (ii >= 1 && nx < 162) hi[nx % PD] = trd(WADDR(FN(nx), FC(nx)) + 4608);
        if constexpr (ck1) { chk = mx2(chk, asv4(xb[n + 1][c - 5]).z, asv4(xb[n + 1][c - 5]).w); asm volatile("" : "+v"(chk)); }
        if constexpr (ck2) { chk2 = mx2(chk2, asv4(xb[(k2 >> 1) % 12][k2 & 1]).z, asv4(xb[(k2 >> 1) % 12][k2 & 1]).w); asm volatile("" : "+v"(chk2)); }
        SB();
        if constexpr (ii < 99 && c == 8) { if (RARE(bad != 0)) xfix(IC<2 * n + 2>{}, IC<2 * n + 4>{}); SB(); }
        if constexpr (ii == 100) { if (RARE(bad != 0)) xfix(IC<24>{}, IC<36>{}); SB(); }
        if constexpr (ii < 28 && ii % 2 == 0) { xload(IC<10 + ii / 2>{}); SB(); }
        if constexpr (ii >= 30 && ii < 66 && ii % 3 == 0) { xload(IC<14 + ii / 3>{}); SB(); }
        if constexpr (ii == 18 || ii == 81 || ii == 108 || ii == 159) { post(pa + P_FREE, ii == 18 ? 6 : ii == 81 ? 7 : ii == 108 ? 8 : 9); SB(); }
        if constexpr (ii == 8) STAMP(23);
        if constexpr (ii == 35) STAMP(24);
        if constexpr (ii == 71) STAMP(25);
        if constexpr (ii == 58 || ii == 96) { fv = peek4(L_FULL); SB(); }
        if constexpr (ii == 64) { if (RARE(sv < 7)) wait_full(7); CLOB(); SB(); }
        if constexpr (ii == 97) { rv = peek(L_RDY + 4 * R_LN); SB(); }
        if constexpr (ii == 102) { if (RARE(sv < 10)) wait_full(10); CLOB(); SB(); }
        if constexpr (ii == 104) {
          if (RARE(sv == 0)) wait_word(L_RDY + 4 * R_LN, 1);
          CLOB();
          lc[0] = *(const LAS f32x4*)la; lc[1] = *(const LAS f32x4*)(la + 256);
          SB(); STAMP(10);
        }
        if constexpr (ii >= 108 && n == 14 && c >= 1) { csv = *(const LAS f32x4*)(ca + (c - 1) * 64); SB(); }
        if constexpr (ii >= 108 && n == 15 && (c == 4 || c == 6)) { rv = peek(L_AGR); SB(); }
        if constexpr (ii >= 108 && n == 17 && c >= 1) epi(IC<c - 1>{});
        if constexpr (ii == 150) { fv = peek4(L_FULL); hv = peek(L_RDY + 4 * R_HH); SB(); }
        if constexpr (ii == 155) { if (RARE(!(rfl(hv) & 1u))) wait_bit(L_RDY + 4 * R_HH, 1u); CLOB(); SB(); STAMP(27); }
        if constexpr (ii >= 155 && ii < 161) { hload(IC<8 + ii - 155>{}); SB(); }
      });
      csv = *(const LAS f32x4*)(ca + 8 * 64);
      if (RARE(min3(fv) < 11)) wait_full(11);
      CLOB();
#pragma unroll
      for (int k = 0; k < PD; ++k) { lo[k] = trd(WADDR(18, k)); hi[k] = trd(WADDR(18, k) + 4608); }
      SB();
      epi(IC<8>{});
      STAMP(11);
    }
    {
      f32x4 acc[2][9];
      unsigned pw = L_PS + 2 * i * 1024 + lane * 16, pw8 = L_PS + 32768 + 2 * i * 512 + lane * 8;
      asm volatile("" : "+v"(pw), "+v"(pw8));
      int po[2];
#pragma unroll
      for (int tt = 0; tt < 2; ++tt) { po[tt] = (2 * i + tt == fr) ? 0x7ff00000 : lane * 8; asm volatile("" : "+v"(po[tt])); }
      const int pdst = OFF_P + (((g * 4 + s) * 8 + 2 * i) * 8 + fr) * PUNIT;
      v4u fv;
      unsigned sv;
      auto epi = [&](auto Cc) INL {
        constexpr int c = Cc.value;
        if constexpr ((c & 1) && c < 8) {
#pragma unroll
          for (int tt = 0; tt < 2; ++tt) {
            const f32x4 a0 = acc[tt][c - 1], a1 = acc[tt][c];
            const v4u v = {pk(a0.x, a0.y), pk(a0.z, a0.w), pk(a1.x, a1.y), pk(a1.z, a1.w)};
            if constexpr (c == 7) ST16(v, 2 * po[tt] , pdst + tt * (8 * PUNIT) + 3 * 1024);
            *(LAS v4u*)(pw + tt * 1024 + (c >> 1) * 8192) = v;
          }
        }
        if constexpr (c == 8) {
#pragma unroll
          for (int tt = 0; tt < 2; ++tt) {
            const f32x4 a0 = acc[tt][8];
            const v2u v = {pk(a0.x, a0.y), pk(a0.z, a0.w)};
            ST8(v, po[tt], pdst + tt * (8 * PUNIT) + 4096);
            *(LAS v2u*)(pw8 + tt * 512) = v;
          }
        }
        if constexpr ((c & 1) && c < 7) post(pa + P_RQ, 3 + c / 2 + 1);
        SB();
      };
      constexpr int HS1 = 8, SIGS = SIGSV, HS2 = 28, HS3 = HS3V;
      sfor<162>([&](auto I) INL {
        constexpr int ii = I.value, n = FN(ii), c = FC(ii), nx = ii - 1 + PD;
        constexpr int G = ii == HS1 ? 1 : ii == HS2 ? 2 : ii == HS3 ? 3 : -1;
        const bf16x8 cur = cat(lo[ii % PD], hi[ii % PD]);
        mfa<(n == 0)>(acc[0][c], cur, hb[n][0]); SB();
        if constexpr (ii >= 1 && nx < 162) lo[nx % PD] = trd(WADDR(18 + FN(nx), FC(nx)));
        if constexpr (G >= 0) { sv = rfl(rv); asm volatile("" : "+s"(sv)); }
        if constexpr (ii == 84 || ii == 102) { sv = min3(fv); asm volatile("" : "+s"(sv)); }
        if constexpr (ii == 106) { sv = min4(fv); asm volatile("" : "+s"(sv)); }
        SB();
        mfa<(n == 0)>(acc[1][c], cur, hb[n][1]); SB();
        if constexpr (ii >= 1 && nx < 162) hi[nx % PD] = trd(WADDR(18 + FN(nx), FC(nx)) + 4608);
        SB();
        if constexpr (ii == SIGS) { VMW(6); SIGNAL(FC, 3 + i); SB(); }
        if constexpr (ii + 4 == HS1 || ii + 4 == HS2 || ii + 4 == HS3) { rv = peek(L_RDY + 4 * R_HH); SB(); }
        if constexpr (G >= 0) {
          const unsigned need = G < 3 ? 1u << G : 8u << i;
          if (RARE((sv & need) != need)) wait_bit(L_RDY + 4 * R_HH, need);
          CLOB(); SB(); STAMP(G == 1 ? 28 : G == 2 ? 13 : 14);
        }
        if constexpr (ii >= HS1 && ii < HS1 + 6) { hload(IC<14 + ii - HS1>{}); SB(); }
        if constexpr (ii >= HS2 && ii < HS2 + 6) { hload(IC<20 + ii - HS2>{}); SB(); }
        if constexpr (ii >= HS3 && ii < HS3 + 10) { hload(IC<26 + ii - HS3>{}); SB(); }
        if constexpr (ii == 18 || ii == 54) { post(pa + P_FREE, ii == 18 ? 10 : 11); SB(); }
        if constexpr (ii == 79 || ii == 97) { fv = peek4(L_FULL); SB(); }
        if constexpr (ii == 84) { if (RARE(sv < 12)) wait_full(12); CLOB(); SB(); }
        if constexpr (ii == 102) { if (RARE(sv < 13)) wait_full(13); fv = peek4(L_FREE); SB(); STAMP(26); }
        if constexpr (ii == 106) { if (RARE(sv < 11)) wait_free(11); CLOB(); SB(); }
        if constexpr (ii >= 108 && n == 17 && c >= 1) epi(IC<c - 1>{});
      });
      MFMA_DONE();
      epi(IC<8>{});
      STAMP(15);
      asm volatile("s_waitcnt vmcnt(0) lgkmcnt(0)" ::: "memory");
      SIGNAL(FD, 3 + i);
      STAMP(16);
    }
  } else if (w < 7) {
    const int ld = w - 4;
    const unsigned fullw = L_FULL + 4 * ld;
    unsigned v3[3], v4a[9];
#pragma unroll
    for (int e = 0; e < 3; ++e) {
      const int idx = (3 * ld + e) * 64 + lane, row = idx / 18, ch = idx % 18;
      const int sc = (ch + 2 * (s & 1)) % 18;
      v3[e] = row * (F_ * 2) + sc * 16;
    }
#pragma unroll
    for (int e = 0; e < 9; ++e) { const int idx = e * 64 + lane; v4a[e] = (idx / 18) * (D_ * 2) + ((idx % 18 + 2 * (s & 1)) % 18) * 16; }
    const char* w3b = w3 + (576 * fr + 144 * s) * 2;
    const char* w4b = w4 + (size_t)(576 * fr) * (D_ * 2) + 144 * s * 2;
    auto w3slice = [&](int n) INL {
#pragma unroll
      for (int e = 0; e < 3; ++e) dma(w3b + (size_t)n * (32 * F_ * 2), v3[e], (n & 15) * SLOT + (3 * ld + e) * 1024);
    };
    auto w4whole = [&](int m, int u, auto CAP) INL {
      const char* src = w4b + u * (32 * D_ * 2);
#pragma unroll
      for (int e = 0; e < 9; ++e) { dma(src, v4a[e], ((18 + m) & 15) * SLOT + e * 1024); if constexpr (CAP.value) VMW(DCAP); }
    };
    const __amdgpu_buffer_rsrc_t r4 = __builtin_amdgcn_make_buffer_rsrc((void*)w4b, 0, 576 * D_ * 2 - 144 * s * 2, 0x00020000);
    v4u w4r[4][9];
    auto w4get = [&](auto K) INL {
      const int so = dstep(6 + ld + 3 * K.value, s) * (32 * D_ * 2);
#pragma unroll
      for (int e = 0; e < 9; ++e) { constexpr int eo[9] = {0, 2, 4, 6, 8, 1, 3, 5, 7}; w4r[K.value][eo[e]] = __builtin_amdgcn_raw_buffer_load_b128(r4, v4a[eo[e]], so, 0); }
      CLOB(); SB();
    };
    auto w4put = [&](auto K) INL {
      const unsigned a = ((24 + ld + 3 * K.value) & 15) * SLOT + lane * 16;
#pragma unroll
      for (int e = 0; e < 9; ++e) *(LAS v4u*)(a + e * 1024) = w4r[K.value][e];
      CLOB(); SB();
    };
    f32x4 cs[3] = {{0, 0, 0, 0}, {0, 0, 0, 0}, {0, 0, 0, 0}};
    const unsigned cb = (4 * q + (t >> 2)) * 288 + (t & 3) * 8 + ld * 96;
    auto colsum3 = [&](int n0, int n1) INL {
      __builtin_amdgcn_s_setprio(1);
      NOUNROLL for (int n = n0; n < n1; n += 2) {
        const unsigned a0 = cb + (n & 15) * SLOT;
        s16x4 l[6], h[6];
#pragma unroll
        for (int k = 0; k < 6; ++k) { l[k] = trd(a0 + (k / 3) * SLOT + (k % 3) * 32); h[k] = trd(a0 + (k / 3) * SLOT + 4608 + (k % 3) * 32); }
        SB();
#pragma unroll
        for (int k = 0; k < 6; ++k) cs[k % 3] = mfma(cat(l[k], h[k]), ones, cs[k % 3]);
      }
      __builtin_amdgcn_s_setprio(0);
      CLOB();
    };
    if (ab) {
      const unsigned v1 = (8 * (lane >> 5) + ((lane >> 3) & 3)) * (D_ * 2) + (((lane & 7) ^ ((q & 3) << 1)) << 4);
      auto w1units = [&](int u0) INL {
#pragma unroll
        for (int e = 0; e < 8; ++e) {
          const int u = u0 + 3 * (e >> 2);
          dma(w1 + 128 * ct + (32 * u + 16 * (e & 1) + 4 * ((e >> 1) & 1)) * (D_ * 2), v1, u * 4096 + (e & 3) * 1024);
        }
      };
      w1units(ld); w1units(ld + 6);
      VMW(8); lds_set(fullw, 1, lane);
      w1units(ld + 12);
      VMW(8); lds_set(fullw, 2, lane);
      STAMP(1);
      DRAIN(); lds_set(fullw, 3, lane);
      wait_free(2);
      const unsigned v2 = (lane >> 3) * (D_ * 2) + (((lane & 7) ^ ((q & 3) << 1)) << 4);
      NOUNROLL for (int u = ld; u < 18; u += 3) {
#pragma unroll
        for (int e = 0; e < 4; ++e) dma(w2 + 128 * ct + (32 * u + 8 * e) * (D_ * 2), v2, 8 * SLOT + u * 4096 + e * 1024);
      }
      w4get(IC<0>{});
      STAMP(2);
      VMW(9); lds_set(fullw, 4, lane);
      STAMP(3);
      wait_free(3);
      NOUNROLL for (int n = 0; n < 8; ++n) w3slice(n);
      STAMP(4);
      DRAIN(); lds_set(fullw, 6, lane);
      wait_full(6);
      colsum3(0, 8);
      wait_free(4);
      NOUNROLL for (int n = 8; n < 11; ++n) w3slice(n);
      wait_free(5);
      NOUNROLL for (int n = 11; n < 16; ++n) w3slice(n);
      w4get(IC<1>{});
      STAMP(5);
      VMW(9); lds_set(fullw, 7, lane);
      STAMP(6);
      wait_full(7);
      colsum3(8, 16);
    } else {
      __builtin_amdgcn_s_sleep(SL2);
      NOUNROLL for (int n = 0; n < 16; ++n) w3slice(n);
      DRAIN(); lds_set(fullw, 7, lane);
      w4get(IC<0>{}); w4get(IC<1>{});
      wait_full(7);
      colsum3(0, 16);
    }
    lds_set(fullw, 8, lane);
    STAMP(7);
    wait_free(6); wait_full(8);
    STAMP(11);
    w3slice(16); w3slice(17);
    DRAIN(); lds_set(fullw, 9, lane);
    STAMP(12);
    wait_full(9);
    colsum3(16, 18);
    if (t == 0) {
#pragma unroll
      for (int c = 0; c < 3; ++c) *(LAS f32x4*)(L_CS3 + (3 * ld + c) * 64 + q * 16) = cs[c];
    }
    CLOB();
    lds_set(fullw, 10, lane);
    STAMP(8);
    const u64 tag = get_tag();
    auto hround = [&](int p) INL {
      wait_min4(L_RQ, p + 1);
      v4u d[8];
#pragma unroll
      for (int k = 0; k < 8; ++k) d[k] = *(const LAS v4u*)(L_HS + k * 1024 + lane * 16);
      CLOB();
      lds_set(L_AGR, p + 1, lane);
#pragma unroll
      for (int k = 0; k < 8; ++k) ST16(d[k], lane * 16 + k * (144 * 1024), OFF_HH + (8 * g * 144 + fr * 18 + pbase(s) + p) * 1024);
      CLOB();
    };
    auto pround = [&](int p) INL {
      wait_min4(L_RQ, 3 + p + 1);
      const int so = OFF_P + ((g * 4 + s) * 64 + fr) * PUNIT + p * 1024;
      v4u d[7];
#pragma unroll
      for (int k = 0; k < 7; ++k) d[k] = *(const LAS v4u*)(L_PS + p * 8192 + ((fr + 1 + k) & 7) * 1024 + lane * 16);
#pragma unroll
      for (int k = 0; k < 7; ++k) ST16(d[k], lane * 16, so + ((fr + 1 + k) & 7) * (8 * PUNIT));
      DRAIN();
      SIGNAL(FD, p);
    };
    wait_free(7);
    NOUNROLL for (int m = ld; m < 6; m += 3) w4whole(m, dstep(m, s), IC<0>{});
    if (!LATE || ld > 0) { w4get(IC<2>{}); w4get(IC<3>{}); }
    if (ld > 0) { wait_free(8); w4put(IC<0>{}); DRAIN(); lds_set(fullw, 11, lane); }
    hround(ld);
    DRAIN();
    SIGNAL(FC, ld);
    if (LATE && ld == 0) { w4get(IC<2>{}); w4get(IC<3>{}); }
    if (ld == 0) { w4put(IC<0>{}); w4put(IC<1>{}); }
    lds_set(fullw, 11, lane);
    STAMP(13);
    wait_free(9); wait_full(10);
    if (ld > 0) w4put(IC<1>{});
    w4put(IC<2>{});
    if (ld == 0) w4put(IC<3>{});
    lds_set(fullw, 12, lane);
    if (ld > 0) { wait_free(10); w4put(IC<3>{}); }
    lds_set(fullw, 13, lane);
    STAMP(10);
    pround(ld);
    STAMP(23);
  } else {
    const u64 tag = __hip_atomic_load((GAS u64*)(ws + OFF_EPOCH), __ATOMIC_RELAXED, __HIP_MEMORY_SCOPE_AGENT) + 1ull;
    if (lane == 0) {
      *(volatile LAS u64*)L_TAG = tag;
      *(volatile LAS unsigned*)(L_RDY + 4 * R_TAG) = 1u;
      __hip_atomic_store(flagp(ws, FS, b, 0), tag, __ATOMIC_RELAXED, __HIP_MEMORY_SCOPE_AGENT);
    }
    {
      const v4u e4 = {EMPTY, EMPTY, EMPTY, EMPTY};
      if (ab) {
        wait_free(3);
#pragma unroll
        for (int k = 0; k < 8; ++k) ST16(e4, lane * 16 + (k & 1) * 1024 + (k >> 1) * (18 * 1024), OFF_X1 + (4 * rt * 18 + 2 * ct) * 1024);
        if (lane < 32) ST16(e4, OFF_STAT + (lane >> 3) * (9 * 128) + (lane & 7) * 16, (4 * rt * 9 + ct) * 128);
        DRAIN();
        lds_set(L_RDY + 4 * R_Z, 1, lane);
      }
    }
    STAMP(1);
    auto poll = [&](GAS u64* p) INL {
      NOUNROLL for (unsigned n = 0; n < GLB_CAP; ++n) {
        const u64 v = __hip_atomic_load(p, __ATOMIC_RELAXED, __HIP_MEMORY_SCOPE_AGENT);
        if (__all(v == tag)) break;
        __builtin_amdgcn_s_sleep(1);
      }
      CLOB();
    };
    auto poll_bits = [&](auto TWO, GAS u64* p0, GAS u64* p1, int word, u64 ma, u64 mb, u64 mc, u64 md) INL {
      unsigned done = 0;
      NOUNROLL for (unsigned n = 0; n < GLB_CAP; ++n) {
        const u64 v0 = __hip_atomic_load(p0, __ATOMIC_RELAXED, __HIP_MEMORY_SCOPE_AGENT);
        u64 v1 = v0;
        if constexpr (TWO.value) v1 = __hip_atomic_load(p1, __ATOMIC_RELAXED, __HIP_MEMORY_SCOPE_AGENT);
        const u64 bad0 = __ballot(v0 != tag), bad1 = __ballot(v1 != tag);
        const unsigned now = ((bad0 & ma) ? 0u : 1u) | ((bad0 & mb) ? 0u : 2u) | ((bad1 & mc) ? 0u : 4u) | ((bad1 & md) ? 0u : 8u);
        if (now != done) { done = now; lds_set(L_RDY + 4 * word, now, lane); }
        if (now == 15u) break;
        __builtin_amdgcn_s_sleep(1);
      }
      lds_set(L_RDY + 4 * word, 15u, lane);
      CLOB();
    };
    constexpr u64 M01 = 0x3333333333333333ull, M23 = 0xccccccccccccccccull;
    const int pl = (lane % 36) >> 2;
    if (ab) poll_bits(IC<0>{}, flagp(ws, FA, j + 8 * ((r & 1) + 2 * pl), lane & 3), nullptr, R_H, M01, M23, M01, M23);
    STAMP(2);
    poll_bits(IC<1>{}, flagp(ws, FB, g + 16 * pl, lane & 3), flagp(ws, FB, g + 8 + 16 * pl, lane & 3), R_X, M01, M23, M01, M23);
    STAMP(3);
    {
      v4u st[9];
      NOUNROLL for (unsigned n = 0; n < TRY_CAP; ++n) { CLOB();
        unsigned m = 0;
#pragma unroll
        for (int k = 0; k < 9; ++k) st[k] = LD16(OFF_STAT + ((lane >> 3) * 9 + k) * 128 + (lane & 7) * 16, g * (8 * 9 * 128));
#pragma unroll
        for (int k = 0; k < 9; ++k) m = mx4(m, st[k]);
        if (!missing(m)) break;
      }
      f32x4 a = __builtin_bit_cast(f32x4, st[0]);
#pragma unroll
      for (int k = 1; k < 9; ++k) a += __builtin_bit_cast(f32x4, st[k]);
      *(LAS f32x4*)(L_LN + lane * 32) = ln_consts(a.x, a.y);
      *(LAS f32x4*)(L_LN + lane * 32 + 16) = ln_consts(a.z, a.w);
      CLOB();
      lds_set(L_RDY + 4 * R_LN, 1, lane);
    }
    if (b == 0) {
      NOUNROLL for (int k = 0; k < 4; ++k) poll(flagp(ws, FS, k * 64 + lane, 0));
      if (lane == 0) __hip_atomic_store((GAS u64*)(ws + OFF_EPOCH), tag, __ATOMIC_RELAXED, __HIP_MEMORY_SCOPE_AGENT);
    }
    auto poll5 = [&](GAS u64* p0, int word, auto NB) INL {
      constexpr unsigned all = (1u << NB.value) - 1u;
      unsigned done = 0;
      NOUNROLL for (unsigned n = 0; n < GLB_CAP; ++n) {
        const u64 bad = __ballot(__hip_atomic_load(p0, __ATOMIC_RELAXED, __HIP_MEMORY_SCOPE_AGENT) != tag);
        unsigned now = 0;
#pragma unroll
        for (int k = 0; k < NB.value; ++k) now |= (bad & (0x0101010101010101ull << k)) ? 0u : 1u << k;
        if (now != done) { done = now; lds_set(L_RDY + 4 * word, now, lane); }
        if (now == all) break;
        __builtin_amdgcn_s_sleep(1);
      }
      lds_set(L_RDY + 4 * word, all, lane);
    };
    poll5(flagp(ws, FC, j + 8 * ((r & ~3) + ((lane >> 3) & 3)), lane & 7), R_HH, IC<7>{});
    STAMP(4);
    const int wr = lane >> 3;
    poll5(flagp(ws, FD, MAP ? (j & 1) + (wr & 6) + 8 * (((wr & 1) << 4) + (r & 15)) : wr + 8 * r, (lane & 7) < 3 ? (lane & 7) : 3 + (fr >> 1)), R_P, IC<5>{});
    STAMP(5);
  }
  if (w < 2 || (w >= 4 && w < 7)) {
    const int p = w < 2 ? w + 3 : w - 4;
    const int T = 8 * g + fr;
    const int u = p < 4 ? pbase(s) + p : (s < 2 ? 4 : 13);
    v4u xv;
    NOUNROLL for (unsigned n = 0; n < TRY_CAP; ++n) { CLOB(); xv = LD16(lane * 16, OFF_X1 + (T * 18 + u) * 1024); if (!missing(mx4(0u, xv))) break; }
    const float rstd = *(const LAS float*)(L_LN + (16 * fr + t) * 16 + 12);
    wait_bit(L_RDY + 4 * R_P, 1u << p);
    STAMP(20);
    const int psrc = OFF_P + ((g * 4 + s) * 8 + fr) * (8 * PUNIT) + p * 1024;
    char* orow = out + (size_t)(16 * T + t) * (D_ * 2) + (32 * u + 4 * q) * 2;
    if (p < 4) {
      v4u pv[8];
#pragma unroll
      for (int k = 1; k < 8; ++k) pv[k] = LD16(lane * 16, psrc + ((fr + k) & 7) * PUNIT);
      pv[0] = *(const LAS v4u*)(L_PS + p * 8192 + fr * 1024 + lane * 16);
      float a[8];
#pragma unroll
      for (int e = 0; e < 8; ++e) a[e] = 0.f;
#pragma unroll
      for (int k = 0; k < 8; ++k)
#pragma unroll
        for (int e = 0; e < 4; ++e) { const unsigned d = pv[k][e]; a[2 * e] += lo16(d); a[2 * e + 1] += hi16(d); }
      unsigned o[4];
#pragma unroll
      for (int e = 0; e < 4; ++e) {
        const unsigned h2 = pk(a[2 * e] * rstd, a[2 * e + 1] * rstd), xe = xv[e];
        o[e] = pk(lo16(xe) + lo16(h2), hi16(xe) + hi16(h2));
      }
      *(GAS v2u*)orow = v2u{o[0], o[1]};
      *(GAS v2u*)(orow + 32) = v2u{o[2], o[3]};
    } else {
      v2u pv[8];
#pragma unroll
      for (int k = 1; k < 8; ++k) pv[k] = LD8(lane * 8, psrc + ((fr + k) & 7) * PUNIT);
      pv[0] = *(const LAS v2u*)(L_PS + 32768 + fr * 512 + lane * 8);
      float a[4] = {0.f, 0.f, 0.f, 0.f};
#pragma unroll
      for (int k = 0; k < 8; ++k) { a[0] += lo16(pv[k].x); a[1] += hi16(pv[k].x); a[2] += lo16(pv[k].y); a[3] += hi16(pv[k].y); }
      const unsigned x0 = (s & 1) ? xv.z : xv.x, x1v = (s & 1) ? xv.w : xv.y;
      const unsigned h0 = pk(a[0] * rstd, a[1] * rstd), h1 = pk(a[2] * rstd, a[3] * rstd);
      *(GAS v2u*)(orow + (s & 1) * 32) = v2u{pk(lo16(x0) + lo16(h0), hi16(x0) + hi16(h0)), pk(lo16(x1v) + lo16(h1), hi16(x1v) + hi16(h1))};
    }
    STAMP(21);
  }
#endif
}
}

extern "C" void kernel_launch(void* const* d_in, const int* in_sizes, int n_in, void* d_out, int out_size, void* d_ws, size_t ws_size,
                              hipStream_t stream) {
  mx::mixer_kernel<<<256, 512, 0, stream>>>((const char*)d_in[0], (const char*)d_in[1], (const char*)d_in[2], (const char*)d_in[3],
                                            (const char*)d_in[4], (char*)d_out, (char*)d_ws);
}
```

```cpp
#include <hip/hip_runtime.h>
#include <utility>

#define STAMPS 0
#define HS0V 10
#define HS1V 36
#define HS2V 62
#define SIGSV 30
#define HS3V 90
#define LATE 0
#define PRIO 1
#define MAP 0
#define SL1 32
#define SL2 64

#define LAS __attribute__((address_space(3)))
#define GAS __attribute__((address_space(1)))
#define DI __device__ __forceinline__
typedef unsigned v4u __attribute__((ext_vector_type(4)));
typedef unsigned v2u __attribute__((ext_vector_type(2)));
typedef float f32x4 __attribute__((ext_vector_type(4)));
typedef float f32x2 __attribute__((ext_vector_type(2)));
typedef short bf16x8 __attribute__((ext_vector_type(8)));
typedef short s16x4 __attribute__((ext_vector_type(4)));
typedef __bf16 bf16x2_t __attribute__((ext_vector_type(2)));
typedef unsigned long long u64;

namespace mx {
constexpr int D_ = 576, F_ = 4608;
constexpr int OFF_EPOCH = 0;
constexpr int OFF_FLAGS = 4096;
constexpr int OFF_STAT = 344064;
constexpr int OFF_H1 = 524288;
constexpr int OFF_X1 = OFF_H1 + 64 * 18 * 1024;
constexpr int OFF_HH = OFF_X1 + 64 * 18 * 1024;
constexpr int PUNIT = 4608;
constexpr int OFF_P = OFF_HH + 64 * 144 * 1024;
constexpr int OFF_DBG = OFF_P + 8 * 4 * 8 * 8 * PUNIT;
constexpr int WS_BYTES = OFF_DBG + 2 * 256 * 8 * 32 * 8 + 256;
enum { FS = 0, FA = 1, FB = 2, FC = 3, FD = 4 };
constexpr int SLOT = 9216, MISC = 16 * SLOT;
constexpr int L_FULL = MISC, L_FREE = MISC + 16, L_RQ = MISC + 32, L_RDY = MISC + 48  , L_SY = MISC + 96, L_AGR = MISC + 112, L_TAG = MISC + 120;
constexpr int L_DUM = MISC + 128  , L_CS1 = MISC + 512  , L_CS3 = MISC + 1024  , L_LN = MISC + 2048  ,
              L_HS = MISC + 4096  , L_LN1 = L_HS  , L_DBG = MISC + 12288, L_PS = 4 * SLOT  ;
enum { R_H = 0, R_X = 1, R_HH = 2, R_P = 3, R_TAG = 4, R_LN = 5, R_Z = 6 };
constexpr unsigned EMPTY = 0xffffffffu;
constexpr unsigned TRY_CAP = 1u << 12;
constexpr unsigned LDS_CAP = 1u << 21, GLB_CAP = 1u << 16;
#define DCAPV 6
constexpr int PD = 4, DCAP = DCAPV;

template <class F, int... I> DI void sfor_impl(F&& f, std::integer_sequence<int, I...>) { (f(std::integral_constant<int, I>{}), ...); }
template <int N, class F> DI void sfor(F&& f) { sfor_impl(f, std::make_integer_sequence<int, N>{}); }
template <int V> using IC = std::integral_constant<int, V>;
#define INL __attribute__((always_inline))
#define NOUNROLL _Pragma("nounroll")
#define RARE(x) __builtin_expect(!!(x), 0)
#define SB() __builtin_amdgcn_sched_barrier(0)
#define CLOB() asm volatile("" ::: "memory")
#define DRAIN() asm volatile("s_waitcnt vmcnt(0)" ::: "memory")
#define VMW(n) asm volatile("s_waitcnt vmcnt(%0)" ::"i"(n) : "memory")

DI unsigned rfl(unsigned v) { return __builtin_amdgcn_readfirstlane(v); }
DI f32x4 mfma(bf16x8 a, bf16x8 b, f32x4 c) { return __builtin_amdgcn_mfma_f32_16x16x32_bf16(a, b, c, 0, 0, 0); }
template <bool FIRST> DI void mfa(f32x4& c, bf16x8 a, bf16x8 b) {
  if constexpr (FIRST) asm volatile("v_mfma_f32_16x16x32_bf16 %0, %1, %2, 0" : "=&v"(c) : "v"(a), "v"(b));
  else asm volatile("v_mfma_f32_16x16x32_bf16 %0, %1, %2, %0" : "+v"(c) : "v"(a), "v"(b));
}
#define MFMA_DONE() asm volatile("s_nop 7\n\ts_nop 3" ::: "memory")
DI bf16x8 asfrag(v4u v) { return __builtin_bit_cast(bf16x8, v); }
DI s16x4 trd(unsigned addr) { return __builtin_amdgcn_ds_read_tr16_b64_v4i16((LAS s16x4*)addr); }
DI bf16x8 cat(s16x4 lo, s16x4 hi) { return __builtin_shufflevector(lo, hi, 0, 1, 2, 3, 4, 5, 6, 7); }
DI unsigned pk(float a, float b) { bf16x2_t r = __builtin_convertvector(f32x2{a, b}, bf16x2_t); return __builtin_bit_cast(unsigned, r); }
DI unsigned pkc(float a, float b) { return min(pk(a, b), EMPTY - 1u); }
DI unsigned mx2(unsigned m, unsigned a, unsigned b) { return max(max(m, a), b); }
DI unsigned mx4(unsigned m, v4u d) { return mx2(mx2(m, d.x, d.y), d.z, d.w); }
DI bool missing(unsigned m) { return __any(m == EMPTY); }
DI v4u asv4(bf16x8 f) { return __builtin_bit_cast(v4u, f); }
DI float lo16(unsigned p) { return __uint_as_float(p << 16); }
DI float hi16(unsigned p) { return __uint_as_float(p & 0xffff0000u); }
DI float bfr(float a) { return lo16(pk(a, a)); }
DI float qsum(float v) {
  auto r = __builtin_amdgcn_permlane16_swap(__float_as_uint(v), __float_as_uint(v), false, false);
  float a = __uint_as_float(r[0]) + __uint_as_float(r[1]);
  auto r2 = __builtin_amdgcn_permlane32_swap(__float_as_uint(a), __float_as_uint(a), false, false);
  return __uint_as_float(r2[0]) + __uint_as_float(r2[1]);
}
constexpr float GA = -2.302208198144325f;
constexpr float GB = -0.1029432397357593f;
DI f32x4 ln_consts(float s, float ss) {
  const float m = s * (1.0f / D_);
  float var = __builtin_fmaf(-m, m, ss * (1.0f / D_));
  var = var > 0.f ? var : 0.f;
  const float sd = bfr(__builtin_amdgcn_sqrtf(bfr(bfr(var) + 1e-5f)));
  const float r = __builtin_amdgcn_rcpf(sd);
  return f32x4{-bfr(m), GA * r, GB * r * r * r, r};
}
template <int N> DI void geluN(f32x2 (&z)[N], const float (&a)[N], const float (&b)[N]) {
  f32x2 p[N];
#pragma unroll
  for (int k = 0; k < N; ++k) p[k] = z[k] * z[k];
  SB();
#pragma unroll
  for (int k = 0; k < N; ++k) p[k] = p[k] * b[k] + a[k];
  SB();
#pragma unroll
  for (int k = 0; k < N; ++k) p[k] = z[k] * p[k];
  SB();
#pragma unroll
  for (int k = 0; k < N; ++k) { p[k].x = __builtin_amdgcn_exp2f(p[k].x); p[k].y = __builtin_amdgcn_exp2f(p[k].y); }
  SB();
#pragma unroll
  for (int k = 0; k < N; ++k) p[k] = p[k] + 1.0f;
  SB();
#pragma unroll
  for (int k = 0; k < N; ++k) { p[k].x = __builtin_amdgcn_rcpf(p[k].x); p[k].y = __builtin_amdgcn_rcpf(p[k].y); }
  SB();
#pragma unroll
  for (int k = 0; k < N; ++k) z[k] = z[k] * p[k];
  SB();
}

DI unsigned ldsw(int addr) { return *(volatile LAS unsigned*)addr; }
DI v4u lds4(int addr) { return *(volatile LAS v4u*)addr; }
DI void lds_set(int addr, unsigned v, int lane) { if (lane == 0) *(volatile LAS unsigned*)addr = v; }
DI v4u peek4(int addr) { CLOB(); return *(const LAS v4u*)addr; }
DI unsigned peek(int addr) { CLOB(); return *(const LAS unsigned*)addr; }
DI void post(unsigned addr, unsigned v) { CLOB(); *(LAS unsigned*)addr = v; CLOB(); }
DI void wait_word(int addr, unsigned v) {
  NOUNROLL for (unsigned n = 0; n < LDS_CAP; ++n) { if (rfl(ldsw(addr)) >= v) break; __builtin_amdgcn_s_sleep(1); }
  CLOB();
}
DI unsigned min3(v4u x) { return rfl(min(min(x.x, x.y), x.z)); }
DI unsigned min4(v4u x) { return rfl(min(min(x.x, x.y), min(x.z, x.w))); }
DI void wait_full(unsigned v) {
  NOUNROLL for (unsigned n = 0; n < LDS_CAP; ++n) { if (min3(lds4(L_FULL)) >= v) break; __builtin_amdgcn_s_sleep(1); }
  CLOB();
}
DI void wait_min4(int addr, unsigned v) {
  NOUNROLL for (unsigned n = 0; n < LDS_CAP; ++n) { if (min4(lds4(addr)) >= v) break; __builtin_amdgcn_s_sleep(1); }
  CLOB();
}
DI void wait_free(unsigned v) { wait_min4(L_FREE, v); }
DI void wait_bit(int addr, unsigned bit) {
  NOUNROLL for (unsigned n = 0; n < LDS_CAP; ++n) { if ((rfl(ldsw(addr)) & bit) == bit) break; __builtin_amdgcn_s_sleep(1); }
  CLOB();
}
DI GAS u64* flagp(char* ws, int kind, int b, int wv) { return (GAS u64*)(ws + OFF_FLAGS + (kind * 256 + b) * 128 + wv * 8); }
DI u64 get_tag() {
  wait_word(L_RDY + 4 * R_TAG, 1);
  return ((u64)rfl(ldsw(L_TAG + 4)) << 32) | rfl(ldsw(L_TAG));
}
#define LD16(off, soff) __builtin_amdgcn_raw_buffer_load_b128(rs, (off), (soff), 16)
#define LD8(off, soff) __builtin_amdgcn_raw_buffer_load_b64(rs, (off), (soff), 16)
#define ST16(v, off, soff) do { v4u d_ = (v); asm volatile("" : "+v"(d_)); __builtin_amdgcn_raw_buffer_store_b128(d_, rs, (off), (soff), 16); asm volatile("s_nop 1" ::"v"(d_) : "memory"); } while (0)
#define ST8(v, off, soff) __builtin_amdgcn_raw_buffer_store_b64((v), rs, (off), (soff), 16)
#define SIGNAL(kind, wv) do { CLOB(); ST8((v2u{(unsigned)tag, (unsigned)(tag >> 32)}), lane ? 0x7ffffff0 : 0, OFF_FLAGS + ((kind) * 256 + b) * 128 + (wv) * 8); CLOB(); } while (0)
DI void dma(const char* sbase, unsigned voff, unsigned ldsaddr) {
  asm volatile("s_mov_b32 m0, %0\n\ts_nop 0\n\tglobal_load_lds_dwordx4 %1, %2" ::"s"(ldsaddr), "v"(voff), "s"(sbase) : "memory");
}
DI void dmac(const char* sbase, unsigned voff, unsigned ldsaddr) { dma(sbase, voff, ldsaddr); VMW(DCAP); }
DI int pbase(int s) { return (9 * s + (s & 1)) >> 1; }
DI int dstep(int M, int s) {
  if (M < 4) return pbase(s) + M;
  else if (M < 16) return pbase((s + 1 + (M - 4) % 3) & 3) + (M - 4) / 3;
  else return M == 16 ? 4 : 13;
}
#define STAMP(k) do { } while (0)
#define COUNT(k) do { } while (0)

__global__ __launch_bounds__(512, 2) void mixer_kernel(const char* __restrict__ x, const char* __restrict__ w1, const char* __restrict__ w2,
                                                       const char* __restrict__ w3, const char* __restrict__ w4, char* __restrict__ out, char* ws) {
#if defined(__HIP_DEVICE_COMPILE__)
  __shared__ __attribute__((aligned(16))) char smem[163840];
  const int tid = threadIdx.x, lane = tid & 63, w = rfl(tid >> 6);
  const int t = lane & 15, q = lane >> 4;
  const int b = blockIdx.x, j = b & 7, r = b >> 3;
  const bool ab = r < 18;
  const int rt = 2 * j + (r & 1), ct = r >> 1;
  const int g = MAP ? 4 * (j & 1) + ((r >> 2) & 3) : r >> 2, fr = MAP ? (j & 6) + (r >> 4) : j, s = r & 3;
  const __amdgpu_buffer_rsrc_t rs = __builtin_amdgcn_make_buffer_rsrc(ws, 0, WS_BYTES, 0x00020000);
  const bf16x8 ones = {0x3F80, 0x3F80, 0x3F80, 0x3F80, 0x3F80, 0x3F80, 0x3F80, 0x3F80};
  asm volatile("" ::"v"(smem));
  STAMP(0);
  if (tid < 32) *(volatile LAS unsigned*)(MISC + tid * 4) = 0u;
  asm volatile("s_waitcnt lgkmcnt(0)\n\ts_barrier" ::: "memory");
  if (ab) {
    if (w < 4) {
      const char* xsrc = x + (size_t)(4 * rt + w) * (16 * D_ * 2);
#pragma unroll
      for (int e = 0; e < 18; ++e) {
        const unsigned f = (((unsigned)(lane + 64 * e) * 911u) >> 17) & 7u;
        dma(xsrc + e * 1024, (lane * 16) ^ (f << 4), (8 + 2 * w) * SLOT + e * 1024);
      }
    }
  }

  if (w < 4) {
    const int i = w;
    unsigned pa = lane ? L_DUM + 4 * lane : L_FREE + 4 * i;
    asm volatile("" : "+v"(pa));
    const u64 tag = get_tag();
    constexpr int P_FREE = 0, P_RQ = L_RQ - L_FREE, P_SY = L_SY - L_FREE;
    if (ab) {
      unsigned ab4[4];
#pragma unroll
      for (int c = 0; c < 4; ++c) ab4[c] = (4 * q + (t >> 2)) * 128 + ((c ^ ((2 * q + (t >> 3)) & 3)) << 5) + (t & 3) * 8;
      const int T = 4 * rt + i;
      v2u xres[4];
      {
        const int tp = i >> 1, ch = i & 1;
        const unsigned xa = (8 + 2 * i) * SLOT + t * (D_ * 2), xs = ((t >> 1) & 7) << 4;
        unsigned xe[2][2], wb[2][2];
#pragma unroll
        for (int e = 0; e < 2; ++e) {
          xe[0][e] = xa + ((64 * (e ^ tp) + q * 16) ^ xs);
          xe[1][e] = xe[0][e] + (ch ? -2 * SLOT : 2 * SLOT);
          wb[e][0] = (ch ? ab4[2] : ab4[0]) + (e ^ tp) * 4096;
          wb[e][1] = (ch ? ab4[3] : ab4[1]) + (e ^ tp) * 4096;
          asm volatile("" : "+v"(xe[0][e]), "+v"(xe[1][e]), "+v"(wb[e][0]), "+v"(wb[e][1]));
        }
        int dst[4];
        unsigned keep[4], dm[4];
#pragma unroll
        for (int k = 0; k < 4; ++k) {
          const int gt = 4 * ct + 2 * ch + (k & 1), grp = gt / 9;
          const int sh = grp == 0 ? 1 : grp == 1 ? -1 : grp == 2 ? 2 : -2;
          const int ts = 16 * (ch ^ (k >> 1)) + t + sh;
          keep[k] = (ts >= 0 && ts < 32) ? 0xffffffffu : 0u;
          dst[k] = OFF_H1 + ((T & ~1) + ((ts >> 4) & 1)) * (18 * 1024) + ((ts & 15) + 16 * q) * 16 + (k & 1) * 8;
          dm[k] = (q == (t >> 2) && k == (t & 3)) ? 0xffffffffu : 0u;
          asm volatile("" : "+v"(dst[k]), "+v"(keep[k]), "+v"(dm[k]));
        }
        DRAIN();
        post(pa + P_FREE, 1);
        wait_word(L_FREE + 4 * (i ^ 1), 1);
        STAMP(1);
        bf16x8 xf[2][18];
        f32x4 aS = {0, 0, 0, 0}, aG = {0, 0, 0, 0};
#pragma unroll
        for (int p = 0; p < 6; ++p) xf[0][p] = *(const LAS bf16x8*)(xe[0][p & 1] + (p >> 1) * 128);
        SB();
        sfor<18>([&](auto P) INL {
          constexpr int p = P.value;
          aS = mfma(ones, xf[0][p], aS); SB();
          if constexpr (p + 6 < 18) xf[0][p + 6] = *(const LAS bf16x8*)(xe[0][p & 1] + ((p + 6) >> 1) * 128);
          else if constexpr (p + 6 < 22) xres[p - 12] = *(const LAS v2u*)(xa + 128 * ct + ((32 * (p - 12) + 8 * q) ^ xs));
          SB();
          aG = mfma(xf[0][p], xf[0][p], aG); SB();
          xf[1][p] = *(const LAS bf16x8*)(xe[1][p & 1] + (p >> 1) * 128);
          SB();
        });
        CLOB();
        post(pa + P_FREE, 2);
        f32x4 lc[2];
        {
          const float gd = __uint_as_float(((__float_as_uint(aG.x) & dm[0]) | (__float_as_uint(aG.y) & dm[1])) | ((__float_as_uint(aG.z) & dm[2]) | (__float_as_uint(aG.w) & dm[3])));
          lc[0] = ln_consts(aS[0], qsum(gd));
          if (q == 0) *(LAS f32x4*)(L_LN1 + i * 256 + t * 16) = lc[0];
          CLOB();
          post(pa + P_SY, 1);
        }
        SB();
        STAMP(17);
        wait_full(1);
        constexpr int PA = 4;
        s16x4 wl[PA], wh[PA];
#define AADDR(k) (wb[((k) >> 1) & 1][(k) & 1] + ((k) >> 2) * 8192)
#pragma unroll
        for (int k = 0; k < PA; ++k) { wl[k] = trd(AADDR(k)); wh[k] = trd(AADDR(k) + 2048); }
        bf16x8 one2 = ones;
        asm volatile("" : "+v"(one2));
        SB();
        f32x4 acc[2][2], cs[2];
        v4u fv;
        sfor<36>([&](auto K) INL {
          constexpr int k = K.value, p = k >> 1, l = k & 1, nx = k - 1 + PA;
          const bf16x8 cur = cat(wl[k % PA], wh[k % PA]);
          mfa<(p == 0)>(acc[0][l], cur, xf[0][p]); SB();
          if constexpr (nx == 12 || nx == 24) { if (RARE(min3(fv) < nx / 12 + 1)) wait_full(nx / 12 + 1); CLOB(); SB(); }
          if constexpr (k >= 1 && nx < 36) wl[nx % PA] = trd(AADDR(nx));
          SB();
          mfa<(p == 0)>(acc[1][l], cur, xf[1][p]); SB();
          if constexpr (k >= 1 && nx < 36) wh[nx % PA] = trd(AADDR(nx) + 2048);
          if constexpr (nx == 9 || nx == 21) fv = peek4(L_FULL);
          SB();
          if constexpr ((p & 1) == 0) { mfa<(p == 0)>(cs[l], cur, one2); SB(); }
        });
        MFMA_DONE();
        if (t == 0) { *(LAS f32x4*)(L_CS1 + i * 128 + q * 16) = cs[0]; *(LAS f32x4*)(L_CS1 + i * 128 + 64 + q * 16) = cs[1]; }
        CLOB();
        post(pa + P_SY, 2);
        STAMP(2);
        wait_word(L_SY + 4 * (i ^ 1), 1);
        lc[1] = *(const LAS f32x4*)(L_LN1 + (i ^ 1) * 256 + t * 16);
        wait_word(L_SY + 4 * (i ^ 2), 2);
        const f32x4 o0 = *(const LAS f32x4*)(L_CS1 + (i ^ 2) * 128 + q * 16), o1 = *(const LAS f32x4*)(L_CS1 + (i ^ 2) * 128 + 64 + q * 16);
        cs[0] += o0; cs[1] += o1;
        f32x2 z[8];
        float ga8[8], gb8[8];
#pragma unroll
        for (int k = 0; k < 4; ++k) {
          const f32x4 zz = cs[k & 1] * lc[k >> 1].x + acc[k >> 1][k & 1];
          z[2 * k] = f32x2{zz.x, zz.y}; z[2 * k + 1] = f32x2{zz.z, zz.w};
          ga8[2 * k] = ga8[2 * k + 1] = lc[k >> 1].y; gb8[2 * k] = gb8[2 * k + 1] = lc[k >> 1].z;
        }
        SB();
        geluN<8>(z, ga8, gb8);
        unsigned hp[8];
#pragma unroll
        for (int k = 0; k < 8; ++k) { const f32x2 v = z[k] * lc[k >> 2].w; hp[k] = pk(v.x, v.y) & keep[k >> 1]; }
        const int up = 2 * ct + ch;
        if (RARE(up == 4 || up == 13)) {
#pragma unroll
          for (int k = 0; k < 4; ++k) ST8((v2u{hp[2 * k], hp[2 * k + 1]}), dst[k], up * 1024);
        } else {
#pragma unroll
          for (int tt = 0; tt < 2; ++tt) ST16((v4u{hp[4 * tt], hp[4 * tt + 1], hp[4 * tt + 2], hp[4 * tt + 3]}), dst[2 * tt], up * 1024);
        }
        STAMP(3);
        DRAIN();
        SIGNAL(FA, i);
        post(pa + P_FREE, 3);
        STAMP(4);
      }
      {
        wait_full(4);
#pragma unroll
        for (int c = 0; c < 4; ++c) { ab4[c] += 8 * SLOT; asm volatile("" : "+v"(ab4[c])); }
        constexpr int PB = 32;
        s16x4 wl[PB], wh[PB];
#pragma unroll
        for (int k = 0; k < PB; ++k) { wl[k] = trd(ab4[k & 3] + (k >> 2) * 4096); wh[k] = trd(ab4[k & 3] + (k >> 2) * 4096 + 2048); }
        SB();
        f32x2 xr[8];
#pragma unroll
        for (int l = 0; l < 4; ++l) { xr[2 * l] = f32x2{lo16(xres[l].x), hi16(xres[l].x)}; xr[2 * l + 1] = f32x2{lo16(xres[l].y), hi16(xres[l].y)}; }
#pragma unroll
        for (int k = 0; k < 8; ++k) asm volatile("" : "+v"(xr[k]));
        wait_bit(L_RDY + 4 * R_H, 1u << (i >> 1));
        STAMP(5);
        bf16x8 hf[18];
        const int hsrc = OFF_H1 + T * (18 * 1024);
#pragma unroll
        for (int u = 0; u < 9; ++u) hf[u] = asfrag(LD16(lane * 16 + u * 1024, hsrc));
        SB();
        f32x4 acc[4];
        unsigned zv;
        sfor<72>([&](auto K) INL {
          constexpr int k = K.value, u = k >> 2;
          mfa<(k < 4)>(acc[k & 3], cat(wl[k % PB], wh[k % PB]), hf[u]); SB();
          if constexpr (k == 17) post(pa + P_FREE, 4);
          if constexpr (k == 60) zv = peek(L_RDY + 4 * R_Z);
          if constexpr ((k & 1) == 0 && k / 2 + 9 < 18) hf[k / 2 + 9] = asfrag(LD16(lane * 16 + (k / 2 + 9) * 1024, hsrc));
          if constexpr (k + PB < 72) { wl[k % PB] = trd(ab4[k & 3] + ((k + PB) >> 2) * 4096); wh[k % PB] = trd(ab4[k & 3] + ((k + PB) >> 2) * 4096 + 2048); }
          SB();
        });
        MFMA_DONE();
        STAMP(6);
        unsigned xp[8];
#pragma unroll
        for (int l = 0; l < 4; ++l) {
          const f32x2 v0 = xr[2 * l] + f32x2{acc[l].x, acc[l].y}, v1 = xr[2 * l + 1] + f32x2{acc[l].z, acc[l].w};
          xp[2 * l] = pkc(v0.x, v0.y); xp[2 * l + 1] = pkc(v1.x, v1.y);
        }
        if (RARE(rfl(zv) == 0)) wait_word(L_RDY + 4 * R_Z, 1);
        CLOB();
        const int dst = OFF_X1 + (T * 18 + 2 * ct) * 1024;
        ST16((v4u{xp[0], xp[1], xp[2], xp[3]}), lane * 16, dst);
        ST16((v4u{xp[4], xp[5], xp[6], xp[7]}), lane * 16 + 1024, dst);
        SB();
        float sm = 0.f, sq = 0.f;
#pragma unroll
        for (int k = 0; k < 8; ++k) {
          const bf16x2_t v = __builtin_bit_cast(bf16x2_t, xp[k]);
          sm = __builtin_amdgcn_fdot2_f32_bf16(v, bf16x2_t{(__bf16)1.0f, (__bf16)1.0f}, sm, false);
          sq = __builtin_amdgcn_fdot2_f32_bf16(v, v, sq, false);
        }
        sm = qsum(sm); sq = qsum(sq);
        ST8((v2u{min(__float_as_uint(sm), EMPTY - 1u), min(__float_as_uint(sq), EMPTY - 1u)}), q ? 0x7ffffff0 : t * 8, OFF_STAT + (T * 9 + ct) * 128);
        SIGNAL(FB, i);
        post(pa + P_FREE, 5);
        STAMP(7);
      }
    } else {
      const int lt = ((r - 18) * 4 + i) * 64 + lane;
      unsigned sink = 0;
      auto touch = [&](const char* base, unsigned off) INL { asm volatile("global_load_dword %0, %1, %2\n\tglobal_load_dword %0, %1, %2 offset:64" : "+v"(sink) : "v"(off), "s"(base) : "memory"); };
      __builtin_amdgcn_s_sleep(SL1);
#pragma unroll
      for (int k = 0; k < 2; ++k) touch(w2, min(lt + 3584 * k, 5183) * 128);
      DRAIN();
      if (MAP) {
#pragma unroll
        for (int k = 0; k < 2; ++k) { const int l = min(lt + 3584 * k, 5183); touch(w3 + (j & 6) * (576 * 2), (l / 9) * (F_ * 2) + (l % 9) * 128); }
        DRAIN();
      }
      __builtin_amdgcn_s_sleep(SL2);
#pragma unroll
      for (int k = 0; k < (MAP ? 3 : 2); ++k) touch(w4 + (size_t)(MAP ? (j & 6) : j) * (576 * D_ * 2), min(lt + 3584 * k, MAP ? 10367 : 5183) * 128);
      DRAIN();
      asm volatile("" ::"v"(sink));
    }
    unsigned b0 = (4 * q + (t >> 2)) * 288 + (t & 3) * 8, b1 = b0 + 6 * SLOT, b2 = b0 + 12 * SLOT;
    asm volatile("" : "+v"(b0), "+v"(b1), "+v"(b2));
#define WADDR(z, c) ((((z) % 16) < 6 ? b0 : ((z) % 16) < 12 ? b1 : b2) + (((z) % 16) % 6) * SLOT + (c) * 32)
#define FN(i_) ((i_) < 108 ? (i_) / 9 : 12 + ((i_) - 108) % 6)
#define FC(i_) ((i_) < 108 ? (i_) % 9 : ((i_) - 108) / 6)
    const int T0 = 8 * g + 2 * i;
    const int hdst = OFF_HH + (T0 * 144 + fr * 18) * 1024;
    bf16x8 hb[18][2];
    auto hload = [&](auto K) INL {
      constexpr int m = K.value >> 1, tt = K.value & 1;
      hb[m][tt] = asfrag(LD16(lane * 16 + tt * (144 * 1024), hdst + dstep(m, s) * 1024));
    };
    s16x4 lo[PD], hi[PD];
    unsigned rv, hw = L_HS + 2 * i * 1024 + lane * 16;
    asm volatile("" : "+v"(hw));
    {
      wait_full(6);
#pragma unroll
      for (int k = 0; k < PD; ++k) { lo[k] = trd(WADDR(0, k)); hi[k] = trd(WADDR(0, k) + 4608); }
      f32x4 acc[2][9];
      const int xsrc = OFF_X1 + T0 * (18 * 1024);
      int xo[2] = {lane * 16, lane * 16 + 18 * 1024};
      asm volatile("" : "+v"(xo[0]), "+v"(xo[1]));
      bf16x8 xb[12][2];
      auto xload = [&](auto K) INL {
        constexpr int n = K.value >> 1, tt = K.value & 1;
        xb[n % 12][tt] = asfrag(LD16(xo[tt] + (n & 3) * 1024, xsrc + (n >> 2) * 4096));
      };
      SB();
      auto xfix = [&](auto K0, auto K1) INL {
        NOUNROLL for (unsigned n = 0; n < TRY_CAP; ++n) { CLOB();
          unsigned m = 0;
          COUNT(1);
          sfor<K1.value - K0.value>([&](auto K) INL { xload(IC<K0.value + K.value>{}); });
          sfor<K1.value - K0.value>([&](auto K) INL { constexpr int kk = K0.value + K.value; m = mx4(m, asv4(xb[(kk >> 1) % 12][kk & 1])); });
          if (!missing(m)) break;
        }
      };
      wait_bit(L_RDY + 4 * R_X, 1u << i);
      STAMP(9);
      sfor<10>([&](auto K) INL { xload(K); });
      SB();
      unsigned chk = mx4(mx4(0u, asv4(xb[0][0])), asv4(xb[0][1])), chk2 = 0;
      if (RARE(missing(chk))) xfix(IC<0>{}, IC<2>{});
      SB();
      f32x4 lc[2], csv;
      unsigned hp[2][2][2];
      unsigned la = L_LN + (32 * i + t) * 16, ca = L_CS3 + q * 16;
      asm volatile("" : "+v"(la), "+v"(ca));
      v4u fv;
      u64 bad;
      unsigned sv;
      auto epi = [&](auto Cc) INL {
        constexpr int c = Cc.value;
        f32x2 z[4];
#pragma unroll
        for (int tt = 0; tt < 2; ++tt) {
          const f32x4 zz = csv * lc[tt].x + acc[tt][c];
          z[2 * tt] = f32x2{zz.x, zz.y}; z[2 * tt + 1] = f32x2{zz.z, zz.w};
        }
        if constexpr (c == 3 || c == 5) sv = rfl(rv);
        SB();
        const float ga4[4] = {lc[0].y, lc[0].y, lc[1].y, lc[1].y}, gb4[4] = {lc[0].z, lc[0].z, lc[1].z, lc[1].z};
        geluN<4>(z, ga4, gb4);
#pragma unroll
        for (int tt = 0; tt < 2; ++tt) {
          { hp[tt][c & 1][0] = pk(z[2 * tt].x, z[2 * tt].y); hp[tt][c & 1][1] = pk(z[2 * tt + 1].x, z[2 * tt + 1].y); }
        }
        asm volatile("" : "+v"(hp[0][c & 1][0]), "+v"(hp[0][c & 1][1]), "+v"(hp[1][c & 1][0]), "+v"(hp[1][c & 1][1]));
        if constexpr (c == 3 || c == 5) { if (RARE(sv < c / 2)) wait_word(L_AGR, c / 2); CLOB(); }
        if constexpr ((c & 1) && c < 8) {
#pragma unroll
          for (int tt = 0; tt < 2; ++tt) {
            const v4u v = {hp[tt][0][0], hp[tt][0][1], hp[tt][1][0], hp[tt][1][1]};
            hb[c >> 1][tt] = asfrag(v);
            if constexpr (c < 7) *(LAS v4u*)(hw + tt * 1024) = v;
            else ST16(v, lane * 16 + tt * (144 * 1024), hdst + (pbase(s) + 3) * 1024);
          }
        }
        if constexpr (c == 8) {
#pragma unroll
          for (int tt = 0; tt < 2; ++tt)
            ST8((v2u{hp[tt][0][0], hp[tt][0][1]}), lane * 16 + tt * (144 * 1024) + (s & 1) * 8, hdst + (s < 2 ? 4 : 13) * 1024);
        }
        if constexpr ((c & 1) && c < 7) post(pa + P_RQ, c / 2 + 1);
        SB();
      };
      sfor<162>([&](auto I) INL {
        constexpr int ii = I.value, n = FN(ii), c = FC(ii), nx = ii - 1 + PD;
        const bf16x8 cur = cat(lo[ii % PD], hi[ii % PD]);
        mfa<(n == 0)>(acc[0][c], cur, xb[n % 12][0]); SB();
        constexpr bool ck1 = ii < 99 && (c == 5 || c == 6);
        constexpr bool ck2 = ii >= 72 && ii < 99 && c >= 1 && c <= 4;
        constexpr int k2 = 24 + 4 * (n - 8) + c - 1;
        if constexpr (ii >= 1 && nx < 162) lo[nx % PD] = trd(WADDR(FN(nx), FC(nx)));
        if constexpr (ck1) { chk = mx2(c == 5 ? 0u : chk, asv4(xb[n + 1][c - 5]).x, asv4(xb[n + 1][c - 5]).y); asm volatile("" : "+v"(chk)); }
        if constexpr (ck2) { chk2 = mx2(chk2, asv4(xb[(k2 >> 1) % 12][k2 & 1]).x, asv4(xb[(k2 >> 1) % 12][k2 & 1]).y); asm volatile("" : "+v"(chk2)); }
        if constexpr (ii < 99 && c == 7) { bad = __ballot(chk == EMPTY); asm volatile("" : "+s"(bad)); }
        if constexpr (ii == 99) { bad = __ballot(chk2 == EMPTY); asm volatile("" : "+s"(bad)); }
        if constexpr (ii == 63 || ii == 101) { sv = min3(fv); asm volatile("" : "+s"(sv)); }
        if constexpr (ii == 103) { sv = rfl(rv); asm volatile("" : "+s"(sv)); }
        SB();
        mfa<(n == 0)>(acc[1][c], cur, xb[n % 12][1]); SB();
        if constexpr (ii >= 1 && nx < 162) hi[nx % PD] = trd(WADDR(FN(nx), FC(nx)) + 4608);
        if constexpr (ck1) { chk = mx2(chk, asv4(xb[n + 1][c - 5]).z, asv4(xb[n + 1][c - 5]).w); asm volatile("" : "+v"(chk)); }
        if constexpr (ck2) { chk2 = mx2(chk2, asv4(xb[(k2 >> 1) % 12][k2 & 1]).z, asv4(xb[(k2 >> 1) % 12][k2 & 1]).w); asm volatile("" : "+v"(chk2)); }
        SB();
        if constexpr (ii < 99 && c == 8) { if (RARE(bad != 0)) xfix(IC<2 * n + 2>{}, IC<2 * n + 4>{}); SB(); }
        if constexpr (ii == 100) { if (RARE(bad != 0)) xfix(IC<24>{}, IC<36>{}); SB(); }
        if constexpr (ii < 28 && ii % 2 == 0) { xload(IC<10 + ii / 2>{}); SB(); }
        if constexpr (ii >= 30 && ii < 66 && ii % 3 == 0) { xload(IC<14 + ii / 3>{}); SB(); }
        if constexpr (ii == 18 || ii == 81 || ii == 108 || ii == 159) { post(pa + P_FREE, ii == 18 ? 6 : ii == 81 ? 7 : ii == 108 ? 8 : 9); SB(); }
        if constexpr (ii == 8) STAMP(23);
        if constexpr (ii == 35) STAMP(24);
        if constexpr (ii == 71) STAMP(25);
        if constexpr (ii == 58 || ii == 96) { fv = peek4(L_FULL); SB(); }
        if constexpr (ii == 64) { if (RARE(sv < 7)) wait_full(7); CLOB(); SB(); }
        if constexpr (ii == 97) { rv = peek(L_RDY + 4 * R_LN); SB(); }
        if constexpr (ii == 102) { if (RARE(sv < 10)) wait_full(10); CLOB(); SB(); }
        if constexpr (ii == 104) {
          if (RARE(sv == 0)) wait_word(L_RDY + 4 * R_LN, 1);
          CLOB();
          lc[0] = *(const LAS f32x4*)la; lc[1] = *(const LAS f32x4*)(la + 256);
          SB(); STAMP(10);
        }
        if constexpr (ii >= 108 && n == 14 && c >= 1) { csv = *(const LAS f32x4*)(ca + (c - 1) * 64); SB(); }
        if constexpr (ii >= 108 && n == 15 && (c == 4 || c == 6)) { rv = peek(L_AGR); SB(); }
        if constexpr (ii >= 108 && n == 17 && c >= 1) epi(IC<c - 1>{});
        if constexpr (ii == 150) { fv = peek4(L_FULL); SB(); }
      });
      csv = *(const LAS f32x4*)(ca + 8 * 64);
      if (RARE(min3(fv) < 11)) wait_full(11);
      CLOB();
#pragma unroll
      for (int k = 0; k < PD; ++k) { lo[k] = trd(WADDR(18, k)); hi[k] = trd(WADDR(18, k) + 4608); }
      SB();
      epi(IC<8>{});
      STAMP(11);
    }
    {
      f32x4 acc[2][9];
      unsigned pw = L_PS + 2 * i * 1024 + lane * 16, pw8 = L_PS + 32768 + 2 * i * 512 + lane * 8;
      asm volatile("" : "+v"(pw), "+v"(pw8));
      int po[2];
#pragma unroll
      for (int tt = 0; tt < 2; ++tt) { po[tt] = (2 * i + tt == fr) ? 0x7ff00000 : lane * 8; asm volatile("" : "+v"(po[tt])); }
      const int pdst = OFF_P + (((g * 4 + s) * 8 + 2 * i) * 8 + fr) * PUNIT;
      v4u fv;
      unsigned sv;
      auto epi = [&](auto Cc) INL {
        constexpr int c = Cc.value;
        if constexpr ((c & 1) && c < 8) {
#pragma unroll
          for (int tt = 0; tt < 2; ++tt) {
            const f32x4 a0 = acc[tt][c - 1], a1 = acc[tt][c];
            const v4u v = {pk(a0.x, a0.y), pk(a0.z, a0.w), pk(a1.x, a1.y), pk(a1.z, a1.w)};
            if constexpr (c == 7) ST16(v, 2 * po[tt] , pdst + tt * (8 * PUNIT) + 3 * 1024);
            *(LAS v4u*)(pw + tt * 1024 + (c >> 1) * 8192) = v;
          }
        }
        if constexpr (c == 8) {
#pragma unroll
          for (int tt = 0; tt < 2; ++tt) {
            const f32x4 a0 = acc[tt][8];
            const v2u v = {pk(a0.x, a0.y), pk(a0.z, a0.w)};
            ST8(v, po[tt], pdst + tt * (8 * PUNIT) + 4096);
            *(LAS v2u*)(pw8 + tt * 512) = v;
          }
        }
        if constexpr ((c & 1) && c < 7) post(pa + P_RQ, 3 + c / 2 + 1);
        SB();
      };
      constexpr int HS0 = HS0V, HS1 = HS1V, SIGS = SIGSV, HS2 = HS2V, HS3 = HS3V;
      sfor<162>([&](auto I) INL {
        constexpr int ii = I.value, n = FN(ii), c = FC(ii), nx = ii - 1 + PD;
        constexpr int G = ii == HS0 ? 0 : ii == HS1 ? 1 : ii == HS2 ? 2 : ii == HS3 ? 3 : -1;
        const bf16x8 cur = cat(lo[ii % PD], hi[ii % PD]);
        mfa<(n == 0)>(acc[0][c], cur, hb[n][0]); SB();
        if constexpr (ii >= 1 && nx < 162) lo[nx % PD] = trd(WADDR(18 + FN(nx), FC(nx)));
        if constexpr (G >= 0) { sv = rfl(rv); asm volatile("" : "+s"(sv)); }
        if constexpr (ii == 84 || ii == 102) { sv = min3(fv); asm volatile("" : "+s"(sv)); }
        if constexpr (ii == 106) { sv = min4(fv); asm volatile("" : "+s"(sv)); }
        SB();
        mfa<(n == 0)>(acc[1][c], cur, hb[n][1]); SB();
        if constexpr (ii >= 1 && nx < 162) hi[nx % PD] = trd(WADDR(18 + FN(nx), FC(nx)) + 4608);
        SB();
        if constexpr (ii == SIGS) { VMW(6); SIGNAL(FC, 3 + i); SB(); }
        if constexpr (ii + 4 == HS0 || ii + 4 == HS1 || ii + 4 == HS2 || ii + 4 == HS3) { rv = peek(L_RDY + 4 * R_HH); SB(); }
        if constexpr (G >= 0) {
          const unsigned need = G < 3 ? 1u << G : 8u << i;
          if (RARE((sv & need) != need)) wait_bit(L_RDY + 4 * R_HH, need);
          CLOB(); SB(); STAMP(G == 0 ? 27 : G == 1 ? 28 : G == 2 ? 13 : 14);
        }
        if constexpr (ii >= HS0 && ii < HS0 + 6) { hload(IC<8 + ii - HS0>{}); SB(); }
        if constexpr (ii >= HS1 && ii < HS1 + 6) { hload(IC<14 + ii - HS1>{}); SB(); }
        if constexpr (ii >= HS2 && ii < HS2 + 6) { hload(IC<20 + ii - HS2>{}); SB(); }
        if constexpr (ii >= HS3 && ii < HS3 + 10) { hload(IC<26 + ii - HS3>{}); SB(); }
        if constexpr (ii == 18 || ii == 54) { post(pa + P_FREE, ii == 18 ? 10 : 11); SB(); }
        if constexpr (ii == 79 || ii == 97) { fv = peek4(L_FULL); SB(); }
        if constexpr (ii == 84) { if (RARE(sv < 12)) wait_full(12); CLOB(); SB(); }
        if constexpr (ii == 102) { if (RARE(sv < 13)) wait_full(13); fv = peek4(L_FREE); SB(); STAMP(26); }
        if constexpr (ii == 106) { if (RARE(sv < 11)) wait_free(11); CLOB(); SB(); }
        if constexpr (ii >= 108 && n == 17 && c >= 1) epi(IC<c - 1>{});
      });
      MFMA_DONE();
      epi(IC<8>{});
      STAMP(15);
      asm volatile("s_waitcnt vmcnt(0) lgkmcnt(0)" ::: "memory");
      SIGNAL(FD, 3 + i);
      STAMP(16);
    }
  } else if (w < 7) {
    const int ld = w - 4;
    const unsigned fullw = L_FULL + 4 * ld;
    unsigned v3[3], v4a[9];
#pragma unroll
    for (int e = 0; e < 3; ++e) {
      const int idx = (3 * ld + e) * 64 + lane, row = idx / 18, ch = idx % 18;
      const int sc = (ch + 2 * (s & 1)) % 18;
      v3[e] = row * (F_ * 2) + sc * 16;
    }
#pragma unroll
    for (int e = 0; e < 9; ++e) { const int idx = e * 64 + lane; v4a[e] = (idx / 18) * (D_ * 2) + ((idx % 18 + 2 * (s & 1)) % 18) * 16; }
    const char* w3b = w3 + (576 * fr + 144 * s) * 2;
    const char* w4b = w4 + (size_t)(576 * fr) * (D_ * 2) + 144 * s * 2;
    auto w3slice = [&](int n) INL {
#pragma unroll
      for (int e = 0; e < 3; ++e) dma(w3b + (size_t)n * (32 * F_ * 2), v3[e], (n & 15) * SLOT + (3 * ld + e) * 1024);
    };
    auto w4whole = [&](int m, int u, auto CAP) INL {
      const char* src = w4b + u * (32 * D_ * 2);
#pragma unroll
      for (int e = 0; e < 9; ++e) { dma(src, v4a[e], ((18 + m) & 15) * SLOT + e * 1024); if constexpr (CAP.value) VMW(DCAP); }
    };
    const __amdgpu_buffer_rsrc_t r4 = __builtin_amdgcn_make_buffer_rsrc((void*)w4b, 0, 576 * D_ * 2 - 144 * s * 2, 0x00020000);
    v4u w4r[4][9];
    auto w4get = [&](auto K) INL {
      const int so = dstep(6 + ld + 3 * K.value, s) * (32 * D_ * 2);
#pragma unroll
      for (int e = 0; e < 9; ++e) { constexpr int eo[9] = {0, 2, 4, 6, 8, 1, 3, 5, 7}; w4r[K.value][eo[e]] = __builtin_amdgcn_raw_buffer_load_b128(r4, v4a[eo[e]], so, 0); }
      CLOB(); SB();
    };
    auto w4put = [&](auto K) INL {
      const unsigned a = ((24 + ld + 3 * K.value) & 15) * SLOT + lane * 16;
#pragma unroll
      for (int e = 0; e < 9; ++e) *(LAS v4u*)(a + e * 1024) = w4r[K.value][e];
      CLOB(); SB();
    };
    f32x4 cs[3] = {{0, 0, 0, 0}, {0, 0, 0, 0}, {0, 0, 0, 0}};
    const unsigned cb = (4 * q + (t >> 2)) * 288 + (t & 3) * 8 + ld * 96;
    auto colsum3 = [&](int n0, int n1) INL {
      __builtin_amdgcn_s_setprio(1);
      NOUNROLL for (int n = n0; n < n1; n += 2) {
        const unsigned a0 = cb + (n & 15) * SLOT;
        s16x4 l[6], h[6];
#pragma unroll
        for (int k = 0; k < 6; ++k) { l[k] = trd(a0 + (k / 3) * SLOT + (k % 3) * 32); h[k] = trd(a0 + (k / 3) * SLOT + 4608 + (k % 3) * 32); }
        SB();
#pragma unroll
        for (int k = 0; k < 6; ++k) cs[k % 3] = mfma(cat(l[k], h[k]), ones, cs[k % 3]);
      }
      __builtin_amdgcn_s_setprio(0);
      CLOB();
    };
    if (ab) {
      const unsigned v1 = (8 * (lane >> 5) + ((lane >> 3) & 3)) * (D_ * 2) + (((lane & 7) ^ ((q & 3) << 1)) << 4);
      auto w1units = [&](int u0) INL {
#pragma unroll
        for (int e = 0; e < 8; ++e) {
          const int u = u0 + 3 * (e >> 2);
          dma(w1 + 128 * ct + (32 * u + 16 * (e & 1) + 4 * ((e >> 1) & 1)) * (D_ * 2), v1, u * 4096 + (e & 3) * 1024);
        }
      };
      w1units(ld); w1units(ld + 6);
      VMW(8); lds_set(fullw, 1, lane);
      w1units(ld + 12);
      VMW(8); lds_set(fullw, 2, lane);
      STAMP(1);
      DRAIN(); lds_set(fullw, 3, lane);
      wait_free(2);
      const unsigned v2 = (lane >> 3) * (D_ * 2) + (((lane & 7) ^ ((q & 3) << 1)) << 4);
      NOUNROLL for (int u = ld; u < 18; u += 3) {
#pragma unroll
        for (int e = 0; e < 4; ++e) dma(w2 + 128 * ct + (32 * u + 8 * e) * (D_ * 2), v2, 8 * SLOT + u * 4096 + e * 1024);
      }
      w4get(IC<0>{});
      STAMP(2);
      VMW(9); lds_set(fullw, 4, lane);
      STAMP(3);
      wait_free(3);
      NOUNROLL for (int n = 0; n < 8; ++n) w3slice(n);
      STAMP(4);
      DRAIN(); lds_set(fullw, 6, lane);
      wait_full(6);
      colsum3(0, 8);
      wait_free(4);
      NOUNROLL for (int n = 8; n < 11; ++n) w3slice(n);
      wait_free(5);
      NOUNROLL for (int n = 11; n < 16; ++n) w3slice(n);
      w4get(IC<1>{});
      STAMP(5);
      VMW(9); lds_set(fullw, 7, lane);
      STAMP(6);
      wait_full(7);
      colsum3(8, 16);
    } else {
      __builtin_amdgcn_s_sleep(SL2);
      NOUNROLL for (int n = 0; n < 16; ++n) w3slice(n);
      DRAIN(); lds_set(fullw, 7, lane);
      w4get(IC<0>{}); w4get(IC<1>{});
      wait_full(7);
      colsum3(0, 16);
    }
    lds_set(fullw, 8, lane);
    STAMP(7);
    wait_free(6); wait_full(8);
    STAMP(11);
    w3slice(16); w3slice(17);
    DRAIN(); lds_set(fullw, 9, lane);
    STAMP(12);
    wait_full(9);
    colsum3(16, 18);
    if (t == 0) {
#pragma unroll
      for (int c = 0; c < 3; ++c) *(LAS f32x4*)(L_CS3 + (3 * ld + c) * 64 + q * 16) = cs[c];
    }
    CLOB();
    lds_set(fullw, 10, lane);
    STAMP(8);
    if (PRIO & 2) __builtin_amdgcn_s_setprio(2);
    const u64 tag = get_tag();
    auto hround = [&](int p) INL {
      wait_min4(L_RQ, p + 1);
      v4u d[8];
#pragma unroll
      for (int k = 0; k < 8; ++k) d[k] = *(const LAS v4u*)(L_HS + k * 1024 + lane * 16);
      CLOB();
      lds_set(L_AGR, p + 1, lane);
#pragma unroll
      for (int k = 0; k < 8; ++k) ST16(d[k], lane * 16 + k * (144 * 1024), OFF_HH + (8 * g * 144 + fr * 18 + pbase(s) + p) * 1024);
      CLOB();
    };
    auto pround = [&](int p) INL {
      wait_min4(L_RQ, 3 + p + 1);
      const int so = OFF_P + ((g * 4 + s) * 64 + fr) * PUNIT + p * 1024;
      v4u d[7];
#pragma unroll
      for (int k = 0; k < 7; ++k) d[k] = *(const LAS v4u*)(L_PS + p * 8192 + ((fr + 1 + k) & 7) * 1024 + lane * 16);
#pragma unroll
      for (int k = 0; k < 7; ++k) ST16(d[k], lane * 16, so + ((fr + 1 + k) & 7) * (8 * PUNIT));
      DRAIN();
      SIGNAL(FD, p);
    };
    wait_free(7);
    NOUNROLL for (int m = ld; m < 6; m += 3) w4whole(m, dstep(m, s), IC<0>{});
    if (!LATE || ld > 0) { w4get(IC<2>{}); w4get(IC<3>{}); }
    if (ld > 0) { wait_free(8); w4put(IC<0>{}); DRAIN(); lds_set(fullw, 11, lane); }
    hround(ld);
    DRAIN();
    SIGNAL(FC, ld);
    if (LATE && ld == 0) { w4get(IC<2>{}); w4get(IC<3>{}); }
    if (ld == 0) { w4put(IC<0>{}); w4put(IC<1>{}); }
    lds_set(fullw, 11, lane);
    STAMP(13);
    wait_free(9); wait_full(10);
    if (ld > 0) w4put(IC<1>{});
    w4put(IC<2>{});
    if (ld == 0) w4put(IC<3>{});
    lds_set(fullw, 12, lane);
    if (ld > 0) { wait_free(10); w4put(IC<3>{}); }
    lds_set(fullw, 13, lane);
    STAMP(10);
    pround(ld);
    STAMP(23);
  } else {
    if (PRIO & 1) __builtin_amdgcn_s_setprio(3);
    const u64 tag = __hip_atomic_load((GAS u64*)(ws + OFF_EPOCH), __ATOMIC_RELAXED, __HIP_MEMORY_SCOPE_AGENT) + 1ull;
    if (lane == 0) {
      *(volatile LAS u64*)L_TAG = tag;
      *(volatile LAS unsigned*)(L_RDY + 4 * R_TAG) = 1u;
      __hip_atomic_store(flagp(ws, FS, b, 0), tag, __ATOMIC_RELAXED, __HIP_MEMORY_SCOPE_AGENT);
    }
    {
      const v4u e4 = {EMPTY, EMPTY, EMPTY, EMPTY};
      if (ab) {
        wait_free(3);
#pragma unroll
        for (int k = 0; k < 8; ++k) ST16(e4, lane * 16 + (k & 1) * 1024 + (k >> 1) * (18 * 1024), OFF_X1 + (4 * rt * 18 + 2 * ct) * 1024);
        if (lane < 32) ST16(e4, OFF_STAT + (lane >> 3) * (9 * 128) + (lane & 7) * 16, (4 * rt * 9 + ct) * 128);
        DRAIN();
        lds_set(L_RDY + 4 * R_Z, 1, lane);
      }
    }
    STAMP(1);
    auto poll = [&](GAS u64* p) INL {
      NOUNROLL for (unsigned n = 0; n < GLB_CAP; ++n) {
        const u64 v = __hip_atomic_load(p, __ATOMIC_RELAXED, __HIP_MEMORY_SCOPE_AGENT);
        if (__all(v == tag)) break;
        __builtin_amdgcn_s_sleep(1);
      }
      CLOB();
    };
    auto poll_bits = [&](auto TWO, GAS u64* p0, GAS u64* p1, int word, u64 ma, u64 mb, u64 mc, u64 md) INL {
      unsigned done = 0;
      NOUNROLL for (unsigned n = 0; n < GLB_CAP; ++n) {
        const u64 v0 = __hip_atomic_load(p0, __ATOMIC_RELAXED, __HIP_MEMORY_SCOPE_AGENT);
        u64 v1 = v0;
        if constexpr (TWO.value) v1 = __hip_atomic_load(p1, __ATOMIC_RELAXED, __HIP_MEMORY_SCOPE_AGENT);
        const u64 bad0 = __ballot(v0 != tag), bad1 = __ballot(v1 != tag);
        const unsigned now = ((bad0 & ma) ? 0u : 1u) | ((bad0 & mb) ? 0u : 2u) | ((bad1 & mc) ? 0u : 4u) | ((bad1 & md) ? 0u : 8u);
        if (now != done) { done = now; lds_set(L_RDY + 4 * word, now, lane); }
        if (now == 15u) break;
        __builtin_amdgcn_s_sleep(1);
      }
      lds_set(L_RDY + 4 * word, 15u, lane);
      CLOB();
    };
    constexpr u64 M01 = 0x3333333333333333ull, M23 = 0xccccccccccccccccull;
    const int pl = (lane % 36) >> 2;
    if (ab) poll_bits(IC<0>{}, flagp(ws, FA, j + 8 * ((r & 1) + 2 * pl), lane & 3), nullptr, R_H, M01, M23, M01, M23);
    STAMP(2);
    poll_bits(IC<1>{}, flagp(ws, FB, g + 16 * pl, lane & 3), flagp(ws, FB, g + 8 + 16 * pl, lane & 3), R_X, M01, M23, M01, M23);
    STAMP(3);
    {
      v4u st[9];
      NOUNROLL for (unsigned n = 0; n < TRY_CAP; ++n) { CLOB();
        unsigned m = 0;
#pragma unroll
        for (int k = 0; k < 9; ++k) st[k] = LD16(OFF_STAT + ((lane >> 3) * 9 + k) * 128 + (lane & 7) * 16, g * (8 * 9 * 128));
#pragma unroll
        for (int k = 0; k < 9; ++k) m = mx4(m, st[k]);
        if (!missing(m)) break;
      }
      f32x4 a = __builtin_bit_cast(f32x4, st[0]);
#pragma unroll
      for (int k = 1; k < 9; ++k) a += __builtin_bit_cast(f32x4, st[k]);
      *(LAS f32x4*)(L_LN + lane * 32) = ln_consts(a.x, a.y);
      *(LAS f32x4*)(L_LN + lane * 32 + 16) = ln_consts(a.z, a.w);
      CLOB();
      lds_set(L_RDY + 4 * R_LN, 1, lane);
    }
    if (b == 0) {
      NOUNROLL for (int k = 0; k < 4; ++k) poll(flagp(ws, FS, k * 64 + lane, 0));
      if (lane == 0) __hip_atomic_store((GAS u64*)(ws + OFF_EPOCH), tag, __ATOMIC_RELAXED, __HIP_MEMORY_SCOPE_AGENT);
    }
    auto poll5 = [&](GAS u64* p0, int word, auto NB) INL {
      constexpr unsigned all = (1u << NB.value) - 1u;
      unsigned done = 0;
      NOUNROLL for (unsigned n = 0; n < GLB_CAP; ++n) {
        const u64 bad = __ballot(__hip_atomic_load(p0, __ATOMIC_RELAXED, __HIP_MEMORY_SCOPE_AGENT) != tag);
        unsigned now = 0;
#pragma unroll
        for (int k = 0; k < NB.value; ++k) now |= (bad & (0x0101010101010101ull << k)) ? 0u : 1u << k;
        if (now != done) { done = now; lds_set(L_RDY + 4 * word, now, lane); }
        if (now == all) break;
        __builtin_amdgcn_s_sleep(1);
      }
      lds_set(L_RDY + 4 * word, all, lane);
    };
    poll5(flagp(ws, FC, j + 8 * ((r & ~3) + ((lane >> 3) & 3)), lane & 7), R_HH, IC<7>{});
    STAMP(4);
    const int wr = lane >> 3;
    poll5(flagp(ws, FD, MAP ? (j & 1) + (wr & 6) + 8 * (((wr & 1) << 4) + (r & 15)) : wr + 8 * r, (lane & 7) < 3 ? (lane & 7) : 3 + (fr >> 1)), R_P, IC<5>{});
    STAMP(5);
  }
  if (w < 2 || (w >= 4 && w < 7)) {
    const int p = w < 2 ? w + 3 : w - 4;
    const int T = 8 * g + fr;
    const int u = p < 4 ? pbase(s) + p : (s < 2 ? 4 : 13);
    v4u xv;
    NOUNROLL for (unsigned n = 0; n < TRY_CAP; ++n) { CLOB(); xv = LD16(lane * 16, OFF_X1 + (T * 18 + u) * 1024); if (!missing(mx4(0u, xv))) break; }
    const float rstd = *(const LAS float*)(L_LN + (16 * fr + t) * 16 + 12);
    wait_bit(L_RDY + 4 * R_P, 1u << p);
    STAMP(20);
    const int psrc = OFF_P + ((g * 4 + s) * 8 + fr) * (8 * PUNIT) + p * 1024;
    char* orow = out + (size_t)(16 * T + t) * (D_ * 2) + (32 * u + 4 * q) * 2;
    if (p < 4) {
      v4u pv[8];
#pragma unroll
      for (int k = 1; k < 8; ++k) pv[k] = LD16(lane * 16, psrc + ((fr + k) & 7) * PUNIT);
      pv[0] = *(const LAS v4u*)(L_PS + p * 8192 + fr * 1024 + lane * 16);
      float a[8];
#pragma unroll
      for (int e = 0; e < 8; ++e) a[e] = 0.f;
#pragma unroll
      for (int k = 0; k < 8; ++k)
#pragma unroll
        for (int e = 0; e < 4; ++e) { const unsigned d = pv[k][e]; a[2 * e] += lo16(d); a[2 * e + 1] += hi16(d); }
      unsigned o[4];
#pragma unroll
      for (int e = 0; e < 4; ++e) {
        const unsigned h2 = pk(a[2 * e] * rstd, a[2 * e + 1] * rstd), xe = xv[e];
        o[e] = pk(lo16(xe) + lo16(h2), hi16(xe) + hi16(h2));
      }
      *(GAS v2u*)orow = v2u{o[0], o[1]};
      *(GAS v2u*)(orow + 32) = v2u{o[2], o[3]};
    } else {
      v2u pv[8];
#pragma unroll
      for (int k = 1; k < 8; ++k) pv[k] = LD8(lane * 8, psrc + ((fr + k) & 7) * PUNIT);
      pv[0] = *(const LAS v2u*)(L_PS + 32768 + fr * 512 + lane * 8);
      float a[4] = {0.f, 0.f, 0.f, 0.f};
#pragma unroll
      for (int k = 0; k < 8; ++k) { a[0] += lo16(pv[k].x); a[1] += hi16(pv[k].x); a[2] += lo16(pv[k].y); a[3] += hi16(pv[k].y); }
      const unsigned x0 = (s & 1) ? xv.z : xv.x, x1v = (s & 1) ? xv.w : xv.y;
      const unsigned h0 = pk(a[0] * rstd, a[1] * rstd), h1 = pk(a[2] * rstd, a[3] * rstd);
      *(GAS v2u*)(orow + (s & 1) * 32) = v2u{pk(lo16(x0) + lo16(h0), hi16(x0) + hi16(h0)), pk(lo16(x1v) + lo16(h1), hi16(x1v) + hi16(h1))};
    }
    STAMP(21);
  }
#endif
}
}

extern "C" void kernel_launch(void* const* d_in, const int* in_sizes, int n_in, void* d_out, int out_size, void* d_ws, size_t ws_size,
                              hipStream_t stream) {
  mx::mixer_kernel<<<256, 512, 0, stream>>>((const char*)d_in[0], (const char*)d_in[1], (const char*)d_in[2], (const char*)d_in[3],
                                            (const char*)d_in[4], (char*)d_out, (char*)d_ws);
}
```

```cpp
#include <hip/hip_runtime.h>
#include <utility>

#define STAMPS 0
#define HS0V 10
#define HS1V 36
#define HS2V 62
#define SIGSV 30
#define HS3V 90
#define LATE 0
#define PRIO 1
#define CO 3
#define MAP 1
#define SL1 32
#define SL2 64

#define LAS __attribute__((address_space(3)))
#define GAS __attribute__((address_space(1)))
#define DI __device__ __forceinline__
typedef unsigned v4u __attribute__((ext_vector_type(4)));
typedef unsigned v2u __attribute__((ext_vector_type(2)));
typedef float f32x4 __attribute__((ext_vector_type(4)));
typedef float f32x2 __attribute__((ext_vector_type(2)));
typedef short bf16x8 __attribute__((ext_vector_type(8)));
typedef short s16x4 __attribute__((ext_vector_type(4)));
typedef __bf16 bf16x2_t __attribute__((ext_vector_type(2)));
typedef unsigned long long u64;

namespace mx {
constexpr int D_ = 576, F_ = 4608;
constexpr int OFF_EPOCH = 0;
constexpr int OFF_FLAGS = 4096;
constexpr int OFF_STAT = 344064;
constexpr int OFF_H1 = 524288;
constexpr int OFF_X1 = OFF_H1 + 64 * 18 * 1024;
constexpr int OFF_HH = OFF_X1 + 64 * 18 * 1024;
constexpr int PUNIT = 4608;
constexpr int OFF_P = OFF_HH + 64 * 144 * 1024;
constexpr int OFF_DBG = OFF_P + 8 * 4 * 8 * 8 * PUNIT;
constexpr int WS_BYTES = OFF_DBG + 2 * 256 * 8 * 32 * 8 + 256;
enum { FS = 0, FA = 1, FB = 2, FC = 3, FD = 4 };
constexpr int SLOT = 9216, MISC = 16 * SLOT;
constexpr int L_FULL = MISC, L_FREE = MISC + 16, L_RQ = MISC + 32, L_RDY = MISC + 48  , L_SY = MISC + 96, L_AGR = MISC + 112, L_TAG = MISC + 120;
constexpr int L_DUM = MISC + 128  , L_CS1 = MISC + 512  , L_CS3 = MISC + 1024  , L_LN = MISC + 2048  ,
              L_HS = MISC + 4096  , L_LN1 = L_HS  , L_DBG = MISC + 12288, L_PS = 4 * SLOT  ;
enum { R_H = 0, R_X = 1, R_HH = 2, R_P = 3, R_TAG = 4, R_LN = 5, R_Z = 6 };
constexpr unsigned EMPTY = 0xffffffffu;
constexpr unsigned TRY_CAP = 1u << 12;
constexpr unsigned LDS_CAP = 1u << 21, GLB_CAP = 1u << 16;
#define DCAPV 6
constexpr int PD = 4, DCAP = DCAPV;

template <class F, int... I> DI void sfor_impl(F&& f, std::integer_sequence<int, I...>) { (f(std::integral_constant<int, I>{}), ...); }
template <int N, class F> DI void sfor(F&& f) { sfor_impl(f, std::make_integer_sequence<int, N>{}); }
template <int V> using IC = std::integral_constant<int, V>;
#define INL __attribute__((always_inline))
#define NOUNROLL _Pragma("nounroll")
#define RARE(x) __builtin_expect(!!(x), 0)
#define SB() __builtin_amdgcn_sched_barrier(0)
#define CLOB() asm volatile("" ::: "memory")
#define DRAIN() asm volatile("s_waitcnt vmcnt(0)" ::: "memory")
#define VMW(n) asm volatile("s_waitcnt vmcnt(%0)" ::"i"(n) : "memory")

DI unsigned rfl(unsigned v) { return __builtin_amdgcn_readfirstlane(v); }
DI f32x4 mfma(bf16x8 a, bf16x8 b, f32x4 c) { return __builtin_amdgcn_mfma_f32_16x16x32_bf16(a, b, c, 0, 0, 0); }
template <bool FIRST> DI void mfa(f32x4& c, bf16x8 a, bf16x8 b) {
  if constexpr (FIRST) asm volatile("v_mfma_f32_16x16x32_bf16 %0, %1, %2, 0" : "=&v"(c) : "v"(a), "v"(b));
  else asm volatile("v_mfma_f32_16x16x32_bf16 %0, %1, %2, %0" : "+v"(c) : "v"(a), "v"(b));
}
#define MFMA_DONE() asm volatile("s_nop 7\n\ts_nop 3" ::: "memory")
DI bf16x8 asfrag(v4u v) { return __builtin_bit_cast(bf16x8, v); }
DI s16x4 trd(unsigned addr) { return __builtin_amdgcn_ds_read_tr16_b64_v4i16((LAS s16x4*)addr); }
DI bf16x8 cat(s16x4 lo, s16x4 hi) { return __builtin_shufflevector(lo, hi, 0, 1, 2, 3, 4, 5, 6, 7); }
DI unsigned pk(float a, float b) { bf16x2_t r = __builtin_convertvector(f32x2{a, b}, bf16x2_t); return __builtin_bit_cast(unsigned, r); }
DI unsigned pkc(float a, float b) { return min(pk(a, b), EMPTY - 1u); }
DI unsigned mx2(unsigned m, unsigned a, unsigned b) { return max(max(m, a), b); }
DI unsigned mx4(unsigned m, v4u d) { return mx2(mx2(m, d.x, d.y), d.z, d.w); }
DI bool missing(unsigned m) { return __any(m == EMPTY); }
DI v4u asv4(bf16x8 f) { return __builtin_bit_cast(v4u, f); }
DI float lo16(unsigned p) { return __uint_as_float(p << 16); }
DI float hi16(unsigned p) { return __uint_as_float(p & 0xffff0000u); }
DI float bfr(float a) { return lo16(pk(a, a)); }
DI float qsum(float v) {
  auto r = __builtin_amdgcn_permlane16_swap(__float_as_uint(v), __float_as_uint(v), false, false);
  float a = __uint_as_float(r[0]) + __uint_as_float(r[1]);
  auto r2 = __builtin_amdgcn_permlane32_swap(__float_as_uint(a), __float_as_uint(a), false, false);
  return __uint_as_float(r2[0]) + __uint_as_float(r2[1]);
}
constexpr float GA = -2.302208198144325f;
constexpr float GB = -0.1029432397357593f;
DI f32x4 ln_consts(float s, float ss) {
  const float m = s * (1.0f / D_);
  float var = __builtin_fmaf(-m, m, ss * (1.0f / D_));
  var = var > 0.f ? var : 0.f;
  const float sd = bfr(__builtin_amdgcn_sqrtf(bfr(bfr(var) + 1e-5f)));
  const float r = __builtin_amdgcn_rcpf(sd);
  return f32x4{-bfr(m), GA * r, GB * r * r * r, r};
}
template <int N> DI void geluN(f32x2 (&z)[N], const float (&a)[N], const float (&b)[N]) {
  f32x2 p[N];
#pragma unroll
  for (int k = 0; k < N; ++k) p[k] = z[k] * z[k];
  SB();
#pragma unroll
  for (int k = 0; k < N; ++k) p[k] = p[k] * b[k] + a[k];
  SB();
#pragma unroll
  for (int k = 0; k < N; ++k) p[k] = z[k] * p[k];
  SB();
#pragma unroll
  for (int k = 0; k < N; ++k) { p[k].x = __builtin_amdgcn_exp2f(p[k].x); p[k].y = __builtin_amdgcn_exp2f(p[k].y); }
  SB();
#pragma unroll
  for (int k = 0; k < N; ++k) p[k] = p[k] + 1.0f;
  SB();
#pragma unroll
  for (int k = 0; k < N; ++k) { p[k].x = __builtin_amdgcn_rcpf(p[k].x); p[k].y = __builtin_amdgcn_rcpf(p[k].y); }
  SB();
#pragma unroll
  for (int k = 0; k < N; ++k) z[k] = z[k] * p[k];
  SB();
}

DI unsigned ldsw(int addr) { return *(volatile LAS unsigned*)addr; }
DI v4u lds4(int addr) { return *(volatile LAS v4u*)addr; }
DI void lds_set(int addr, unsigned v, int lane) { if (lane == 0) *(volatile LAS unsigned*)addr = v; }
DI v4u peek4(int addr) { CLOB(); return *(const LAS v4u*)addr; }
DI unsigned peek(int addr) { CLOB(); return *(const LAS unsigned*)addr; }
DI void post(unsigned addr, unsigned v) { CLOB(); *(LAS unsigned*)addr = v; CLOB(); }
DI void wait_word(int addr, unsigned v) {
  NOUNROLL for (unsigned n = 0; n < LDS_CAP; ++n) { if (rfl(ldsw(addr)) >= v) break; __builtin_amdgcn_s_sleep(1); }
  CLOB();
}
DI unsigned min3(v4u x) { return rfl(min(min(x.x, x.y), x.z)); }
DI unsigned min4(v4u x) { return rfl(min(min(x.x, x.y), min(x.z, x.w))); }
DI void wait_full(unsigned v) {
  NOUNROLL for (unsigned n = 0; n < LDS_CAP; ++n) { if (min3(lds4(L_FULL)) >= v) break; __builtin_amdgcn_s_sleep(1); }
  CLOB();
}
DI void wait_min4(int addr, unsigned v) {
  NOUNROLL for (unsigned n = 0; n < LDS_CAP; ++n) { if (min4(lds4(addr)) >= v) break; __builtin_amdgcn_s_sleep(1); }
  CLOB();
}
DI void wait_free(unsigned v) { wait_min4(L_FREE, v); }
DI void wait_bit(int addr, unsigned bit) {
  NOUNROLL for (unsigned n = 0; n < LDS_CAP; ++n) { if ((rfl(ldsw(addr)) & bit) == bit) break; __builtin_amdgcn_s_sleep(1); }
  CLOB();
}
DI GAS u64* flagp(char* ws, int kind, int b, int wv) { return (GAS u64*)(ws + OFF_FLAGS + (kind * 256 + b) * 128 + wv * 8); }
DI u64 get_tag() {
  wait_word(L_RDY + 4 * R_TAG, 1);
  return ((u64)rfl(ldsw(L_TAG + 4)) << 32) | rfl(ldsw(L_TAG));
}
#define LD16(off, soff) __builtin_amdgcn_raw_buffer_load_b128(rs, (off), (soff), 16)
#define LD8(off, soff) __builtin_amdgcn_raw_buffer_load_b64(rs, (off), (soff), 16)
#define ST16(v, off, soff) do { v4u d_ = (v); asm volatile("" : "+v"(d_)); __builtin_amdgcn_raw_buffer_store_b128(d_, rs, (off), (soff), 16); asm volatile("s_nop 1" ::"v"(d_) : "memory"); } while (0)
#define ST8(v, off, soff) __builtin_amdgcn_raw_buffer_store_b64((v), rs, (off), (soff), 16)
#define SIGNAL(kind, wv) do { CLOB(); ST8((v2u{(unsigned)tag, (unsigned)(tag >> 32)}), lane ? 0x7ffffff0 : 0, OFF_FLAGS + ((kind) * 256 + b) * 128 + (wv) * 8); CLOB(); } while (0)
DI void dma(const char* sbase, unsigned voff, unsigned ldsaddr) {
  asm volatile("s_mov_b32 m0, %0\n\ts_nop 0\n\tglobal_load_lds_dwordx4 %1, %2" ::"s"(ldsaddr), "v"(voff), "s"(sbase) : "memory");
}
DI void dmac(const char* sbase, unsigned voff, unsigned ldsaddr) { dma(sbase, voff, ldsaddr); VMW(DCAP); }
DI int pbase(int s) { return (9 * s + (s & 1)) >> 1; }
DI int dstep(int M, int s) {
  if (M < 4) return pbase(s) + M;
  else if (M < 16) return pbase((s + 1 + (M - 4) % 3) & 3) + (M - 4) / 3;
  else return M == 16 ? 4 : 13;
}
#define STAMP(k) do { } while (0)
#define COUNT(k) do { } while (0)

__global__ __launch_bounds__(512, 2) void mixer_kernel(const char* __restrict__ x, const char* __restrict__ w1, const char* __restrict__ w2,
                                                       const char* __restrict__ w3, const char* __restrict__ w4, char* __restrict__ out, char* ws) {
#if defined(__HIP_DEVICE_COMPILE__)
  __shared__ __attribute__((aligned(16))) char smem[163840];
  const int tid = threadIdx.x, lane = tid & 63, w = rfl(tid >> 6);
  const int t = lane & 15, q = lane >> 4;
  const int b = blockIdx.x, j = b & 7, r = b >> 3;
  const bool ab = r < 18;
  const int rt = 2 * j + (r & 1), ct = r >> 1;
  const int g = MAP ? 4 * (j & 1) + ((r >> 2) & 3) : r >> 2, fr = MAP ? (j & 6) + (r >> 4) : j, s = r & 3;
  const __amdgpu_buffer_rsrc_t rs = __builtin_amdgcn_make_buffer_rsrc(ws, 0, WS_BYTES, 0x00020000);
  const bf16x8 ones = {0x3F80, 0x3F80, 0x3F80, 0x3F80, 0x3F80, 0x3F80, 0x3F80, 0x3F80};
  asm volatile("" ::"v"(smem));
  STAMP(0);
  if (tid < 32) *(volatile LAS unsigned*)(MISC + tid * 4) = 0u;
  asm volatile("s_waitcnt lgkmcnt(0)\n\ts_barrier" ::: "memory");
  if (ab) {
    if (w < 4) {
      const char* xsrc = x + (size_t)(4 * rt + w) * (16 * D_ * 2);
#pragma unroll
      for (int e = 0; e < 18; ++e) {
        const unsigned f = (((unsigned)(lane + 64 * e) * 911u) >> 17) & 7u;
        dma(xsrc + e * 1024, (lane * 16) ^ (f << 4), (8 + 2 * w) * SLOT + e * 1024);
      }
    }
  }

  if (w < 4) {
    const int i = w;
    unsigned pa = lane ? L_DUM + 4 * lane : L_FREE + 4 * i;
    asm volatile("" : "+v"(pa));
    const u64 tag = get_tag();
    constexpr int P_FREE = 0, P_RQ = L_RQ - L_FREE, P_SY = L_SY - L_FREE;
    if (ab) {
      unsigned ab4[4];
#pragma unroll
      for (int c = 0; c < 4; ++c) ab4[c] = (4 * q + (t >> 2)) * 128 + ((c ^ ((2 * q + (t >> 3)) & 3)) << 5) + (t & 3) * 8;
      const int T = 4 * rt + i;
      v2u xres[4];
      {
        const int tp = i >> 1, ch = i & 1;
        const unsigned xa = (8 + 2 * i) * SLOT + t * (D_ * 2), xs = ((t >> 1) & 7) << 4;
        unsigned xe[2][2], wb[2][2];
#pragma unroll
        for (int e = 0; e < 2; ++e) {
          xe[0][e] = xa + ((64 * (e ^ tp) + q * 16) ^ xs);
          xe[1][e] = xe[0][e] + (ch ? -2 * SLOT : 2 * SLOT);
          wb[e][0] = (ch ? ab4[2] : ab4[0]) + (e ^ tp) * 4096;
          wb[e][1] = (ch ? ab4[3] : ab4[1]) + (e ^ tp) * 4096;
          asm volatile("" : "+v"(xe[0][e]), "+v"(xe[1][e]), "+v"(wb[e][0]), "+v"(wb[e][1]));
        }
        int dst[4];
        unsigned keep[4], dm[4];
#pragma unroll
        for (int k = 0; k < 4; ++k) {
          const int gt = 4 * ct + 2 * ch + (k & 1), grp = gt / 9;
          const int sh = grp == 0 ? 1 : grp == 1 ? -1 : grp == 2 ? 2 : -2;
          const int ts = 16 * (ch ^ (k >> 1)) + t + sh;
          keep[k] = (ts >= 0 && ts < 32) ? 0xffffffffu : 0u;
          dst[k] = OFF_H1 + ((T & ~1) + ((ts >> 4) & 1)) * (18 * 1024) + ((ts & 15) + 16 * q) * 16 + (k & 1) * 8;
          dm[k] = (q == (t >> 2) && k == (t & 3)) ? 0xffffffffu : 0u;
          asm volatile("" : "+v"(dst[k]), "+v"(keep[k]), "+v"(dm[k]));
        }
        DRAIN();
        post(pa + P_FREE, 1);
        wait_word(L_FREE + 4 * (i ^ 1), 1);
        STAMP(1);
        bf16x8 xf[2][18];
        f32x4 aS = {0, 0, 0, 0}, aG = {0, 0, 0, 0};
#pragma unroll
        for (int p = 0; p < 6; ++p) xf[0][p] = *(const LAS bf16x8*)(xe[0][p & 1] + (p >> 1) * 128);
        SB();
        sfor<18>([&](auto P) INL {
          constexpr int p = P.value;
          aS = mfma(ones, xf[0][p], aS); SB();
          if constexpr (p + 6 < 18) xf[0][p + 6] = *(const LAS bf16x8*)(xe[0][p & 1] + ((p + 6) >> 1) * 128);
          else if constexpr (p + 6 < 22) xres[p - 12] = *(const LAS v2u*)(xa + 128 * ct + ((32 * (p - 12) + 8 * q) ^ xs));
          SB();
          aG = mfma(xf[0][p], xf[0][p], aG); SB();
          xf[1][p] = *(const LAS bf16x8*)(xe[1][p & 1] + (p >> 1) * 128);
          SB();
        });
        CLOB();
        post(pa + P_FREE, 2);
        f32x4 lc[2];
        {
          const float gd = __uint_as_float(((__float_as_uint(aG.x) & dm[0]) | (__float_as_uint(aG.y) & dm[1])) | ((__float_as_uint(aG.z) & dm[2]) | (__float_as_uint(aG.w) & dm[3])));
          lc[0] = ln_consts(aS[0], qsum(gd));
          if (q == 0) *(LAS f32x4*)(L_LN1 + i * 256 + t * 16) = lc[0];
          CLOB();
          post(pa + P_SY, 1);
        }
        SB();
        STAMP(17);
        wait_full(1);
        constexpr int PA = 4;
        s16x4 wl[PA], wh[PA];
#define AADDR(k) (wb[((k) >> 1) & 1][(k) & 1] + ((k) >> 2) * 8192)
#pragma unroll
        for (int k = 0; k < PA; ++k) { wl[k] = trd(AADDR(k)); wh[k] = trd(AADDR(k) + 2048); }
        bf16x8 one2 = ones;
        asm volatile("" : "+v"(one2));
        SB();
        f32x4 acc[2][2], cs[2];
        v4u fv;
        sfor<36>([&](auto K) INL {
          constexpr int k = K.value, p = k >> 1, l = k & 1, nx = k - 1 + PA;
          const bf16x8 cur = cat(wl[k % PA], wh[k % PA]);
          mfa<(p == 0)>(acc[0][l], cur, xf[0][p]); SB();
          if constexpr (nx == 12 || nx == 24) { if (RARE(min3(fv) < nx / 12 + 1)) wait_full(nx / 12 + 1); CLOB(); SB(); }
          if constexpr (k >= 1 && nx < 36) wl[nx % PA] = trd(AADDR(nx));
          SB();
          mfa<(p == 0)>(acc[1][l], cur, xf[1][p]); SB();
          if constexpr (k >= 1 && nx < 36) wh[nx % PA] = trd(AADDR(nx) + 2048);
          if constexpr (nx == 9 || nx == 21) fv = peek4(L_FULL);
          SB();
          if constexpr ((p & 1) == 0) { mfa<(p == 0)>(cs[l], cur, one2); SB(); }
        });
        MFMA_DONE();
        if (t == 0) { *(LAS f32x4*)(L_CS1 + i * 128 + q * 16) = cs[0]; *(LAS f32x4*)(L_CS1 + i * 128 + 64 + q * 16) = cs[1]; }
        CLOB();
        post(pa + P_SY, 2);
        STAMP(2);
        wait_word(L_SY + 4 * (i ^ 1), 1);
        lc[1] = *(const LAS f32x4*)(L_LN1 + (i ^ 1) * 256 + t * 16);
        wait_word(L_SY + 4 * (i ^ 2), 2);
        const f32x4 o0 = *(const LAS f32x4*)(L_CS1 + (i ^ 2) * 128 + q * 16), o1 = *(const LAS f32x4*)(L_CS1 + (i ^ 2) * 128 + 64 + q * 16);
        cs[0] += o0; cs[1] += o1;
        f32x2 z[8];
        float ga8[8], gb8[8];
#pragma unroll
        for (int k = 0; k < 4; ++k) {
          const f32x4 zz = cs[k & 1] * lc[k >> 1].x + acc[k >> 1][k & 1];
          z[2 * k] = f32x2{zz.x, zz.y}; z[2 * k + 1] = f32x2{zz.z, zz.w};
          ga8[2 * k] = ga8[2 * k + 1] = lc[k >> 1].y; gb8[2 * k] = gb8[2 * k + 1] = lc[k >> 1].z;
        }
        SB();
        geluN<8>(z, ga8, gb8);
        unsigned hp[8];
#pragma unroll
        for (int k = 0; k < 8; ++k) { const f32x2 v = z[k] * lc[k >> 2].w; hp[k] = pk(v.x, v.y) & keep[k >> 1]; }
        const int up = 2 * ct + ch;
        if (RARE(up == 4 || up == 13)) {
#pragma unroll
          for (int k = 0; k < 4; ++k) ST8((v2u{hp[2 * k], hp[2 * k + 1]}), dst[k], up * 1024);
        } else {
#pragma unroll
          for (int tt = 0; tt < 2; ++tt) ST16((v4u{hp[4 * tt], hp[4 * tt + 1], hp[4 * tt + 2], hp[4 * tt + 3]}), dst[2 * tt], up * 1024);
        }
        STAMP(3);
        DRAIN();
        SIGNAL(FA, i);
        post(pa + P_FREE, 3);
        STAMP(4);
      }
      {
        wait_full(4);
#pragma unroll
        for (int c = 0; c < 4; ++c) { ab4[c] += 8 * SLOT; asm volatile("" : "+v"(ab4[c])); }
        constexpr int PB = 32;
        s16x4 wl[PB], wh[PB];
#pragma unroll
        for (int k = 0; k < PB; ++k) { wl[k] = trd(ab4[k & 3] + (k >> 2) * 4096); wh[k] = trd(ab4[k & 3] + (k >> 2) * 4096 + 2048); }
        SB();
        f32x2 xr[8];
#pragma unroll
        for (int l = 0; l < 4; ++l) { xr[2 * l] = f32x2{lo16(xres[l].x), hi16(xres[l].x)}; xr[2 * l + 1] = f32x2{lo16(xres[l].y), hi16(xres[l].y)}; }
#pragma unroll
        for (int k = 0; k < 8; ++k) asm volatile("" : "+v"(xr[k]));
        wait_bit(L_RDY + 4 * R_H, 1u << (i >> 1));
        STAMP(5);
        bf16x8 hf[18];
        const int hsrc = OFF_H1 + T * (18 * 1024);
#pragma unroll
        for (int u = 0; u < 9; ++u) hf[u] = asfrag(LD16(lane * 16 + u * 1024, hsrc));
        SB();
        f32x4 acc[4];
        unsigned zv;
        sfor<72>([&](auto K) INL {
          constexpr int k = K.value, u = k >> 2;
          mfa<(k < 4)>(acc[k & 3], cat(wl[k % PB], wh[k % PB]), hf[u]); SB();
          if constexpr (k == 17) post(pa + P_FREE, 4);
          if constexpr (k == 60) zv = peek(L_RDY + 4 * R_Z);
          if constexpr ((k & 1) == 0 && k / 2 + 9 < 18) hf[k / 2 + 9] = asfrag(LD16(lane * 16 + (k / 2 + 9) * 1024, hsrc));
          if constexpr (k + PB < 72) { wl[k % PB] = trd(ab4[k & 3] + ((k + PB) >> 2) * 4096); wh[k % PB] = trd(ab4[k & 3] + ((k + PB) >> 2) * 4096 + 2048); }
          SB();
        });
        MFMA_DONE();
        STAMP(6);
        unsigned xp[8];
#pragma unroll
        for (int l = 0; l < 4; ++l) {
          const f32x2 v0 = xr[2 * l] + f32x2{acc[l].x, acc[l].y}, v1 = xr[2 * l + 1] + f32x2{acc[l].z, acc[l].w};
          xp[2 * l] = pkc(v0.x, v0.y); xp[2 * l + 1] = pkc(v1.x, v1.y);
        }
        if (RARE(rfl(zv) == 0)) wait_word(L_RDY + 4 * R_Z, 1);
        CLOB();
        const int dst = OFF_X1 + (T * 18 + 2 * ct) * 1024;
        ST16((v4u{xp[0], xp[1], xp[2], xp[3]}), lane * 16, dst);
        ST16((v4u{xp[4], xp[5], xp[6], xp[7]}), lane * 16 + 1024, dst);
        SB();
        float sm = 0.f, sq = 0.f;
#pragma unroll
        for (int k = 0; k < 8; ++k) {
          const bf16x2_t v = __builtin_bit_cast(bf16x2_t, xp[k]);
          sm = __builtin_amdgcn_fdot2_f32_bf16(v, bf16x2_t{(__bf16)1.0f, (__bf16)1.0f}, sm, false);
          sq = __builtin_amdgcn_fdot2_f32_bf16(v, v, sq, false);
        }
        sm = qsum(sm); sq = qsum(sq);
        ST8((v2u{min(__float_as_uint(sm), EMPTY - 1u), min(__float_as_uint(sq), EMPTY - 1u)}), q ? 0x7ffffff0 : t * 8, OFF_STAT + (T * 9 + ct) * 128);
        SIGNAL(FB, i);
        post(pa + P_FREE, 5);
        STAMP(7);
      }
    } else {
      const int lt = ((r - 18) * 4 + i) * 64 + lane;
      unsigned sink = 0;
      auto touch = [&](const char* base, unsigned off) INL { asm volatile("global_load_dword %0, %1, %2" : "+v"(sink) : "v"(off), "s"(base) : "memory"); };
      if (CO & 1) {
#pragma unroll
        for (int k = 0; k < 2; ++k) touch(w1, (5183 - min(lt + 3584 * k, 5183)) * 128);
        touch(x + (size_t)j * (128 * D_ * 2), min(lt, 1151) * 128);
      }
      if (CO & 4) DRAIN(); else __builtin_amdgcn_s_sleep(SL1);
#pragma unroll
      for (int k = 0; k < 2; ++k) touch(w2, min(lt + 3584 * k, 5183) * 128);
      if (!(CO & 4)) DRAIN();
      if (MAP) {
#pragma unroll
        for (int k = 0; k < 2; ++k) { const int l = min(lt + 3584 * k, 5183); touch(w3 + (j & 6) * (576 * 2), (l / 9) * (F_ * 2) + (l % 9) * 128); }
      }
      DRAIN();
      if (!(CO & 4)) __builtin_amdgcn_s_sleep(SL2);
#pragma unroll
      for (int k = 0; k < (MAP ? 3 : 2); ++k) touch(w4 + (size_t)(MAP ? (j & 6) : j) * (576 * D_ * 2), min(lt + 3584 * k, MAP ? 10367 : 5183) * 128);
      DRAIN();
      asm volatile("" ::"v"(sink));
    }
    unsigned b0 = (4 * q + (t >> 2)) * 288 + (t & 3) * 8, b1 = b0 + 6 * SLOT, b2 = b0 + 12 * SLOT;
    asm volatile("" : "+v"(b0), "+v"(b1), "+v"(b2));
#define WADDR(z, c) ((((z) % 16) < 6 ? b0 : ((z) % 16) < 12 ? b1 : b2) + (((z) % 16) % 6) * SLOT + (c) * 32)
#define FN(i_) ((i_) < 108 ? (i_) / 9 : 12 + ((i_) - 108) % 6)
#define FC(i_) ((i_) < 108 ? (i_) % 9 : ((i_) - 108) / 6)
    const int T0 = 8 * g + 2 * i;
    const int hdst = OFF_HH + (T0 * 144 + fr * 18) * 1024;
    bf16x8 hb[18][2];
    auto hload = [&](auto K) INL {
      constexpr int m = K.value >> 1, tt = K.value & 1;
      hb[m][tt] = asfrag(LD16(lane * 16 + tt * (144 * 1024), hdst + dstep(m, s) * 1024));
    };
    s16x4 lo[PD], hi[PD];
    unsigned rv, hw = L_HS + 2 * i * 1024 + lane * 16;
    asm volatile("" : "+v"(hw));
    {
      wait_full(6);
#pragma unroll
      for (int k = 0; k < PD; ++k) { lo[k] = trd(WADDR(0, k)); hi[k] = trd(WADDR(0, k) + 4608); }
      f32x4 acc[2][9];
      const int xsrc = OFF_X1 + T0 * (18 * 1024);
      int xo[2] = {lane * 16, lane * 16 + 18 * 1024};
      asm volatile("" : "+v"(xo[0]), "+v"(xo[1]));
      bf16x8 xb[12][2];
      auto xload = [&](auto K) INL {
        constexpr int n = K.value >> 1, tt = K.value & 1;
        xb[n % 12][tt] = asfrag(LD16(xo[tt] + (n & 3) * 1024, xsrc + (n >> 2) * 4096));
      };
      SB();
      auto xfix = [&](auto K0, auto K1) INL {
        NOUNROLL for (unsigned n = 0; n < TRY_CAP; ++n) { CLOB();
          unsigned m = 0;
          COUNT(1);
          sfor<K1.value - K0.value>([&](auto K) INL { xload(IC<K0.value + K.value>{}); });
          sfor<K1.value - K0.value>([&](auto K) INL { constexpr int kk = K0.value + K.value; m = mx4(m, asv4(xb[(kk >> 1) % 12][kk & 1])); });
          if (!missing(m)) break;
        }
      };
      wait_bit(L_RDY + 4 * R_X, 1u << i);
      STAMP(9);
      sfor<10>([&](auto K) INL { xload(K); });
      SB();
      unsigned chk = mx4(mx4(0u, asv4(xb[0][0])), asv4(xb[0][1])), chk2 = 0;
      if (RARE(missing(chk))) xfix(IC<0>{}, IC<2>{});
      SB();
      f32x4 lc[2], csv;
      unsigned hp[2][2][2];
      unsigned la = L_LN + (32 * i + t) * 16, ca = L_CS3 + q * 16;
      asm volatile("" : "+v"(la), "+v"(ca));
      v4u fv;
      u64 bad;
      unsigned sv;
      auto epi = [&](auto Cc) INL {
        constexpr int c = Cc.value;
        f32x2 z[4];
#pragma unroll
        for (int tt = 0; tt < 2; ++tt) {
          const f32x4 zz = csv * lc[tt].x + acc[tt][c];
          z[2 * tt] = f32x2{zz.x, zz.y}; z[2 * tt + 1] = f32x2{zz.z, zz.w};
        }
        if constexpr (c == 3 || c == 5) sv = rfl(rv);
        SB();
        const float ga4[4] = {lc[0].y, lc[0].y, lc[1].y, lc[1].y}, gb4[4] = {lc[0].z, lc[0].z, lc[1].z, lc[1].z};
        geluN<4>(z, ga4, gb4);
#pragma unroll
        for (int tt = 0; tt < 2; ++tt) {
          { hp[tt][c & 1][0] = pk(z[2 * tt].x, z[2 * tt].y); hp[tt][c & 1][1] = pk(z[2 * tt + 1].x, z[2 * tt + 1].y); }
        }
        asm volatile("" : "+v"(hp[0][c & 1][0]), "+v"(hp[0][c & 1][1]), "+v"(hp[1][c & 1][0]), "+v"(hp[1][c & 1][1]));
        if constexpr (c == 3 || c == 5) { if (RARE(sv < c / 2)) wait_word(L_AGR, c / 2); CLOB(); }
        if constexpr ((c & 1) && c < 8) {
#pragma unroll
          for (int tt = 0; tt < 2; ++tt) {
            const v4u v = {hp[tt][0][0], hp[tt][0][1], hp[tt][1][0], hp[tt][1][1]};
            hb[c >> 1][tt] = asfrag(v);
            if constexpr (c < 7) *(LAS v4u*)(hw + tt * 1024) = v;
            else ST16(v, lane * 16 + tt * (144 * 1024), hdst + (pbase(s) + 3) * 1024);
          }
        }
        if constexpr (c == 8) {
#pragma unroll
          for (int tt = 0; tt < 2; ++tt)
            ST8((v2u{hp[tt][0][0], hp[tt][0][1]}), lane * 16 + tt * (144 * 1024) + (s & 1) * 8, hdst + (s < 2 ? 4 : 13) * 1024);
        }
        if constexpr ((c & 1) && c < 7) post(pa + P_RQ, c / 2 + 1);
        SB();
      };
      sfor<162>([&](auto I) INL {
        constexpr int ii = I.value, n = FN(ii), c = FC(ii), nx = ii - 1 + PD;
        const bf16x8 cur = cat(lo[ii % PD], hi[ii % PD]);
        mfa<(n == 0)>(acc[0][c], cur, xb[n % 12][0]); SB();
        constexpr bool ck1 = ii < 99 && (c == 5 || c == 6);
        constexpr bool ck2 = ii >= 72 && ii < 99 && c >= 1 && c <= 4;
        constexpr int k2 = 24 + 4 * (n - 8) + c - 1;
        if constexpr (ii >= 1 && nx < 162) lo[nx % PD] = trd(WADDR(FN(nx), FC(nx)));
        if constexpr (ck1) { chk = mx2(c == 5 ? 0u : chk, asv4(xb[n + 1][c - 5]).x, asv4(xb[n + 1][c - 5]).y); asm volatile("" : "+v"(chk)); }
        if constexpr (ck2) { chk2 = mx2(chk2, asv4(xb[(k2 >> 1) % 12][k2 & 1]).x, asv4(xb[(k2 >> 1) % 12][k2 & 1]).y); asm volatile("" : "+v"(chk2)); }
        if constexpr (ii < 99 && c == 7) { bad = __ballot(chk == EMPTY); asm volatile("" : "+s"(bad)); }
        if constexpr (ii == 99) { bad = __ballot(chk2 == EMPTY); asm volatile("" : "+s"(bad)); }
        if constexpr (ii == 63 || ii == 101) { sv = min3(fv); asm volatile("" : "+s"(sv)); }
        if constexpr (ii == 103) { sv = rfl(rv); asm volatile("" : "+s"(sv)); }
        SB();
        mfa<(n == 0)>(acc[1][c], cur, xb[n % 12][1]); SB();
        if constexpr (ii >= 1 && nx < 162) hi[nx % PD] = trd(WADDR(FN(nx), FC(nx)) + 4608);
        if constexpr (ck1) { chk = mx2(chk, asv4(xb[n + 1][c - 5]).z, asv4(xb[n + 1][c - 5]).w); asm volatile("" : "+v"(chk)); }
        if constexpr (ck2) { chk2 = mx2(chk2, asv4(xb[(k2 >> 1) % 12][k2 & 1]).z, asv4(xb[(k2 >> 1) % 12][k2 & 1]).w); asm volatile("" : "+v"(chk2)); }
        SB();
        if constexpr (ii < 99 && c == 8) { if (RARE(bad != 0)) xfix(IC<2 * n + 2>{}, IC<2 * n + 4>{}); SB(); }
        if constexpr (ii == 100) { if (RARE(bad != 0)) xfix(IC<24>{}, IC<36>{}); SB(); }
        if constexpr (ii < 28 && ii % 2 == 0) { xload(IC<10 + ii / 2>{}); SB(); }
        if constexpr (ii >= 30 && ii < 66 && ii % 3 == 0) { xload(IC<14 + ii / 3>{}); SB(); }
        if constexpr (ii == 18 || ii == 81 || ii == 108 || ii == 159) { post(pa + P_FREE, ii == 18 ? 6 : ii == 81 ? 7 : ii == 108 ? 8 : 9); SB(); }
        if constexpr (ii == 8) STAMP(23);
        if constexpr (ii == 35) STAMP(24);
        if constexpr (ii == 71) STAMP(25);
        if constexpr (ii == 58 || ii == 96) { fv = peek4(L_FULL); SB(); }
        if constexpr (ii == 64) { if (RARE(sv < 7)) wait_full(7); CLOB(); SB(); }
        if constexpr (ii == 97) { rv = peek(L_RDY + 4 * R_LN); SB(); }
        if constexpr (ii == 102) { if (RARE(sv < 10)) wait_full(10); CLOB(); SB(); }
        if constexpr (ii == 104) {
          if (RARE(sv == 0)) wait_word(L_RDY + 4 * R_LN, 1);
          CLOB();
          lc[0] = *(const LAS f32x4*)la; lc[1] = *(const LAS f32x4*)(la + 256);
          SB(); STAMP(10);
        }
        if constexpr (ii >= 108 && n == 14 && c >= 1) { csv = *(const LAS f32x4*)(ca + (c - 1) * 64); SB(); }
        if constexpr (ii >= 108 && n == 15 && (c == 4 || c == 6)) { rv = peek(L_AGR); SB(); }
        if constexpr (ii >= 108 && n == 17 && c >= 1) epi(IC<c - 1>{});
        if constexpr (ii == 150) { fv = peek4(L_FULL); SB(); }
      });
      csv = *(const LAS f32x4*)(ca + 8 * 64);
      if (RARE(min3(fv) < 11)) wait_full(11);
      CLOB();
#pragma unroll
      for (int k = 0; k < PD; ++k) { lo[k] = trd(WADDR(18, k)); hi[k] = trd(WADDR(18, k) + 4608); }
      SB();
      epi(IC<8>{});
      STAMP(11);
    }
    {
      f32x4 acc[2][9];
      unsigned pw = L_PS + 2 * i * 1024 + lane * 16, pw8 = L_PS + 32768 + 2 * i * 512 + lane * 8;
      asm volatile("" : "+v"(pw), "+v"(pw8));
      int po[2];
#pragma unroll
      for (int tt = 0; tt < 2; ++tt) { po[tt] = (2 * i + tt == fr) ? 0x7ff00000 : lane * 8; asm volatile("" : "+v"(po[tt])); }
      const int pdst = OFF_P + (((g * 4 + s) * 8 + 2 * i) * 8 + fr) * PUNIT;
      v4u fv;
      unsigned sv;
      auto epi = [&](auto Cc) INL {
        constexpr int c = Cc.value;
        if constexpr ((c & 1) && c < 8) {
#pragma unroll
          for (int tt = 0; tt < 2; ++tt) {
            const f32x4 a0 = acc[tt][c - 1], a1 = acc[tt][c];
            const v4u v = {pk(a0.x, a0.y), pk(a0.z, a0.w), pk(a1.x, a1.y), pk(a1.z, a1.w)};
            if constexpr (c == 7) ST16(v, 2 * po[tt] , pdst + tt * (8 * PUNIT) + 3 * 1024);
            *(LAS v4u*)(pw + tt * 1024 + (c >> 1) * 8192) = v;
          }
        }
        if constexpr (c == 8) {
#pragma unroll
          for (int tt = 0; tt < 2; ++tt) {
            const f32x4 a0 = acc[tt][8];
            const v2u v = {pk(a0.x, a0.y), pk(a0.z, a0.w)};
            ST8(v, po[tt], pdst + tt * (8 * PUNIT) + 4096);
            *(LAS v2u*)(pw8 + tt * 512) = v;
          }
        }
        if constexpr ((c & 1) && c < 7) post(pa + P_RQ, 3 + c / 2 + 1);
        SB();
      };
      constexpr int HS0 = HS0V, HS1 = HS1V, SIGS = SIGSV, HS2 = HS2V, HS3 = HS3V;
      sfor<162>([&](auto I) INL {
        constexpr int ii = I.value, n = FN(ii), c = FC(ii), nx = ii - 1 + PD;
        constexpr int G = ii == HS0 ? 0 : ii == HS1 ? 1 : ii == HS2 ? 2 : ii == HS3 ? 3 : -1;
        const bf16x8 cur = cat(lo[ii % PD], hi[ii % PD]);
        mfa<(n == 0)>(acc[0][c], cur, hb[n][0]); SB();
        if constexpr (ii >= 1 && nx < 162) lo[nx % PD] = trd(WADDR(18 + FN(nx), FC(nx)));
        if constexpr (G >= 0) { sv = rfl(rv); asm volatile("" : "+s"(sv)); }
        if constexpr (ii == 84 || ii == 102) { sv = min3(fv); asm volatile("" : "+s"(sv)); }
        if constexpr (ii == 106) { sv = min4(fv); asm volatile("" : "+s"(sv)); }
        SB();
        mfa<(n == 0)>(acc[1][c], cur, hb[n][1]); SB();
        if constexpr (ii >= 1 && nx < 162) hi[nx % PD] = trd(WADDR(18 + FN(nx), FC(nx)) + 4608);
        SB();
        if constexpr (ii == SIGS) { VMW(6); SIGNAL(FC, 3 + i); SB(); }
        if constexpr (ii + 4 == HS0 || ii + 4 == HS1 || ii + 4 == HS2 || ii + 4 == HS3) { rv = peek(L_RDY + 4 * R_HH); SB(); }
        if constexpr (G >= 0) {
          const unsigned need = G < 3 ? 1u << G : 8u << i;
          if (RARE((sv & need) != need)) wait_bit(L_RDY + 4 * R_HH, need);
          CLOB(); SB(); STAMP(G == 0 ? 27 : G == 1 ? 28 : G == 2 ? 13 : 14);
        }
        if constexpr (ii >= HS0 && ii < HS0 + 6) { hload(IC<8 + ii - HS0>{}); SB(); }
        if constexpr (ii >= HS1 && ii < HS1 + 6) { hload(IC<14 + ii - HS1>{}); SB(); }
        if constexpr (ii >= HS2 && ii < HS2 + 6) { hload(IC<20 + ii - HS2>{}); SB(); }
        if constexpr (ii >= HS3 && ii < HS3 + 10) { hload(IC<26 + ii - HS3>{}); SB(); }
        if constexpr (ii == 18 || ii == 54) { post(pa + P_FREE, ii == 18 ? 10 : 11); SB(); }
        if constexpr (ii == 79 || ii == 97) { fv = peek4(L_FULL); SB(); }
        if constexpr (ii == 84) { if (RARE(sv < 12)) wait_full(12); CLOB(); SB(); }
        if constexpr (ii == 102) { if (RARE(sv < 13)) wait_full(13); fv = peek4(L_FREE); SB(); STAMP(26); }
        if constexpr (ii == 106) { if (RARE(sv < 11)) wait_free(11); CLOB(); SB(); }
        if constexpr (ii >= 108 && n == 17 && c >= 1) epi(IC<c - 1>{});
      });
      MFMA_DONE();
      epi(IC<8>{});
      STAMP(15);
      asm volatile("s_waitcnt vmcnt(0) lgkmcnt(0)" ::: "memory");
      SIGNAL(FD, 3 + i);
      STAMP(16);
    }
  } else if (w < 7) {
    const int ld = w - 4;
    const unsigned fullw = L_FULL + 4 * ld;
    unsigned v3[3], v4a[9];
#pragma unroll
    for (int e = 0; e < 3; ++e) {
      const int idx = (3 * ld + e) * 64 + lane, row = idx / 18, ch = idx % 18;
      const int sc = (ch + 2 * (s & 1)) % 18;
      v3[e] = row * (F_ * 2) + sc * 16;
    }
#pragma unroll
    for (int e = 0; e < 9; ++e) { const int idx = e * 64 + lane; v4a[e] = (idx / 18) * (D_ * 2) + ((idx % 18 + 2 * (s & 1)) % 18) * 16; }
    const char* w3b = w3 + (576 * fr + 144 * s) * 2;
    const char* w4b = w4 + (size_t)(576 * fr) * (D_ * 2) + 144 * s * 2;
    auto w3slice = [&](int n) INL {
#pragma unroll
      for (int e = 0; e < 3; ++e) dma(w3b + (size_t)n * (32 * F_ * 2), v3[e], (n & 15) * SLOT + (3 * ld + e) * 1024);
    };
    auto w4whole = [&](int m, int u, auto CAP) INL {
      const char* src = w4b + u * (32 * D_ * 2);
#pragma unroll
      for (int e = 0; e < 9; ++e) { dma(src, v4a[e], ((18 + m) & 15) * SLOT + e * 1024); if constexpr (CAP.value) VMW(DCAP); }
    };
    const __amdgpu_buffer_rsrc_t r4 = __builtin_amdgcn_make_buffer_rsrc((void*)w4b, 0, 576 * D_ * 2 - 144 * s * 2, 0x00020000);
    v4u w4r[4][9];
    auto w4get = [&](auto K) INL {
      const int so = dstep(6 + ld + 3 * K.value, s) * (32 * D_ * 2);
#pragma unroll
      for (int e = 0; e < 9; ++e) { constexpr int eo[9] = {0, 2, 4, 6, 8, 1, 3, 5, 7}; w4r[K.value][eo[e]] = __builtin_amdgcn_raw_buffer_load_b128(r4, v4a[eo[e]], so, 0); }
      CLOB(); SB();
    };
    auto w4put = [&](auto K) INL {
      const unsigned a = ((24 + ld + 3 * K.value) & 15) * SLOT + lane * 16;
#pragma unroll
      for (int e = 0; e < 9; ++e) *(LAS v4u*)(a + e * 1024) = w4r[K.value][e];
      CLOB(); SB();
    };
    f32x4 cs[3] = {{0, 0, 0, 0}, {0, 0, 0, 0}, {0, 0, 0, 0}};
    const unsigned cb = (4 * q + (t >> 2)) * 288 + (t & 3) * 8 + ld * 96;
    auto colsum3 = [&](int n0, int n1) INL {
      __builtin_amdgcn_s_setprio(1);
      NOUNROLL for (int n = n0; n < n1; n += 2) {
        const unsigned a0 = cb + (n & 15) * SLOT;
        s16x4 l[6], h[6];
#pragma unroll
        for (int k = 0; k < 6; ++k) { l[k] = trd(a0 + (k / 3) * SLOT + (k % 3) * 32); h[k] = trd(a0 + (k / 3) * SLOT + 4608 + (k % 3) * 32); }
        SB();
#pragma unroll
        for (int k = 0; k < 6; ++k) cs[k % 3] = mfma(cat(l[k], h[k]), ones, cs[k % 3]);
      }
      __builtin_amdgcn_s_setprio(0);
      CLOB();
    };
    if (ab) {
      const unsigned v1 = (8 * (lane >> 5) + ((lane >> 3) & 3)) * (D_ * 2) + (((lane & 7) ^ ((q & 3) << 1)) << 4);
      auto w1units = [&](int u0) INL {
#pragma unroll
        for (int e = 0; e < 8; ++e) {
          const int u = u0 + 3 * (e >> 2);
          dma(w1 + 128 * ct + (32 * u + 16 * (e & 1) + 4 * ((e >> 1) & 1)) * (D_ * 2), v1, u * 4096 + (e & 3) * 1024);
        }
      };
      w1units(ld); w1units(ld + 6);
      VMW(8); lds_set(fullw, 1, lane);
      w1units(ld + 12);
      VMW(8); lds_set(fullw, 2, lane);
      STAMP(1);
      DRAIN(); lds_set(fullw, 3, lane);
      wait_free(2);
      const unsigned v2 = (lane >> 3) * (D_ * 2) + (((lane & 7) ^ ((q & 3) << 1)) << 4);
      NOUNROLL for (int u = ld; u < 18; u += 3) {
#pragma unroll
        for (int e = 0; e < 4; ++e) dma(w2 + 128 * ct + (32 * u + 8 * e) * (D_ * 2), v2, 8 * SLOT + u * 4096 + e * 1024);
      }
      if (!(CO & 2)) w4get(IC<0>{});
      STAMP(2);
      if (CO & 2) DRAIN(); else VMW(9);
      lds_set(fullw, 4, lane);
      STAMP(3);
      wait_free(3);
      NOUNROLL for (int n = 0; n < 8; ++n) w3slice(n);
      STAMP(4);
      DRAIN(); lds_set(fullw, 6, lane);
      wait_full(6);
      colsum3(0, 8);
      wait_free(4);
      NOUNROLL for (int n = 8; n < 11; ++n) w3slice(n);
      wait_free(5);
      NOUNROLL for (int n = 11; n < 16; ++n) w3slice(n);
      if (CO & 2) w4get(IC<0>{}); else w4get(IC<1>{});
      STAMP(5);
      VMW(9); lds_set(fullw, 7, lane);
      STAMP(6);
      wait_full(7);
      colsum3(8, 16);
    } else {
      __builtin_amdgcn_s_sleep(SL2);
      NOUNROLL for (int n = 0; n < 16; ++n) w3slice(n);
      DRAIN(); lds_set(fullw, 7, lane);
      w4get(IC<0>{}); if (!(CO & 2)) w4get(IC<1>{});
      wait_full(7);
      colsum3(0, 16);
    }
    lds_set(fullw, 8, lane);
    STAMP(7);
    wait_free(6); wait_full(8);
    STAMP(11);
    w3slice(16); w3slice(17);
    DRAIN(); lds_set(fullw, 9, lane);
    STAMP(12);
    wait_full(9);
    colsum3(16, 18);
    if (t == 0) {
#pragma unroll
      for (int c = 0; c < 3; ++c) *(LAS f32x4*)(L_CS3 + (3 * ld + c) * 64 + q * 16) = cs[c];
    }
    CLOB();
    lds_set(fullw, 10, lane);
    STAMP(8);
    if (PRIO & 2) __builtin_amdgcn_s_setprio(2);
    const u64 tag = get_tag();
    auto hround = [&](int p) INL {
      wait_min4(L_RQ, p + 1);
      v4u d[8];
#pragma unroll
      for (int k = 0; k < 8; ++k) d[k] = *(const LAS v4u*)(L_HS + k * 1024 + lane * 16);
      CLOB();
      lds_set(L_AGR, p + 1, lane);
#pragma unroll
      for (int k = 0; k < 8; ++k) ST16(d[k], lane * 16 + k * (144 * 1024), OFF_HH + (8 * g * 144 + fr * 18 + pbase(s) + p) * 1024);
      CLOB();
    };
    auto pround = [&](int p) INL {
      wait_min4(L_RQ, 3 + p + 1);
      const int so = OFF_P + ((g * 4 + s) * 64 + fr) * PUNIT + p * 1024;
      v4u d[7];
#pragma unroll
      for (int k = 0; k < 7; ++k) d[k] = *(const LAS v4u*)(L_PS + p * 8192 + ((fr + 1 + k) & 7) * 1024 + lane * 16);
#pragma unroll
      for (int k = 0; k < 7; ++k) ST16(d[k], lane * 16, so + ((fr + 1 + k) & 7) * (8 * PUNIT));
      DRAIN();
      SIGNAL(FD, p);
    };
    wait_free(7);
    NOUNROLL for (int m = ld; m < 6; m += 3) w4whole(m, dstep(m, s), IC<0>{});
    if (CO & 2) w4get(IC<1>{});
    if (!LATE || ld > 0) { w4get(IC<2>{}); w4get(IC<3>{}); }
    if (ld > 0) { wait_free(8); w4put(IC<0>{}); DRAIN(); lds_set(fullw, 11, lane); }
    hround(ld);
    DRAIN();
    SIGNAL(FC, ld);
    if (LATE && ld == 0) { w4get(IC<2>{}); w4get(IC<3>{}); }
    if (ld == 0) { w4put(IC<0>{}); w4put(IC<1>{}); }
    lds_set(fullw, 11, lane);
    STAMP(13);
    wait_free(9); wait_full(10);
    if (ld > 0) w4put(IC<1>{});
    w4put(IC<2>{});
    if (ld == 0) w4put(IC<3>{});
    lds_set(fullw, 12, lane);
    if (ld > 0) { wait_free(10); w4put(IC<3>{}); }
    lds_set(fullw, 13, lane);
    STAMP(10);
    pround(ld);
    STAMP(23);
  } else {
    if (PRIO & 1) __builtin_amdgcn_s_setprio(3);
    const u64 tag = __hip_atomic_load((GAS u64*)(ws + OFF_EPOCH), __ATOMIC_RELAXED, __HIP_MEMORY_SCOPE_AGENT) + 1ull;
    if (lane == 0) {
      *(volatile LAS u64*)L_TAG = tag;
      *(volatile LAS unsigned*)(L_RDY + 4 * R_TAG) = 1u;
      __hip_atomic_store(flagp(ws, FS, b, 0), tag, __ATOMIC_RELAXED, __HIP_MEMORY_SCOPE_AGENT);
    }
    {
      const v4u e4 = {EMPTY, EMPTY, EMPTY, EMPTY};
      if (ab) {
        wait_free(3);
#pragma unroll
        for (int k = 0; k < 8; ++k) ST16(e4, lane * 16 + (k & 1) * 1024 + (k >> 1) * (18 * 1024), OFF_X1 + (4 * rt * 18 + 2 * ct) * 1024);
        if (lane < 32) ST16(e4, OFF_STAT + (lane >> 3) * (9 * 128) + (lane & 7) * 16, (4 * rt * 9 + ct) * 128);
        DRAIN();
        lds_set(L_RDY + 4 * R_Z, 1, lane);
      }
    }
    STAMP(1);
    auto poll = [&](GAS u64* p) INL {
      NOUNROLL for (unsigned n = 0; n < GLB_CAP; ++n) {
        const u64 v = __hip_atomic_load(p, __ATOMIC_RELAXED, __HIP_MEMORY_SCOPE_AGENT);
        if (__all(v == tag)) break;
        __builtin_amdgcn_s_sleep(1);
      }
      CLOB();
    };
    auto poll_bits = [&](auto TWO, GAS u64* p0, GAS u64* p1, int word, u64 ma, u64 mb, u64 mc, u64 md) INL {
      unsigned done = 0;
      NOUNROLL for (unsigned n = 0; n < GLB_CAP; ++n) {
        const u64 v0 = __hip_atomic_load(p0, __ATOMIC_RELAXED, __HIP_MEMORY_SCOPE_AGENT);
        u64 v1 = v0;
        if constexpr (TWO.value) v1 = __hip_atomic_load(p1, __ATOMIC_RELAXED, __HIP_MEMORY_SCOPE_AGENT);
        const u64 bad0 = __ballot(v0 != tag), bad1 = __ballot(v1 != tag);
        const unsigned now = ((bad0 & ma) ? 0u : 1u) | ((bad0 & mb) ? 0u : 2u) | ((bad1 & mc) ? 0u : 4u) | ((bad1 & md) ? 0u : 8u);
        if (now != done) { done = now; lds_set(L_RDY + 4 * word, now, lane); }
        if (now == 15u) break;
        __builtin_amdgcn_s_sleep(1);
      }
      lds_set(L_RDY + 4 * word, 15u, lane);
      CLOB();
    };
    constexpr u64 M01 = 0x3333333333333333ull, M23 = 0xccccccccccccccccull;
    const int pl = (lane % 36) >> 2;
    if (ab) poll_bits(IC<0>{}, flagp(ws, FA, j + 8 * ((r & 1) + 2 * pl), lane & 3), nullptr, R_H, M01, M23, M01, M23);
    STAMP(2);
    poll_bits(IC<1>{}, flagp(ws, FB, g + 16 * pl, lane & 3), flagp(ws, FB, g + 8 + 16 * pl, lane & 3), R_X, M01, M23, M01, M23);
    STAMP(3);
    {
      v4u st[9];
      NOUNROLL for (unsigned n = 0; n < TRY_CAP; ++n) { CLOB();
        unsigned m = 0;
#pragma unroll
        for (int k = 0; k < 9; ++k) st[k] = LD16(OFF_STAT + ((lane >> 3) * 9 + k) * 128 + (lane & 7) * 16, g * (8 * 9 * 128));
#pragma unroll
        for (int k = 0; k < 9; ++k) m = mx4(m, st[k]);
        if (!missing(m)) break;
      }
      f32x4 a = __builtin_bit_cast(f32x4, st[0]);
#pragma unroll
      for (int k = 1; k < 9; ++k) a += __builtin_bit_cast(f32x4, st[k]);
      *(LAS f32x4*)(L_LN + lane * 32) = ln_consts(a.x, a.y);
      *(LAS f32x4*)(L_LN + lane * 32 + 16) = ln_consts(a.z, a.w);
      CLOB();
      lds_set(L_RDY + 4 * R_LN, 1, lane);
    }
    if (b == 0) {
      NOUNROLL for (int k = 0; k < 4; ++k) poll(flagp(ws, FS, k * 64 + lane, 0));
      if (lane == 0) __hip_atomic_store((GAS u64*)(ws + OFF_EPOCH), tag, __ATOMIC_RELAXED, __HIP_MEMORY_SCOPE_AGENT);
    }
    auto poll5 = [&](GAS u64* p0, int word, auto NB) INL {
      constexpr unsigned all = (1u << NB.value) - 1u;
      unsigned done = 0;
      NOUNROLL for (unsigned n = 0; n < GLB_CAP; ++n) {
        const u64 bad = __ballot(__hip_atomic_load(p0, __ATOMIC_RELAXED, __HIP_MEMORY_SCOPE_AGENT) != tag);
        unsigned now = 0;
#pragma unroll
        for (int k = 0; k < NB.value; ++k) now |= (bad & (0x0101010101010101ull << k)) ? 0u : 1u << k;
        if (now != done) { done = now; lds_set(L_RDY + 4 * word, now, lane); }
        if (now == all) break;
        __builtin_amdgcn_s_sleep(1);
      }
      lds_set(L_RDY + 4 * word, all, lane);
    };
    poll5(flagp(ws, FC, j + 8 * ((r & ~3) + ((lane >> 3) & 3)), lane & 7), R_HH, IC<7>{});
    STAMP(4);
    const int wr = lane >> 3;
    poll5(flagp(ws, FD, MAP ? (j & 1) + (wr & 6) + 8 * (((wr & 1) << 4) + (r & 15)) : wr + 8 * r, (lane & 7) < 3 ? (lane & 7) : 3 + (fr >> 1)), R_P, IC<5>{});
    STAMP(5);
  }
  if (w < 2 || (w >= 4 && w < 7)) {
    const int p = w < 2 ? w + 3 : w - 4;
    const int T = 8 * g + fr;
    const int u = p < 4 ? pbase(s) + p : (s < 2 ? 4 : 13);
    v4u xv;
    NOUNROLL for (unsigned n = 0; n < TRY_CAP; ++n) { CLOB(); xv = LD16(lane * 16, OFF_X1 + (T * 18 + u) * 1024); if (!missing(mx4(0u, xv))) break; }
    const float rstd = *(const LAS float*)(L_LN + (16 * fr + t) * 16 + 12);
    wait_bit(L_RDY + 4 * R_P, 1u << p);
    STAMP(20);
    const int psrc = OFF_P + ((g * 4 + s) * 8 + fr) * (8 * PUNIT) + p * 1024;
    char* orow = out + (size_t)(16 * T + t) * (D_ * 2) + (32 * u + 4 * q) * 2;
    if (p < 4) {
      v4u pv[8];
#pragma unroll
      for (int k = 1; k < 8; ++k) pv[k] = LD16(lane * 16, psrc + ((fr + k) & 7) * PUNIT);
      pv[0] = *(const LAS v4u*)(L_PS + p * 8192 + fr * 1024 + lane * 16);
      float a[8];
#pragma unroll
      for (int e = 0; e < 8; ++e) a[e] = 0.f;
#pragma unroll
      for (int k = 0; k < 8; ++k)
#pragma unroll
        for (int e = 0; e < 4; ++e) { const unsigned d = pv[k][e]; a[2 * e] += lo16(d); a[2 * e + 1] += hi16(d); }
      unsigned o[4];
#pragma unroll
      for (int e = 0; e < 4; ++e) {
        const unsigned h2 = pk(a[2 * e] * rstd, a[2 * e + 1] * rstd), xe = xv[e];
        o[e] = pk(lo16(xe) + lo16(h2), hi16(xe) + hi16(h2));
      }
      *(GAS v2u*)orow = v2u{o[0], o[1]};
      *(GAS v2u*)(orow + 32) = v2u{o[2], o[3]};
    } else {
      v2u pv[8];
#pragma unroll
      for (int k = 1; k < 8; ++k) pv[k] = LD8(lane * 8, psrc + ((fr + k) & 7) * PUNIT);
      pv[0] = *(const LAS v2u*)(L_PS + 32768 + fr * 512 + lane * 8);
      float a[4] = {0.f, 0.f, 0.f, 0.f};
#pragma unroll
      for (int k = 0; k < 8; ++k) { a[0] += lo16(pv[k].x); a[1] += hi16(pv[k].x); a[2] += lo16(pv[k].y); a[3] += hi16(pv[k].y); }
      const unsigned x0 = (s & 1) ? xv.z : xv.x, x1v = (s & 1) ? xv.w : xv.y;
      const unsigned h0 = pk(a[0] * rstd, a[1] * rstd), h1 = pk(a[2] * rstd, a[3] * rstd);
      *(GAS v2u*)(orow + (s & 1) * 32) = v2u{pk(lo16(x0) + lo16(h0), hi16(x0) + hi16(h0)), pk(lo16(x1v) + lo16(h1), hi16(x1v) + hi16(h1))};
    }
    STAMP(21);
  }
#endif
}
}

extern "C" void kernel_launch(void* const* d_in, const int* in_sizes, int n_in, void* d_out, int out_size, void* d_ws, size_t ws_size,
                              hipStream_t stream) {
  mx::mixer_kernel<<<256, 512, 0, stream>>>((const char*)d_in[0], (const char*)d_in[1], (const char*)d_in[2], (const char*)d_in[3],
                                            (const char*)d_in[4], (char*)d_out, (char*)d_ws);
}
```

```cpp
#include <hip/hip_runtime.h>
#include <utility>

#define STAMPS 0
#define HS0V 10
#define HS1V 36
#define HS2V 62
#define SIGSV 30
#define HS3V 90
#define LATE 0
#define PRIO 1
#define CO 3
#define MAP 1
#define SL1 32
#define SL2 64

#define LAS __attribute__((address_space(3)))
#define GAS __attribute__((address_space(1)))
#define DI __device__ __forceinline__
typedef unsigned v4u __attribute__((ext_vector_type(4)));
typedef unsigned v2u __attribute__((ext_vector_type(2)));
typedef float f32x4 __attribute__((ext_vector_type(4)));
typedef float f32x2 __attribute__((ext_vector_type(2)));
typedef short bf16x8 __attribute__((ext_vector_type(8)));
typedef short s16x4 __attribute__((ext_vector_type(4)));
typedef __bf16 bf16x2_t __attribute__((ext_vector_type(2)));
typedef unsigned long long u64;

namespace mx {
constexpr int D_ = 576, F_ = 4608;
constexpr int OFF_EPOCH = 0;
constexpr int OFF_FLAGS = 4096;
constexpr int OFF_STAT = 344064;
constexpr int OFF_H1 = 524288;
constexpr int OFF_X1 = OFF_H1 + 64 * 18 * 1024;
constexpr int OFF_HH = OFF_X1 + 64 * 18 * 1024;
constexpr int PUNIT = 4608;
constexpr int OFF_P = OFF_HH + 64 * 144 * 1024;
constexpr int OFF_DBG = OFF_P + 8 * 4 * 8 * 8 * PUNIT;
constexpr int WS_BYTES = OFF_DBG + 2 * 256 * 8 * 32 * 8 + 256;
enum { FS = 0, FA = 1, FB = 2, FC = 3, FD = 4 };
constexpr int SLOT = 9216, MISC = 16 * SLOT;
constexpr int L_FULL = MISC, L_FREE = MISC + 16, L_RQ = MISC + 32, L_RDY = MISC + 48  , L_SY = MISC + 96, L_AGR = MISC + 112, L_TAG = MISC + 120;
constexpr int L_DUM = MISC + 128  , L_CS1 = MISC + 512  , L_CS3 = MISC + 1024  , L_LN = MISC + 2048  ,
              L_HS = MISC + 4096  , L_LN1 = L_HS  , L_DBG = MISC + 12288, L_PS = 4 * SLOT  ;
enum { R_H = 0, R_X = 1, R_HH = 2, R_P = 3, R_TAG = 4, R_LN = 5, R_Z = 6 };
constexpr unsigned EMPTY = 0xffffffffu;
constexpr unsigned TRY_CAP = 1u << 12;
constexpr unsigned LDS_CAP = 1u << 21, GLB_CAP = 1u << 16;
#define DCAPV 6
constexpr int PD = 4, DCAP = DCAPV;

template <class F, int... I> DI void sfor_impl(F&& f, std::integer_sequence<int, I...>) { (f(std::integral_constant<int, I>{}), ...); }
template <int N, class F> DI void sfor(F&& f) { sfor_impl(f, std::make_integer_sequence<int, N>{}); }
template <int V> using IC = std::integral_constant<int, V>;
#define INL __attribute__((always_inline))
#define NOUNROLL _Pragma("nounroll")
#define RARE(x) __builtin_expect(!!(x), 0)
#define SB() __builtin_amdgcn_sched_barrier(0)
#define CLOB() asm volatile("" ::: "memory")
#define DRAIN() asm volatile("s_waitcnt vmcnt(0)" ::: "memory")
#define VMW(n) asm volatile("s_waitcnt vmcnt(%0)" ::"i"(n) : "memory")

DI unsigned rfl(unsigned v) { return __builtin_amdgcn_readfirstlane(v); }
DI f32x4 mfma(bf16x8 a, bf16x8 b, f32x4 c) { return __builtin_amdgcn_mfma_f32_16x16x32_bf16(a, b, c, 0, 0, 0); }
template <bool FIRST> DI void mfa(f32x4& c, bf16x8 a, bf16x8 b) {
  if constexpr (FIRST) asm volatile("v_mfma_f32_16x16x32_bf16 %0, %1, %2, 0" : "=&v"(c) : "v"(a), "v"(b));
  else asm volatile("v_mfma_f32_16x16x32_bf16 %0, %1, %2, %0" : "+v"(c) : "v"(a), "v"(b));
}
#define MFMA_DONE() asm volatile("s_nop 7\n\ts_nop 3" ::: "memory")
DI bf16x8 asfrag(v4u v) { return __builtin_bit_cast(bf16x8, v); }
DI s16x4 trd(unsigned addr) { return __builtin_amdgcn_ds_read_tr16_b64_v4i16((LAS s16x4*)addr); }
DI bf16x8 cat(s16x4 lo, s16x4 hi) { return __builtin_shufflevector(lo, hi, 0, 1, 2, 3, 4, 5, 6, 7); }
DI unsigned pk(float a, float b) { bf16x2_t r = __builtin_convertvector(f32x2{a, b}, bf16x2_t); return __builtin_bit_cast(unsigned, r); }
DI unsigned pkc(float a, float b) { return min(pk(a, b), EMPTY - 1u); }
DI unsigned mx2(unsigned m, unsigned a, unsigned b) { return max(max(m, a), b); }
DI unsigned mx4(unsigned m, v4u d) { return mx2(mx2(m, d.x, d.y), d.z, d.w); }
DI bool missing(unsigned m) { return __any(m == EMPTY); }
DI v4u asv4(bf16x8 f) { return __builtin_bit_cast(v4u, f); }
DI float lo16(unsigned p) { return __uint_as_float(p << 16); }
DI float hi16(unsigned p) { return __uint_as_float(p & 0xffff0000u); }
DI float bfr(float a) { return lo16(pk(a, a)); }
DI float qsum(float v) {
  auto r = __builtin_amdgcn_permlane16_swap(__float_as_uint(v), __float_as_uint(v), false, false);
  float a = __uint_as_float(r[0]) + __uint_as_float(r[1]);
  auto r2 = __builtin_amdgcn_permlane32_swap(__float_as_uint(a), __float_as_uint(a), false, false);
  return __uint_as_float(r2[0]) + __uint_as_float(r2[1]);
}
constexpr float GA = -2.302208198144325f;
constexpr float GB = -0.1029432397357593f;
DI f32x4 ln_consts(float s, float ss) {
  const float m = s * (1.0f / D_);
  float var = __builtin_fmaf(-m, m, ss * (1.0f / D_));
  var = var > 0.f ? var : 0.f;
  const float sd = bfr(__builtin_amdgcn_sqrtf(bfr(bfr(var) + 1e-5f)));
  const float r = __builtin_amdgcn_rcpf(sd);
  return f32x4{-bfr(m), GA * r, GB * r * r * r, r};
}
template <int N> DI void geluN(f32x2 (&z)[N], const float (&a)[N], const float (&b)[N]) {
  f32x2 p[N];
#pragma unroll
  for (int k = 0; k < N; ++k) p[k] = z[k] * z[k];
  SB();
#pragma unroll
  for (int k = 0; k < N; ++k) p[k] = p[k] * b[k] + a[k];
  SB();
#pragma unroll
  for (int k = 0; k < N; ++k) p[k] = z[k] * p[k];
  SB();
#pragma unroll
  for (int k = 0; k < N; ++k) { p[k].x = __builtin_amdgcn_exp2f(p[k].x); p[k].y = __builtin_amdgcn_exp2f(p[k].y); }
  SB();
#pragma unroll
  for (int k = 0; k < N; ++k) p[k] = p[k] + 1.0f;
  SB();
#pragma unroll
  for (int k = 0; k < N; ++k) { p[k].x = __builtin_amdgcn_rcpf(p[k].x); p[k].y = __builtin_amdgcn_rcpf(p[k].y); }
  SB();
#pragma unroll
  for (int k = 0; k < N; ++k) z[k] = z[k] * p[k];
  SB();
}

DI unsigned ldsw(int addr) { return *(volatile LAS unsigned*)addr; }
DI v4u lds4(int addr) { return *(volatile LAS v4u*)addr; }
DI void lds_set(int addr, unsigned v, int lane) { if (lane == 0) *(volatile LAS unsigned*)addr = v; }
DI v4u peek4(int addr) { CLOB(); return *(const LAS v4u*)addr; }
DI unsigned peek(int addr) { CLOB(); return *(const LAS unsigned*)addr; }
DI void post(unsigned addr, unsigned v) { CLOB(); *(LAS unsigned*)addr = v; CLOB(); }
DI void wait_word(int addr, unsigned v) {
  NOUNROLL for (unsigned n = 0; n < LDS_CAP; ++n) { if (rfl(ldsw(addr)) >= v) break; __builtin_amdgcn_s_sleep(1); }
  CLOB();
}
DI unsigned min3(v4u x) { return rfl(min(min(x.x, x.y), x.z)); }
DI unsigned min4(v4u x) { return rfl(min(min(x.x, x.y), min(x.z, x.w))); }
DI void wait_full(unsigned v) {
  NOUNROLL for (unsigned n = 0; n < LDS_CAP; ++n) { if (min3(lds4(L_FULL)) >= v) break; __builtin_amdgcn_s_sleep(1); }
  CLOB();
}
DI void wait_min4(int addr, unsigned v) {
  NOUNROLL for (unsigned n = 0; n < LDS_CAP; ++n) { if (min4(lds4(addr)) >= v) break; __builtin_amdgcn_s_sleep(1); }
  CLOB();
}
DI void wait_free(unsigned v) { wait_min4(L_FREE, v); }
DI void wait_bit(int addr, unsigned bit) {
  NOUNROLL for (unsigned n = 0; n < LDS_CAP; ++n) { if ((rfl(ldsw(addr)) & bit) == bit) break; __builtin_amdgcn_s_sleep(1); }
  CLOB();
}
DI GAS u64* flagp(char* ws, int kind, int b, int wv) { return (GAS u64*)(ws + OFF_FLAGS + (kind * 256 + b) * 128 + wv * 8); }
DI u64 get_tag() {
  wait_word(L_RDY + 4 * R_TAG, 1);
  return ((u64)rfl(ldsw(L_TAG + 4)) << 32) | rfl(ldsw(L_TAG));
}
#define LD16(off, soff) __builtin_amdgcn_raw_buffer_load_b128(rs, (off), (soff), 16)
#define LD8(off, soff) __builtin_amdgcn_raw_buffer_load_b64(rs, (off), (soff), 16)
#define ST16(v, off, soff) do { v4u d_ = (v); asm volatile("" : "+v"(d_)); __builtin_amdgcn_raw_buffer_store_b128(d_, rs, (off), (soff), 16); asm volatile("s_nop 1" ::"v"(d_) : "memory"); } while (0)
#define ST8(v, off, soff) __builtin_amdgcn_raw_buffer_store_b64((v), rs, (off), (soff), 16)
#define SIGNAL(kind, wv) do { CLOB(); ST8((v2u{(unsigned)tag, (unsigned)(tag >> 32)}), lane ? 0x7ffffff0 : 0, OFF_FLAGS + ((kind) * 256 + b) * 128 + (wv) * 8); CLOB(); } while (0)
DI void dma(const char* sbase, unsigned voff, unsigned ldsaddr) {
  asm volatile("s_mov_b32 m0, %0\n\ts_nop 0\n\tglobal_load_lds_dwordx4 %1, %2" ::"s"(ldsaddr), "v"(voff), "s"(sbase) : "memory");
}
DI void dmac(const char* sbase, unsigned voff, unsigned ldsaddr) { dma(sbase, voff, ldsaddr); VMW(DCAP); }
DI int pbase(int s) { return (9 * s + (s & 1)) >> 1; }
DI int dstep(int M, int s) {
  if (M < 4) return pbase(s) + M;
  else if (M < 16) return pbase((s + 1 + (M - 4) % 3) & 3) + (M - 4) / 3;
  else return M == 16 ? 4 : 13;
}
#define STAMP(k) do { } while (0)
#define COUNT(k) do { } while (0)

__global__ __launch_bounds__(512, 2) void mixer_kernel(const char* __restrict__ x, const char* __restrict__ w1, const char* __restrict__ w2,
                                                       const char* __restrict__ w3, const char* __restrict__ w4, char* __restrict__ out, char* ws) {
#if defined(__HIP_DEVICE_COMPILE__)
  __shared__ __attribute__((aligned(16))) char smem[163840];
  const int tid = threadIdx.x, lane = tid & 63, w = rfl(tid >> 6);
  const int t = lane & 15, q = lane >> 4;
  const int b = blockIdx.x, j = b & 7, r = b >> 3;
  const bool ab = r < 18;
  const int rt = 2 * j + (r & 1), ct = r >> 1;
  const int g = MAP ? 4 * (j & 1) + ((r >> 2) & 3) : r >> 2, fr = MAP ? (j & 6) + (r >> 4) : j, s = r & 3;
  const __amdgpu_buffer_rsrc_t rs = __builtin_amdgcn_make_buffer_rsrc(ws, 0, WS_BYTES, 0x00020000);
  const bf16x8 ones = {0x3F80, 0x3F80, 0x3F80, 0x3F80, 0x3F80, 0x3F80, 0x3F80, 0x3F80};
  asm volatile("" ::"v"(smem));
  STAMP(0);
  if (tid < 32) *(volatile LAS unsigned*)(MISC + tid * 4) = 0u;
  asm volatile("s_waitcnt lgkmcnt(0)\n\ts_barrier" ::: "memory");
  if (ab) {
    if (w < 4) {
      const char* xsrc = x + (size_t)(4 * rt + w) * (16 * D_ * 2);
#pragma unroll
      for (int e = 0; e < 18; ++e) {
        const unsigned f = (((unsigned)(lane + 64 * e) * 911u) >> 17) & 7u;
        dma(xsrc + e * 1024, (lane * 16) ^ (f << 4), (8 + 2 * w) * SLOT + e * 1024);
      }
    }
  }

  if (w < 4) {
    const int i = w;
    unsigned pa = lane ? L_DUM + 4 * lane : L_FREE + 4 * i;
    asm volatile("" : "+v"(pa));
    const u64 tag = get_tag();
    constexpr int P_FREE = 0, P_RQ = L_RQ - L_FREE, P_SY = L_SY - L_FREE;
    if (ab) {
      unsigned ab4[4];
#pragma unroll
      for (int c = 0; c < 4; ++c) ab4[c] = (4 * q + (t >> 2)) * 128 + ((c ^ ((2 * q + (t >> 3)) & 3)) << 5) + (t & 3) * 8;
      const int T = 4 * rt + i;
      v2u xres[4];
      {
        const int tp = i >> 1, ch = i & 1;
        const unsigned xa = (8 + 2 * i) * SLOT + t * (D_ * 2), xs = ((t >> 1) & 7) << 4;
        unsigned xe[2][2], wb[2][2];
#pragma unroll
        for (int e = 0; e < 2; ++e) {
          xe[0][e] = xa + ((64 * (e ^ tp) + q * 16) ^ xs);
          xe[1][e] = xe[0][e] + (ch ? -2 * SLOT : 2 * SLOT);
          wb[e][0] = (ch ? ab4[2] : ab4[0]) + (e ^ tp) * 4096;
          wb[e][1] = (ch ? ab4[3] : ab4[1]) + (e ^ tp) * 4096;
          asm volatile("" : "+v"(xe[0][e]), "+v"(xe[1][e]), "+v"(wb[e][0]), "+v"(wb[e][1]));
        }
        int dst[4];
        unsigned keep[4], dm[4];
#pragma unroll
        for (int k = 0; k < 4; ++k) {
          const int gt = 4 * ct + 2 * ch + (k & 1), grp = gt / 9;
          const int sh = grp == 0 ? 1 : grp == 1 ? -1 : grp == 2 ? 2 : -2;
          const int ts = 16 * (ch ^ (k >> 1)) + t + sh;
          keep[k] = (ts >= 0 && ts < 32) ? 0xffffffffu : 0u;
          dst[k] = OFF_H1 + ((T & ~1) + ((ts >> 4) & 1)) * (18 * 1024) + ((ts & 15) + 16 * q) * 16 + (k & 1) * 8;
          dm[k] = (q == (t >> 2) && k == (t & 3)) ? 0xffffffffu : 0u;
          asm volatile("" : "+v"(dst[k]), "+v"(keep[k]), "+v"(dm[k]));
        }
        DRAIN();
        post(pa + P_FREE, 1);
        wait_word(L_FREE + 4 * (i ^ 1), 1);
        STAMP(1);
        bf16x8 xf[2][18];
        f32x4 aS = {0, 0, 0, 0}, aG = {0, 0, 0, 0};
#pragma unroll
        for (int p = 0; p < 6; ++p) xf[0][p] = *(const LAS bf16x8*)(xe[0][p & 1] + (p >> 1) * 128);
        SB();
        sfor<18>([&](auto P) INL {
          constexpr int p = P.value;
          aS = mfma(ones, xf[0][p], aS); SB();
          if constexpr (p + 6 < 18) xf[0][p + 6] = *(const LAS bf16x8*)(xe[0][p & 1] + ((p + 6) >> 1) * 128);
          else if constexpr (p + 6 < 22) xres[p - 12] = *(const LAS v2u*)(xa + 128 * ct + ((32 * (p - 12) + 8 * q) ^ xs));
          SB();
          aG = mfma(xf[0][p], xf[0][p], aG); SB();
          xf[1][p] = *(const LAS bf16x8*)(xe[1][p & 1] + (p >> 1) * 128);
          SB();
        });
        CLOB();
        post(pa + P_FREE, 2);
        f32x4 lc[2];
        {
          const float gd = __uint_as_float(((__float_as_uint(aG.x) & dm[0]) | (__float_as_uint(aG.y) & dm[1])) | ((__float_as_uint(aG.z) & dm[2]) | (__float_as_uint(aG.w) & dm[3])));
          lc[0] = ln_consts(aS[0], qsum(gd));
          if (q == 0) *(LAS f32x4*)(L_LN1 + i * 256 + t * 16) = lc[0];
          CLOB();
          post(pa + P_SY, 1);
        }
        SB();
        STAMP(17);
        wait_full(1);
        constexpr int PA = 4;
        s16x4 wl[PA], wh[PA];
#define AADDR(k) (wb[((k) >> 1) & 1][(k) & 1] + ((k) >> 2) * 8192)
#pragma unroll
        for (int k = 0; k < PA; ++k) { wl[k] = trd(AADDR(k)); wh[k] = trd(AADDR(k) + 2048); }
        bf16x8 one2 = ones;
        asm volatile("" : "+v"(one2));
        SB();
        f32x4 acc[2][2], cs[2];
        v4u fv;
        sfor<36>([&](auto K) INL {
          constexpr int k = K.value, p = k >> 1, l = k & 1, nx = k - 1 + PA;
          const bf16x8 cur = cat(wl[k % PA], wh[k % PA]);
          mfa<(p == 0)>(acc[0][l], cur, xf[0][p]); SB();
          if constexpr (nx == 12 || nx == 24) { if (RARE(min3(fv) < nx / 12 + 1)) wait_full(nx / 12 + 1); CLOB(); SB(); }
          if constexpr (k >= 1 && nx < 36) wl[nx % PA] = trd(AADDR(nx));
          SB();
          mfa<(p == 0)>(acc[1][l], cur, xf[1][p]); SB();
          if constexpr (k >= 1 && nx < 36) wh[nx % PA] = trd(AADDR(nx) + 2048);
          if constexpr (nx == 9 || nx == 21) fv = peek4(L_FULL);
          SB();
          if constexpr ((p & 1) == 0) { mfa<(p == 0)>(cs[l], cur, one2); SB(); }
        });
        MFMA_DONE();
        if (t == 0) { *(LAS f32x4*)(L_CS1 + i * 128 + q * 16) = cs[0]; *(LAS f32x4*)(L_CS1 + i * 128 + 64 + q * 16) = cs[1]; }
        CLOB();
        post(pa + P_SY, 2);
        STAMP(2);
        wait_word(L_SY + 4 * (i ^ 1), 1);
        lc[1] = *(const LAS f32x4*)(L_LN1 + (i ^ 1) * 256 + t * 16);
        wait_word(L_SY + 4 * (i ^ 2), 2);
        const f32x4 o0 = *(const LAS f32x4*)(L_CS1 + (i ^ 2) * 128 + q * 16), o1 = *(const LAS f32x4*)(L_CS1 + (i ^ 2) * 128 + 64 + q * 16);
        cs[0] += o0; cs[1] += o1;
        f32x2 z[8];
        float ga8[8], gb8[8];
#pragma unroll
        for (int k = 0; k < 4; ++k) {
          const f32x4 zz = cs[k & 1] * lc[k >> 1].x + acc[k >> 1][k & 1];
          z[2 * k] = f32x2{zz.x, zz.y}; z[2 * k + 1] = f32x2{zz.z, zz.w};
          ga8[2 * k] = ga8[2 * k + 1] = lc[k >> 1].y; gb8[2 * k] = gb8[2 * k + 1] = lc[k >> 1].z;
        }
        SB();
        geluN<8>(z, ga8, gb8);
        unsigned hp[8];
#pragma unroll
        for (int k = 0; k < 8; ++k) { const f32x2 v = z[k] * lc[k >> 2].w; hp[k] = pk(v.x, v.y) & keep[k >> 1]; }
        const int up = 2 * ct + ch;
        if (RARE(up == 4 || up == 13)) {
#pragma unroll
          for (int k = 0; k < 4; ++k) ST8((v2u{hp[2 * k], hp[2 * k + 1]}), dst[k], up * 1024);
        } else {
#pragma unroll
          for (int tt = 0; tt < 2; ++tt) ST16((v4u{hp[4 * tt], hp[4 * tt + 1], hp[4 * tt + 2], hp[4 * tt + 3]}), dst[2 * tt], up * 1024);
        }
        STAMP(3);
        DRAIN();
        SIGNAL(FA, i);
        post(pa + P_FREE, 3);
        STAMP(4);
      }
      {
        wait_full(4);
#pragma unroll
        for (int c = 0; c < 4; ++c) { ab4[c] += 8 * SLOT; asm volatile("" : "+v"(ab4[c])); }
        constexpr int PB = 32;
        s16x4 wl[PB], wh[PB];
#pragma unroll
        for (int k = 0; k < PB; ++k) { wl[k] = trd(ab4[k & 3] + (k >> 2) * 4096); wh[k] = trd(ab4[k & 3] + (k >> 2) * 4096 + 2048); }
        SB();
        f32x2 xr[8];
#pragma unroll
        for (int l = 0; l < 4; ++l) { xr[2 * l] = f32x2{lo16(xres[l].x), hi16(xres[l].x)}; xr[2 * l + 1] = f32x2{lo16(xres[l].y), hi16(xres[l].y)}; }
#pragma unroll
        for (int k = 0; k < 8; ++k) asm volatile("" : "+v"(xr[k]));
        wait_bit(L_RDY + 4 * R_H, 1u << (i >> 1));
        STAMP(5);
        bf16x8 hf[18];
        const int hsrc = OFF_H1 + T * (18 * 1024);
#pragma unroll
        for (int u = 0; u < 9; ++u) hf[u] = asfrag(LD16(lane * 16 + u * 1024, hsrc));
        SB();
        f32x4 acc[4];
        unsigned zv;
        sfor<72>([&](auto K) INL {
          constexpr int k = K.value, u = k >> 2;
          mfa<(k < 4)>(acc[k & 3], cat(wl[k % PB], wh[k % PB]), hf[u]); SB();
          if constexpr (k == 17) post(pa + P_FREE, 4);
          if constexpr (k == 60) zv = peek(L_RDY + 4 * R_Z);
          if constexpr ((k & 1) == 0 && k / 2 + 9 < 18) hf[k / 2 + 9] = asfrag(LD16(lane * 16 + (k / 2 + 9) * 1024, hsrc));
          if constexpr (k + PB < 72) { wl[k % PB] = trd(ab4[k & 3] + ((k + PB) >> 2) * 4096); wh[k % PB] = trd(ab4[k & 3] + ((k + PB) >> 2) * 4096 + 2048); }
          SB();
        });
        MFMA_DONE();
        STAMP(6);
        unsigned xp[8];
#pragma unroll
        for (int l = 0; l < 4; ++l) {
          const f32x2 v0 = xr[2 * l] + f32x2{acc[l].x, acc[l].y}, v1 = xr[2 * l + 1] + f32x2{acc[l].z, acc[l].w};
          xp[2 * l] = pkc(v0.x, v0.y); xp[2 * l + 1] = pkc(v1.x, v1.y);
        }
        if (RARE(rfl(zv) == 0)) wait_word(L_RDY + 4 * R_Z, 1);
        CLOB();
        const int dst = OFF_X1 + (T * 18 + 2 * ct) * 1024;
        ST16((v4u{xp[0], xp[1], xp[2], xp[3]}), lane * 16, dst);
        ST16((v4u{xp[4], xp[5], xp[6], xp[7]}), lane * 16 + 1024, dst);
        SB();
        float sm = 0.f, sq = 0.f;
#pragma unroll
        for (int k = 0; k < 8; ++k) {
          const bf16x2_t v = __builtin_bit_cast(bf16x2_t, xp[k]);
          sm = __builtin_amdgcn_fdot2_f32_bf16(v, bf16x2_t{(__bf16)1.0f, (__bf16)1.0f}, sm, false);
          sq = __builtin_amdgcn_fdot2_f32_bf16(v, v, sq, false);
        }
        sm = qsum(sm); sq = qsum(sq);
        ST8((v2u{min(__float_as_uint(sm), EMPTY - 1u), min(__float_as_uint(sq), EMPTY - 1u)}), q ? 0x7ffffff0 : t * 8, OFF_STAT + (T * 9 + ct) * 128);
        SIGNAL(FB, i);
        post(pa + P_FREE, 5);
        STAMP(7);
      }
    } else {
      const int lt = ((r - 18) * 4 + i) * 64 + lane;
      unsigned sink = 0;
      auto touch = [&](const char* base, unsigned off) INL { asm volatile("global_load_dword %0, %1, %2" : "+v"(sink) : "v"(off), "s"(base) : "memory"); };
      if (CO & 1) {
#pragma unroll
        for (int k = 0; k < 2; ++k) touch(w1, (5183 - min(lt + 3584 * k, 5183)) * 128);
        touch(x + (size_t)j * (128 * D_ * 2), min(lt, 1151) * 128);
      }
      if (CO & 4) DRAIN(); else __builtin_amdgcn_s_sleep(SL1);
#pragma unroll
      for (int k = 0; k < 2; ++k) touch(w2, min(lt + 3584 * k, 5183) * 128);
      if (!(CO & 4)) DRAIN();
      if (MAP) {
#pragma unroll
        for (int k = 0; k < 2; ++k) { const int l = min(lt + 3584 * k, 5183); touch(w3 + (j & 6) * (576 * 2), (l / 9) * (F_ * 2) + (l % 9) * 128); }
      }
      DRAIN();
      if (!(CO & 4)) __builtin_amdgcn_s_sleep(SL2);
#pragma unroll
      for (int k = 0; k < (MAP ? 3 : 2); ++k) touch(w4 + (size_t)(MAP ? (j & 6) : j) * (576 * D_ * 2), min(lt + 3584 * k, MAP ? 10367 : 5183) * 128);
      DRAIN();
      asm volatile("" ::"v"(sink));
    }
    unsigned b0 = (4 * q + (t >> 2)) * 288 + (t & 3) * 8, b1 = b0 + 6 * SLOT, b2 = b0 + 12 * SLOT;
    asm volatile("" : "+v"(b0), "+v"(b1), "+v"(b2));
#define WADDR(z, c) ((((z) % 16) < 6 ? b0 : ((z) % 16) < 12 ? b1 : b2) + (((z) % 16) % 6) * SLOT + (c) * 32)
#define FN(i_) ((i_) < 108 ? (i_) / 9 : 12 + ((i_) - 108) % 6)
#define FC(i_) ((i_) < 108 ? (i_) % 9 : ((i_) - 108) / 6)
    const int T0 = 8 * g + 2 * i;
    const int hdst = OFF_HH + (T0 * 144 + fr * 18) * 1024;
    bf16x8 hb[18][2];
    auto hload = [&](auto K) INL {
      constexpr int m = K.value >> 1, tt = K.value & 1;
      hb[m][tt] = asfrag(LD16(lane * 16 + tt * (144 * 1024), hdst + dstep(m, s) * 1024));
    };
    s16x4 lo[PD], hi[PD];
    unsigned rv, hw = L_HS + 2 * i * 1024 + lane * 16, hw2 = 9 * SLOT + 2 * i * 1024 + lane * 16;
    asm volatile("" : "+v"(hw), "+v"(hw2));
    {
      wait_full(6);
#pragma unroll
      for (int k = 0; k < PD; ++k) { lo[k] = trd(WADDR(0, k)); hi[k] = trd(WADDR(0, k) + 4608); }
      f32x4 acc[2][9];
      const int xsrc = OFF_X1 + T0 * (18 * 1024);
      int xo[2] = {lane * 16, lane * 16 + 18 * 1024};
      asm volatile("" : "+v"(xo[0]), "+v"(xo[1]));
      bf16x8 xb[12][2];
      auto xload = [&](auto K) INL {
        constexpr int n = K.value >> 1, tt = K.value & 1;
        xb[n % 12][tt] = asfrag(LD16(xo[tt] + (n & 3) * 1024, xsrc + (n >> 2) * 4096));
      };
      SB();
      auto xfix = [&](auto K0, auto K1) INL {
        NOUNROLL for (unsigned n = 0; n < TRY_CAP; ++n) { CLOB();
          unsigned m = 0;
          COUNT(1);
          sfor<K1.value - K0.value>([&](auto K) INL { xload(IC<K0.value + K.value>{}); });
          sfor<K1.value - K0.value>([&](auto K) INL { constexpr int kk = K0.value + K.value; m = mx4(m, asv4(xb[(kk >> 1) % 12][kk & 1])); });
          if (!missing(m)) break;
        }
      };
      wait_bit(L_RDY + 4 * R_X, 1u << i);
      STAMP(9);
      sfor<10>([&](auto K) INL { xload(K); });
      SB();
      unsigned chk = mx4(mx4(0u, asv4(xb[0][0])), asv4(xb[0][1])), chk2 = 0;
      if (RARE(missing(chk))) xfix(IC<0>{}, IC<2>{});
      SB();
      f32x4 lc[2], csv;
      unsigned hp[2][2][2];
      unsigned la = L_LN + (32 * i + t) * 16, ca = L_CS3 + q * 16;
      asm volatile("" : "+v"(la), "+v"(ca));
      v4u fv;
      u64 bad;
      unsigned sv;
      auto epi = [&](auto Cc) INL {
        constexpr int c = Cc.value;
        f32x2 z[4];
#pragma unroll
        for (int tt = 0; tt < 2; ++tt) {
          const f32x4 zz = csv * lc[tt].x + acc[tt][c];
          z[2 * tt] = f32x2{zz.x, zz.y}; z[2 * tt + 1] = f32x2{zz.z, zz.w};
        }
        if constexpr (c == 3) sv = min4(fv);
        SB();
        const float ga4[4] = {lc[0].y, lc[0].y, lc[1].y, lc[1].y}, gb4[4] = {lc[0].z, lc[0].z, lc[1].z, lc[1].z};
        geluN<4>(z, ga4, gb4);
#pragma unroll
        for (int tt = 0; tt < 2; ++tt) {
          { hp[tt][c & 1][0] = pk(z[2 * tt].x, z[2 * tt].y); hp[tt][c & 1][1] = pk(z[2 * tt + 1].x, z[2 * tt + 1].y); }
        }
        asm volatile("" : "+v"(hp[0][c & 1][0]), "+v"(hp[0][c & 1][1]), "+v"(hp[1][c & 1][0]), "+v"(hp[1][c & 1][1]));
        if constexpr (c == 3) { if (RARE(sv < 8)) wait_free(8); CLOB(); }
        if constexpr ((c & 1) && c < 8) {
#pragma unroll
          for (int tt = 0; tt < 2; ++tt) {
            const v4u v = {hp[tt][0][0], hp[tt][0][1], hp[tt][1][0], hp[tt][1][1]};
            hb[c >> 1][tt] = asfrag(v);
            if constexpr (c == 1) *(LAS v4u*)(hw + tt * 1024) = v;
            else if constexpr (c < 7) *(LAS v4u*)(hw2 + (c >> 2) * SLOT + tt * 1024) = v;
            else ST16(v, lane * 16 + tt * (144 * 1024), hdst + (pbase(s) + 3) * 1024);
          }
        }
        if constexpr (c == 8) {
#pragma unroll
          for (int tt = 0; tt < 2; ++tt)
            ST8((v2u{hp[tt][0][0], hp[tt][0][1]}), lane * 16 + tt * (144 * 1024) + (s & 1) * 8, hdst + (s < 2 ? 4 : 13) * 1024);
        }
        if constexpr ((c & 1) && c < 7) post(pa + P_RQ, c / 2 + 1);
        SB();
      };
      sfor<162>([&](auto I) INL {
        constexpr int ii = I.value, n = FN(ii), c = FC(ii), nx = ii - 1 + PD;
        const bf16x8 cur = cat(lo[ii % PD], hi[ii % PD]);
        mfa<(n == 0)>(acc[0][c], cur, xb[n % 12][0]); SB();
        constexpr bool ck1 = ii < 99 && (c == 5 || c == 6);
        constexpr bool ck2 = ii >= 72 && ii < 99 && c >= 1 && c <= 4;
        constexpr int k2 = 24 + 4 * (n - 8) + c - 1;
        if constexpr (ii >= 1 && nx < 162) lo[nx % PD] = trd(WADDR(FN(nx), FC(nx)));
        if constexpr (ck1) { chk = mx2(c == 5 ? 0u : chk, asv4(xb[n + 1][c - 5]).x, asv4(xb[n + 1][c - 5]).y); asm volatile("" : "+v"(chk)); }
        if constexpr (ck2) { chk2 = mx2(chk2, asv4(xb[(k2 >> 1) % 12][k2 & 1]).x, asv4(xb[(k2 >> 1) % 12][k2 & 1]).y); asm volatile("" : "+v"(chk2)); }
        if constexpr (ii < 99 && c == 7) { bad = __ballot(chk == EMPTY); asm volatile("" : "+s"(bad)); }
        if constexpr (ii == 99) { bad = __ballot(chk2 == EMPTY); asm volatile("" : "+s"(bad)); }
        if constexpr (ii == 63 || ii == 101) { sv = min3(fv); asm volatile("" : "+s"(sv)); }
        if constexpr (ii == 103) { sv = rfl(rv); asm volatile("" : "+s"(sv)); }
        SB();
        mfa<(n == 0)>(acc[1][c], cur, xb[n % 12][1]); SB();
        if constexpr (ii >= 1 && nx < 162) hi[nx % PD] = trd(WADDR(FN(nx), FC(nx)) + 4608);
        if constexpr (ck1) { chk = mx2(chk, asv4(xb[n + 1][c - 5]).z, asv4(xb[n + 1][c - 5]).w); asm volatile("" : "+v"(chk)); }
        if constexpr (ck2) { chk2 = mx2(chk2, asv4(xb[(k2 >> 1) % 12][k2 & 1]).z, asv4(xb[(k2 >> 1) % 12][k2 & 1]).w); asm volatile("" : "+v"(chk2)); }
        SB();
        if constexpr (ii < 99 && c == 8) { if (RARE(bad != 0)) xfix(IC<2 * n + 2>{}, IC<2 * n + 4>{}); SB(); }
        if constexpr (ii == 100) { if (RARE(bad != 0)) xfix(IC<24>{}, IC<36>{}); SB(); }
        if constexpr (ii < 28 && ii % 2 == 0) { xload(IC<10 + ii / 2>{}); SB(); }
        if constexpr (ii >= 30 && ii < 66 && ii % 3 == 0) { xload(IC<14 + ii / 3>{}); SB(); }
        if constexpr (ii == 18 || ii == 81 || ii == 108 || ii == 159) { post(pa + P_FREE, ii == 18 ? 6 : ii == 81 ? 7 : ii == 108 ? 8 : 9); SB(); }
        if constexpr (ii == 8) STAMP(23);
        if constexpr (ii == 35) STAMP(24);
        if constexpr (ii == 71) STAMP(25);
        if constexpr (ii == 58 || ii == 96) { fv = peek4(L_FULL); SB(); }
        if constexpr (ii == 64) { if (RARE(sv < 7)) wait_full(7); CLOB(); SB(); }
        if constexpr (ii == 97) { rv = peek(L_RDY + 4 * R_LN); SB(); }
        if constexpr (ii == 102) { if (RARE(sv < 10)) wait_full(10); CLOB(); SB(); }
        if constexpr (ii == 104) {
          if (RARE(sv == 0)) wait_word(L_RDY + 4 * R_LN, 1);
          CLOB();
          lc[0] = *(const LAS f32x4*)la; lc[1] = *(const LAS f32x4*)(la + 256);
          SB(); STAMP(10);
        }
        if constexpr (ii >= 108 && n == 14 && c >= 1) { csv = *(const LAS f32x4*)(ca + (c - 1) * 64); SB(); }
        if constexpr (ii >= 108 && n == 15 && c == 4) { fv = peek4(L_FREE); SB(); }
        if constexpr (ii >= 108 && n == 17 && c >= 1) epi(IC<c - 1>{});
        if constexpr (ii == 150) { fv = peek4(L_FULL); SB(); }
      });
      csv = *(const LAS f32x4*)(ca + 8 * 64);
      if (RARE(min3(fv) < 11)) wait_full(11);
      CLOB();
#pragma unroll
      for (int k = 0; k < PD; ++k) { lo[k] = trd(WADDR(18, k)); hi[k] = trd(WADDR(18, k) + 4608); }
      SB();
      epi(IC<8>{});
      STAMP(11);
    }
    {
      f32x4 acc[2][9];
      unsigned pw = L_PS + 2 * i * 1024 + lane * 16, pw8 = L_PS + 32768 + 2 * i * 512 + lane * 8;
      asm volatile("" : "+v"(pw), "+v"(pw8));
      int po[2];
#pragma unroll
      for (int tt = 0; tt < 2; ++tt) { po[tt] = (2 * i + tt == fr) ? 0x7ff00000 : lane * 8; asm volatile("" : "+v"(po[tt])); }
      const int pdst = OFF_P + (((g * 4 + s) * 8 + 2 * i) * 8 + fr) * PUNIT;
      v4u fv;
      unsigned sv;
      auto epi = [&](auto Cc) INL {
        constexpr int c = Cc.value;
        if constexpr ((c & 1) && c < 8) {
#pragma unroll
          for (int tt = 0; tt < 2; ++tt) {
            const f32x4 a0 = acc[tt][c - 1], a1 = acc[tt][c];
            const v4u v = {pk(a0.x, a0.y), pk(a0.z, a0.w), pk(a1.x, a1.y), pk(a1.z, a1.w)};
            if constexpr (c == 7) ST16(v, 2 * po[tt] , pdst + tt * (8 * PUNIT) + 3 * 1024);
            *(LAS v4u*)(pw + tt * 1024 + (c >> 1) * 8192) = v;
          }
        }
        if constexpr (c == 8) {
#pragma unroll
          for (int tt = 0; tt < 2; ++tt) {
            const f32x4 a0 = acc[tt][8];
            const v2u v = {pk(a0.x, a0.y), pk(a0.z, a0.w)};
            ST8(v, po[tt], pdst + tt * (8 * PUNIT) + 4096);
            *(LAS v2u*)(pw8 + tt * 512) = v;
          }
        }
        if constexpr ((c & 1) && c < 7) post(pa + P_RQ, 3 + c / 2 + 1);
        SB();
      };
      constexpr int HS0 = HS0V, HS1 = HS1V, SIGS = SIGSV, HS2 = HS2V, HS3 = HS3V;
      sfor<162>([&](auto I) INL {
        constexpr int ii = I.value, n = FN(ii), c = FC(ii), nx = ii - 1 + PD;
        constexpr int G = ii == HS0 ? 0 : ii == HS1 ? 1 : ii == HS2 ? 2 : ii == HS3 ? 3 : -1;
        const bf16x8 cur = cat(lo[ii % PD], hi[ii % PD]);
        mfa<(n == 0)>(acc[0][c], cur, hb[n][0]); SB();
        if constexpr (ii >= 1 && nx < 162) lo[nx % PD] = trd(WADDR(18 + FN(nx), FC(nx)));
        if constexpr (G >= 0) { sv = rfl(rv); asm volatile("" : "+s"(sv)); }
        if constexpr (ii == 48 || ii == 84 || ii == 102) { sv = min3(fv); asm volatile("" : "+s"(sv)); }
        if constexpr (ii == 106) { sv = min4(fv); asm volatile("" : "+s"(sv)); }
        SB();
        mfa<(n == 0)>(acc[1][c], cur, hb[n][1]); SB();
        if constexpr (ii >= 1 && nx < 162) hi[nx % PD] = trd(WADDR(18 + FN(nx), FC(nx)) + 4608);
        SB();
        if constexpr (ii == SIGS) { VMW(6); SIGNAL(FC, 3 + i); SB(); }
        if constexpr (ii + 4 == HS0 || ii + 4 == HS1 || ii + 4 == HS2 || ii + 4 == HS3) { rv = peek(L_RDY + 4 * R_HH); SB(); }
        if constexpr (G >= 0) {
          const unsigned need = G < 3 ? 1u << G : 8u << i;
          if (RARE((sv & need) != need)) wait_bit(L_RDY + 4 * R_HH, need);
          CLOB(); SB(); STAMP(G == 0 ? 27 : G == 1 ? 28 : G == 2 ? 13 : 14);
        }
        if constexpr (ii >= HS0 && ii < HS0 + 6) { hload(IC<8 + ii - HS0>{}); SB(); }
        if constexpr (ii >= HS1 && ii < HS1 + 6) { hload(IC<14 + ii - HS1>{}); SB(); }
        if constexpr (ii >= HS2 && ii < HS2 + 6) { hload(IC<20 + ii - HS2>{}); SB(); }
        if constexpr (ii >= HS3 && ii < HS3 + 10) { hload(IC<26 + ii - HS3>{}); SB(); }
        if constexpr (ii == 18 || ii == 54) { post(pa + P_FREE, ii == 18 ? 10 : 11); SB(); }
        if constexpr (ii == 44 || ii == 79 || ii == 97) { fv = peek4(L_FULL); SB(); }
        if constexpr (ii == 48) { if (RARE(sv < 12)) wait_full(12); CLOB(); SB(); }
        if constexpr (ii == 84) { if (RARE(sv < 13)) wait_full(13); CLOB(); SB(); }
        if constexpr (ii == 102) { if (RARE(sv < 14)) wait_full(14); fv = peek4(L_FREE); SB(); STAMP(26); }
        if constexpr (ii == 106) { if (RARE(sv < 11)) wait_free(11); CLOB(); SB(); }
        if constexpr (ii >= 108 && n == 17 && c >= 1) epi(IC<c - 1>{});
      });
      MFMA_DONE();
      epi(IC<8>{});
      STAMP(15);
      asm volatile("s_waitcnt vmcnt(0) lgkmcnt(0)" ::: "memory");
      SIGNAL(FD, 3 + i);
      STAMP(16);
    }
  } else if (w < 7) {
    const int ld = w - 4;
    const unsigned fullw = L_FULL + 4 * ld;
    unsigned v3[3], v4a[9];
#pragma unroll
    for (int e = 0; e < 3; ++e) {
      const int idx = (3 * ld + e) * 64 + lane, row = idx / 18, ch = idx % 18;
      const int sc = (ch + 2 * (s & 1)) % 18;
      v3[e] = row * (F_ * 2) + sc * 16;
    }
#pragma unroll
    for (int e = 0; e < 9; ++e) { const int idx = e * 64 + lane; v4a[e] = (idx / 18) * (D_ * 2) + ((idx % 18 + 2 * (s & 1)) % 18) * 16; }
    const char* w3b = w3 + (576 * fr + 144 * s) * 2;
    const char* w4b = w4 + (size_t)(576 * fr) * (D_ * 2) + 144 * s * 2;
    auto w3slice = [&](int n) INL {
#pragma unroll
      for (int e = 0; e < 3; ++e) dma(w3b + (size_t)n * (32 * F_ * 2), v3[e], (n & 15) * SLOT + (3 * ld + e) * 1024);
    };
    auto w4whole = [&](int m, int u, auto CAP) INL {
      const char* src = w4b + u * (32 * D_ * 2);
#pragma unroll
      for (int e = 0; e < 9; ++e) { dma(src, v4a[e], ((18 + m) & 15) * SLOT + e * 1024); if constexpr (CAP.value) VMW(DCAP); }
    };
    const __amdgpu_buffer_rsrc_t r4 = __builtin_amdgcn_make_buffer_rsrc((void*)w4b, 0, 576 * D_ * 2 - 144 * s * 2, 0x00020000);
    v4u w4r[4][9];
    auto w4get = [&](auto K) INL {
      const int so = dstep(6 + ld + 3 * K.value, s) * (32 * D_ * 2);
#pragma unroll
      for (int e = 0; e < 9; ++e) { constexpr int eo[9] = {0, 2, 4, 6, 8, 1, 3, 5, 7}; w4r[K.value][eo[e]] = __builtin_amdgcn_raw_buffer_load_b128(r4, v4a[eo[e]], so, 0); }
      CLOB(); SB();
    };
    auto w4put = [&](auto K) INL {
      const unsigned a = ((24 + ld + 3 * K.value) & 15) * SLOT + lane * 16;
#pragma unroll
      for (int e = 0; e < 9; ++e) *(LAS v4u*)(a + e * 1024) = w4r[K.value][e];
      CLOB(); SB();
    };
    f32x4 cs[3] = {{0, 0, 0, 0}, {0, 0, 0, 0}, {0, 0, 0, 0}};
    const unsigned cb = (4 * q + (t >> 2)) * 288 + (t & 3) * 8 + ld * 96;
    auto colsum3 = [&](int n0, int n1) INL {
      __builtin_amdgcn_s_setprio(1);
      NOUNROLL for (int n = n0; n < n1; n += 2) {
        const unsigned a0 = cb + (n & 15) * SLOT;
        s16x4 l[6], h[6];
#pragma unroll
        for (int k = 0; k < 6; ++k) { l[k] = trd(a0 + (k / 3) * SLOT + (k % 3) * 32); h[k] = trd(a0 + (k / 3) * SLOT + 4608 + (k % 3) * 32); }
        SB();
#pragma unroll
        for (int k = 0; k < 6; ++k) cs[k % 3] = mfma(cat(l[k], h[k]), ones, cs[k % 3]);
      }
      __builtin_amdgcn_s_setprio(0);
      CLOB();
    };
    if (ab) {
      const unsigned v1 = (8 * (lane >> 5) + ((lane >> 3) & 3)) * (D_ * 2) + (((lane & 7) ^ ((q & 3) << 1)) << 4);
      auto w1units = [&](int u0) INL {
#pragma unroll
        for (int e = 0; e < 8; ++e) {
          const int u = u0 + 3 * (e >> 2);
          dma(w1 + 128 * ct + (32 * u + 16 * (e & 1) + 4 * ((e >> 1) & 1)) * (D_ * 2), v1, u * 4096 + (e & 3) * 1024);
        }
      };
      w1units(ld); w1units(ld + 6);
      VMW(8); lds_set(fullw, 1, lane);
      w1units(ld + 12);
      VMW(8); lds_set(fullw, 2, lane);
      STAMP(1);
      DRAIN(); lds_set(fullw, 3, lane);
      wait_free(2);
      const unsigned v2 = (lane >> 3) * (D_ * 2) + (((lane & 7) ^ ((q & 3) << 1)) << 4);
      NOUNROLL for (int u = ld; u < 18; u += 3) {
#pragma unroll
        for (int e = 0; e < 4; ++e) dma(w2 + 128 * ct + (32 * u + 8 * e) * (D_ * 2), v2, 8 * SLOT + u * 4096 + e * 1024);
      }
      if (!(CO & 2)) w4get(IC<0>{});
      STAMP(2);
      if (CO & 2) DRAIN(); else VMW(9);
      lds_set(fullw, 4, lane);
      STAMP(3);
      wait_free(3);
      NOUNROLL for (int n = 0; n < 8; ++n) w3slice(n);
      STAMP(4);
      DRAIN(); lds_set(fullw, 6, lane);
      wait_full(6);
      colsum3(0, 8);
      wait_free(4);
      NOUNROLL for (int n = 8; n < 11; ++n) w3slice(n);
      wait_free(5);
      NOUNROLL for (int n = 11; n < 16; ++n) w3slice(n);
      if (CO & 2) w4get(IC<0>{}); else w4get(IC<1>{});
      STAMP(5);
      VMW(9); lds_set(fullw, 7, lane);
      STAMP(6);
      wait_full(7);
      colsum3(8, 16);
    } else {
      __builtin_amdgcn_s_sleep(SL2);
      NOUNROLL for (int n = 0; n < 16; ++n) w3slice(n);
      DRAIN(); lds_set(fullw, 7, lane);
      w4get(IC<0>{}); if (!(CO & 2)) w4get(IC<1>{});
      wait_full(7);
      colsum3(0, 16);
    }
    lds_set(fullw, 8, lane);
    STAMP(7);
    wait_free(6); wait_full(8);
    STAMP(11);
    w3slice(16); w3slice(17);
    DRAIN(); lds_set(fullw, 9, lane);
    STAMP(12);
    wait_full(9);
    colsum3(16, 18);
    if (t == 0) {
#pragma unroll
      for (int c = 0; c < 3; ++c) *(LAS f32x4*)(L_CS3 + (3 * ld + c) * 64 + q * 16) = cs[c];
    }
    CLOB();
    lds_set(fullw, 10, lane);
    STAMP(8);
    if (PRIO & 2) __builtin_amdgcn_s_setprio(2);
    const u64 tag = get_tag();
    auto hround = [&](int p) INL {
      wait_min4(L_RQ, p + 1);
      v4u d[8];
#pragma unroll
      for (int k = 0; k < 8; ++k) d[k] = *(const LAS v4u*)((p ? (8 + p) * SLOT : L_HS) + k * 1024 + lane * 16);
#pragma unroll
      for (int k = 0; k < 8; ++k) ST16(d[k], lane * 16 + k * (144 * 1024), OFF_HH + (8 * g * 144 + fr * 18 + pbase(s) + p) * 1024);
      CLOB();
    };
    auto pround = [&](int p) INL {
      wait_min4(L_RQ, 3 + p + 1);
      const int so = OFF_P + ((g * 4 + s) * 64 + fr) * PUNIT + p * 1024;
      v4u d[7];
#pragma unroll
      for (int k = 0; k < 7; ++k) d[k] = *(const LAS v4u*)(L_PS + p * 8192 + ((fr + 1 + k) & 7) * 1024 + lane * 16);
#pragma unroll
      for (int k = 0; k < 7; ++k) ST16(d[k], lane * 16, so + ((fr + 1 + k) & 7) * (8 * PUNIT));
      DRAIN();
      SIGNAL(FD, p);
    };
    wait_free(7);
    NOUNROLL for (int m = ld; m < 6; m += 3) w4whole(m, dstep(m, s), IC<0>{});
    if (CO & 2) w4get(IC<1>{});
    w4get(IC<2>{}); w4get(IC<3>{});
    if (ld > 0) { DRAIN(); lds_set(fullw, 11, lane); }
    hround(ld);
    DRAIN();
    SIGNAL(FC, ld);
    lds_set(fullw, 11, lane);
    w4put(IC<0>{});
    if (ld == 0) w4put(IC<1>{});
    lds_set(fullw, 12, lane);
    STAMP(13);
    wait_free(9); wait_full(10);
    if (ld > 0) w4put(IC<1>{});
    w4put(IC<2>{});
    if (ld == 0) w4put(IC<3>{});
    lds_set(fullw, 13, lane);
    if (ld > 0) { wait_free(10); w4put(IC<3>{}); }
    lds_set(fullw, 14, lane);
    STAMP(10);
    pround(ld);
    STAMP(23);
  } else {
    if (PRIO & 1) __builtin_amdgcn_s_setprio(3);
    const u64 tag = __hip_atomic_load((GAS u64*)(ws + OFF_EPOCH), __ATOMIC_RELAXED, __HIP_MEMORY_SCOPE_AGENT) + 1ull;
    if (lane == 0) {
      *(volatile LAS u64*)L_TAG = tag;
      *(volatile LAS unsigned*)(L_RDY + 4 * R_TAG) = 1u;
      __hip_atomic_store(flagp(ws, FS, b, 0), tag, __ATOMIC_RELAXED, __HIP_MEMORY_SCOPE_AGENT);
    }
    {
      const v4u e4 = {EMPTY, EMPTY, EMPTY, EMPTY};
      if (ab) {
        wait_free(3);
#pragma unroll
        for (int k = 0; k < 8; ++k) ST16(e4, lane * 16 + (k & 1) * 1024 + (k >> 1) * (18 * 1024), OFF_X1 + (4 * rt * 18 + 2 * ct) * 1024);
        if (lane < 32) ST16(e4, OFF_STAT + (lane >> 3) * (9 * 128) + (lane & 7) * 16, (4 * rt * 9 + ct) * 128);
        DRAIN();
        lds_set(L_RDY + 4 * R_Z, 1, lane);
      }
    }
    STAMP(1);
    auto poll = [&](GAS u64* p) INL {
      NOUNROLL for (unsigned n = 0; n < GLB_CAP; ++n) {
        const u64 v = __hip_atomic_load(p, __ATOMIC_RELAXED, __HIP_MEMORY_SCOPE_AGENT);
        if (__all(v == tag)) break;
        __builtin_amdgcn_s_sleep(1);
      }
      CLOB();
    };
    auto poll_bits = [&](auto TWO, GAS u64* p0, GAS u64* p1, int word, u64 ma, u64 mb, u64 mc, u64 md) INL {
      unsigned done = 0;
      NOUNROLL for (unsigned n = 0; n < GLB_CAP; ++n) {
        const u64 v0 = __hip_atomic_load(p0, __ATOMIC_RELAXED, __HIP_MEMORY_SCOPE_AGENT);
        u64 v1 = v0;
        if constexpr (TWO.value) v1 = __hip_atomic_load(p1, __ATOMIC_RELAXED, __HIP_MEMORY_SCOPE_AGENT);
        const u64 bad0 = __ballot(v0 != tag), bad1 = __ballot(v1 != tag);
        const unsigned now = ((bad0 & ma) ? 0u : 1u) | ((bad0 & mb) ? 0u : 2u) | ((bad1 & mc) ? 0u : 4u) | ((bad1 & md) ? 0u : 8u);
        if (now != done) { done = now; lds_set(L_RDY + 4 * word, now, lane); }
        if (now == 15u) break;
        __builtin_amdgcn_s_sleep(1);
      }
      lds_set(L_RDY + 4 * word, 15u, lane);
      CLOB();
    };
    constexpr u64 M01 = 0x3333333333333333ull, M23 = 0xccccccccccccccccull;
    const int pl = (lane % 36) >> 2;
    if (ab) poll_bits(IC<0>{}, flagp(ws, FA, j + 8 * ((r & 1) + 2 * pl), lane & 3), nullptr, R_H, M01, M23, M01, M23);
    STAMP(2);
    poll_bits(IC<1>{}, flagp(ws, FB, g + 16 * pl, lane & 3), flagp(ws, FB, g + 8 + 16 * pl, lane & 3), R_X, M01, M23, M01, M23);
    STAMP(3);
    {
      v4u st[9];
      NOUNROLL for (unsigned n = 0; n < TRY_CAP; ++n) { CLOB();
        unsigned m = 0;
#pragma unroll
        for (int k = 0; k < 9; ++k) st[k] = LD16(OFF_STAT + ((lane >> 3) * 9 + k) * 128 + (lane & 7) * 16, g * (8 * 9 * 128));
#pragma unroll
        for (int k = 0; k < 9; ++k) m = mx4(m, st[k]);
        if (!missing(m)) break;
      }
      f32x4 a = __builtin_bit_cast(f32x4, st[0]);
#pragma unroll
      for (int k = 1; k < 9; ++k) a += __builtin_bit_cast(f32x4, st[k]);
      *(LAS f32x4*)(L_LN + lane * 32) = ln_consts(a.x, a.y);
      *(LAS f32x4*)(L_LN + lane * 32 + 16) = ln_consts(a.z, a.w);
      CLOB();
      lds_set(L_RDY + 4 * R_LN, 1, lane);
    }
    if (b == 0) {
      NOUNROLL for (int k = 0; k < 4; ++k) poll(flagp(ws, FS, k * 64 + lane, 0));
      if (lane == 0) __hip_atomic_store((GAS u64*)(ws + OFF_EPOCH), tag, __ATOMIC_RELAXED, __HIP_MEMORY_SCOPE_AGENT);
    }
    auto poll5 = [&](GAS u64* p0, int word, auto NB) INL {
      constexpr unsigned all = (1u << NB.value) - 1u;
      unsigned done = 0;
      NOUNROLL for (unsigned n = 0; n < GLB_CAP; ++n) {
        const u64 bad = __ballot(__hip_atomic_load(p0, __ATOMIC_RELAXED, __HIP_MEMORY_SCOPE_AGENT) != tag);
        unsigned now = 0;
#pragma unroll
        for (int k = 0; k < NB.value; ++k) now |= (bad & (0x0101010101010101ull << k)) ? 0u : 1u << k;
        if (now != done) { done = now; lds_set(L_RDY + 4 * word, now, lane); }
        if (now == all) break;
        __builtin_amdgcn_s_sleep(1);
      }
      lds_set(L_RDY + 4 * word, all, lane);
    };
    poll5(flagp(ws, FC, j + 8 * ((r & ~3) + ((lane >> 3) & 3)), lane & 7), R_HH, IC<7>{});
    STAMP(4);
    const int wr = lane >> 3;
    poll5(flagp(ws, FD, MAP ? (j & 1) + (wr & 6) + 8 * (((wr & 1) << 4) + (r & 15)) : wr + 8 * r, (lane & 7) < 3 ? (lane & 7) : 3 + (fr >> 1)), R_P, IC<5>{});
    STAMP(5);
  }
  if (w < 2 || (w >= 4 && w < 7)) {
    const int p = w < 2 ? w + 3 : w - 4;
    const int T = 8 * g + fr;
    const int u = p < 4 ? pbase(s) + p : (s < 2 ? 4 : 13);
    v4u xv;
    NOUNROLL for (unsigned n = 0; n < TRY_CAP; ++n) { CLOB(); xv = LD16(lane * 16, OFF_X1 + (T * 18 + u) * 1024); if (!missing(mx4(0u, xv))) break; }
    const float rstd = *(const LAS float*)(L_LN + (16 * fr + t) * 16 + 12);
    wait_bit(L_RDY + 4 * R_P, 1u << p);
    STAMP(20);
    const int psrc = OFF_P + ((g * 4 + s) * 8 + fr) * (8 * PUNIT) + p * 1024;
    char* orow = out + (size_t)(16 * T + t) * (D_ * 2) + (32 * u + 4 * q) * 2;
    if (p < 4) {
      v4u pv[8];
#pragma unroll
      for (int k = 1; k < 8; ++k) pv[k] = LD16(lane * 16, psrc + ((fr + k) & 7) * PUNIT);
      pv[0] = *(const LAS v4u*)(L_PS + p * 8192 + fr * 1024 + lane * 16);
      float a[8];
#pragma unroll
      for (int e = 0; e < 8; ++e) a[e] = 0.f;
#pragma unroll
      for (int k = 0; k < 8; ++k)
#pragma unroll
        for (int e = 0; e < 4; ++e) { const unsigned d = pv[k][e]; a[2 * e] += lo16(d); a[2 * e + 1] += hi16(d); }
      unsigned o[4];
#pragma unroll
      for (int e = 0; e < 4; ++e) {
        const unsigned h2 = pk(a[2 * e] * rstd, a[2 * e + 1] * rstd), xe = xv[e];
        o[e] = pk(lo16(xe) + lo16(h2), hi16(xe) + hi16(h2));
      }
      *(GAS v2u*)orow = v2u{o[0], o[1]};
      *(GAS v2u*)(orow + 32) = v2u{o[2], o[3]};
    } else {
      v2u pv[8];
#pragma unroll
      for (int k = 1; k < 8; ++k) pv[k] = LD8(lane * 8, psrc + ((fr + k) & 7) * PUNIT);
      pv[0] = *(const LAS v2u*)(L_PS + 32768 + fr * 512 + lane * 8);
      float a[4] = {0.f, 0.f, 0.f, 0.f};
#pragma unroll
      for (int k = 0; k < 8; ++k) { a[0] += lo16(pv[k].x); a[1] += hi16(pv[k].x); a[2] += lo16(pv[k].y); a[3] += hi16(pv[k].y); }
      const unsigned x0 = (s & 1) ? xv.z : xv.x, x1v = (s & 1) ? xv.w : xv.y;
      const unsigned h0 = pk(a[0] * rstd, a[1] * rstd), h1 = pk(a[2] * rstd, a[3] * rstd);
      *(GAS v2u*)(orow + (s & 1) * 32) = v2u{pk(lo16(x0) + lo16(h0), hi16(x0) + hi16(h0)), pk(lo16(x1v) + lo16(h1), hi16(x1v) + hi16(h1))};
    }
    STAMP(21);
  }
#endif
}
}

extern "C" void kernel_launch(void* const* d_in, const int* in_sizes, int n_in, void* d_out, int out_size, void* d_ws, size_t ws_size,
                              hipStream_t stream) {
  mx::mixer_kernel<<<256, 512, 0, stream>>>((const char*)d_in[0], (const char*)d_in[1], (const char*)d_in[2], (const char*)d_in[3],
                                            (const char*)d_in[4], (char*)d_out, (char*)d_ws);
}
```

```cpp
#include <hip/hip_runtime.h>
#include <utility>

#define STAMPS 0
#define HS0V 10
#define HS1V 36
#define HS2V 62
#define SIGSV 30
#define HS3V 90
#define LATE 0
#define PRIO 1
#define CO 3
#define MAP 1
#define SL1 32
#define SL2 64

#define LAS __attribute__((address_space(3)))
#define GAS __attribute__((address_space(1)))
#define DI __device__ __forceinline__
typedef unsigned v4u __attribute__((ext_vector_type(4)));
typedef unsigned v2u __attribute__((ext_vector_type(2)));
typedef float f32x4 __attribute__((ext_vector_type(4)));
typedef float f32x2 __attribute__((ext_vector_type(2)));
typedef short bf16x8 __attribute__((ext_vector_type(8)));
typedef short s16x4 __attribute__((ext_vector_type(4)));
typedef __bf16 bf16x2_t __attribute__((ext_vector_type(2)));
typedef unsigned long long u64;

namespace mx {
constexpr int D_ = 576, F_ = 4608;
constexpr int OFF_EPOCH = 0;
constexpr int OFF_FLAGS = 4096;
constexpr int OFF_STAT = 344064;
constexpr int OFF_H1 = 524288;
constexpr int OFF_X1 = OFF_H1 + 64 * 18 * 1024;
constexpr int OFF_HH = OFF_X1 + 64 * 18 * 1024;
constexpr int PUNIT = 4608;
constexpr int OFF_P = OFF_HH + 64 * 144 * 1024;
constexpr int OFF_DBG = OFF_P + 8 * 4 * 8 * 8 * PUNIT;
constexpr int WS_BYTES = OFF_DBG + 2 * 256 * 8 * 32 * 8 + 256;
enum { FS = 0, FA = 1, FB = 2, FC = 3, FD = 4 };
constexpr int SLOT = 9216, MISC = 16 * SLOT;
constexpr int L_FULL = MISC, L_FREE = MISC + 16, L_RQ = MISC + 32, L_RDY = MISC + 48  , L_SY = MISC + 96, L_AGR = MISC + 112, L_TAG = MISC + 120;
constexpr int L_DUM = MISC + 128  , L_CS1 = MISC + 512  , L_CS3 = MISC + 1024  , L_LN = MISC + 2048  ,
              L_HS = MISC + 4096  , L_LN1 = L_HS  , L_DBG = MISC + 12288, L_PS = 4 * SLOT  ;
enum { R_H = 0, R_X = 1, R_HH = 2, R_P = 3, R_TAG = 4, R_LN = 5, R_Z = 6 };
constexpr unsigned EMPTY = 0xffffffffu;
constexpr unsigned TRY_CAP = 1u << 12;
constexpr unsigned LDS_CAP = 1u << 21, GLB_CAP = 1u << 16;
#define DCAPV 6
constexpr int PD = 4, DCAP = DCAPV;

template <class F, int... I> DI void sfor_impl(F&& f, std::integer_sequence<int, I...>) { (f(std::integral_constant<int, I>{}), ...); }
template <int N, class F> DI void sfor(F&& f) { sfor_impl(f, std::make_integer_sequence<int, N>{}); }
template <int V> using IC = std::integral_constant<int, V>;
#define INL __attribute__((always_inline))
#define NOUNROLL _Pragma("nounroll")
#define RARE(x) __builtin_expect(!!(x), 0)
#define SB() __builtin_amdgcn_sched_barrier(0)
#define CLOB() asm volatile("" ::: "memory")
#define DRAIN() asm volatile("s_waitcnt vmcnt(0)" ::: "memory")
#define VMW(n) asm volatile("s_waitcnt vmcnt(%0)" ::"i"(n) : "memory")

DI unsigned rfl(unsigned v) { return __builtin_amdgcn_readfirstlane(v); }
DI f32x4 mfma(bf16x8 a, bf16x8 b, f32x4 c) { return __builtin_amdgcn_mfma_f32_16x16x32_bf16(a, b, c, 0, 0, 0); }
template <bool FIRST> DI void mfa(f32x4& c, bf16x8 a, bf16x8 b) {
  if constexpr (FIRST) asm volatile("v_mfma_f32_16x16x32_bf16 %0, %1, %2, 0" : "=&v"(c) : "v"(a), "v"(b));
  else asm volatile("v_mfma_f32_16x16x32_bf16 %0, %1, %2, %0" : "+v"(c) : "v"(a), "v"(b));
}
#define MFMA_DONE() asm volatile("s_nop 7\n\ts_nop 3" ::: "memory")
DI bf16x8 asfrag(v4u v) { return __builtin_bit_cast(bf16x8, v); }
DI s16x4 trd(unsigned addr) { return __builtin_amdgcn_ds_read_tr16_b64_v4i16((LAS s16x4*)addr); }
DI bf16x8 cat(s16x4 lo, s16x4 hi) { return __builtin_shufflevector(lo, hi, 0, 1, 2, 3, 4, 5, 6, 7); }
DI unsigned pk(float a, float b) { bf16x2_t r = __builtin_convertvector(f32x2{a, b}, bf16x2_t); return __builtin_bit_cast(unsigned, r); }
DI unsigned pkc(float a, float b) { return min(pk(a, b), EMPTY - 1u); }
DI unsigned mx2(unsigned m, unsigned a, unsigned b) { return max(max(m, a), b); }
DI unsigned mx4(unsigned m, v4u d) { return mx2(mx2(m, d.x, d.y), d.z, d.w); }
DI bool missing(unsigned m) { return __any(m == EMPTY); }
DI v4u asv4(bf16x8 f) { return __builtin_bit_cast(v4u, f); }
DI float lo16(unsigned p) { return __uint_as_float(p << 16); }
DI float hi16(unsigned p) { return __uint_as_float(p & 0xffff0000u); }
DI float bfr(float a) { return lo16(pk(a, a)); }
DI float qsum(float v) {
  auto r = __builtin_amdgcn_permlane16_swap(__float_as_uint(v), __float_as_uint(v), false, false);
  float a = __uint_as_float(r[0]) + __uint_as_float(r[1]);
  auto r2 = __builtin_amdgcn_permlane32_swap(__float_as_uint(a), __float_as_uint(a), false, false);
  return __uint_as_float(r2[0]) + __uint_as_float(r2[1]);
}
constexpr float GA = -2.302208198144325f;
constexpr float GB = -0.1029432397357593f;
DI f32x4 ln_consts(float s, float ss) {
  const float m = s * (1.0f / D_);
  float var = __builtin_fmaf(-m, m, ss * (1.0f / D_));
  var = var > 0.f ? var : 0.f;
  const float sd = bfr(__builtin_amdgcn_sqrtf(bfr(bfr(var) + 1e-5f)));
  const float r = __builtin_amdgcn_rcpf(sd);
  return f32x4{-bfr(m), GA * r, GB * r * r * r, r};
}
template <int N> DI void geluN(f32x2 (&z)[N], const float (&a)[N], const float (&b)[N]) {
  f32x2 p[N];
#pragma unroll
  for (int k = 0; k < N; ++k) p[k] = z[k] * z[k];
  SB();
#pragma unroll
  for (int k = 0; k < N; ++k) p[k] = p[k] * b[k] + a[k];
  SB();
#pragma unroll
  for (int k = 0; k < N; ++k) p[k] = z[k] * p[k];
  SB();
#pragma unroll
  for (int k = 0; k < N; ++k) { p[k].x = __builtin_amdgcn_exp2f(p[k].x); p[k].y = __builtin_amdgcn_exp2f(p[k].y); }
  SB();
#pragma unroll
  for (int k = 0; k < N; ++k) p[k] = p[k] + 1.0f;
  SB();
#pragma unroll
  for (int k = 0; k < N; ++k) { p[k].x = __builtin_amdgcn_rcpf(p[k].x); p[k].y = __builtin_amdgcn_rcpf(p[k].y); }
  SB();
#pragma unroll
  for (int k = 0; k < N; ++k) z[k] = z[k] * p[k];
  SB();
}

DI unsigned ldsw(int addr) { return *(volatile LAS unsigned*)addr; }
DI v4u lds4(int addr) { return *(volatile LAS v4u*)addr; }
DI void lds_set(int addr, unsigned v, int lane) { if (lane == 0) *(volatile LAS unsigned*)addr = v; }
DI v4u peek4(int addr) { CLOB(); return *(const LAS v4u*)addr; }
DI unsigned peek(int addr) { CLOB(); return *(const LAS unsigned*)addr; }
DI void post(unsigned addr, unsigned v) { CLOB(); *(LAS unsigned*)addr = v; CLOB(); }
DI void wait_word(int addr, unsigned v) {
  NOUNROLL for (unsigned n = 0; n < LDS_CAP; ++n) { if (rfl(ldsw(addr)) >= v) break; __builtin_amdgcn_s_sleep(1); }
  CLOB();
}
DI unsigned min3(v4u x) { return rfl(min(min(x.x, x.y), x.z)); }
DI unsigned min4(v4u x) { return rfl(min(min(x.x, x.y), min(x.z, x.w))); }
DI void wait_full(unsigned v) {
  NOUNROLL for (unsigned n = 0; n < LDS_CAP; ++n) { if (min3(lds4(L_FULL)) >= v) break; __builtin_amdgcn_s_sleep(1); }
  CLOB();
}
DI void wait_min4(int addr, unsigned v) {
  NOUNROLL for (unsigned n = 0; n < LDS_CAP; ++n) { if (min4(lds4(addr)) >= v) break; __builtin_amdgcn_s_sleep(1); }
  CLOB();
}
DI void wait_free(unsigned v) { wait_min4(L_FREE, v); }
DI void wait_bit(int addr, unsigned bit) {
  NOUNROLL for (unsigned n = 0; n < LDS_CAP; ++n) { if ((rfl(ldsw(addr)) & bit) == bit) break; __builtin_amdgcn_s_sleep(1); }
  CLOB();
}
DI GAS u64* flagp(char* ws, int kind, int b, int wv) { return (GAS u64*)(ws + OFF_FLAGS + (kind * 256 + b) * 128 + wv * 8); }
DI u64 get_tag() {
  wait_word(L_RDY + 4 * R_TAG, 1);
  return ((u64)rfl(ldsw(L_TAG + 4)) << 32) | rfl(ldsw(L_TAG));
}
#define LD16(off, soff) __builtin_amdgcn_raw_buffer_load_b128(rs, (off), (soff), 16)
#define LD8(off, soff) __builtin_amdgcn_raw_buffer_load_b64(rs, (off), (soff), 16)
#define ST16(v, off, soff) do { v4u d_ = (v); asm volatile("" : "+v"(d_)); __builtin_amdgcn_raw_buffer_store_b128(d_, rs, (off), (soff), 16); asm volatile("s_nop 1" ::"v"(d_) : "memory"); } while (0)
#define ST8(v, off, soff) __builtin_amdgcn_raw_buffer_store_b64((v), rs, (off), (soff), 16)
#define SIGNAL(kind, wv) do { CLOB(); ST8((v2u{(unsigned)tag, (unsigned)(tag >> 32)}), lane ? 0x7ffffff0 : 0, OFF_FLAGS + ((kind) * 256 + b) * 128 + (wv) * 8); CLOB(); } while (0)
DI void dma(const char* sbase, unsigned voff, unsigned ldsaddr) {
  asm volatile("s_mov_b32 m0, %0\n\ts_nop 0\n\tglobal_load_lds_dwordx4 %1, %2" ::"s"(ldsaddr), "v"(voff), "s"(sbase) : "memory");
}
DI void dmac(const char* sbase, unsigned voff, unsigned ldsaddr) { dma(sbase, voff, ldsaddr); VMW(DCAP); }
DI int pbase(int s) { return (9 * s + (s & 1)) >> 1; }
DI int dstep(int M, int s) {
  if (M < 4) return pbase(s) + M;
  else if (M < 16) return pbase((s + 1 + (M - 4) % 3) & 3) + (M - 4) / 3;
  else return M == 16 ? 4 : 13;
}
#define STAMP(k) do { } while (0)
#define COUNT(k) do { } while (0)

__global__ __launch_bounds__(512, 2) void mixer_kernel(const char* __restrict__ x, const char* __restrict__ w1, const char* __restrict__ w2,
                                                       const char* __restrict__ w3, const char* __restrict__ w4, char* __restrict__ out, char* ws) {
#if defined(__HIP_DEVICE_COMPILE__)
  __shared__ __attribute__((aligned(16))) char smem[163840];
  const int tid = threadIdx.x, lane = tid & 63, w = rfl(tid >> 6);
  const int t = lane & 15, q = lane >> 4;
  const int b = blockIdx.x, j = b & 7, r = b >> 3;
  const bool ab = r < 18;
  const int rt = 2 * j + (r & 1), ct = r >> 1;
  const int g = MAP ? 4 * (j & 1) + ((r >> 2) & 3) : r >> 2, fr = MAP ? (j & 6) + (r >> 4) : j, s = r & 3;
  const __amdgpu_buffer_rsrc_t rs = __builtin_amdgcn_make_buffer_rsrc(ws, 0, WS_BYTES, 0x00020000);
  const bf16x8 ones = {0x3F80, 0x3F80, 0x3F80, 0x3F80, 0x3F80, 0x3F80, 0x3F80, 0x3F80};
  asm volatile("" ::"v"(smem));
  STAMP(0);
  if (tid < 32) *(volatile LAS unsigned*)(MISC + tid * 4) = 0u;
  asm volatile("s_waitcnt lgkmcnt(0)\n\ts_barrier" ::: "memory");
  if (ab) {
    if (w < 4) {
      const char* xsrc = x + (size_t)(4 * rt + w) * (16 * D_ * 2);
#pragma unroll
      for (int e = 0; e < 18; ++e) {
        const unsigned f = (((unsigned)(lane + 64 * e) * 911u) >> 17) & 7u;
        dma(xsrc + e * 1024, (lane * 16) ^ (f << 4), (8 + 2 * w) * SLOT + e * 1024);
      }
    }
  }

  if (w == 4 && !ab) {
    unsigned len, sink = 0;
    const char* pc;
    asm volatile("s_getpc_b64 %0\nmx_pf_begin:\n\ts_mov_b32 %1, mx_pf_end-mx_pf_begin" : "=s"(pc), "=s"(len));
    asm volatile("global_load_dword %0, %1, %2" : "+v"(sink) : "v"(min((unsigned)(((r - 18) & 7) * 64 + lane) * 128u, len - 4u) & ~3u), "s"(pc) : "memory");
    DRAIN();
    asm volatile("" ::"v"(sink));
  }

  if (w < 4) {
    const int i = w;
    unsigned pa = lane ? L_DUM + 4 * lane : L_FREE + 4 * i;
    asm volatile("" : "+v"(pa));
    const u64 tag = get_tag();
    constexpr int P_FREE = 0, P_RQ = L_RQ - L_FREE, P_SY = L_SY - L_FREE;
    if (ab) {
      unsigned ab4[4];
#pragma unroll
      for (int c = 0; c < 4; ++c) ab4[c] = (4 * q + (t >> 2)) * 128 + ((c ^ ((2 * q + (t >> 3)) & 3)) << 5) + (t & 3) * 8;
      const int T = 4 * rt + i;
      v2u xres[4];
      {
        const int tp = i >> 1, ch = i & 1;
        const unsigned xa = (8 + 2 * i) * SLOT + t * (D_ * 2), xs = ((t >> 1) & 7) << 4;
        unsigned xe[2][2], wb[2][2];
#pragma unroll
        for (int e = 0; e < 2; ++e) {
          xe[0][e] = xa + ((64 * (e ^ tp) + q * 16) ^ xs);
          xe[1][e] = xe[0][e] + (ch ? -2 * SLOT : 2 * SLOT);
          wb[e][0] = (ch ? ab4[2] : ab4[0]) + (e ^ tp) * 4096;
          wb[e][1] = (ch ? ab4[3] : ab4[1]) + (e ^ tp) * 4096;
          asm volatile("" : "+v"(xe[0][e]), "+v"(xe[1][e]), "+v"(wb[e][0]), "+v"(wb[e][1]));
        }
        int dst[4];
        unsigned keep[4], dm[4];
#pragma unroll
        for (int k = 0; k < 4; ++k) {
          const int gt = 4 * ct + 2 * ch + (k & 1), grp = gt / 9;
          const int sh = grp == 0 ? 1 : grp == 1 ? -1 : grp == 2 ? 2 : -2;
          const int ts = 16 * (ch ^ (k >> 1)) + t + sh;
          keep[k] = (ts >= 0 && ts < 32) ? 0xffffffffu : 0u;
          dst[k] = OFF_H1 + ((T & ~1) + ((ts >> 4) & 1)) * (18 * 1024) + ((ts & 15) + 16 * q) * 16 + (k & 1) * 8;
          dm[k] = (q == (t >> 2) && k == (t & 3)) ? 0xffffffffu : 0u;
          asm volatile("" : "+v"(dst[k]), "+v"(keep[k]), "+v"(dm[k]));
        }
        DRAIN();
        post(pa + P_FREE, 1);
        wait_word(L_FREE + 4 * (i ^ 1), 1);
        STAMP(1);
        bf16x8 xf[2][18];
        f32x4 aS = {0, 0, 0, 0}, aG = {0, 0, 0, 0};
#pragma unroll
        for (int p = 0; p < 6; ++p) xf[0][p] = *(const LAS bf16x8*)(xe[0][p & 1] + (p >> 1) * 128);
        SB();
        sfor<18>([&](auto P) INL {
          constexpr int p = P.value;
          aS = mfma(ones, xf[0][p], aS); SB();
          if constexpr (p + 6 < 18) xf[0][p + 6] = *(const LAS bf16x8*)(xe[0][p & 1] + ((p + 6) >> 1) * 128);
          else if constexpr (p + 6 < 22) xres[p - 12] = *(const LAS v2u*)(xa + 128 * ct + ((32 * (p - 12) + 8 * q) ^ xs));
          SB();
          aG = mfma(xf[0][p], xf[0][p], aG); SB();
          xf[1][p] = *(const LAS bf16x8*)(xe[1][p & 1] + (p >> 1) * 128);
          SB();
        });
        CLOB();
        post(pa + P_FREE, 2);
        f32x4 lc[2];
        {
          const float gd = __uint_as_float(((__float_as_uint(aG.x) & dm[0]) | (__float_as_uint(aG.y) & dm[1])) | ((__float_as_uint(aG.z) & dm[2]) | (__float_as_uint(aG.w) & dm[3])));
          lc[0] = ln_consts(aS[0], qsum(gd));
          if (q == 0) *(LAS f32x4*)(L_LN1 + i * 256 + t * 16) = lc[0];
          CLOB();
          post(pa + P_SY, 1);
        }
        SB();
        STAMP(17);
        wait_full(1);
        constexpr int PA = 4;
        s16x4 wl[PA], wh[PA];
#define AADDR(k) (wb[((k) >> 1) & 1][(k) & 1] + ((k) >> 2) * 8192)
#pragma unroll
        for (int k = 0; k < PA; ++k) { wl[k] = trd(AADDR(k)); wh[k] = trd(AADDR(k) + 2048); }
        bf16x8 one2 = ones;
        asm volatile("" : "+v"(one2));
        SB();
        f32x4 acc[2][2], cs[2];
        v4u fv;
        sfor<36>([&](auto K) INL {
          constexpr int k = K.value, p = k >> 1, l = k & 1, nx = k - 1 + PA;
          const bf16x8 cur = cat(wl[k % PA], wh[k % PA]);
          mfa<(p == 0)>(acc[0][l], cur, xf[0][p]); SB();
          if constexpr (nx == 12 || nx == 24) { if (RARE(min3(fv) < nx / 12 + 1)) wait_full(nx / 12 + 1); CLOB(); SB(); }
          if constexpr (k >= 1 && nx < 36) wl[nx % PA] = trd(AADDR(nx));
          SB();
          mfa<(p == 0)>(acc[1][l], cur, xf[1][p]); SB();
          if constexpr (k >= 1 && nx < 36) wh[nx % PA] = trd(AADDR(nx) + 2048);
          if constexpr (nx == 9 || nx == 21) fv = peek4(L_FULL);
          SB();
          if constexpr ((p & 1) == 0) { mfa<(p == 0)>(cs[l], cur, one2); SB(); }
        });
        MFMA_DONE();
        if (t == 0) { *(LAS f32x4*)(L_CS1 + i * 128 + q * 16) = cs[0]; *(LAS f32x4*)(L_CS1 + i * 128 + 64 + q * 16) = cs[1]; }
        CLOB();
        post(pa + P_SY, 2);
        STAMP(2);
        wait_word(L_SY + 4 * (i ^ 1), 1);
        lc[1] = *(const LAS f32x4*)(L_LN1 + (i ^ 1) * 256 + t * 16);
        wait_word(L_SY + 4 * (i ^ 2), 2);
        const f32x4 o0 = *(const LAS f32x4*)(L_CS1 + (i ^ 2) * 128 + q * 16), o1 = *(const LAS f32x4*)(L_CS1 + (i ^ 2) * 128 + 64 + q * 16);
        cs[0] += o0; cs[1] += o1;
        f32x2 z[8];
        float ga8[8], gb8[8];
#pragma unroll
        for (int k = 0; k < 4; ++k) {
          const f32x4 zz = cs[k & 1] * lc[k >> 1].x + acc[k >> 1][k & 1];
          z[2 * k] = f32x2{zz.x, zz.y}; z[2 * k + 1] = f32x2{zz.z, zz.w};
          ga8[2 * k] = ga8[2 * k + 1] = lc[k >> 1].y; gb8[2 * k] = gb8[2 * k + 1] = lc[k >> 1].z;
        }
        SB();
        geluN<8>(z, ga8, gb8);
        unsigned hp[8];
#pragma unroll
        for (int k = 0; k < 8; ++k) { const f32x2 v = z[k] * lc[k >> 2].w; hp[k] = pk(v.x, v.y) & keep[k >> 1]; }
        const int up = 2 * ct + ch;
        if (RARE(up == 4 || up == 13)) {
#pragma unroll
          for (int k = 0; k < 4; ++k) ST8((v2u{hp[2 * k], hp[2 * k + 1]}), dst[k], up * 1024);
        } else {
#pragma unroll
          for (int tt = 0; tt < 2; ++tt) ST16((v4u{hp[4 * tt], hp[4 * tt + 1], hp[4 * tt + 2], hp[4 * tt + 3]}), dst[2 * tt], up * 1024);
        }
        STAMP(3);
        DRAIN();
        SIGNAL(FA, i);
        post(pa + P_FREE, 3);
        STAMP(4);
      }
      {
        wait_full(4);
#pragma unroll
        for (int c = 0; c < 4; ++c) { ab4[c] += 8 * SLOT; asm volatile("" : "+v"(ab4[c])); }
        constexpr int PB = 32;
        s16x4 wl[PB], wh[PB];
#pragma unroll
        for (int k = 0; k < PB; ++k) { wl[k] = trd(ab4[k & 3] + (k >> 2) * 4096); wh[k] = trd(ab4[k & 3] + (k >> 2) * 4096 + 2048); }
        SB();
        f32x2 xr[8];
#pragma unroll
        for (int l = 0; l < 4; ++l) { xr[2 * l] = f32x2{lo16(xres[l].x), hi16(xres[l].x)}; xr[2 * l + 1] = f32x2{lo16(xres[l].y), hi16(xres[l].y)}; }
#pragma unroll
        for (int k = 0; k < 8; ++k) asm volatile("" : "+v"(xr[k]));
        wait_bit(L_RDY + 4 * R_H, 1u << (i >> 1));
        STAMP(5);
        bf16x8 hf[18];
        const int hsrc = OFF_H1 + T * (18 * 1024);
#pragma unroll
        for (int u = 0; u < 9; ++u) hf[u] = asfrag(LD16(lane * 16 + u * 1024, hsrc));
        SB();
        f32x4 acc[4];
        unsigned zv;
        sfor<72>([&](auto K) INL {
          constexpr int k = K.value, u = k >> 2;
          mfa<(k < 4)>(acc[k & 3], cat(wl[k % PB], wh[k % PB]), hf[u]); SB();
          if constexpr (k == 17) post(pa + P_FREE, 4);
          if constexpr (k == 60) zv = peek(L_RDY + 4 * R_Z);
          if constexpr ((k & 1) == 0 && k / 2 + 9 < 18) hf[k / 2 + 9] = asfrag(LD16(lane * 16 + (k / 2 + 9) * 1024, hsrc));
          if constexpr (k + PB < 72) { wl[k % PB] = trd(ab4[k & 3] + ((k + PB) >> 2) * 4096); wh[k % PB] = trd(ab4[k & 3] + ((k + PB) >> 2) * 4096 + 2048); }
          SB();
        });
        MFMA_DONE();
        STAMP(6);
        unsigned xp[8];
#pragma unroll
        for (int l = 0; l < 4; ++l) {
          const f32x2 v0 = xr[2 * l] + f32x2{acc[l].x, acc[l].y}, v1 = xr[2 * l + 1] + f32x2{acc[l].z, acc[l].w};
          xp[2 * l] = pkc(v0.x, v0.y); xp[2 * l + 1] = pkc(v1.x, v1.y);
        }
        if (RARE(rfl(zv) == 0)) wait_word(L_RDY + 4 * R_Z, 1);
        CLOB();
        const int dst = OFF_X1 + (T * 18 + 2 * ct) * 1024;
        ST16((v4u{xp[0], xp[1], xp[2], xp[3]}), lane * 16, dst);
        ST16((v4u{xp[4], xp[5], xp[6], xp[7]}), lane * 16 + 1024, dst);
        SB();
        float sm = 0.f, sq = 0.f;
#pragma unroll
        for (int k = 0; k < 8; ++k) {
          const bf16x2_t v = __builtin_bit_cast(bf16x2_t, xp[k]);
          sm = __builtin_amdgcn_fdot2_f32_bf16(v, bf16x2_t{(__bf16)1.0f, (__bf16)1.0f}, sm, false);
          sq = __builtin_amdgcn_fdot2_f32_bf16(v, v, sq, false);
        }
        sm = qsum(sm); sq = qsum(sq);
        ST8((v2u{min(__float_as_uint(sm), EMPTY - 1u), min(__float_as_uint(sq), EMPTY - 1u)}), q ? 0x7ffffff0 : t * 8, OFF_STAT + (T * 9 + ct) * 128);
        SIGNAL(FB, i);
        post(pa + P_FREE, 5);
        STAMP(7);
      }
    } else {
      const int lt = ((r - 18) * 4 + i) * 64 + lane;
      unsigned sink = 0;
      auto touch = [&](const char* base, unsigned off) INL { asm volatile("global_load_dword %0, %1, %2" : "+v"(sink) : "v"(off), "s"(base) : "memory"); };
      if (CO & 1) {
#pragma unroll
        for (int k = 0; k < 2; ++k) touch(w1, (5183 - min(lt + 3584 * k, 5183)) * 128);
        touch(x + (size_t)j * (128 * D_ * 2), min(lt, 1151) * 128);
      }
      if (CO & 4) DRAIN(); else __builtin_amdgcn_s_sleep(SL1);
#pragma unroll
      for (int k = 0; k < 2; ++k) touch(w2, min(lt + 3584 * k, 5183) * 128);
      if (!(CO & 4)) DRAIN();
      if (MAP) {
#pragma unroll
        for (int k = 0; k < 2; ++k) { const int l = min(lt + 3584 * k, 5183); touch(w3 + (j & 6) * (576 * 2), (l / 9) * (F_ * 2) + (l % 9) * 128); }
      }
      DRAIN();
      if (!(CO & 4)) __builtin_amdgcn_s_sleep(SL2);
#pragma unroll
      for (int k = 0; k < (MAP ? 3 : 2); ++k) touch(w4 + (size_t)(MAP ? (j & 6) : j) * (576 * D_ * 2), min(lt + 3584 * k, MAP ? 10367 : 5183) * 128);
      DRAIN();
      asm volatile("" ::"v"(sink));
    }
    unsigned b0 = (4 * q + (t >> 2)) * 288 + (t & 3) * 8, b1 = b0 + 6 * SLOT, b2 = b0 + 12 * SLOT;
    asm volatile("" : "+v"(b0), "+v"(b1), "+v"(b2));
#define WADDR(z, c) ((((z) % 16) < 6 ? b0 : ((z) % 16) < 12 ? b1 : b2) + (((z) % 16) % 6) * SLOT + (c) * 32)
#define FN(i_) ((i_) < 108 ? (i_) / 9 : 12 + ((i_) - 108) % 6)
#define FC(i_) ((i_) < 108 ? (i_) % 9 : ((i_) - 108) / 6)
    const int T0 = 8 * g + 2 * i;
    const int hdst = OFF_HH + (T0 * 144 + fr * 18) * 1024;
    bf16x8 hb[18][2];
    auto hload = [&](auto K) INL {
      constexpr int m = K.value >> 1, tt = K.value & 1;
      hb[m][tt] = asfrag(LD16(lane * 16 + tt * (144 * 1024), hdst + dstep(m, s) * 1024));
    };
    s16x4 lo[PD], hi[PD];
    unsigned rv, hw = L_HS + 2 * i * 1024 + lane * 16, hw2 = 9 * SLOT + 2 * i * 1024 + lane * 16;
    asm volatile("" : "+v"(hw), "+v"(hw2));
    {
      wait_full(6);
#pragma unroll
      for (int k = 0; k < PD; ++k) { lo[k] = trd(WADDR(0, k)); hi[k] = trd(WADDR(0, k) + 4608); }
      f32x4 acc[2][9];
      const int xsrc = OFF_X1 + T0 * (18 * 1024);
      int xo[2] = {lane * 16, lane * 16 + 18 * 1024};
      asm volatile("" : "+v"(xo[0]), "+v"(xo[1]));
      bf16x8 xb[12][2];
      auto xload = [&](auto K) INL {
        constexpr int n = K.value >> 1, tt = K.value & 1;
        xb[n % 12][tt] = asfrag(LD16(xo[tt] + (n & 3) * 1024, xsrc + (n >> 2) * 4096));
      };
      SB();
      auto xfix = [&](auto K0, auto K1) INL {
        NOUNROLL for (unsigned n = 0; n < TRY_CAP; ++n) { CLOB();
          unsigned m = 0;
          COUNT(1);
          sfor<K1.value - K0.value>([&](auto K) INL { xload(IC<K0.value + K.value>{}); });
          sfor<K1.value - K0.value>([&](auto K) INL { constexpr int kk = K0.value + K.value; m = mx4(m, asv4(xb[(kk >> 1) % 12][kk & 1])); });
          if (!missing(m)) break;
        }
      };
      wait_bit(L_RDY + 4 * R_X, 1u << i);
      STAMP(9);
      sfor<10>([&](auto K) INL { xload(K); });
      SB();
      unsigned chk = mx4(mx4(0u, asv4(xb[0][0])), asv4(xb[0][1])), chk2 = 0;
      if (RARE(missing(chk))) xfix(IC<0>{}, IC<2>{});
      SB();
      f32x4 lc[2], csv;
      unsigned hp[2][2][2];
      unsigned la = L_LN + (32 * i + t) * 16, ca = L_CS3 + q * 16;
      asm volatile("" : "+v"(la), "+v"(ca));
      v4u fv;
      u64 bad;
      unsigned sv;
      auto epi = [&](auto Cc) INL {
        constexpr int c = Cc.value;
        f32x2 z[4];
#pragma unroll
        for (int tt = 0; tt < 2; ++tt) {
          const f32x4 zz = csv * lc[tt].x + acc[tt][c];
          z[2 * tt] = f32x2{zz.x, zz.y}; z[2 * tt + 1] = f32x2{zz.z, zz.w};
        }
        if constexpr (c == 3) sv = min4(fv);
        SB();
        const float ga4[4] = {lc[0].y, lc[0].y, lc[1].y, lc[1].y}, gb4[4] = {lc[0].z, lc[0].z, lc[1].z, lc[1].z};
        geluN<4>(z, ga4, gb4);
#pragma unroll
        for (int tt = 0; tt < 2; ++tt) {
          { hp[tt][c & 1][0] = pk(z[2 * tt].x, z[2 * tt].y); hp[tt][c & 1][1] = pk(z[2 * tt + 1].x, z[2 * tt + 1].y); }
        }
        asm volatile("" : "+v"(hp[0][c & 1][0]), "+v"(hp[0][c & 1][1]), "+v"(hp[1][c & 1][0]), "+v"(hp[1][c & 1][1]));
        if constexpr (c == 3) { if (RARE(sv < 8)) wait_free(8); CLOB(); }
        if constexpr ((c & 1) && c < 8) {
#pragma unroll
          for (int tt = 0; tt < 2; ++tt) {
            const v4u v = {hp[tt][0][0], hp[tt][0][1], hp[tt][1][0], hp[tt][1][1]};
            hb[c >> 1][tt] = asfrag(v);
            if constexpr (c == 1) *(LAS v4u*)(hw + tt * 1024) = v;
            else if constexpr (c < 7) *(LAS v4u*)(hw2 + (c >> 2) * SLOT + tt * 1024) = v;
            else ST16(v, lane * 16 + tt * (144 * 1024), hdst + (pbase(s) + 3) * 1024);
          }
        }
        if constexpr (c == 8) {
#pragma unroll
          for (int tt = 0; tt < 2; ++tt)
            ST8((v2u{hp[tt][0][0], hp[tt][0][1]}), lane * 16 + tt * (144 * 1024) + (s & 1) * 8, hdst + (s < 2 ? 4 : 13) * 1024);
        }
        if constexpr ((c & 1) && c < 7) post(pa + P_RQ, c / 2 + 1);
        SB();
      };
      sfor<162>([&](auto I) INL {
        constexpr int ii = I.value, n = FN(ii), c = FC(ii), nx = ii - 1 + PD;
        const bf16x8 cur = cat(lo[ii % PD], hi[ii % PD]);
        mfa<(n == 0)>(acc[0][c], cur, xb[n % 12][0]); SB();
        constexpr bool ck1 = ii < 99 && (c == 5 || c == 6);
        constexpr bool ck2 = ii >= 72 && ii < 99 && c >= 1 && c <= 4;
        constexpr int k2 = 24 + 4 * (n - 8) + c - 1;
        if constexpr (ii >= 1 && nx < 162) lo[nx % PD] = trd(WADDR(FN(nx), FC(nx)));
        if constexpr (ck1) { chk = mx2(c == 5 ? 0u : chk, asv4(xb[n + 1][c - 5]).x, asv4(xb[n + 1][c - 5]).y); asm volatile("" : "+v"(chk)); }
        if constexpr (ck2) { chk2 = mx2(chk2, asv4(xb[(k2 >> 1) % 12][k2 & 1]).x, asv4(xb[(k2 >> 1) % 12][k2 & 1]).y); asm volatile("" : "+v"(chk2)); }
        if constexpr (ii < 99 && c == 7) { bad = __ballot(chk == EMPTY); asm volatile("" : "+s"(bad)); }
        if constexpr (ii == 99) { bad = __ballot(chk2 == EMPTY); asm volatile("" : "+s"(bad)); }
        if constexpr (ii == 63 || ii == 101) { sv = min3(fv); asm volatile("" : "+s"(sv)); }
        if constexpr (ii == 103) { sv = rfl(rv); asm volatile("" : "+s"(sv)); }
        SB();
        mfa<(n == 0)>(acc[1][c], cur, xb[n % 12][1]); SB();
        if constexpr (ii >= 1 && nx < 162) hi[nx % PD] = trd(WADDR(FN(nx), FC(nx)) + 4608);
        if constexpr (ck1) { chk = mx2(chk, asv4(xb[n + 1][c - 5]).z, asv4(xb[n + 1][c - 5]).w); asm volatile("" : "+v"(chk)); }
        if constexpr (ck2) { chk2 = mx2(chk2, asv4(xb[(k2 >> 1) % 12][k2 & 1]).z, asv4(xb[(k2 >> 1) % 12][k2 & 1]).w); asm volatile("" : "+v"(chk2)); }
        SB();
        if constexpr (ii < 99 && c == 8) { if (RARE(bad != 0)) xfix(IC<2 * n + 2>{}, IC<2 * n + 4>{}); SB(); }
        if constexpr (ii == 100) { if (RARE(bad != 0)) xfix(IC<24>{}, IC<36>{}); SB(); }
        if constexpr (ii < 28 && ii % 2 == 0) { xload(IC<10 + ii / 2>{}); SB(); }
        if constexpr (ii >= 30 && ii < 66 && ii % 3 == 0) { xload(IC<14 + ii / 3>{}); SB(); }
        if constexpr (ii == 18 || ii == 81 || ii == 108 || ii == 159) { post(pa + P_FREE, ii == 18 ? 6 : ii == 81 ? 7 : ii == 108 ? 8 : 9); SB(); }
        if constexpr (ii == 8) STAMP(23);
        if constexpr (ii == 35) STAMP(24);
        if constexpr (ii == 71) STAMP(25);
        if constexpr (ii == 58 || ii == 96) { fv = peek4(L_FULL); SB(); }
        if constexpr (ii == 64) { if (RARE(sv < 7)) wait_full(7); CLOB(); SB(); }
        if constexpr (ii == 97) { rv = peek(L_RDY + 4 * R_LN); SB(); }
        if constexpr (ii == 102) { if (RARE(sv < 10)) wait_full(10); CLOB(); SB(); }
        if constexpr (ii == 104) {
          if (RARE(sv == 0)) wait_word(L_RDY + 4 * R_LN, 1);
          CLOB();
          lc[0] = *(const LAS f32x4*)la; lc[1] = *(const LAS f32x4*)(la + 256);
          SB(); STAMP(10);
        }
        if constexpr (ii >= 108 && n == 14 && c >= 1) { csv = *(const LAS f32x4*)(ca + (c - 1) * 64); SB(); }
        if constexpr (ii >= 108 && n == 15 && c == 4) { fv = peek4(L_FREE); SB(); }
        if constexpr (ii >= 108 && n == 17 && c >= 1) epi(IC<c - 1>{});
        if constexpr (ii == 150) { fv = peek4(L_FULL); SB(); }
      });
      csv = *(const LAS f32x4*)(ca + 8 * 64);
      if (RARE(min3(fv) < 11)) wait_full(11);
      CLOB();
#pragma unroll
      for (int k = 0; k < PD; ++k) { lo[k] = trd(WADDR(18, k)); hi[k] = trd(WADDR(18, k) + 4608); }
      SB();
      epi(IC<8>{});
      STAMP(11);
    }
    {
      f32x4 acc[2][9];
      unsigned pw = L_PS + 2 * i * 1024 + lane * 16, pw8 = L_PS + 32768 + 2 * i * 512 + lane * 8;
      asm volatile("" : "+v"(pw), "+v"(pw8));
      int po[2];
#pragma unroll
      for (int tt = 0; tt < 2; ++tt) { po[tt] = (2 * i + tt == fr) ? 0x7ff00000 : lane * 8; asm volatile("" : "+v"(po[tt])); }
      const int pdst = OFF_P + (((g * 4 + s) * 8 + 2 * i) * 8 + fr) * PUNIT;
      v4u fv;
      unsigned sv;
      auto epi = [&](auto Cc) INL {
        constexpr int c = Cc.value;
        if constexpr ((c & 1) && c < 8) {
#pragma unroll
          for (int tt = 0; tt < 2; ++tt) {
            const f32x4 a0 = acc[tt][c - 1], a1 = acc[tt][c];
            const v4u v = {pk(a0.x, a0.y), pk(a0.z, a0.w), pk(a1.x, a1.y), pk(a1.z, a1.w)};
            if constexpr (c == 7) ST16(v, 2 * po[tt] , pdst + tt * (8 * PUNIT) + 3 * 1024);
            *(LAS v4u*)(pw + tt * 1024 + (c >> 1) * 8192) = v;
          }
        }
        if constexpr (c == 8) {
#pragma unroll
          for (int tt = 0; tt < 2; ++tt) {
            const f32x4 a0 = acc[tt][8];
            const v2u v = {pk(a0.x, a0.y), pk(a0.z, a0.w)};
            ST8(v, po[tt], pdst + tt * (8 * PUNIT) + 4096);
            *(LAS v2u*)(pw8 + tt * 512) = v;
          }
        }
        if constexpr ((c & 1) && c < 7) post(pa + P_RQ, 3 + c / 2 + 1);
        SB();
      };
      constexpr int HS0 = HS0V, HS1 = HS1V, SIGS = SIGSV, HS2 = HS2V, HS3 = HS3V;
      sfor<162>([&](auto I) INL {
        constexpr int ii = I.value, n = FN(ii), c = FC(ii), nx = ii - 1 + PD;
        constexpr int G = ii == HS0 ? 0 : ii == HS1 ? 1 : ii == HS2 ? 2 : ii == HS3 ? 3 : -1;
        const bf16x8 cur = cat(lo[ii % PD], hi[ii % PD]);
        mfa<(n == 0)>(acc[0][c], cur, hb[n][0]); SB();
        if constexpr (ii >= 1 && nx < 162) lo[nx % PD] = trd(WADDR(18 + FN(nx), FC(nx)));
        if constexpr (G >= 0) { sv = rfl(rv); asm volatile("" : "+s"(sv)); }
        if constexpr (ii == 48 || ii == 84 || ii == 102) { sv = min3(fv); asm volatile("" : "+s"(sv)); }
        if constexpr (ii == 106) { sv = min4(fv); asm volatile("" : "+s"(sv)); }
        SB();
        mfa<(n == 0)>(acc[1][c], cur, hb[n][1]); SB();
        if constexpr (ii >= 1 && nx < 162) hi[nx % PD] = trd(WADDR(18 + FN(nx), FC(nx)) + 4608);
        SB();
        if constexpr (ii == SIGS) { VMW(6); SIGNAL(FC, 3 + i); SB(); }
        if constexpr (ii + 4 == HS0 || ii + 4 == HS1 || ii + 4 == HS2 || ii + 4 == HS3) { rv = peek(L_RDY + 4 * R_HH); SB(); }
        if constexpr (G >= 0) {
          const unsigned need = G < 3 ? 1u << G : 8u << i;
          if (RARE((sv & need) != need)) wait_bit(L_RDY + 4 * R_HH, need);
          CLOB(); SB(); STAMP(G == 0 ? 27 : G == 1 ? 28 : G == 2 ? 13 : 14);
        }
        if constexpr (ii >= HS0 && ii < HS0 + 6) { hload(IC<8 + ii - HS0>{}); SB(); }
        if constexpr (ii >= HS1 && ii < HS1 + 6) { hload(IC<14 + ii - HS1>{}); SB(); }
        if constexpr (ii >= HS2 && ii < HS2 + 6) { hload(IC<20 + ii - HS2>{}); SB(); }
        if constexpr (ii >= HS3 && ii < HS3 + 10) { hload(IC<26 + ii - HS3>{}); SB(); }
        if constexpr (ii == 18 || ii == 54) { post(pa + P_FREE, ii == 18 ? 10 : 11); SB(); }
        if constexpr (ii == 44 || ii == 79 || ii == 97) { fv = peek4(L_FULL); SB(); }
        if constexpr (ii == 48) { if (RARE(sv < 12)) wait_full(12); CLOB(); SB(); }
        if constexpr (ii == 84) { if (RARE(sv < 13)) wait_full(13); CLOB(); SB(); }
        if constexpr (ii == 102) { if (RARE(sv < 14)) wait_full(14); fv = peek4(L_FREE); SB(); STAMP(26); }
        if constexpr (ii == 106) { if (RARE(sv < 11)) wait_free(11); CLOB(); SB(); }
        if constexpr (ii >= 108 && n == 17 && c >= 1) epi(IC<c - 1>{});
      });
      MFMA_DONE();
      epi(IC<8>{});
      STAMP(15);
      asm volatile("s_waitcnt vmcnt(0) lgkmcnt(0)" ::: "memory");
      SIGNAL(FD, 3 + i);
      STAMP(16);
    }
  } else if (w < 7) {
    const int ld = w - 4;
    const unsigned fullw = L_FULL + 4 * ld;
    unsigned v3[3], v4a[9];
#pragma unroll
    for (int e = 0; e < 3; ++e) {
      const int idx = (3 * ld + e) * 64 + lane, row = idx / 18, ch = idx % 18;
      const int sc = (ch + 2 * (s & 1)) % 18;
      v3[e] = row * (F_ * 2) + sc * 16;
    }
#pragma unroll
    for (int e = 0; e < 9; ++e) { const int idx = e * 64 + lane; v4a[e] = (idx / 18) * (D_ * 2) + ((idx % 18 + 2 * (s & 1)) % 18) * 16; }
    const char* w3b = w3 + (576 * fr + 144 * s) * 2;
    const char* w4b = w4 + (size_t)(576 * fr) * (D_ * 2) + 144 * s * 2;
    auto w3slice = [&](int n) INL {
#pragma unroll
      for (int e = 0; e < 3; ++e) dma(w3b + (size_t)n * (32 * F_ * 2), v3[e], (n & 15) * SLOT + (3 * ld + e) * 1024);
    };
    auto w4whole = [&](int m, int u, auto CAP) INL {
      const char* src = w4b + u * (32 * D_ * 2);
#pragma unroll
      for (int e = 0; e < 9; ++e) { dma(src, v4a[e], ((18 + m) & 15) * SLOT + e * 1024); if constexpr (CAP.value) VMW(DCAP); }
    };
    const __amdgpu_buffer_rsrc_t r4 = __builtin_amdgcn_make_buffer_rsrc((void*)w4b, 0, 576 * D_ * 2 - 144 * s * 2, 0x00020000);
    v4u w4r[4][9];
    auto w4get = [&](auto K) INL {
      const int so = dstep(6 + ld + 3 * K.value, s) * (32 * D_ * 2);
#pragma unroll
      for (int e = 0; e < 9; ++e) { constexpr int eo[9] = {0, 2, 4, 6, 8, 1, 3, 5, 7}; w4r[K.value][eo[e]] = __builtin_amdgcn_raw_buffer_load_b128(r4, v4a[eo[e]], so, 0); }
      CLOB(); SB();
    };
    auto w4put = [&](auto K) INL {
      const unsigned a = ((24 + ld + 3 * K.value) & 15) * SLOT + lane * 16;
#pragma unroll
      for (int e = 0; e < 9; ++e) *(LAS v4u*)(a + e * 1024) = w4r[K.value][e];
      CLOB(); SB();
    };
    f32x4 cs[3] = {{0, 0, 0, 0}, {0, 0, 0, 0}, {0, 0, 0, 0}};
    const unsigned cb = (4 * q + (t >> 2)) * 288 + (t & 3) * 8 + ld * 96;
    auto colsum3 = [&](int n0, int n1) INL {
      __builtin_amdgcn_s_setprio(1);
      NOUNROLL for (int n = n0; n < n1; n += 2) {
        const unsigned a0 = cb + (n & 15) * SLOT;
        s16x4 l[6], h[6];
#pragma unroll
        for (int k = 0; k < 6; ++k) { l[k] = trd(a0 + (k / 3) * SLOT + (k % 3) * 32); h[k] = trd(a0 + (k / 3) * SLOT + 4608 + (k % 3) * 32); }
        SB();
#pragma unroll
        for (int k = 0; k < 6; ++k) cs[k % 3] = mfma(cat(l[k], h[k]), ones, cs[k % 3]);
      }
      __builtin_amdgcn_s_setprio(0);
      CLOB();
    };
    if (ab) {
      const unsigned v1 = (8 * (lane >> 5) + ((lane >> 3) & 3)) * (D_ * 2) + (((lane & 7) ^ ((q & 3) << 1)) << 4);
      auto w1units = [&](int u0) INL {
#pragma unroll
        for (int e = 0; e < 8; ++e) {
          const int u = u0 + 3 * (e >> 2);
          dma(w1 + 128 * ct + (32 * u + 16 * (e & 1) + 4 * ((e >> 1) & 1)) * (D_ * 2), v1, u * 4096 + (e & 3) * 1024);
        }
      };
      w1units(ld); w1units(ld + 6);
      VMW(8); lds_set(fullw, 1, lane);
      w1units(ld + 12);
      VMW(8); lds_set(fullw, 2, lane);
      STAMP(1);
      DRAIN(); lds_set(fullw, 3, lane);
      wait_free(2);
      const unsigned v2 = (lane >> 3) * (D_ * 2) + (((lane & 7) ^ ((q & 3) << 1)) << 4);
      NOUNROLL for (int u = ld; u < 18; u += 3) {
#pragma unroll
        for (int e = 0; e < 4; ++e) dma(w2 + 128 * ct + (32 * u + 8 * e) * (D_ * 2), v2, 8 * SLOT + u * 4096 + e * 1024);
      }
      if (!(CO & 2)) w4get(IC<0>{});
      STAMP(2);
      if (CO & 2) DRAIN(); else VMW(9);
      lds_set(fullw, 4, lane);
      STAMP(3);
      wait_free(3);
      NOUNROLL for (int n = 0; n < 8; ++n) w3slice(n);
      STAMP(4);
      DRAIN(); lds_set(fullw, 6, lane);
      wait_full(6);
      colsum3(0, 8);
      wait_free(4);
      NOUNROLL for (int n = 8; n < 11; ++n) w3slice(n);
      wait_free(5);
      NOUNROLL for (int n = 11; n < 16; ++n) w3slice(n);
      if (CO & 2) w4get(IC<0>{}); else w4get(IC<1>{});
      STAMP(5);
      VMW(9); lds_set(fullw, 7, lane);
      STAMP(6);
      wait_full(7);
      colsum3(8, 16);
    } else {
      __builtin_amdgcn_s_sleep(SL2);
      NOUNROLL for (int n = 0; n < 16; ++n) w3slice(n);
      DRAIN(); lds_set(fullw, 7, lane);
      w4get(IC<0>{}); if (!(CO & 2)) w4get(IC<1>{});
      wait_full(7);
      colsum3(0, 16);
    }
    lds_set(fullw, 8, lane);
    STAMP(7);
    wait_free(6); wait_full(8);
    STAMP(11);
    w3slice(16); w3slice(17);
    DRAIN(); lds_set(fullw, 9, lane);
    STAMP(12);
    wait_full(9);
    colsum3(16, 18);
    if (t == 0) {
#pragma unroll
      for (int c = 0; c < 3; ++c) *(LAS f32x4*)(L_CS3 + (3 * ld + c) * 64 + q * 16) = cs[c];
    }
    CLOB();
    lds_set(fullw, 10, lane);
    STAMP(8);
    if (PRIO & 2) __builtin_amdgcn_s_setprio(2);
    const u64 tag = get_tag();
    auto hround = [&](int p) INL {
      wait_min4(L_RQ, p + 1);
      v4u d[8];
#pragma unroll
      for (int k = 0; k < 8; ++k) d[k] = *(const LAS v4u*)((p ? (8 + p) * SLOT : L_HS) + k * 1024 + lane * 16);
#pragma unroll
      for (int k = 0; k < 8; ++k) ST16(d[k], lane * 16 + k * (144 * 1024), OFF_HH + (8 * g * 144 + fr * 18 + pbase(s) + p) * 1024);
      CLOB();
    };
    auto pround = [&](int p) INL {
      wait_min4(L_RQ, 3 + p + 1);
      const int so = OFF_P + ((g * 4 + s) * 64 + fr) * PUNIT + p * 1024;
      v4u d[7];
#pragma unroll
      for (int k = 0; k < 7; ++k) d[k] = *(const LAS v4u*)(L_PS + p * 8192 + ((fr + 1 + k) & 7) * 1024 + lane * 16);
#pragma unroll
      for (int k = 0; k < 7; ++k) ST16(d[k], lane * 16, so + ((fr + 1 + k) & 7) * (8 * PUNIT));
      DRAIN();
      SIGNAL(FD, p);
    };
    wait_free(7);
    NOUNROLL for (int m = ld; m < 6; m += 3) w4whole(m, dstep(m, s), IC<0>{});
    if (CO & 2) w4get(IC<1>{});
    w4get(IC<2>{}); w4get(IC<3>{});
    if (ld > 0) { DRAIN(); lds_set(fullw, 11, lane); }
    hround(ld);
    DRAIN();
    SIGNAL(FC, ld);
    lds_set(fullw, 11, lane);
    w4put(IC<0>{});
    if (ld == 0) w4put(IC<1>{});
    lds_set(fullw, 12, lane);
    STAMP(13);
    wait_free(9); wait_full(10);
    if (ld > 0) w4put(IC<1>{});
    w4put(IC<2>{});
    if (ld == 0) w4put(IC<3>{});
    lds_set(fullw, 13, lane);
    if (ld > 0) { wait_free(10); w4put(IC<3>{}); }
    lds_set(fullw, 14, lane);
    STAMP(10);
    pround(ld);
    STAMP(23);
  } else {
    if (PRIO & 1) __builtin_amdgcn_s_setprio(3);
    const u64 tag = __hip_atomic_load((GAS u64*)(ws + OFF_EPOCH), __ATOMIC_RELAXED, __HIP_MEMORY_SCOPE_AGENT) + 1ull;
    if (lane == 0) {
      *(volatile LAS u64*)L_TAG = tag;
      *(volatile LAS unsigned*)(L_RDY + 4 * R_TAG) = 1u;
      __hip_atomic_store(flagp(ws, FS, b, 0), tag, __ATOMIC_RELAXED, __HIP_MEMORY_SCOPE_AGENT);
    }
    {
      const v4u e4 = {EMPTY, EMPTY, EMPTY, EMPTY};
      if (ab) {
        wait_free(3);
#pragma unroll
        for (int k = 0; k < 8; ++k) ST16(e4, lane * 16 + (k & 1) * 1024 + (k >> 1) * (18 * 1024), OFF_X1 + (4 * rt * 18 + 2 * ct) * 1024);
        if (lane < 32) ST16(e4, OFF_STAT + (lane >> 3) * (9 * 128) + (lane & 7) * 16, (4 * rt * 9 + ct) * 128);
        DRAIN();
        lds_set(L_RDY + 4 * R_Z, 1, lane);
      }
    }
    STAMP(1);
    auto poll = [&](GAS u64* p) INL {
      NOUNROLL for (unsigned n = 0; n < GLB_CAP; ++n) {
        const u64 v = __hip_atomic_load(p, __ATOMIC_RELAXED, __HIP_MEMORY_SCOPE_AGENT);
        if (__all(v == tag)) break;
        __builtin_amdgcn_s_sleep(1);
      }
      CLOB();
    };
    auto poll_bits = [&](auto TWO, GAS u64* p0, GAS u64* p1, int word, u64 ma, u64 mb, u64 mc, u64 md) INL {
      unsigned done = 0;
      NOUNROLL for (unsigned n = 0; n < GLB_CAP; ++n) {
        const u64 v0 = __hip_atomic_load(p0, __ATOMIC_RELAXED, __HIP_MEMORY_SCOPE_AGENT);
        u64 v1 = v0;
        if constexpr (TWO.value) v1 = __hip_atomic_load(p1, __ATOMIC_RELAXED, __HIP_MEMORY_SCOPE_AGENT);
        const u64 bad0 = __ballot(v0 != tag), bad1 = __ballot(v1 != tag);
        const unsigned now = ((bad0 & ma) ? 0u : 1u) | ((bad0 & mb) ? 0u : 2u) | ((bad1 & mc) ? 0u : 4u) | ((bad1 & md) ? 0u : 8u);
        if (now != done) { done = now; lds_set(L_RDY + 4 * word, now, lane); }
        if (now == 15u) break;
        __builtin_amdgcn_s_sleep(1);
      }
      lds_set(L_RDY + 4 * word, 15u, lane);
      CLOB();
    };
    constexpr u64 M01 = 0x3333333333333333ull, M23 = 0xccccccccccccccccull;
    const int pl = (lane % 36) >> 2;
    if (ab) poll_bits(IC<0>{}, flagp(ws, FA, j + 8 * ((r & 1) + 2 * pl), lane & 3), nullptr, R_H, M01, M23, M01, M23);
    STAMP(2);
    poll_bits(IC<1>{}, flagp(ws, FB, g + 16 * pl, lane & 3), flagp(ws, FB, g + 8 + 16 * pl, lane & 3), R_X, M01, M23, M01, M23);
    STAMP(3);
    {
      v4u st[9];
      NOUNROLL for (unsigned n = 0; n < TRY_CAP; ++n) { CLOB();
        unsigned m = 0;
#pragma unroll
        for (int k = 0; k < 9; ++k) st[k] = LD16(OFF_STAT + ((lane >> 3) * 9 + k) * 128 + (lane & 7) * 16, g * (8 * 9 * 128));
#pragma unroll
        for (int k = 0; k < 9; ++k) m = mx4(m, st[k]);
        if (!missing(m)) break;
      }
      f32x4 a = __builtin_bit_cast(f32x4, st[0]);
#pragma unroll
      for (int k = 1; k < 9; ++k) a += __builtin_bit_cast(f32x4, st[k]);
      *(LAS f32x4*)(L_LN + lane * 32) = ln_consts(a.x, a.y);
      *(LAS f32x4*)(L_LN + lane * 32 + 16) = ln_consts(a.z, a.w);
      CLOB();
      lds_set(L_RDY + 4 * R_LN, 1, lane);
    }
    if (b == 0) {
      NOUNROLL for (int k = 0; k < 4; ++k) poll(flagp(ws, FS, k * 64 + lane, 0));
      if (lane == 0) __hip_atomic_store((GAS u64*)(ws + OFF_EPOCH), tag, __ATOMIC_RELAXED, __HIP_MEMORY_SCOPE_AGENT);
    }
    auto poll5 = [&](GAS u64* p0, int word, auto NB) INL {
      constexpr unsigned all = (1u << NB.value) - 1u;
      unsigned done = 0;
      NOUNROLL for (unsigned n = 0; n < GLB_CAP; ++n) {
        const u64 bad = __ballot(__hip_atomic_load(p0, __ATOMIC_RELAXED, __HIP_MEMORY_SCOPE_AGENT) != tag);
        unsigned now = 0;
#pragma unroll
        for (int k = 0; k < NB.value; ++k) now |= (bad & (0x0101010101010101ull << k)) ? 0u : 1u << k;
        if (now != done) { done = now; lds_set(L_RDY + 4 * word, now, lane); }
        if (now == all) break;
        __builtin_amdgcn_s_sleep(1);
      }
      lds_set(L_RDY + 4 * word, all, lane);
    };
    poll5(flagp(ws, FC, j + 8 * ((r & ~3) + ((lane >> 3) & 3)), lane & 7), R_HH, IC<7>{});
    STAMP(4);
    const int wr = lane >> 3;
    poll5(flagp(ws, FD, MAP ? (j & 1) + (wr & 6) + 8 * (((wr & 1) << 4) + (r & 15)) : wr + 8 * r, (lane & 7) < 3 ? (lane & 7) : 3 + (fr >> 1)), R_P, IC<5>{});
    STAMP(5);
  }
  if (w < 2 || (w >= 4 && w < 7)) {
    const int p = w < 2 ? w + 3 : w - 4;
    const int T = 8 * g + fr;
    const int u = p < 4 ? pbase(s) + p : (s < 2 ? 4 : 13);
    v4u xv;
    NOUNROLL for (unsigned n = 0; n < TRY_CAP; ++n) { CLOB(); xv = LD16(lane * 16, OFF_X1 + (T * 18 + u) * 1024); if (!missing(mx4(0u, xv))) break; }
    const float rstd = *(const LAS float*)(L_LN + (16 * fr + t) * 16 + 12);
    wait_bit(L_RDY + 4 * R_P, 1u << p);
    asm volatile("mx_pf_end:");
    STAMP(20);
    const int psrc = OFF_P + ((g * 4 + s) * 8 + fr) * (8 * PUNIT) + p * 1024;
    char* orow = out + (size_t)(16 * T + t) * (D_ * 2) + (32 * u + 4 * q) * 2;
    if (p < 4) {
      v4u pv[8];
#pragma unroll
      for (int k = 1; k < 8; ++k) pv[k] = LD16(lane * 16, psrc + ((fr + k) & 7) * PUNIT);
      pv[0] = *(const LAS v4u*)(L_PS + p * 8192 + fr * 1024 + lane * 16);
      float a[8];
#pragma unroll
      for (int e = 0; e < 8; ++e) a[e] = 0.f;
#pragma unroll
      for (int k = 0; k < 8; ++k)
#pragma unroll
        for (int e = 0; e < 4; ++e) { const unsigned d = pv[k][e]; a[2 * e] += lo16(d); a[2 * e + 1] += hi16(d); }
      unsigned o[4];
#pragma unroll
      for (int e = 0; e < 4; ++e) {
        const unsigned h2 = pk(a[2 * e] * rstd, a[2 * e + 1] * rstd), xe = xv[e];
        o[e] = pk(lo16(xe) + lo16(h2), hi16(xe) + hi16(h2));
      }
      *(GAS v2u*)orow = v2u{o[0], o[1]};
      *(GAS v2u*)(orow + 32) = v2u{o[2], o[3]};
    } else {
      v2u pv[8];
#pragma unroll
      for (int k = 1; k < 8; ++k) pv[k] = LD8(lane * 8, psrc + ((fr + k) & 7) * PUNIT);
      pv[0] = *(const LAS v2u*)(L_PS + 32768 + fr * 512 + lane * 8);
      float a[4] = {0.f, 0.f, 0.f, 0.f};
#pragma unroll
      for (int k = 0; k < 8; ++k) { a[0] += lo16(pv[k].x); a[1] += hi16(pv[k].x); a[2] += lo16(pv[k].y); a[3] += hi16(pv[k].y); }
      const unsigned x0 = (s & 1) ? xv.z : xv.x, x1v = (s & 1) ? xv.w : xv.y;
      const unsigned h0 = pk(a[0] * rstd, a[1] * rstd), h1 = pk(a[2] * rstd, a[3] * rstd);
      *(GAS v2u*)(orow + (s & 1) * 32) = v2u{pk(lo16(x0) + lo16(h0), hi16(x0) + hi16(h0)), pk(lo16(x1v) + lo16(h1), hi16(x1v) + hi16(h1))};
    }
    STAMP(21);
  }
#endif
}
}

extern "C" void kernel_launch(void* const* d_in, const int* in_sizes, int n_in, void* d_out, int out_size, void* d_ws, size_t ws_size,
                              hipStream_t stream) {
  mx::mixer_kernel<<<256, 512, 0, stream>>>((const char*)d_in[0], (const char*)d_in[1], (const char*)d_in[2], (const char*)d_in[3],
                                            (const char*)d_in[4], (char*)d_out, (char*)d_ws);
}
```

```cpp
#include <hip/hip_runtime.h>
#include <utility>

#define STAMPS 0
#define HS0V 16
#define HS1V 44
#define HS2V 70
#define SIGSV 34
#define HS3V 92
#define LATE 0
#define PRIO 1
#define CO 3
#define NT 0
#define MAP 1
#define SL1 32
#define SL2 64

#define LAS __attribute__((address_space(3)))
#define GAS __attribute__((address_space(1)))
#define DI __device__ __forceinline__
typedef unsigned v4u __attribute__((ext_vector_type(4)));
typedef unsigned v2u __attribute__((ext_vector_type(2)));
typedef float f32x4 __attribute__((ext_vector_type(4)));
typedef float f32x2 __attribute__((ext_vector_type(2)));
typedef short bf16x8 __attribute__((ext_vector_type(8)));
typedef short s16x4 __attribute__((ext_vector_type(4)));
typedef __bf16 bf16x2_t __attribute__((ext_vector_type(2)));
typedef unsigned long long u64;

namespace mx {
constexpr int D_ = 576, F_ = 4608;
constexpr int OFF_EPOCH = 0;
constexpr int OFF_FLAGS = 4096;
constexpr int OFF_STAT = 344064;
constexpr int OFF_H1 = 524288;
constexpr int OFF_X1 = OFF_H1 + 64 * 18 * 1024;
constexpr int OFF_HH = OFF_X1 + 64 * 18 * 1024;
constexpr int PUNIT = 4608;
constexpr int OFF_P = OFF_HH + 64 * 144 * 1024;
constexpr int OFF_DBG = OFF_P + 8 * 4 * 8 * 8 * PUNIT;
constexpr int WS_BYTES = OFF_DBG + 2 * 256 * 8 * 32 * 8 + 256;
enum { FS = 0, FA = 1, FB = 2, FC = 3, FD = 4 };
constexpr int SLOT = 9216, MISC = 16 * SLOT;
constexpr int L_FULL = MISC, L_FREE = MISC + 16, L_RQ = MISC + 32, L_RDY = MISC + 48  , L_SY = MISC + 96, L_AGR = MISC + 112, L_TAG = MISC + 120;
constexpr int L_DUM = MISC + 128  , L_CS1 = MISC + 512  , L_CS3 = MISC + 1024  , L_LN = MISC + 2048  ,
              L_HS = MISC + 4096  , L_LN1 = L_HS  , L_DBG = MISC + 12288, L_PS = 4 * SLOT  ;
enum { R_H = 0, R_X = 1, R_HH = 2, R_P = 3, R_TAG = 4, R_LN = 5, R_Z = 6 };
constexpr unsigned EMPTY = 0xffffffffu;
constexpr unsigned TRY_CAP = 1u << 12;
constexpr unsigned LDS_CAP = 1u << 21, GLB_CAP = 1u << 16;
#define DCAPV 6
constexpr int PD = 4, DCAP = DCAPV;

template <class F, int... I> DI void sfor_impl(F&& f, std::integer_sequence<int, I...>) { (f(std::integral_constant<int, I>{}), ...); }
template <int N, class F> DI void sfor(F&& f) { sfor_impl(f, std::make_integer_sequence<int, N>{}); }
template <int V> using IC = std::integral_constant<int, V>;
#define INL __attribute__((always_inline))
#define NOUNROLL _Pragma("nounroll")
#define RARE(x) __builtin_expect(!!(x), 0)
#define SB() __builtin_amdgcn_sched_barrier(0)
#define CLOB() asm volatile("" ::: "memory")
#define DRAIN() asm volatile("s_waitcnt vmcnt(0)" ::: "memory")
#define VMW(n) asm volatile("s_waitcnt vmcnt(%0)" ::"i"(n) : "memory")

DI unsigned rfl(unsigned v) { return __builtin_amdgcn_readfirstlane(v); }
DI f32x4 mfma(bf16x8 a, bf16x8 b, f32x4 c) { return __builtin_amdgcn_mfma_f32_16x16x32_bf16(a, b, c, 0, 0, 0); }
template <bool FIRST> DI void mfa(f32x4& c, bf16x8 a, bf16x8 b) {
  if constexpr (FIRST) asm volatile("v_mfma_f32_16x16x32_bf16 %0, %1, %2, 0" : "=&v"(c) : "v"(a), "v"(b));
  else asm volatile("v_mfma_f32_16x16x32_bf16 %0, %1, %2, %0" : "+v"(c) : "v"(a), "v"(b));
}
#define MFMA_DONE() asm volatile("s_nop 7\n\ts_nop 3" ::: "memory")
DI bf16x8 asfrag(v4u v) { return __builtin_bit_cast(bf16x8, v); }
DI s16x4 trd(unsigned addr) { return __builtin_amdgcn_ds_read_tr16_b64_v4i16((LAS s16x4*)addr); }
DI bf16x8 cat(s16x4 lo, s16x4 hi) { return __builtin_shufflevector(lo, hi, 0, 1, 2, 3, 4, 5, 6, 7); }
DI unsigned pk(float a, float b) { bf16x2_t r = __builtin_convertvector(f32x2{a, b}, bf16x2_t); return __builtin_bit_cast(unsigned, r); }
DI unsigned pkc(float a, float b) { return min(pk(a, b), EMPTY - 1u); }
DI unsigned mx2(unsigned m, unsigned a, unsigned b) { return max(max(m, a), b); }
DI unsigned mx4(unsigned m, v4u d) { return mx2(mx2(m, d.x, d.y), d.z, d.w); }
DI bool missing(unsigned m) { return __any(m == EMPTY); }
DI v4u asv4(bf16x8 f) { return __builtin_bit_cast(v4u, f); }
DI float lo16(unsigned p) { return __uint_as_float(p << 16); }
DI float hi16(unsigned p) { return __uint_as_float(p & 0xffff0000u); }
DI float bfr(float a) { return lo16(pk(a, a)); }
DI float qsum(float v) {
  auto r = __builtin_amdgcn_permlane16_swap(__float_as_uint(v), __float_as_uint(v), false, false);
  float a = __uint_as_float(r[0]) + __uint_as_float(r[1]);
  auto r2 = __builtin_amdgcn_permlane32_swap(__float_as_uint(a), __float_as_uint(a), false, false);
  return __uint_as_float(r2[0]) + __uint_as_float(r2[1]);
}
constexpr float GA = -2.302208198144325f;
constexpr float GB = -0.1029432397357593f;
DI f32x4 ln_consts(float s, float ss) {
  const float m = s * (1.0f / D_);
  float var = __builtin_fmaf(-m, m, ss * (1.0f / D_));
  var = var > 0.f ? var : 0.f;
  const float sd = bfr(__builtin_amdgcn_sqrtf(bfr(bfr(var) + 1e-5f)));
  const float r = __builtin_amdgcn_rcpf(sd);
  return f32x4{-bfr(m), GA * r, GB * r * r * r, r};
}
template <int N> DI void geluN(f32x2 (&z)[N], const float (&a)[N], const float (&b)[N]) {
  f32x2 p[N];
#pragma unroll
  for (int k = 0; k < N; ++k) p[k] = z[k] * z[k];
  SB();
#pragma unroll
  for (int k = 0; k < N; ++k) p[k] = p[k] * b[k] + a[k];
  SB();
#pragma unroll
  for (int k = 0; k < N; ++k) p[k] = z[k] * p[k];
  SB();
#pragma unroll
  for (int k = 0; k < N; ++k) { p[k].x = __builtin_amdgcn_exp2f(p[k].x); p[k].y = __builtin_amdgcn_exp2f(p[k].y); }
  SB();
#pragma unroll
  for (int k = 0; k < N; ++k) p[k] = p[k] + 1.0f;
  SB();
#pragma unroll
  for (int k = 0; k < N; ++k) { p[k].x = __builtin_amdgcn_rcpf(p[k].x); p[k].y = __builtin_amdgcn_rcpf(p[k].y); }
  SB();
#pragma unroll
  for (int k = 0; k < N; ++k) z[k] = z[k] * p[k];
  SB();
}

DI unsigned ldsw(int addr) { return *(volatile LAS unsigned*)addr; }
DI v4u lds4(int addr) { return *(volatile LAS v4u*)addr; }
DI void lds_set(int addr, unsigned v, int lane) { if (lane == 0) *(volatile LAS unsigned*)addr = v; }
DI v4u peek4(int addr) { CLOB(); return *(const LAS v4u*)addr; }
DI unsigned peek(int addr) { CLOB(); return *(const LAS unsigned*)addr; }
DI void post(unsigned addr, unsigned v) { CLOB(); *(LAS unsigned*)addr = v; CLOB(); }
DI void wait_word(int addr, unsigned v) {
  NOUNROLL for (unsigned n = 0; n < LDS_CAP; ++n) { if (rfl(ldsw(addr)) >= v) break; __builtin_amdgcn_s_sleep(1); }
  CLOB();
}
DI unsigned min3(v4u x) { return rfl(min(min(x.x, x.y), x.z)); }
DI unsigned min4(v4u x) { return rfl(min(min(x.x, x.y), min(x.z, x.w))); }
DI void wait_full(unsigned v) {
  NOUNROLL for (unsigned n = 0; n < LDS_CAP; ++n) { if (min3(lds4(L_FULL)) >= v) break; __builtin_amdgcn_s_sleep(1); }
  CLOB();
}
DI void wait_min4(int addr, unsigned v) {
  NOUNROLL for (unsigned n = 0; n < LDS_CAP; ++n) { if (min4(lds4(addr)) >= v) break; __builtin_amdgcn_s_sleep(1); }
  CLOB();
}
DI void wait_free(unsigned v) { wait_min4(L_FREE, v); }
DI void wait_bit(int addr, unsigned bit) {
  NOUNROLL for (unsigned n = 0; n < LDS_CAP; ++n) { if ((rfl(ldsw(addr)) & bit) == bit) break; __builtin_amdgcn_s_sleep(1); }
  CLOB();
}
DI GAS u64* flagp(char* ws, int kind, int b, int wv) { return (GAS u64*)(ws + OFF_FLAGS + (kind * 256 + b) * 128 + wv * 8); }
DI u64 get_tag() {
  wait_word(L_RDY + 4 * R_TAG, 1);
  return ((u64)rfl(ldsw(L_TAG + 4)) << 32) | rfl(ldsw(L_TAG));
}
#define LD16(off, soff) __builtin_amdgcn_raw_buffer_load_b128(rs, (off), (soff), 16)
#define LD8(off, soff) __builtin_amdgcn_raw_buffer_load_b64(rs, (off), (soff), 16)
#define ST16(v, off, soff) do { v4u d_ = (v); asm volatile("" : "+v"(d_)); __builtin_amdgcn_raw_buffer_store_b128(d_, rs, (off), (soff), 16); asm volatile("s_nop 1" ::"v"(d_) : "memory"); } while (0)
#define ST8(v, off, soff) __builtin_amdgcn_raw_buffer_store_b64((v), rs, (off), (soff), 16)
#define SIGNAL(kind, wv) do { CLOB(); ST8((v2u{(unsigned)tag, (unsigned)(tag >> 32)}), lane ? 0x7ffffff0 : 0, OFF_FLAGS + ((kind) * 256 + b) * 128 + (wv) * 8); CLOB(); } while (0)
DI void dma(const char* sbase, unsigned voff, unsigned ldsaddr) {
  asm volatile("s_mov_b32 m0, %0\n\ts_nop 0\n\tglobal_load_lds_dwordx4 %1, %2" ::"s"(ldsaddr), "v"(voff), "s"(sbase) : "memory");
}
template <bool N> DI void dman(const char* sbase, unsigned voff, unsigned ldsaddr) {
  if constexpr (N) asm volatile("s_mov_b32 m0, %0\n\ts_nop 0\n\tglobal_load_lds_dwordx4 %1, %2 nt" ::"s"(ldsaddr), "v"(voff), "s"(sbase) : "memory");
  else dma(sbase, voff, ldsaddr);
}
DI void dmac(const char* sbase, unsigned voff, unsigned ldsaddr) { dma(sbase, voff, ldsaddr); VMW(DCAP); }
DI int pbase(int s) { return (9 * s + (s & 1)) >> 1; }
DI int dstep(int M, int s) {
  if (M < 4) return pbase(s) + M;
  else if (M < 16) return pbase((s + 1 + (M - 4) % 3) & 3) + (M - 4) / 3;
  else return M == 16 ? 4 : 13;
}
#define STAMP(k) do { } while (0)
#define COUNT(k) do { } while (0)

__global__ __launch_bounds__(512, 2) void mixer_kernel(const char* __restrict__ x, const char* __restrict__ w1, const char* __restrict__ w2,
                                                       const char* __restrict__ w3, const char* __restrict__ w4, char* __restrict__ out, char* ws) {
#if defined(__HIP_DEVICE_COMPILE__)
  __shared__ __attribute__((aligned(16))) char smem[163840];
  const int tid = threadIdx.x, lane = tid & 63, w = rfl(tid >> 6);
  const int t = lane & 15, q = lane >> 4;
  const int b = blockIdx.x, j = b & 7, r = b >> 3;
  const bool ab = r < 18;
  const int rt = 2 * j + (r & 1), ct = r >> 1;
  const int g = MAP ? 4 * (j & 1) + ((r >> 2) & 3) : r >> 2, fr = MAP ? (j & 6) + (r >> 4) : j, s = r & 3;
  const __amdgpu_buffer_rsrc_t rs = __builtin_amdgcn_make_buffer_rsrc(ws, 0, WS_BYTES, 0x00020000);
  const bf16x8 ones = {0x3F80, 0x3F80, 0x3F80, 0x3F80, 0x3F80, 0x3F80, 0x3F80, 0x3F80};
  asm volatile("" ::"v"(smem));
  STAMP(0);
  if (tid < 32) *(volatile LAS unsigned*)(MISC + tid * 4) = 0u;
  asm volatile("s_waitcnt lgkmcnt(0)\n\ts_barrier" ::: "memory");
  if (ab) {
    if (w < 4) {
      const char* xsrc = x + (size_t)(4 * rt + w) * (16 * D_ * 2);
#pragma unroll
      for (int e = 0; e < 18; ++e) {
        const unsigned f = (((unsigned)(lane + 64 * e) * 911u) >> 17) & 7u;
        dman<(NT & 1) != 0>(xsrc + e * 1024, (lane * 16) ^ (f << 4), (8 + 2 * w) * SLOT + e * 1024);
      }
    }
  }

  if (w < 4) {
    const int i = w;
    unsigned pa = lane ? L_DUM + 4 * lane : L_FREE + 4 * i;
    asm volatile("" : "+v"(pa));
    const u64 tag = get_tag();
    constexpr int P_FREE = 0, P_RQ = L_RQ - L_FREE, P_SY = L_SY - L_FREE;
    if (ab) {
      unsigned ab4[4];
#pragma unroll
      for (int c = 0; c < 4; ++c) ab4[c] = (4 * q + (t >> 2)) * 128 + ((c ^ ((2 * q + (t >> 3)) & 3)) << 5) + (t & 3) * 8;
      const int T = 4 * rt + i;
      v2u xres[4];
      {
        const int tp = i >> 1, ch = i & 1;
        const unsigned xa = (8 + 2 * i) * SLOT + t * (D_ * 2), xs = ((t >> 1) & 7) << 4;
        unsigned xe[2][2], wb[2][2];
#pragma unroll
        for (int e = 0; e < 2; ++e) {
          xe[0][e] = xa + ((64 * (e ^ tp) + q * 16) ^ xs);
          xe[1][e] = xe[0][e] + (ch ? -2 * SLOT : 2 * SLOT);
          wb[e][0] = (ch ? ab4[2] : ab4[0]) + (e ^ tp) * 4096;
          wb[e][1] = (ch ? ab4[3] : ab4[1]) + (e ^ tp) * 4096;
          asm volatile("" : "+v"(xe[0][e]), "+v"(xe[1][e]), "+v"(wb[e][0]), "+v"(wb[e][1]));
        }
        int dst[4];
        unsigned keep[4], dm[4];
#pragma unroll
        for (int k = 0; k < 4; ++k) {
          const int gt = 4 * ct + 2 * ch + (k & 1), grp = gt / 9;
          const int sh = grp == 0 ? 1 : grp == 1 ? -1 : grp == 2 ? 2 : -2;
          const int ts = 16 * (ch ^ (k >> 1)) + t + sh;
          keep[k] = (ts >= 0 && ts < 32) ? 0xffffffffu : 0u;
          dst[k] = OFF_H1 + ((T & ~1) + ((ts >> 4) & 1)) * (18 * 1024) + ((ts & 15) + 16 * q) * 16 + (k & 1) * 8;
          dm[k] = (q == (t >> 2) && k == (t & 3)) ? 0xffffffffu : 0u;
          asm volatile("" : "+v"(dst[k]), "+v"(keep[k]), "+v"(dm[k]));
        }
        DRAIN();
        post(pa + P_FREE, 1);
        wait_word(L_FREE + 4 * (i ^ 1), 1);
        STAMP(1);
        bf16x8 xf[2][18];
        f32x4 aS = {0, 0, 0, 0}, aG = {0, 0, 0, 0};
#pragma unroll
        for (int p = 0; p < 6; ++p) xf[0][p] = *(const LAS bf16x8*)(xe[0][p & 1] + (p >> 1) * 128);
        SB();
        sfor<18>([&](auto P) INL {
          constexpr int p = P.value;
          aS = mfma(ones, xf[0][p], aS); SB();
          if constexpr (p + 6 < 18) xf[0][p + 6] = *(const LAS bf16x8*)(xe[0][p & 1] + ((p + 6) >> 1) * 128);
          else if constexpr (p + 6 < 22) xres[p - 12] = *(const LAS v2u*)(xa + 128 * ct + ((32 * (p - 12) + 8 * q) ^ xs));
          SB();
          aG = mfma(xf[0][p], xf[0][p], aG); SB();
          xf[1][p] = *(const LAS bf16x8*)(xe[1][p & 1] + (p >> 1) * 128);
          SB();
        });
        CLOB();
        post(pa + P_FREE, 2);
        f32x4 lc[2];
        {
          const float gd = __uint_as_float(((__float_as_uint(aG.x) & dm[0]) | (__float_as_uint(aG.y) & dm[1])) | ((__float_as_uint(aG.z) & dm[2]) | (__float_as_uint(aG.w) & dm[3])));
          lc[0] = ln_consts(aS[0], qsum(gd));
          if (q == 0) *(LAS f32x4*)(L_LN1 + i * 256 + t * 16) = lc[0];
          CLOB();
          post(pa + P_SY, 1);
        }
        SB();
        STAMP(17);
        wait_full(1);
        constexpr int PA = 4;
        s16x4 wl[PA], wh[PA];
#define AADDR(k) (wb[((k) >> 1) & 1][(k) & 1] + ((k) >> 2) * 8192)
#pragma unroll
        for (int k = 0; k < PA; ++k) { wl[k] = trd(AADDR(k)); wh[k] = trd(AADDR(k) + 2048); }
        bf16x8 one2 = ones;
        asm volatile("" : "+v"(one2));
        SB();
        f32x4 acc[2][2], cs[2];
        v4u fv;
        sfor<36>([&](auto K) INL {
          constexpr int k = K.value, p = k >> 1, l = k & 1, nx = k - 1 + PA;
          const bf16x8 cur = cat(wl[k % PA], wh[k % PA]);
          mfa<(p == 0)>(acc[0][l], cur, xf[0][p]); SB();
          if constexpr (nx == 12 || nx == 24) { if (RARE(min3(fv) < nx / 12 + 1)) wait_full(nx / 12 + 1); CLOB(); SB(); }
          if constexpr (k >= 1 && nx < 36) wl[nx % PA] = trd(AADDR(nx));
          SB();
          mfa<(p == 0)>(acc[1][l], cur, xf[1][p]); SB();
          if constexpr (k >= 1 && nx < 36) wh[nx % PA] = trd(AADDR(nx) + 2048);
          if constexpr (nx == 9 || nx == 21) fv = peek4(L_FULL);
          SB();
          if constexpr ((p & 1) == 0) { mfa<(p == 0)>(cs[l], cur, one2); SB(); }
        });
        MFMA_DONE();
        if (t == 0) { *(LAS f32x4*)(L_CS1 + i * 128 + q * 16) = cs[0]; *(LAS f32x4*)(L_CS1 + i * 128 + 64 + q * 16) = cs[1]; }
        CLOB();
        post(pa + P_SY, 2);
        STAMP(2);
        wait_word(L_SY + 4 * (i ^ 1), 1);
        lc[1] = *(const LAS f32x4*)(L_LN1 + (i ^ 1) * 256 + t * 16);
        wait_word(L_SY + 4 * (i ^ 2), 2);
        const f32x4 o0 = *(const LAS f32x4*)(L_CS1 + (i ^ 2) * 128 + q * 16), o1 = *(const LAS f32x4*)(L_CS1 + (i ^ 2) * 128 + 64 + q * 16);
        cs[0] += o0; cs[1] += o1;
        f32x2 z[8];
        float ga8[8], gb8[8];
#pragma unroll
        for (int k = 0; k < 4; ++k) {
          const f32x4 zz = cs[k & 1] * lc[k >> 1].x + acc[k >> 1][k & 1];
          z[2 * k] = f32x2{zz.x, zz.y}; z[2 * k + 1] = f32x2{zz.z, zz.w};
          ga8[2 * k] = ga8[2 * k + 1] = lc[k >> 1].y; gb8[2 * k] = gb8[2 * k + 1] = lc[k >> 1].z;
        }
        SB();
        geluN<8>(z, ga8, gb8);
        unsigned hp[8];
#pragma unroll
        for (int k = 0; k < 8; ++k) { const f32x2 v = z[k] * lc[k >> 2].w; hp[k] = pk(v.x, v.y) & keep[k >> 1]; }
        const int up = 2 * ct + ch;
        if (RARE(up == 4 || up == 13)) {
#pragma unroll
          for (int k = 0; k < 4; ++k) ST8((v2u{hp[2 * k], hp[2 * k + 1]}), dst[k], up * 1024);
        } else {
#pragma unroll
          for (int tt = 0; tt < 2; ++tt) ST16((v4u{hp[4 * tt], hp[4 * tt + 1], hp[4 * tt + 2], hp[4 * tt + 3]}), dst[2 * tt], up * 1024);
        }
        STAMP(3);
        DRAIN();
        SIGNAL(FA, i);
        post(pa + P_FREE, 3);
        STAMP(4);
      }
      {
        wait_full(4);
#pragma unroll
        for (int c = 0; c < 4; ++c) { ab4[c] += 8 * SLOT; asm volatile("" : "+v"(ab4[c])); }
        constexpr int PB = 32;
        s16x4 wl[PB], wh[PB];
#pragma unroll
        for (int k = 0; k < PB; ++k) { wl[k] = trd(ab4[k & 3] + (k >> 2) * 4096); wh[k] = trd(ab4[k & 3] + (k >> 2) * 4096 + 2048); }
        SB();
        f32x2 xr[8];
#pragma unroll
        for (int l = 0; l < 4; ++l) { xr[2 * l] = f32x2{lo16(xres[l].x), hi16(xres[l].x)}; xr[2 * l + 1] = f32x2{lo16(xres[l].y), hi16(xres[l].y)}; }
#pragma unroll
        for (int k = 0; k < 8; ++k) asm volatile("" : "+v"(xr[k]));
        wait_bit(L_RDY + 4 * R_H, 1u << (i >> 1));
        STAMP(5);
        bf16x8 hf[18];
        const int hsrc = OFF_H1 + T * (18 * 1024);
#pragma unroll
        for (int u = 0; u < 9; ++u) hf[u] = asfrag(LD16(lane * 16 + u * 1024, hsrc));
        SB();
        f32x4 acc[4];
        unsigned zv;
        sfor<72>([&](auto K) INL {
          constexpr int k = K.value, u = k >> 2;
          mfa<(k < 4)>(acc[k & 3], cat(wl[k % PB], wh[k % PB]), hf[u]); SB();
          if constexpr (k == 17) post(pa + P_FREE, 4);
          if constexpr (k == 60) zv = peek(L_RDY + 4 * R_Z);
          if constexpr ((k & 1) == 0 && k / 2 + 9 < 18) hf[k / 2 + 9] = asfrag(LD16(lane * 16 + (k / 2 + 9) * 1024, hsrc));
          if constexpr (k + PB < 72) { wl[k % PB] = trd(ab4[k & 3] + ((k + PB) >> 2) * 4096); wh[k % PB] = trd(ab4[k & 3] + ((k + PB) >> 2) * 4096 + 2048); }
          SB();
        });
        MFMA_DONE();
        STAMP(6);
        unsigned xp[8];
#pragma unroll
        for (int l = 0; l < 4; ++l) {
          const f32x2 v0 = xr[2 * l] + f32x2{acc[l].x, acc[l].y}, v1 = xr[2 * l + 1] + f32x2{acc[l].z, acc[l].w};
          xp[2 * l] = pkc(v0.x, v0.y); xp[2 * l + 1] = pkc(v1.x, v1.y);
        }
        if (RARE(rfl(zv) == 0)) wait_word(L_RDY + 4 * R_Z, 1);
        CLOB();
        const int dst = OFF_X1 + (T * 18 + 2 * ct) * 1024;
        ST16((v4u{xp[0], xp[1], xp[2], xp[3]}), lane * 16, dst);
        ST16((v4u{xp[4], xp[5], xp[6], xp[7]}), lane * 16 + 1024, dst);
        SB();
        float sm = 0.f, sq = 0.f;
#pragma unroll
        for (int k = 0; k < 8; ++k) {
          const bf16x2_t v = __builtin_bit_cast(bf16x2_t, xp[k]);
          sm = __builtin_amdgcn_fdot2_f32_bf16(v, bf16x2_t{(__bf16)1.0f, (__bf16)1.0f}, sm, false);
          sq = __builtin_amdgcn_fdot2_f32_bf16(v, v, sq, false);
        }
        sm = qsum(sm); sq = qsum(sq);
        ST8((v2u{min(__float_as_uint(sm), EMPTY - 1u), min(__float_as_uint(sq), EMPTY - 1u)}), q ? 0x7ffffff0 : t * 8, OFF_STAT + (T * 9 + ct) * 128);
        SIGNAL(FB, i);
        post(pa + P_FREE, 5);
        STAMP(7);
      }
    } else {
      const int lt = ((r - 18) * 4 + i) * 64 + lane;
      unsigned sink = 0;
      auto touch = [&](const char* base, unsigned off) INL {
        if (NT & 4) asm volatile("global_load_dword %0, %1, %2 nt" : "+v"(sink) : "v"(off), "s"(base) : "memory");
        else asm volatile("global_load_dword %0, %1, %2" : "+v"(sink) : "v"(off), "s"(base) : "memory");
      };
      if (CO & 1) {
#pragma unroll
        for (int k = 0; k < 2; ++k) touch(w1, (5183 - min(lt + 3584 * k, 5183)) * 128);
        touch(x + (size_t)j * (128 * D_ * 2), min(lt, 1151) * 128);
      }
      if (CO & 4) DRAIN(); else __builtin_amdgcn_s_sleep(SL1);
#pragma unroll
      for (int k = 0; k < 2; ++k) touch(w2, min(lt + 3584 * k, 5183) * 128);
      if (!(CO & 4)) DRAIN();
      if (MAP) {
#pragma unroll
        for (int k = 0; k < 2; ++k) { const int l = min(lt + 3584 * k, 5183); touch(w3 + (j & 6) * (576 * 2), (l / 9) * (F_ * 2) + (l % 9) * 128); }
      }
      DRAIN();
      if (!(CO & 4)) __builtin_amdgcn_s_sleep(SL2);
#pragma unroll
      for (int k = 0; k < (MAP ? 3 : 2); ++k) touch(w4 + (size_t)(MAP ? (j & 6) : j) * (576 * D_ * 2), min(lt + 3584 * k, MAP ? 10367 : 5183) * 128);
      DRAIN();
      asm volatile("" ::"v"(sink));
    }
    unsigned b0 = (4 * q + (t >> 2)) * 288 + (t & 3) * 8, b1 = b0 + 6 * SLOT, b2 = b0 + 12 * SLOT;
    asm volatile("" : "+v"(b0), "+v"(b1), "+v"(b2));
#define WADDR(z, c) ((((z) % 16) < 6 ? b0 : ((z) % 16) < 12 ? b1 : b2) + (((z) % 16) % 6) * SLOT + (c) * 32)
#define FN(i_) ((i_) < 108 ? (i_) / 9 : 12 + ((i_) - 108) % 6)
#define FC(i_) ((i_) < 108 ? (i_) % 9 : ((i_) - 108) / 6)
    const int T0 = 8 * g + 2 * i;
    const int hdst = OFF_HH + (T0 * 144 + fr * 18) * 1024;
    bf16x8 hb[18][2];
    auto hload = [&](auto K) INL {
      constexpr int m = K.value >> 1, tt = K.value & 1;
      hb[m][tt] = asfrag(LD16(lane * 16 + tt * (144 * 1024), hdst + dstep(m, s) * 1024));
    };
    s16x4 lo[PD], hi[PD];
    unsigned rv, hw = L_HS + 2 * i * 1024 + lane * 16, hw2 = 9 * SLOT + 2 * i * 1024 + lane * 16;
    asm volatile("" : "+v"(hw), "+v"(hw2));
    {
      wait_full(6);
#pragma unroll
      for (int k = 0; k < PD; ++k) { lo[k] = trd(WADDR(0, k)); hi[k] = trd(WADDR(0, k) + 4608); }
      f32x4 acc[2][9];
      const int xsrc = OFF_X1 + T0 * (18 * 1024);
      int xo[2] = {lane * 16, lane * 16 + 18 * 1024};
      asm volatile("" : "+v"(xo[0]), "+v"(xo[1]));
      bf16x8 xb[12][2];
      auto xload = [&](auto K) INL {
        constexpr int n = K.value >> 1, tt = K.value & 1;
        xb[n % 12][tt] = asfrag(LD16(xo[tt] + (n & 3) * 1024, xsrc + (n >> 2) * 4096));
      };
      SB();
      auto xfix = [&](auto K0, auto K1) INL {
        NOUNROLL for (unsigned n = 0; n < TRY_CAP; ++n) { CLOB();
          unsigned m = 0;
          COUNT(1);
          sfor<K1.value - K0.value>([&](auto K) INL { xload(IC<K0.value + K.value>{}); });
          sfor<K1.value - K0.value>([&](auto K) INL { constexpr int kk = K0.value + K.value; m = mx4(m, asv4(xb[(kk >> 1) % 12][kk & 1])); });
          if (!missing(m)) break;
        }
      };
      wait_bit(L_RDY + 4 * R_X, 1u << i);
      STAMP(9);
      sfor<10>([&](auto K) INL { xload(K); });
      SB();
      unsigned chk = mx4(mx4(0u, asv4(xb[0][0])), asv4(xb[0][1])), chk2 = 0;
      if (RARE(missing(chk))) xfix(IC<0>{}, IC<2>{});
      SB();
      f32x4 lc[2], csv;
      unsigned hp[2][2][2];
      unsigned la = L_LN + (32 * i + t) * 16, ca = L_CS3 + q * 16;
      asm volatile("" : "+v"(la), "+v"(ca));
      v4u fv;
      u64 bad;
      unsigned sv;
      auto epi = [&](auto Cc) INL {
        constexpr int c = Cc.value;
        f32x2 z[4];
#pragma unroll
        for (int tt = 0; tt < 2; ++tt) {
          const f32x4 zz = csv * lc[tt].x + acc[tt][c];
          z[2 * tt] = f32x2{zz.x, zz.y}; z[2 * tt + 1] = f32x2{zz.z, zz.w};
        }
        if constexpr (c == 3) sv = min4(fv);
        SB();
        const float ga4[4] = {lc[0].y, lc[0].y, lc[1].y, lc[1].y}, gb4[4] = {lc[0].z, lc[0].z, lc[1].z, lc[1].z};
        geluN<4>(z, ga4, gb4);
#pragma unroll
        for (int tt = 0; tt < 2; ++tt) {
          { hp[tt][c & 1][0] = pk(z[2 * tt].x, z[2 * tt].y); hp[tt][c & 1][1] = pk(z[2 * tt + 1].x, z[2 * tt + 1].y); }
        }
        asm volatile("" : "+v"(hp[0][c & 1][0]), "+v"(hp[0][c & 1][1]), "+v"(hp[1][c & 1][0]), "+v"(hp[1][c & 1][1]));
        if constexpr (c == 3) { if (RARE(sv < 8)) wait_free(8); CLOB(); }
        if constexpr ((c & 1) && c < 8) {
#pragma unroll
          for (int tt = 0; tt < 2; ++tt) {
            const v4u v = {hp[tt][0][0], hp[tt][0][1], hp[tt][1][0], hp[tt][1][1]};
            hb[c >> 1][tt] = asfrag(v);
            if constexpr (c == 1) *(LAS v4u*)(hw + tt * 1024) = v;
            else if constexpr (c < 7) *(LAS v4u*)(hw2 + (c >> 2) * SLOT + tt * 1024) = v;
            else ST16(v, lane * 16 + tt * (144 * 1024), hdst + (pbase(s) + 3) * 1024);
          }
        }
        if constexpr (c == 8) {
#pragma unroll
          for (int tt = 0; tt < 2; ++tt)
            ST8((v2u{hp[tt][0][0], hp[tt][0][1]}), lane * 16 + tt * (144 * 1024) + (s & 1) * 8, hdst + (s < 2 ? 4 : 13) * 1024);
        }
        if constexpr ((c & 1) && c < 7) post(pa + P_RQ, c / 2 + 1);
        SB();
      };
      sfor<162>([&](auto I) INL {
        constexpr int ii = I.value, n = FN(ii), c = FC(ii), nx = ii - 1 + PD;
        const bf16x8 cur = cat(lo[ii % PD], hi[ii % PD]);
        mfa<(n == 0)>(acc[0][c], cur, xb[n % 12][0]); SB();
        constexpr bool ck1 = ii < 99 && (c == 5 || c == 6);
        constexpr bool ck2 = ii >= 72 && ii < 99 && c >= 1 && c <= 4;
        constexpr int k2 = 24 + 4 * (n - 8) + c - 1;
        if constexpr (ii >= 1 && nx < 162) lo[nx % PD] = trd(WADDR(FN(nx), FC(nx)));
        if constexpr (ck1) { chk = mx2(c == 5 ? 0u : chk, asv4(xb[n + 1][c - 5]).x, asv4(xb[n + 1][c - 5]).y); asm volatile("" : "+v"(chk)); }
        if constexpr (ck2) { chk2 = mx2(chk2, asv4(xb[(k2 >> 1) % 12][k2 & 1]).x, asv4(xb[(k2 >> 1) % 12][k2 & 1]).y); asm volatile("" : "+v"(chk2)); }
        if constexpr (ii < 99 && c == 7) { bad = __ballot(chk == EMPTY); asm volatile("" : "+s"(bad)); }
        if constexpr (ii == 99) { bad = __ballot(chk2 == EMPTY); asm volatile("" : "+s"(bad)); }
        if constexpr (ii == 63 || ii == 101) { sv = min3(fv); asm volatile("" : "+s"(sv)); }
        if constexpr (ii == 103) { sv = rfl(rv); asm volatile("" : "+s"(sv)); }
        SB();
        mfa<(n == 0)>(acc[1][c], cur, xb[n % 12][1]); SB();
        if constexpr (ii >= 1 && nx < 162) hi[nx % PD] = trd(WADDR(FN(nx), FC(nx)) + 4608);
        if constexpr (ck1) { chk = mx2(chk, asv4(xb[n + 1][c - 5]).z, asv4(xb[n + 1][c - 5]).w); asm volatile("" : "+v"(chk)); }
        if constexpr (ck2) { chk2 = mx2(chk2, asv4(xb[(k2 >> 1) % 12][k2 & 1]).z, asv4(xb[(k2 >> 1) % 12][k2 & 1]).w); asm volatile("" : "+v"(chk2)); }
        SB();
        if constexpr (ii < 99 && c == 8) { if (RARE(bad != 0)) xfix(IC<2 * n + 2>{}, IC<2 * n + 4>{}); SB(); }
        if constexpr (ii == 100) { if (RARE(bad != 0)) xfix(IC<24>{}, IC<36>{}); SB(); }
        if constexpr (ii < 28 && ii % 2 == 0) { xload(IC<10 + ii / 2>{}); SB(); }
        if constexpr (ii >= 30 && ii < 66 && ii % 3 == 0) { xload(IC<14 + ii / 3>{}); SB(); }
        if constexpr (ii == 18 || ii == 81 || ii == 108 || ii == 159) { post(pa + P_FREE, ii == 18 ? 6 : ii == 81 ? 7 : ii == 108 ? 8 : 9); SB(); }
        if constexpr (ii == 8) STAMP(23);
        if constexpr (ii == 35) STAMP(24);
        if constexpr (ii == 71) STAMP(25);
        if constexpr (ii == 58 || ii == 96) { fv = peek4(L_FULL); SB(); }
        if constexpr (ii == 64) { if (RARE(sv < 7)) wait_full(7); CLOB(); SB(); }
        if constexpr (ii == 97) { rv = peek(L_RDY + 4 * R_LN); SB(); }
        if constexpr (ii == 102) { if (RARE(sv < 10)) wait_full(10); CLOB(); SB(); }
        if constexpr (ii == 104) {
          if (RARE(sv == 0)) wait_word(L_RDY + 4 * R_LN, 1);
          CLOB();
          lc[0] = *(const LAS f32x4*)la; lc[1] = *(const LAS f32x4*)(la + 256);
          SB(); STAMP(10);
        }
        if constexpr (ii >= 108 && n == 14 && c >= 1) { csv = *(const LAS f32x4*)(ca + (c - 1) * 64); SB(); }
        if constexpr (ii >= 108 && n == 15 && c == 4) { fv = peek4(L_FREE); SB(); }
        if constexpr (ii >= 108 && n == 17 && c >= 1) epi(IC<c - 1>{});
        if constexpr (ii == 150) { fv = peek4(L_FULL); SB(); }
      });
      csv = *(const LAS f32x4*)(ca + 8 * 64);
      if (RARE(min3(fv) < 11)) wait_full(11);
      CLOB();
#pragma unroll
      for (int k = 0; k < PD; ++k) { lo[k] = trd(WADDR(18, k)); hi[k] = trd(WADDR(18, k) + 4608); }
      SB();
      epi(IC<8>{});
      STAMP(11);
    }
    {
      f32x4 acc[2][9];
      unsigned pw = L_PS + 2 * i * 1024 + lane * 16, pw8 = L_PS + 32768 + 2 * i * 512 + lane * 8;
      asm volatile("" : "+v"(pw), "+v"(pw8));
      int po[2];
#pragma unroll
      for (int tt = 0; tt < 2; ++tt) { po[tt] = (2 * i + tt == fr) ? 0x7ff00000 : lane * 8; asm volatile("" : "+v"(po[tt])); }
      const int pdst = OFF_P + (((g * 4 + s) * 8 + 2 * i) * 8 + fr) * PUNIT;
      v4u fv;
      unsigned sv;
      auto epi = [&](auto Cc) INL {
        constexpr int c = Cc.value;
        if constexpr ((c & 1) && c < 8) {
#pragma unroll
          for (int tt = 0; tt < 2; ++tt) {
            const f32x4 a0 = acc[tt][c - 1], a1 = acc[tt][c];
            const v4u v = {pk(a0.x, a0.y), pk(a0.z, a0.w), pk(a1.x, a1.y), pk(a1.z, a1.w)};
            if constexpr (c == 7) ST16(v, 2 * po[tt] , pdst + tt * (8 * PUNIT) + 3 * 1024);
            *(LAS v4u*)(pw + tt * 1024 + (c >> 1) * 8192) = v;
          }
        }
        if constexpr (c == 8) {
#pragma unroll
          for (int tt = 0; tt < 2; ++tt) {
            const f32x4 a0 = acc[tt][8];
            const v2u v = {pk(a0.x, a0.y), pk(a0.z, a0.w)};
            ST8(v, po[tt], pdst + tt * (8 * PUNIT) + 4096);
            *(LAS v2u*)(pw8 + tt * 512) = v;
          }
        }
        if constexpr ((c & 1) && c < 7) post(pa + P_RQ, 3 + c / 2 + 1);
        SB();
      };
      constexpr int HS0 = HS0V, HS1 = HS1V, SIGS = SIGSV, HS2 = HS2V, HS3 = HS3V;
      sfor<162>([&](auto I) INL {
        constexpr int ii = I.value, n = FN(ii), c = FC(ii), nx = ii - 1 + PD;
        constexpr int G = ii == HS0 ? 0 : ii == HS1 ? 1 : ii == HS2 ? 2 : ii == HS3 ? 3 : -1;
        const bf16x8 cur = cat(lo[ii % PD], hi[ii % PD]);
        mfa<(n == 0)>(acc[0][c], cur, hb[n][0]); SB();
        if constexpr (ii >= 1 && nx < 162) lo[nx % PD] = trd(WADDR(18 + FN(nx), FC(nx)));
        if constexpr (G >= 0) { sv = rfl(rv); asm volatile("" : "+s"(sv)); }
        if constexpr (ii == 48 || ii == 84 || ii == 102) { sv = min3(fv); asm volatile("" : "+s"(sv)); }
        if constexpr (ii == 106) { sv = min4(fv); asm volatile("" : "+s"(sv)); }
        SB();
        mfa<(n == 0)>(acc[1][c], cur, hb[n][1]); SB();
        if constexpr (ii >= 1 && nx < 162) hi[nx % PD] = trd(WADDR(18 + FN(nx), FC(nx)) + 4608);
        SB();
        if constexpr (ii == SIGS) { VMW(6); SIGNAL(FC, 3 + i); SB(); }
        if constexpr (ii + 4 == HS0 || ii + 4 == HS1 || ii + 4 == HS2 || ii + 4 == HS3) { rv = peek(L_RDY + 4 * R_HH); SB(); }
        if constexpr (G >= 0) {
          const unsigned need = G < 3 ? 1u << G : 8u << i;
          if (RARE((sv & need) != need)) wait_bit(L_RDY + 4 * R_HH, need);
          CLOB(); SB(); STAMP(G == 0 ? 27 : G == 1 ? 28 : G == 2 ? 13 : 14);
        }
        if constexpr (ii >= HS0 && ii < HS0 + 6) { hload(IC<8 + ii - HS0>{}); SB(); }
        if constexpr (ii >= HS1 && ii < HS1 + 6) { hload(IC<14 + ii - HS1>{}); SB(); }
        if constexpr (ii >= HS2 && ii < HS2 + 6) { hload(IC<20 + ii - HS2>{}); SB(); }
        if constexpr (ii >= HS3 && ii < HS3 + 10) { hload(IC<26 + ii - HS3>{}); SB(); }
        if constexpr (ii == 18 || ii == 54) { post(pa + P_FREE, ii == 18 ? 10 : 11); SB(); }
        if constexpr (ii == 44 || ii == 79 || ii == 97) { fv = peek4(L_FULL); SB(); }
        if constexpr (ii == 48) { if (RARE(sv < 12)) wait_full(12); CLOB(); SB(); }
        if constexpr (ii == 84) { if (RARE(sv < 13)) wait_full(13); CLOB(); SB(); }
        if constexpr (ii == 102) { if (RARE(sv < 14)) wait_full(14); fv = peek4(L_FREE); SB(); STAMP(26); }
        if constexpr (ii == 106) { if (RARE(sv < 11)) wait_free(11); CLOB(); SB(); }
        if constexpr (ii >= 108 && n == 17 && c >= 1) epi(IC<c - 1>{});
      });
      MFMA_DONE();
      epi(IC<8>{});
      STAMP(15);
      asm volatile("s_waitcnt vmcnt(0) lgkmcnt(0)" ::: "memory");
      SIGNAL(FD, 3 + i);
      STAMP(16);
    }
  } else if (w < 7) {
    const int ld = w - 4;
    const unsigned fullw = L_FULL + 4 * ld;
    unsigned v3[3], v4a[9];
#pragma unroll
    for (int e = 0; e < 3; ++e) {
      const int idx = (3 * ld + e) * 64 + lane, row = idx / 18, ch = idx % 18;
      const int sc = (ch + 2 * (s & 1)) % 18;
      v3[e] = row * (F_ * 2) + sc * 16;
    }
#pragma unroll
    for (int e = 0; e < 9; ++e) { const int idx = e * 64 + lane; v4a[e] = (idx / 18) * (D_ * 2) + ((idx % 18 + 2 * (s & 1)) % 18) * 16; }
    const char* w3b = w3 + (576 * fr + 144 * s) * 2;
    const char* w4b = w4 + (size_t)(576 * fr) * (D_ * 2) + 144 * s * 2;
    auto w3slice = [&](int n) INL {
#pragma unroll
      for (int e = 0; e < 3; ++e) dma(w3b + (size_t)n * (32 * F_ * 2), v3[e], (n & 15) * SLOT + (3 * ld + e) * 1024);
    };
    auto w4whole = [&](int m, int u, auto CAP) INL {
      const char* src = w4b + u * (32 * D_ * 2);
#pragma unroll
      for (int e = 0; e < 9; ++e) { dma(src, v4a[e], ((18 + m) & 15) * SLOT + e * 1024); if constexpr (CAP.value) VMW(DCAP); }
    };
    const __amdgpu_buffer_rsrc_t r4 = __builtin_amdgcn_make_buffer_rsrc((void*)w4b, 0, 576 * D_ * 2 - 144 * s * 2, 0x00020000);
    v4u w4r[4][9];
    auto w4get = [&](auto K) INL {
      const int so = dstep(6 + ld + 3 * K.value, s) * (32 * D_ * 2);
#pragma unroll
      for (int e = 0; e < 9; ++e) { constexpr int eo[9] = {0, 2, 4, 6, 8, 1, 3, 5, 7}; w4r[K.value][eo[e]] = __builtin_amdgcn_raw_buffer_load_b128(r4, v4a[eo[e]], so, 0); }
      CLOB(); SB();
    };
    auto w4put = [&](auto K) INL {
      const unsigned a = ((24 + ld + 3 * K.value) & 15) * SLOT + lane * 16;
#pragma unroll
      for (int e = 0; e < 9; ++e) *(LAS v4u*)(a + e * 1024) = w4r[K.value][e];
      CLOB(); SB();
    };
    f32x4 cs[3] = {{0, 0, 0, 0}, {0, 0, 0, 0}, {0, 0, 0, 0}};
    const unsigned cb = (4 * q + (t >> 2)) * 288 + (t & 3) * 8 + ld * 96;
    auto colsum3 = [&](int n0, int n1) INL {
      __builtin_amdgcn_s_setprio(1);
      NOUNROLL for (int n = n0; n < n1; n += 2) {
        const unsigned a0 = cb + (n & 15) * SLOT;
        s16x4 l[6], h[6];
#pragma unroll
        for (int k = 0; k < 6; ++k) { l[k] = trd(a0 + (k / 3) * SLOT + (k % 3) * 32); h[k] = trd(a0 + (k / 3) * SLOT + 4608 + (k % 3) * 32); }
        SB();
#pragma unroll
        for (int k = 0; k < 6; ++k) cs[k % 3] = mfma(cat(l[k], h[k]), ones, cs[k % 3]);
      }
      __builtin_amdgcn_s_setprio(0);
      CLOB();
    };
    if (ab) {
      const unsigned v1 = (8 * (lane >> 5) + ((lane >> 3) & 3)) * (D_ * 2) + (((lane & 7) ^ ((q & 3) << 1)) << 4);
      auto w1units = [&](int u0) INL {
#pragma unroll
        for (int e = 0; e < 8; ++e) {
          const int u = u0 + 3 * (e >> 2);
          dman<(NT & 2) != 0>(w1 + 128 * ct + (32 * u + 16 * (e & 1) + 4 * ((e >> 1) & 1)) * (D_ * 2), v1, u * 4096 + (e & 3) * 1024);
        }
      };
      w1units(ld); w1units(ld + 6);
      VMW(8); lds_set(fullw, 1, lane);
      w1units(ld + 12);
      VMW(8); lds_set(fullw, 2, lane);
      STAMP(1);
      DRAIN(); lds_set(fullw, 3, lane);
      wait_free(2);
      const unsigned v2 = (lane >> 3) * (D_ * 2) + (((lane & 7) ^ ((q & 3) << 1)) << 4);
      NOUNROLL for (int u = ld; u < 18; u += 3) {
#pragma unroll
        for (int e = 0; e < 4; ++e) dma(w2 + 128 * ct + (32 * u + 8 * e) * (D_ * 2), v2, 8 * SLOT + u * 4096 + e * 1024);
      }
      if (!(CO & 2)) w4get(IC<0>{});
      STAMP(2);
      if (CO & 2) DRAIN(); else VMW(9);
      lds_set(fullw, 4, lane);
      STAMP(3);
      wait_free(3);
      NOUNROLL for (int n = 0; n < 8; ++n) w3slice(n);
      STAMP(4);
      DRAIN(); lds_set(fullw, 6, lane);
      wait_full(6);
      colsum3(0, 8);
      wait_free(4);
      NOUNROLL for (int n = 8; n < 11; ++n) w3slice(n);
      wait_free(5);
      NOUNROLL for (int n = 11; n < 16; ++n) w3slice(n);
      if (CO & 2) w4get(IC<0>{}); else w4get(IC<1>{});
      STAMP(5);
      VMW(9); lds_set(fullw, 7, lane);
      STAMP(6);
      wait_full(7);
      colsum3(8, 16);
    } else {
      __builtin_amdgcn_s_sleep(SL2);
      NOUNROLL for (int n = 0; n < 16; ++n) w3slice(n);
      DRAIN(); lds_set(fullw, 7, lane);
      w4get(IC<0>{}); if (!(CO & 2)) w4get(IC<1>{});
      wait_full(7);
      colsum3(0, 16);
    }
    lds_set(fullw, 8, lane);
    STAMP(7);
    wait_free(6); wait_full(8);
    STAMP(11);
    w3slice(16); w3slice(17);
    DRAIN(); lds_set(fullw, 9, lane);
    STAMP(12);
    wait_full(9);
    colsum3(16, 18);
    if (t == 0) {
#pragma unroll
      for (int c = 0; c < 3; ++c) *(LAS f32x4*)(L_CS3 + (3 * ld + c) * 64 + q * 16) = cs[c];
    }
    CLOB();
    lds_set(fullw, 10, lane);
    STAMP(8);
    if (PRIO & 2) __builtin_amdgcn_s_setprio(2);
    const u64 tag = get_tag();
    auto hround = [&](int p) INL {
      wait_min4(L_RQ, p + 1);
      v4u d[8];
#pragma unroll
      for (int k = 0; k < 8; ++k) d[k] = *(const LAS v4u*)((p ? (8 + p) * SLOT : L_HS) + k * 1024 + lane * 16);
#pragma unroll
      for (int k = 0; k < 8; ++k) ST16(d[k], lane * 16 + k * (144 * 1024), OFF_HH + (8 * g * 144 + fr * 18 + pbase(s) + p) * 1024);
      CLOB();
    };
    auto pround = [&](int p) INL {
      wait_min4(L_RQ, 3 + p + 1);
      const int so = OFF_P + ((g * 4 + s) * 64 + fr) * PUNIT + p * 1024;
      v4u d[7];
#pragma unroll
      for (int k = 0; k < 7; ++k) d[k] = *(const LAS v4u*)(L_PS + p * 8192 + ((fr + 1 + k) & 7) * 1024 + lane * 16);
#pragma unroll
      for (int k = 0; k < 7; ++k) ST16(d[k], lane * 16, so + ((fr + 1 + k) & 7) * (8 * PUNIT));
      DRAIN();
      SIGNAL(FD, p);
    };
    wait_free(7);
    NOUNROLL for (int m = ld; m < 6; m += 3) w4whole(m, dstep(m, s), IC<0>{});
    if (CO & 2) w4get(IC<1>{});
    w4get(IC<2>{}); w4get(IC<3>{});
    if (ld > 0) { DRAIN(); lds_set(fullw, 11, lane); }
    hround(ld);
    DRAIN();
    SIGNAL(FC, ld);
    lds_set(fullw, 11, lane);
    w4put(IC<0>{});
    if (ld == 0) w4put(IC<1>{});
    lds_set(fullw, 12, lane);
    STAMP(13);
    wait_free(9); wait_full(10);
    if (ld > 0) w4put(IC<1>{});
    w4put(IC<2>{});
    if (ld == 0) w4put(IC<3>{});
    lds_set(fullw, 13, lane);
    if (ld > 0) { wait_free(10); w4put(IC<3>{}); }
    lds_set(fullw, 14, lane);
    STAMP(10);
    pround(ld);
    STAMP(23);
  } else {
    if (PRIO & 1) __builtin_amdgcn_s_setprio(3);
    const u64 tag = __hip_atomic_load((GAS u64*)(ws + OFF_EPOCH), __ATOMIC_RELAXED, __HIP_MEMORY_SCOPE_AGENT) + 1ull;
    if (lane == 0) {
      *(volatile LAS u64*)L_TAG = tag;
      *(volatile LAS unsigned*)(L_RDY + 4 * R_TAG) = 1u;
      __hip_atomic_store(flagp(ws, FS, b, 0), tag, __ATOMIC_RELAXED, __HIP_MEMORY_SCOPE_AGENT);
    }
    {
      const v4u e4 = {EMPTY, EMPTY, EMPTY, EMPTY};
      if (ab) {
        wait_free(3);
#pragma unroll
        for (int k = 0; k < 8; ++k) ST16(e4, lane * 16 + (k & 1) * 1024 + (k >> 1) * (18 * 1024), OFF_X1 + (4 * rt * 18 + 2 * ct) * 1024);
        if (lane < 32) ST16(e4, OFF_STAT + (lane >> 3) * (9 * 128) + (lane & 7) * 16, (4 * rt * 9 + ct) * 128);
        DRAIN();
        lds_set(L_RDY + 4 * R_Z, 1, lane);
      }
    }
    STAMP(1);
    auto poll = [&](GAS u64* p) INL {
      NOUNROLL for (unsigned n = 0; n < GLB_CAP; ++n) {
        const u64 v = __hip_atomic_load(p, __ATOMIC_RELAXED, __HIP_MEMORY_SCOPE_AGENT);
        if (__all(v == tag)) break;
        __builtin_amdgcn_s_sleep(1);
      }
      CLOB();
    };
    auto poll_bits = [&](auto TWO, GAS u64* p0, GAS u64* p1, int word, u64 ma, u64 mb, u64 mc, u64 md) INL {
      unsigned done = 0;
      NOUNROLL for (unsigned n = 0; n < GLB_CAP; ++n) {
        const u64 v0 = __hip_atomic_load(p0, __ATOMIC_RELAXED, __HIP_MEMORY_SCOPE_AGENT);
        u64 v1 = v0;
        if constexpr (TWO.value) v1 = __hip_atomic_load(p1, __ATOMIC_RELAXED, __HIP_MEMORY_SCOPE_AGENT);
        const u64 bad0 = __ballot(v0 != tag), bad1 = __ballot(v1 != tag);
        const unsigned now = ((bad0 & ma) ? 0u : 1u) | ((bad0 & mb) ? 0u : 2u) | ((bad1 & mc) ? 0u : 4u) | ((bad1 & md) ? 0u : 8u);
        if (now != done) { done = now; lds_set(L_RDY + 4 * word, now, lane); }
        if (now == 15u) break;
        __builtin_amdgcn_s_sleep(1);
      }
      lds_set(L_RDY + 4 * word, 15u, lane);
      CLOB();
    };
    constexpr u64 M01 = 0x3333333333333333ull, M23 = 0xccccccccccccccccull;
    const int pl = (lane % 36) >> 2;
    if (ab) poll_bits(IC<0>{}, flagp(ws, FA, j + 8 * ((r & 1) + 2 * pl), lane & 3), nullptr, R_H, M01, M23, M01, M23);
    STAMP(2);
    poll_bits(IC<1>{}, flagp(ws, FB, g + 16 * pl, lane & 3), flagp(ws, FB, g + 8 + 16 * pl, lane & 3), R_X, M01, M23, M01, M23);
    STAMP(3);
    {
      v4u st[9];
      NOUNROLL for (unsigned n = 0; n < TRY_CAP; ++n) { CLOB();
        unsigned m = 0;
#pragma unroll
        for (int k = 0; k < 9; ++k) st[k] = LD16(OFF_STAT + ((lane >> 3) * 9 + k) * 128 + (lane & 7) * 16, g * (8 * 9 * 128));
#pragma unroll
        for (int k = 0; k < 9; ++k) m = mx4(m, st[k]);
        if (!missing(m)) break;
      }
      f32x4 a = __builtin_bit_cast(f32x4, st[0]);
#pragma unroll
      for (int k = 1; k < 9; ++k) a += __builtin_bit_cast(f32x4, st[k]);
      *(LAS f32x4*)(L_LN + lane * 32) = ln_consts(a.x, a.y);
      *(LAS f32x4*)(L_LN + lane * 32 + 16) = ln_consts(a.z, a.w);
      CLOB();
      lds_set(L_RDY + 4 * R_LN, 1, lane);
    }
    if (b == 0) {
      NOUNROLL for (int k = 0; k < 4; ++k) poll(flagp(ws, FS, k * 64 + lane, 0));
      if (lane == 0) __hip_atomic_store((GAS u64*)(ws + OFF_EPOCH), tag, __ATOMIC_RELAXED, __HIP_MEMORY_SCOPE_AGENT);
    }
    auto poll5 = [&](GAS u64* p0, int word, auto NB) INL {
      constexpr unsigned all = (1u << NB.value) - 1u;
      unsigned done = 0;
      NOUNROLL for (unsigned n = 0; n < GLB_CAP; ++n) {
        const u64 bad = __ballot(__hip_atomic_load(p0, __ATOMIC_RELAXED, __HIP_MEMORY_SCOPE_AGENT) != tag);
        unsigned now = 0;
#pragma unroll
        for (int k = 0; k < NB.value; ++k) now |= (bad & (0x0101010101010101ull << k)) ? 0u : 1u << k;
        if (now != done) { done = now; lds_set(L_RDY + 4 * word, now, lane); }
        if (now == all) break;
        __builtin_amdgcn_s_sleep(1);
      }
      lds_set(L_RDY + 4 * word, all, lane);
    };
    poll5(flagp(ws, FC, j + 8 * ((r & ~3) + ((lane >> 3) & 3)), lane & 7), R_HH, IC<7>{});
    STAMP(4);
    const int wr = lane >> 3;
    poll5(flagp(ws, FD, MAP ? (j & 1) + (wr & 6) + 8 * (((wr & 1) << 4) + (r & 15)) : wr + 8 * r, (lane & 7) < 3 ? (lane & 7) : 3 + (fr >> 1)), R_P, IC<5>{});
    STAMP(5);
  }
  if (w < 2 || (w >= 4 && w < 7)) {
    const int p = w < 2 ? w + 3 : w - 4;
    const int T = 8 * g + fr;
    const int u = p < 4 ? pbase(s) + p : (s < 2 ? 4 : 13);
    v4u xv;
    NOUNROLL for (unsigned n = 0; n < TRY_CAP; ++n) { CLOB(); xv = LD16(lane * 16, OFF_X1 + (T * 18 + u) * 1024); if (!missing(mx4(0u, xv))) break; }
    const float rstd = *(const LAS float*)(L_LN + (16 * fr + t) * 16 + 12);
    wait_bit(L_RDY + 4 * R_P, 1u << p);
    STAMP(20);
    const int psrc = OFF_P + ((g * 4 + s) * 8 + fr) * (8 * PUNIT) + p * 1024;
    char* orow = out + (size_t)(16 * T + t) * (D_ * 2) + (32 * u + 4 * q) * 2;
    if (p < 4) {
      v4u pv[8];
#pragma unroll
      for (int k = 1; k < 8; ++k) pv[k] = LD16(lane * 16, psrc + ((fr + k) & 7) * PUNIT);
      pv[0] = *(const LAS v4u*)(L_PS + p * 8192 + fr * 1024 + lane * 16);
      float a[8];
#pragma unroll
      for (int e = 0; e < 8; ++e) a[e] = 0.f;
#pragma unroll
      for (int k = 0; k < 8; ++k)
#pragma unroll
        for (int e = 0; e < 4; ++e) { const unsigned d = pv[k][e]; a[2 * e] += lo16(d); a[2 * e + 1] += hi16(d); }
      unsigned o[4];
#pragma unroll
      for (int e = 0; e < 4; ++e) {
        const unsigned h2 = pk(a[2 * e] * rstd, a[2 * e + 1] * rstd), xe = xv[e];
        o[e] = pk(lo16(xe) + lo16(h2), hi16(xe) + hi16(h2));
      }
      *(GAS v2u*)orow = v2u{o[0], o[1]};
      *(GAS v2u*)(orow + 32) = v2u{o[2], o[3]};
    } else {
      v2u pv[8];
#pragma unroll
      for (int k = 1; k < 8; ++k) pv[k] = LD8(lane * 8, psrc + ((fr + k) & 7) * PUNIT);
      pv[0] = *(const LAS v2u*)(L_PS + 32768 + fr * 512 + lane * 8);
      float a[4] = {0.f, 0.f, 0.f, 0.f};
#pragma unroll
      for (int k = 0; k < 8; ++k) { a[0] += lo16(pv[k].x); a[1] += hi16(pv[k].x); a[2] += lo16(pv[k].y); a[3] += hi16(pv[k].y); }
      const unsigned x0 = (s & 1) ? xv.z : xv.x, x1v = (s & 1) ? xv.w : xv.y;
      const unsigned h0 = pk(a[0] * rstd, a[1] * rstd), h1 = pk(a[2] * rstd, a[3] * rstd);
      *(GAS v2u*)(orow + (s & 1) * 32) = v2u{pk(lo16(x0) + lo16(h0), hi16(x0) + hi16(h0)), pk(lo16(x1v) + lo16(h1), hi16(x1v) + hi16(h1))};
    }
    STAMP(21);
  }
#endif
}
}

extern "C" void kernel_launch(void* const* d_in, const int* in_sizes, int n_in, void* d_out, int out_size, void* d_ws, size_t ws_size,
                              hipStream_t stream) {
  mx::mixer_kernel<<<256, 512, 0, stream>>>((const char*)d_in[0], (const char*)d_in[1], (const char*)d_in[2], (const char*)d_in[3],
                                            (const char*)d_in[4], (char*)d_out, (char*)d_ws);
}
```

```cpp
#include <hip/hip_runtime.h>
#include <utility>

#define LAS __attribute__((address_space(3)))
#define GAS __attribute__((address_space(1)))
#define DI __device__ __forceinline__
typedef unsigned v4u __attribute__((ext_vector_type(4)));
typedef unsigned v2u __attribute__((ext_vector_type(2)));
typedef float f32x4 __attribute__((ext_vector_type(4)));
typedef float f32x2 __attribute__((ext_vector_type(2)));
typedef short bf16x8 __attribute__((ext_vector_type(8)));
typedef short s16x4 __attribute__((ext_vector_type(4)));
typedef __bf16 bf16x2_t __attribute__((ext_vector_type(2)));
typedef unsigned long long u64;

namespace mx {
constexpr int D_ = 576, F_ = 4608;
constexpr int OFF_EPOCH = 0;
constexpr int OFF_FLAGS = 4096;
constexpr int OFF_STAT = 344064;
constexpr int OFF_H1 = 524288;
constexpr int OFF_X1 = OFF_H1 + 64 * 18 * 1024;
constexpr int OFF_HH = OFF_X1 + 64 * 18 * 1024;
constexpr int PUNIT = 4608;
constexpr int OFF_P = OFF_HH + 64 * 144 * 1024;
constexpr int WS_BYTES = OFF_P + 8 * 4 * 8 * 8 * PUNIT;
enum { FS = 0, FA = 1, FB = 2, FC = 3, FD = 4 };
constexpr int SLOT = 9216, MISC = 16 * SLOT;
constexpr int L_FULL = MISC, L_FREE = MISC + 16, L_RQ = MISC + 32, L_RDY = MISC + 48  , L_SY = MISC + 96, L_TAG = MISC + 120;
constexpr int L_DUM = MISC + 128  , L_CS1 = MISC + 512  , L_CS3 = MISC + 1024  , L_LN = MISC + 2048  ,
              L_HS = MISC + 4096  , L_LN1 = L_HS  , L_PS = 4 * SLOT  ;
enum { R_H = 0, R_X = 1, R_HH = 2, R_P = 3, R_TAG = 4, R_LN = 5, R_Z = 6 };
constexpr unsigned EMPTY = 0xffffffffu;
constexpr unsigned TRY_CAP = 1u << 12;
constexpr unsigned LDS_CAP = 1u << 21, GLB_CAP = 1u << 16;
constexpr int PD = 4;

template <class F, int... I> DI void sfor_impl(F&& f, std::integer_sequence<int, I...>) { (f(std::integral_constant<int, I>{}), ...); }
template <int N, class F> DI void sfor(F&& f) { sfor_impl(f, std::make_integer_sequence<int, N>{}); }
template <int V> using IC = std::integral_constant<int, V>;
#define INL __attribute__((always_inline))
#define NOUNROLL _Pragma("nounroll")
#define RARE(x) __builtin_expect(!!(x), 0)
#define SB() __builtin_amdgcn_sched_barrier(0)
#define CLOB() asm volatile("" ::: "memory")
#define DRAIN() asm volatile("s_waitcnt vmcnt(0)" ::: "memory")
#define VMW(n) asm volatile("s_waitcnt vmcnt(%0)" ::"i"(n) : "memory")

DI unsigned rfl(unsigned v) { return __builtin_amdgcn_readfirstlane(v); }
DI f32x4 mfma(bf16x8 a, bf16x8 b, f32x4 c) { return __builtin_amdgcn_mfma_f32_16x16x32_bf16(a, b, c, 0, 0, 0); }
template <bool FIRST> DI void mfa(f32x4& c, bf16x8 a, bf16x8 b) {
  if constexpr (FIRST) asm volatile("v_mfma_f32_16x16x32_bf16 %0, %1, %2, 0" : "=&v"(c) : "v"(a), "v"(b));
  else asm volatile("v_mfma_f32_16x16x32_bf16 %0, %1, %2, %0" : "+v"(c) : "v"(a), "v"(b));
}
#define MFMA_DONE() asm volatile("s_nop 7\n\ts_nop 3" ::: "memory")
DI bf16x8 asfrag(v4u v) { return __builtin_bit_cast(bf16x8, v); }
DI s16x4 trd(unsigned addr) { return __builtin_amdgcn_ds_read_tr16_b64_v4i16((LAS s16x4*)addr); }
DI bf16x8 cat(s16x4 lo, s16x4 hi) { return __builtin_shufflevector(lo, hi, 0, 1, 2, 3, 4, 5, 6, 7); }
DI unsigned pk(float a, float b) { bf16x2_t r = __builtin_convertvector(f32x2{a, b}, bf16x2_t); return __builtin_bit_cast(unsigned, r); }
DI unsigned pkc(float a, float b) { return min(pk(a, b), EMPTY - 1u); }
DI unsigned mx2(unsigned m, unsigned a, unsigned b) { return max(max(m, a), b); }
DI unsigned mx4(unsigned m, v4u d) { return mx2(mx2(m, d.x, d.y), d.z, d.w); }
DI bool missing(unsigned m) { return __any(m == EMPTY); }
DI v4u asv4(bf16x8 f) { return __builtin_bit_cast(v4u, f); }
DI float lo16(unsigned p) { return __uint_as_float(p << 16); }
DI float hi16(unsigned p) { return __uint_as_float(p & 0xffff0000u); }
DI float bfr(float a) { return lo16(pk(a, a)); }
DI float qsum(float v) {
  auto r = __builtin_amdgcn_permlane16_swap(__float_as_uint(v), __float_as_uint(v), false, false);
  float a = __uint_as_float(r[0]) + __uint_as_float(r[1]);
  auto r2 = __builtin_amdgcn_permlane32_swap(__float_as_uint(a), __float_as_uint(a), false, false);
  return __uint_as_float(r2[0]) + __uint_as_float(r2[1]);
}
constexpr float GA = -2.302208198144325f;
constexpr float GB = -0.1029432397357593f;
DI f32x4 ln_consts(float s, float ss) {
  const float m = s * (1.0f / D_);
  float var = __builtin_fmaf(-m, m, ss * (1.0f / D_));
  var = var > 0.f ? var : 0.f;
  const float sd = bfr(__builtin_amdgcn_sqrtf(bfr(bfr(var) + 1e-5f)));
  const float r = __builtin_amdgcn_rcpf(sd);
  return f32x4{-bfr(m), GA * r, GB * r * r * r, r};
}
template <int N> DI void geluN(f32x2 (&z)[N], const float (&a)[N], const float (&b)[N]) {
  f32x2 p[N];
#pragma unroll
  for (int k = 0; k < N; ++k) p[k] = z[k] * z[k];
  SB();
#pragma unroll
  for (int k = 0; k < N; ++k) p[k] = p[k] * b[k] + a[k];
  SB();
#pragma unroll
  for (int k = 0; k < N; ++k) p[k] = z[k] * p[k];
  SB();
#pragma unroll
  for (int k = 0; k < N; ++k) { p[k].x = __builtin_amdgcn_exp2f(p[k].x); p[k].y = __builtin_amdgcn_exp2f(p[k].y); }
  SB();
#pragma unroll
  for (int k = 0; k < N; ++k) p[k] = p[k] + 1.0f;
  SB();
#pragma unroll
  for (int k = 0; k < N; ++k) { p[k].x = __builtin_amdgcn_rcpf(p[k].x); p[k].y = __builtin_amdgcn_rcpf(p[k].y); }
  SB();
#pragma unroll
  for (int k = 0; k < N; ++k) z[k] = z[k] * p[k];
  SB();
}

DI unsigned ldsw(int addr) { return *(volatile LAS unsigned*)addr; }
DI v4u lds4(int addr) { return *(volatile LAS v4u*)addr; }
DI void lds_set(int addr, unsigned v, int lane) { if (lane == 0) *(volatile LAS unsigned*)addr = v; }
DI v4u peek4(int addr) { CLOB(); return *(const LAS v4u*)addr; }
DI unsigned peek(int addr) { CLOB(); return *(const LAS unsigned*)addr; }
DI void post(unsigned addr, unsigned v) { CLOB(); *(LAS unsigned*)addr = v; CLOB(); }
DI void wait_word(int addr, unsigned v) {
  NOUNROLL for (unsigned n = 0; n < LDS_CAP; ++n) { if (rfl(ldsw(addr)) >= v) break; __builtin_amdgcn_s_sleep(1); }
  CLOB();
}
DI unsigned min3(v4u x) { return rfl(min(min(x.x, x.y), x.z)); }
DI unsigned min4(v4u x) { return rfl(min(min(x.x, x.y), min(x.z, x.w))); }
DI void wait_full(unsigned v) {
  NOUNROLL for (unsigned n = 0; n < LDS_CAP; ++n) { if (min3(lds4(L_FULL)) >= v) break; __builtin_amdgcn_s_sleep(1); }
  CLOB();
}
DI void wait_min4(int addr, unsigned v) {
  NOUNROLL for (unsigned n = 0; n < LDS_CAP; ++n) { if (min4(lds4(addr)) >= v) break; __builtin_amdgcn_s_sleep(1); }
  CLOB();
}
DI void wait_free(unsigned v) { wait_min4(L_FREE, v); }
DI void wait_bit(int addr, unsigned bit) {
  NOUNROLL for (unsigned n = 0; n < LDS_CAP; ++n) { if ((rfl(ldsw(addr)) & bit) == bit) break; __builtin_amdgcn_s_sleep(1); }
  CLOB();
}
DI GAS u64* flagp(char* ws, int kind, int b, int wv) { return (GAS u64*)(ws + OFF_FLAGS + (kind * 256 + b) * 128 + wv * 8); }
DI u64 get_tag() {
  wait_word(L_RDY + 4 * R_TAG, 1);
  return ((u64)rfl(ldsw(L_TAG + 4)) << 32) | rfl(ldsw(L_TAG));
}
#define LD16(off, soff) __builtin_amdgcn_raw_buffer_load_b128(rs, (off), (soff), 16)
#define LD8(off, soff) __builtin_amdgcn_raw_buffer_load_b64(rs, (off), (soff), 16)
#define ST16(v, off, soff) do { v4u d_ = (v); asm volatile("" : "+v"(d_)); __builtin_amdgcn_raw_buffer_store_b128(d_, rs, (off), (soff), 16); asm volatile("s_nop 1" ::"v"(d_) : "memory"); } while (0)
#define ST8(v, off, soff) __builtin_amdgcn_raw_buffer_store_b64((v), rs, (off), (soff), 16)
#define SIGNAL(kind, wv) do { CLOB(); ST8((v2u{(unsigned)tag, (unsigned)(tag >> 32)}), lane ? 0x7ffffff0 : 0, OFF_FLAGS + ((kind) * 256 + b) * 128 + (wv) * 8); CLOB(); } while (0)
DI void dma(const char* sbase, unsigned voff, unsigned ldsaddr) {
  asm volatile("s_mov_b32 m0, %0\n\ts_nop 0\n\tglobal_load_lds_dwordx4 %1, %2" ::"s"(ldsaddr), "v"(voff), "s"(sbase) : "memory");
}
DI int pbase(int s) { return (9 * s + (s & 1)) >> 1; }
DI int dstep(int M, int s) {
  if (M < 4) return pbase(s) + M;
  else if (M < 16) return pbase((s + 1 + (M - 4) % 3) & 3) + (M - 4) / 3;
  else return M == 16 ? 4 : 13;
}

__global__ __launch_bounds__(512, 2) void mixer_kernel(const char* __restrict__ x, const char* __restrict__ w1, const char* __restrict__ w2,
                                                       const char* __restrict__ w3, const char* __restrict__ w4, char* __restrict__ out, char* ws) {
#if defined(__HIP_DEVICE_COMPILE__)
  __shared__ __attribute__((aligned(16))) char smem[163840];
  const int tid = threadIdx.x, lane = tid & 63, w = rfl(tid >> 6);
  const int t = lane & 15, q = lane >> 4;
  const int b = blockIdx.x, j = b & 7, r = b >> 3;
  const bool ab = r < 18;
  const int rt = 2 * j + (r & 1), ct = r >> 1;
  const int g = 4 * (j & 1) + ((r >> 2) & 3), fr = (j & 6) + (r >> 4), s = r & 3;
  const __amdgpu_buffer_rsrc_t rs = __builtin_amdgcn_make_buffer_rsrc(ws, 0, WS_BYTES, 0x00020000);
  const bf16x8 ones = {0x3F80, 0x3F80, 0x3F80, 0x3F80, 0x3F80, 0x3F80, 0x3F80, 0x3F80};
  asm volatile("" ::"v"(smem));
  if (tid < 32) *(volatile LAS unsigned*)(MISC + tid * 4) = 0u;
  asm volatile("s_waitcnt lgkmcnt(0)\n\ts_barrier" ::: "memory");
  if (ab) {
    if (w < 4) {
      const char* xsrc = x + (size_t)(4 * rt + w) * (16 * D_ * 2);
#pragma unroll
      for (int e = 0; e < 18; ++e) {
        const unsigned f = (((unsigned)(lane + 64 * e) * 911u) >> 17) & 7u;
        dma(xsrc + e * 1024, (lane * 16) ^ (f << 4), (8 + 2 * w) * SLOT + e * 1024);
      }
    }
  }

  if (w < 4) {
    const int i = w;
    unsigned pa = lane ? L_DUM + 4 * lane : L_FREE + 4 * i;
    asm volatile("" : "+v"(pa));
    const u64 tag = get_tag();
    constexpr int P_FREE = 0, P_RQ = L_RQ - L_FREE, P_SY = L_SY - L_FREE;
    if (ab) {
      unsigned ab4[4];
#pragma unroll
      for (int c = 0; c < 4; ++c) ab4[c] = (4 * q + (t >> 2)) * 128 + ((c ^ ((2 * q + (t >> 3)) & 3)) << 5) + (t & 3) * 8;
      const int T = 4 * rt + i;
      v2u xres[4];
      {
        const int tp = i >> 1, ch = i & 1;
        const unsigned xa = (8 + 2 * i) * SLOT + t * (D_ * 2), xs = ((t >> 1) & 7) << 4;
        unsigned xe[2][2], wb[2][2];
#pragma unroll
        for (int e = 0; e < 2; ++e) {
          xe[0][e] = xa + ((64 * (e ^ tp) + q * 16) ^ xs);
          xe[1][e] = xe[0][e] + (ch ? -2 * SLOT : 2 * SLOT);
          wb[e][0] = (ch ? ab4[2] : ab4[0]) + (e ^ tp) * 4096;
          wb[e][1] = (ch ? ab4[3] : ab4[1]) + (e ^ tp) * 4096;
          asm volatile("" : "+v"(xe[0][e]), "+v"(xe[1][e]), "+v"(wb[e][0]), "+v"(wb[e][1]));
        }
        int dst[4];
        unsigned keep[4], dm[4];
#pragma unroll
        for (int k = 0; k < 4; ++k) {
          const int gt = 4 * ct + 2 * ch + (k & 1), grp = gt / 9;
          const int sh = grp == 0 ? 1 : grp == 1 ? -1 : grp == 2 ? 2 : -2;
          const int ts = 16 * (ch ^ (k >> 1)) + t + sh;
          keep[k] = (ts >= 0 && ts < 32) ? 0xffffffffu : 0u;
          dst[k] = OFF_H1 + ((T & ~1) + ((ts >> 4) & 1)) * (18 * 1024) + ((ts & 15) + 16 * q) * 16 + (k & 1) * 8;
          dm[k] = (q == (t >> 2) && k == (t & 3)) ? 0xffffffffu : 0u;
          asm volatile("" : "+v"(dst[k]), "+v"(keep[k]), "+v"(dm[k]));
        }
        DRAIN();
        post(pa + P_FREE, 1);
        wait_word(L_FREE + 4 * (i ^ 1), 1);
        bf16x8 xf[2][18];
        f32x4 aS = {0, 0, 0, 0}, aG = {0, 0, 0, 0};
#pragma unroll
        for (int p = 0; p < 6; ++p) xf[0][p] = *(const LAS bf16x8*)(xe[0][p & 1] + (p >> 1) * 128);
        SB();
        sfor<18>([&](auto P) INL {
          constexpr int p = P.value;
          aS = mfma(ones, xf[0][p], aS); SB();
          if constexpr (p + 6 < 18) xf[0][p + 6] = *(const LAS bf16x8*)(xe[0][p & 1] + ((p + 6) >> 1) * 128);
          else if constexpr (p + 6 < 22) xres[p - 12] = *(const LAS v2u*)(xa + 128 * ct + ((32 * (p - 12) + 8 * q) ^ xs));
          SB();
          aG = mfma(xf[0][p], xf[0][p], aG); SB();
          xf[1][p] = *(const LAS bf16x8*)(xe[1][p & 1] + (p >> 1) * 128);
          SB();
        });
        CLOB();
        post(pa + P_FREE, 2);
        f32x4 lc[2];
        {
          const float gd = __uint_as_float(((__float_as_uint(aG.x) & dm[0]) | (__float_as_uint(aG.y) & dm[1])) | ((__float_as_uint(aG.z) & dm[2]) | (__float_as_uint(aG.w) & dm[3])));
          lc[0] = ln_consts(aS[0], qsum(gd));
          if (q == 0) *(LAS f32x4*)(L_LN1 + i * 256 + t * 16) = lc[0];
          CLOB();
          post(pa + P_SY, 1);
        }
        SB();
        wait_full(1);
        constexpr int PA = 4;
        s16x4 wl[PA], wh[PA];
#define AADDR(k) (wb[((k) >> 1) & 1][(k) & 1] + ((k) >> 2) * 8192)
#pragma unroll
        for (int k = 0; k < PA; ++k) { wl[k] = trd(AADDR(k)); wh[k] = trd(AADDR(k) + 2048); }
        bf16x8 one2 = ones;
        asm volatile("" : "+v"(one2));
        SB();
        f32x4 acc[2][2], cs[2];
        v4u fv;
        sfor<36>([&](auto K) INL {
          constexpr int k = K.value, p = k >> 1, l = k & 1, nx = k - 1 + PA;
          const bf16x8 cur = cat(wl[k % PA], wh[k % PA]);
          mfa<(p == 0)>(acc[0][l], cur, xf[0][p]); SB();
          if constexpr (nx == 12 || nx == 24) { if (RARE(min3(fv) < nx / 12 + 1)) wait_full(nx / 12 + 1); CLOB(); SB(); }
          if constexpr (k >= 1 && nx < 36) wl[nx % PA] = trd(AADDR(nx));
          SB();
          mfa<(p == 0)>(acc[1][l], cur, xf[1][p]); SB();
          if constexpr (k >= 1 && nx < 36) wh[nx % PA] = trd(AADDR(nx) + 2048);
          if constexpr (nx == 9 || nx == 21) fv = peek4(L_FULL);
          SB();
          if constexpr ((p & 1) == 0) { mfa<(p == 0)>(cs[l], cur, one2); SB(); }
        });
        MFMA_DONE();
        if (t == 0) { *(LAS f32x4*)(L_CS1 + i * 128 + q * 16) = cs[0]; *(LAS f32x4*)(L_CS1 + i * 128 + 64 + q * 16) = cs[1]; }
        CLOB();
        post(pa + P_SY, 2);
        wait_word(L_SY + 4 * (i ^ 1), 1);
        lc[1] = *(const LAS f32x4*)(L_LN1 + (i ^ 1) * 256 + t * 16);
        wait_word(L_SY + 4 * (i ^ 2), 2);
        const f32x4 o0 = *(const LAS f32x4*)(L_CS1 + (i ^ 2) * 128 + q * 16), o1 = *(const LAS f32x4*)(L_CS1 + (i ^ 2) * 128 + 64 + q * 16);
        cs[0] += o0; cs[1] += o1;
        f32x2 z[8];
        float ga8[8], gb8[8];
#pragma unroll
        for (int k = 0; k < 4; ++k) {
          const f32x4 zz = cs[k & 1] * lc[k >> 1].x + acc[k >> 1][k & 1];
          z[2 * k] = f32x2{zz.x, zz.y}; z[2 * k + 1] = f32x2{zz.z, zz.w};
          ga8[2 * k] = ga8[2 * k + 1] = lc[k >> 1].y; gb8[2 * k] = gb8[2 * k + 1] = lc[k >> 1].z;
        }
        SB();
        geluN<8>(z, ga8, gb8);
        unsigned hp[8];
#pragma unroll
        for (int k = 0; k < 8; ++k) { const f32x2 v = z[k] * lc[k >> 2].w; hp[k] = pk(v.x, v.y) & keep[k >> 1]; }
        const int up = 2 * ct + ch;
        if (RARE(up == 4 || up == 13)) {
#pragma unroll
          for (int k = 0; k < 4; ++k) ST8((v2u{hp[2 * k], hp[2 * k + 1]}), dst[k], up * 1024);
        } else {
#pragma unroll
          for (int tt = 0; tt < 2; ++tt) ST16((v4u{hp[4 * tt], hp[4 * tt + 1], hp[4 * tt + 2], hp[4 * tt + 3]}), dst[2 * tt], up * 1024);
        }
        DRAIN();
        SIGNAL(FA, i);
        post(pa + P_FREE, 3);
      }
      {
        wait_full(4);
#pragma unroll
        for (int c = 0; c < 4; ++c) { ab4[c] += 8 * SLOT; asm volatile("" : "+v"(ab4[c])); }
        constexpr int PB = 32;
        s16x4 wl[PB], wh[PB];
#pragma unroll
        for (int k = 0; k < PB; ++k) { wl[k] = trd(ab4[k & 3] + (k >> 2) * 4096); wh[k] = trd(ab4[k & 3] + (k >> 2) * 4096 + 2048); }
        SB();
        f32x2 xr[8];
#pragma unroll
        for (int l = 0; l < 4; ++l) { xr[2 * l] = f32x2{lo16(xres[l].x), hi16(xres[l].x)}; xr[2 * l + 1] = f32x2{lo16(xres[l].y), hi16(xres[l].y)}; }
#pragma unroll
        for (int k = 0; k < 8; ++k) asm volatile("" : "+v"(xr[k]));
        wait_bit(L_RDY + 4 * R_H, 1u << (i >> 1));
        bf16x8 hf[18];
        const int hsrc = OFF_H1 + T * (18 * 1024);
#pragma unroll
        for (int u = 0; u < 9; ++u) hf[u] = asfrag(LD16(lane * 16 + u * 1024, hsrc));
        SB();
        f32x4 acc[4];
        unsigned zv;
        sfor<72>([&](auto K) INL {
          constexpr int k = K.value, u = k >> 2;
          mfa<(k < 4)>(acc[k & 3], cat(wl[k % PB], wh[k % PB]), hf[u]); SB();
          if constexpr (k == 17) post(pa + P_FREE, 4);
          if constexpr (k == 60) zv = peek(L_RDY + 4 * R_Z);
          if constexpr ((k & 1) == 0 && k / 2 + 9 < 18) hf[k / 2 + 9] = asfrag(LD16(lane * 16 + (k / 2 + 9) * 1024, hsrc));
          if constexpr (k + PB < 72) { wl[k % PB] = trd(ab4[k & 3] + ((k + PB) >> 2) * 4096); wh[k % PB] = trd(ab4[k & 3] + ((k + PB) >> 2) * 4096 + 2048); }
          SB();
        });
        MFMA_DONE();
        unsigned xp[8];
#pragma unroll
        for (int l = 0; l < 4; ++l) {
          const f32x2 v0 = xr[2 * l] + f32x2{acc[l].x, acc[l].y}, v1 = xr[2 * l + 1] + f32x2{acc[l].z, acc[l].w};
          xp[2 * l] = pkc(v0.x, v0.y); xp[2 * l + 1] = pkc(v1.x, v1.y);
        }
        if (RARE(rfl(zv) == 0)) wait_word(L_RDY + 4 * R_Z, 1);
        CLOB();
        const int dst = OFF_X1 + (T * 18 + 2 * ct) * 1024;
        ST16((v4u{xp[0], xp[1], xp[2], xp[3]}), lane * 16, dst);
        ST16((v4u{xp[4], xp[5], xp[6], xp[7]}), lane * 16 + 1024, dst);
        SB();
        float sm = 0.f, sq = 0.f;
#pragma unroll
        for (int k = 0; k < 8; ++k) {
          const bf16x2_t v = __builtin_bit_cast(bf16x2_t, xp[k]);
          sm = __builtin_amdgcn_fdot2_f32_bf16(v, bf16x2_t{(__bf16)1.0f, (__bf16)1.0f}, sm, false);
          sq = __builtin_amdgcn_fdot2_f32_bf16(v, v, sq, false);
        }
        sm = qsum(sm); sq = qsum(sq);
        ST8((v2u{min(__float_as_uint(sm), EMPTY - 1u), min(__float_as_uint(sq), EMPTY - 1u)}), q ? 0x7ffffff0 : t * 8, OFF_STAT + (T * 9 + ct) * 128);
        SIGNAL(FB, i);
        post(pa + P_FREE, 5);
      }
    } else {
      const int lt = ((r - 18) * 4 + i) * 64 + lane;
      unsigned sink = 0;
      auto touch = [&](const char* base, unsigned off) INL { asm volatile("global_load_dword %0, %1, %2" : "+v"(sink) : "v"(off), "s"(base) : "memory"); };
#pragma unroll
      for (int k = 0; k < 2; ++k) touch(w1, (5183 - min(lt + 3584 * k, 5183)) * 128);
      touch(x + (size_t)j * (128 * D_ * 2), min(lt, 1151) * 128);
      __builtin_amdgcn_s_sleep(32);
#pragma unroll
      for (int k = 0; k < 2; ++k) touch(w2, min(lt + 3584 * k, 5183) * 128);
      DRAIN();
#pragma unroll
      for (int k = 0; k < 2; ++k) { const int l = min(lt + 3584 * k, 5183); touch(w3 + (j & 6) * (576 * 2), (l / 9) * (F_ * 2) + (l % 9) * 128); }
      DRAIN();
      __builtin_amdgcn_s_sleep(64);
#pragma unroll
      for (int k = 0; k < 3; ++k) touch(w4 + (size_t)(j & 6) * (576 * D_ * 2), min(lt + 3584 * k, 10367) * 128);
      DRAIN();
      asm volatile("" ::"v"(sink));
    }
    unsigned b0 = (4 * q + (t >> 2)) * 288 + (t & 3) * 8, b1 = b0 + 6 * SLOT, b2 = b0 + 12 * SLOT;
    asm volatile("" : "+v"(b0), "+v"(b1), "+v"(b2));
#define WADDR(z, c) ((((z) % 16) < 6 ? b0 : ((z) % 16) < 12 ? b1 : b2) + (((z) % 16) % 6) * SLOT + (c) * 32)
#define FN(i_) ((i_) < 108 ? (i_) / 9 : 12 + ((i_) - 108) % 6)
#define FC(i_) ((i_) < 108 ? (i_) % 9 : ((i_) - 108) / 6)
    const int T0 = 8 * g + 2 * i;
    const int hdst = OFF_HH + (T0 * 144 + fr * 18) * 1024;
    bf16x8 hb[18][2];
    auto hload = [&](auto K) INL {
      constexpr int m = K.value >> 1, tt = K.value & 1;
      hb[m][tt] = asfrag(LD16(lane * 16 + tt * (144 * 1024), hdst + dstep(m, s) * 1024));
    };
    s16x4 lo[PD], hi[PD];
    unsigned rv, hw = L_HS + 2 * i * 1024 + lane * 16, hw2 = 9 * SLOT + 2 * i * 1024 + lane * 16;
    asm volatile("" : "+v"(hw), "+v"(hw2));
    {
      wait_full(6);
#pragma unroll
      for (int k = 0; k < PD; ++k) { lo[k] = trd(WADDR(0, k)); hi[k] = trd(WADDR(0, k) + 4608); }
      f32x4 acc[2][9];
      const int xsrc = OFF_X1 + T0 * (18 * 1024);
      int xo[2] = {lane * 16, lane * 16 + 18 * 1024};
      asm volatile("" : "+v"(xo[0]), "+v"(xo[1]));
      bf16x8 xb[12][2];
      auto xload = [&](auto K) INL {
        constexpr int n = K.value >> 1, tt = K.value & 1;
        xb[n % 12][tt] = asfrag(LD16(xo[tt] + (n & 3) * 1024, xsrc + (n >> 2) * 4096));
      };
      SB();
      auto xfix = [&](auto K0, auto K1) INL {
        NOUNROLL for (unsigned n = 0; n < TRY_CAP; ++n) { CLOB();
          unsigned m = 0;
          sfor<K1.value - K0.value>([&](auto K) INL { xload(IC<K0.value + K.value>{}); });
          sfor<K1.value - K0.value>([&](auto K) INL { constexpr int kk = K0.value + K.value; m = mx4(m, asv4(xb[(kk >> 1) % 12][kk & 1])); });
          if (!missing(m)) break;
        }
      };
      wait_bit(L_RDY + 4 * R_X, 1u << i);
      sfor<10>([&](auto K) INL { xload(K); });
      SB();
      unsigned chk = mx4(mx4(0u, asv4(xb[0][0])), asv4(xb[0][1])), chk2 = 0;
      if (RARE(missing(chk))) xfix(IC<0>{}, IC<2>{});
      SB();
      f32x4 lc[2], csv;
      unsigned hp[2][2][2];
      unsigned la = L_LN + (32 * i + t) * 16, ca = L_CS3 + q * 16;
      asm volatile("" : "+v"(la), "+v"(ca));
      v4u fv;
      u64 bad;
      unsigned sv;
      auto epi = [&](auto Cc) INL {
        constexpr int c = Cc.value;
        f32x2 z[4];
#pragma unroll
        for (int tt = 0; tt < 2; ++tt) {
          const f32x4 zz = csv * lc[tt].x + acc[tt][c];
          z[2 * tt] = f32x2{zz.x, zz.y}; z[2 * tt + 1] = f32x2{zz.z, zz.w};
        }
        if constexpr (c == 3) sv = min4(fv);
        SB();
        const float ga4[4] = {lc[0].y, lc[0].y, lc[1].y, lc[1].y}, gb4[4] = {lc[0].z, lc[0].z, lc[1].z, lc[1].z};
        geluN<4>(z, ga4, gb4);
#pragma unroll
        for (int tt = 0; tt < 2; ++tt) {
          hp[tt][c & 1][0] = pk(z[2 * tt].x, z[2 * tt].y); hp[tt][c & 1][1] = pk(z[2 * tt + 1].x, z[2 * tt + 1].y);
        }
        asm volatile("" : "+v"(hp[0][c & 1][0]), "+v"(hp[0][c & 1][1]), "+v"(hp[1][c & 1][0]), "+v"(hp[1][c & 1][1]));
        if constexpr (c == 3) { if (RARE(sv < 8)) wait_free(8); CLOB(); }
        if constexpr ((c & 1) && c < 8) {
#pragma unroll
          for (int tt = 0; tt < 2; ++tt) {
            const v4u v = {hp[tt][0][0], hp[tt][0][1], hp[tt][1][0], hp[tt][1][1]};
            hb[c >> 1][tt] = asfrag(v);
            if constexpr (c == 1) *(LAS v4u*)(hw + tt * 1024) = v;
            else if constexpr (c < 7) *(LAS v4u*)(hw2 + (c >> 2) * SLOT + tt * 1024) = v;
            else ST16(v, lane * 16 + tt * (144 * 1024), hdst + (pbase(s) + 3) * 1024);
          }
        }
        if constexpr (c == 8) {
#pragma unroll
          for (int tt = 0; tt < 2; ++tt)
            ST8((v2u{hp[tt][0][0], hp[tt][0][1]}), lane * 16 + tt * (144 * 1024) + (s & 1) * 8, hdst + (s < 2 ? 4 : 13) * 1024);
        }
        if constexpr ((c & 1) && c < 7) post(pa + P_RQ, c / 2 + 1);
        SB();
      };
      sfor<162>([&](auto I) INL {
        constexpr int ii = I.value, n = FN(ii), c = FC(ii), nx = ii - 1 + PD;
        const bf16x8 cur = cat(lo[ii % PD], hi[ii % PD]);
        mfa<(n == 0)>(acc[0][c], cur, xb[n % 12][0]); SB();
        constexpr bool ck1 = ii < 99 && (c == 5 || c == 6);
        constexpr bool ck2 = ii >= 72 && ii < 99 && c >= 1 && c <= 4;
        constexpr int k2 = 24 + 4 * (n - 8) + c - 1;
        if constexpr (ii >= 1 && nx < 162) lo[nx % PD] = trd(WADDR(FN(nx), FC(nx)));
        if constexpr (ck1) { chk = mx2(c == 5 ? 0u : chk, asv4(xb[n + 1][c - 5]).x, asv4(xb[n + 1][c - 5]).y); asm volatile("" : "+v"(chk)); }
        if constexpr (ck2) { chk2 = mx2(chk2, asv4(xb[(k2 >> 1) % 12][k2 & 1]).x, asv4(xb[(k2 >> 1) % 12][k2 & 1]).y); asm volatile("" : "+v"(chk2)); }
        if constexpr (ii < 99 && c == 7) { bad = __ballot(chk == EMPTY); asm volatile("" : "+s"(bad)); }
        if constexpr (ii == 99) { bad = __ballot(chk2 == EMPTY); asm volatile("" : "+s"(bad)); }
        if constexpr (ii == 63 || ii == 101) { sv = min3(fv); asm volatile("" : "+s"(sv)); }
        if constexpr (ii == 103) { sv = rfl(rv); asm volatile("" : "+s"(sv)); }
        SB();
        mfa<(n == 0)>(acc[1][c], cur, xb[n % 12][1]); SB();
        if constexpr (ii >= 1 && nx < 162) hi[nx % PD] = trd(WADDR(FN(nx), FC(nx)) + 4608);
        if constexpr (ck1) { chk = mx2(chk, asv4(xb[n + 1][c - 5]).z, asv4(xb[n + 1][c - 5]).w); asm volatile("" : "+v"(chk)); }
        if constexpr (ck2) { chk2 = mx2(chk2, asv4(xb[(k2 >> 1) % 12][k2 & 1]).z, asv4(xb[(k2 >> 1) % 12][k2 & 1]).w); asm volatile("" : "+v"(chk2)); }
        SB();
        if constexpr (ii < 99 && c == 8) { if (RARE(bad != 0)) xfix(IC<2 * n + 2>{}, IC<2 * n + 4>{}); SB(); }
        if constexpr (ii == 100) { if (RARE(bad != 0)) xfix(IC<24>{}, IC<36>{}); SB(); }
        if constexpr (ii < 28 && ii % 2 == 0) { xload(IC<10 + ii / 2>{}); SB(); }
        if constexpr (ii >= 30 && ii < 66 && ii % 3 == 0) { xload(IC<14 + ii / 3>{}); SB(); }
        if constexpr (ii == 18 || ii == 81 || ii == 108 || ii == 159) { post(pa + P_FREE, ii == 18 ? 6 : ii == 81 ? 7 : ii == 108 ? 8 : 9); SB(); }
        if constexpr (ii == 58 || ii == 96) { fv = peek4(L_FULL); SB(); }
        if constexpr (ii == 64) { if (RARE(sv < 7)) wait_full(7); CLOB(); SB(); }
        if constexpr (ii == 97) { rv = peek(L_RDY + 4 * R_LN); SB(); }
        if constexpr (ii == 102) { if (RARE(sv < 10)) wait_full(10); CLOB(); SB(); }
        if constexpr (ii == 104) {
          if (RARE(sv == 0)) wait_word(L_RDY + 4 * R_LN, 1);
          CLOB();
          lc[0] = *(const LAS f32x4*)la; lc[1] = *(const LAS f32x4*)(la + 256);
          SB();
        }
        if constexpr (ii >= 108 && n == 14 && c >= 1) { csv = *(const LAS f32x4*)(ca + (c - 1) * 64); SB(); }
        if constexpr (ii >= 108 && n == 15 && c == 4) { fv = peek4(L_FREE); SB(); }
        if constexpr (ii >= 108 && n == 17 && c >= 1) epi(IC<c - 1>{});
        if constexpr (ii == 150) { fv = peek4(L_FULL); SB(); }
      });
      csv = *(const LAS f32x4*)(ca + 8 * 64);
      if (RARE(min3(fv) < 11)) wait_full(11);
      CLOB();
#pragma unroll
      for (int k = 0; k < PD; ++k) { lo[k] = trd(WADDR(18, k)); hi[k] = trd(WADDR(18, k) + 4608); }
      SB();
      epi(IC<8>{});
    }
    {
      f32x4 acc[2][9];
      unsigned pw = L_PS + 2 * i * 1024 + lane * 16, pw8 = L_PS + 32768 + 2 * i * 512 + lane * 8;
      asm volatile("" : "+v"(pw), "+v"(pw8));
      int po[2];
#pragma unroll
      for (int tt = 0; tt < 2; ++tt) { po[tt] = (2 * i + tt == fr) ? 0x7ff00000 : lane * 8; asm volatile("" : "+v"(po[tt])); }
      const int pdst = OFF_P + (((g * 4 + s) * 8 + 2 * i) * 8 + fr) * PUNIT;
      v4u fv;
      unsigned sv;
      auto epi = [&](auto Cc) INL {
        constexpr int c = Cc.value;
        if constexpr ((c & 1) && c < 8) {
#pragma unroll
          for (int tt = 0; tt < 2; ++tt) {
            const f32x4 a0 = acc[tt][c - 1], a1 = acc[tt][c];
            const v4u v = {pk(a0.x, a0.y), pk(a0.z, a0.w), pk(a1.x, a1.y), pk(a1.z, a1.w)};
            if constexpr (c == 7) ST16(v, 2 * po[tt] , pdst + tt * (8 * PUNIT) + 3 * 1024);
            *(LAS v4u*)(pw + tt * 1024 + (c >> 1) * 8192) = v;
          }
        }
        if constexpr (c == 8) {
#pragma unroll
          for (int tt = 0; tt < 2; ++tt) {
            const f32x4 a0 = acc[tt][8];
            const v2u v = {pk(a0.x, a0.y), pk(a0.z, a0.w)};
            ST8(v, po[tt], pdst + tt * (8 * PUNIT) + 4096);
            *(LAS v2u*)(pw8 + tt * 512) = v;
          }
        }
        if constexpr ((c & 1) && c < 7) post(pa + P_RQ, 3 + c / 2 + 1);
        SB();
      };
      constexpr int HS0 = 16, SIGS = 34, HS1 = 44, HS2 = 70, HS3 = 92;
      sfor<162>([&](auto I) INL {
        constexpr int ii = I.value, n = FN(ii), c = FC(ii), nx = ii - 1 + PD;
        constexpr int G = ii == HS0 ? 0 : ii == HS1 ? 1 : ii == HS2 ? 2 : ii == HS3 ? 3 : -1;
        const bf16x8 cur = cat(lo[ii % PD], hi[ii % PD]);
        mfa<(n == 0)>(acc[0][c], cur, hb[n][0]); SB();
        if constexpr (ii >= 1 && nx < 162) lo[nx % PD] = trd(WADDR(18 + FN(nx), FC(nx)));
        if constexpr (G >= 0) { sv = rfl(rv); asm volatile("" : "+s"(sv)); }
        if constexpr (ii == 48 || ii == 84 || ii == 102) { sv = min3(fv); asm volatile("" : "+s"(sv)); }
        if constexpr (ii == 106) { sv = min4(fv); asm volatile("" : "+s"(sv)); }
        SB();
        mfa<(n == 0)>(acc[1][c], cur, hb[n][1]); SB();
        if constexpr (ii >= 1 && nx < 162) hi[nx % PD] = trd(WADDR(18 + FN(nx), FC(nx)) + 4608);
        SB();
        if constexpr (ii == SIGS) { VMW(6); SIGNAL(FC, 3 + i); SB(); }
        if constexpr (ii + 4 == HS0 || ii + 4 == HS1 || ii + 4 == HS2 || ii + 4 == HS3) { rv = peek(L_RDY + 4 * R_HH); SB(); }
        if constexpr (G >= 0) {
          const unsigned need = G < 3 ? 1u << G : 8u << i;
          if (RARE((sv & need) != need)) wait_bit(L_RDY + 4 * R_HH, need);
          CLOB(); SB();
        }
        if constexpr (ii >= HS0 && ii < HS0 + 6) { hload(IC<8 + ii - HS0>{}); SB(); }
        if constexpr (ii >= HS1 && ii < HS1 + 6) { hload(IC<14 + ii - HS1>{}); SB(); }
        if constexpr (ii >= HS2 && ii < HS2 + 6) { hload(IC<20 + ii - HS2>{}); SB(); }
        if constexpr (ii >= HS3 && ii < HS3 + 10) { hload(IC<26 + ii - HS3>{}); SB(); }
        if constexpr (ii == 18 || ii == 54) { post(pa + P_FREE, ii == 18 ? 10 : 11); SB(); }
        if constexpr (ii == 44 || ii == 79 || ii == 97) { fv = peek4(L_FULL); SB(); }
        if constexpr (ii == 48) { if (RARE(sv < 12)) wait_full(12); CLOB(); SB(); }
        if constexpr (ii == 84) { if (RARE(sv < 13)) wait_full(13); CLOB(); SB(); }
        if constexpr (ii == 102) { if (RARE(sv < 14)) wait_full(14); fv = peek4(L_FREE); SB(); }
        if constexpr (ii == 106) { if (RARE(sv < 11)) wait_free(11); CLOB(); SB(); }
        if constexpr (ii >= 108 && n == 17 && c >= 1) epi(IC<c - 1>{});
      });
      MFMA_DONE();
      epi(IC<8>{});
      asm volatile("s_waitcnt vmcnt(0) lgkmcnt(0)" ::: "memory");
      SIGNAL(FD, 3 + i);
    }
  } else if (w < 7) {
    const int ld = w - 4;
    const unsigned fullw = L_FULL + 4 * ld;
    unsigned v3[3], v4a[9];
#pragma unroll
    for (int e = 0; e < 3; ++e) {
      const int idx = (3 * ld + e) * 64 + lane, row = idx / 18, ch = idx % 18;
      const int sc = (ch + 2 * (s & 1)) % 18;
      v3[e] = row * (F_ * 2) + sc * 16;
    }
#pragma unroll
    for (int e = 0; e < 9; ++e) { const int idx = e * 64 + lane; v4a[e] = (idx / 18) * (D_ * 2) + ((idx % 18 + 2 * (s & 1)) % 18) * 16; }
    const char* w3b = w3 + (576 * fr + 144 * s) * 2;
    const char* w4b = w4 + (size_t)(576 * fr) * (D_ * 2) + 144 * s * 2;
    auto w3slice = [&](int n) INL {
#pragma unroll
      for (int e = 0; e < 3; ++e) dma(w3b + (size_t)n * (32 * F_ * 2), v3[e], (n & 15) * SLOT + (3 * ld + e) * 1024);
    };
    auto w4whole = [&](int m, int u) INL {
      const char* src = w4b + u * (32 * D_ * 2);
#pragma unroll
      for (int e = 0; e < 9; ++e) dma(src, v4a[e], ((18 + m) & 15) * SLOT + e * 1024);
    };
    const __amdgpu_buffer_rsrc_t r4 = __builtin_amdgcn_make_buffer_rsrc((void*)w4b, 0, 576 * D_ * 2 - 144 * s * 2, 0x00020000);
    v4u w4r[4][9];
    auto w4get = [&](auto K) INL {
      const int so = dstep(6 + ld + 3 * K.value, s) * (32 * D_ * 2);
#pragma unroll
      for (int e = 0; e < 9; ++e) { constexpr int eo[9] = {0, 2, 4, 6, 8, 1, 3, 5, 7}; w4r[K.value][eo[e]] = __builtin_amdgcn_raw_buffer_load_b128(r4, v4a[eo[e]], so, 0); }
      CLOB(); SB();
    };
    auto w4put = [&](auto K) INL {
      const unsigned a = ((24 + ld + 3 * K.value) & 15) * SLOT + lane * 16;
#pragma unroll
      for (int e = 0; e < 9; ++e) *(LAS v4u*)(a + e * 1024) = w4r[K.value][e];
      CLOB(); SB();
    };
    f32x4 cs[3] = {{0, 0, 0, 0}, {0, 0, 0, 0}, {0, 0, 0, 0}};
    const unsigned cb = (4 * q + (t >> 2)) * 288 + (t & 3) * 8 + ld * 96;
    auto colsum3 = [&](int n0, int n1) INL {
      __builtin_amdgcn_s_setprio(1);
      NOUNROLL for (int n = n0; n < n1; n += 2) {
        const unsigned a0 = cb + (n & 15) * SLOT;
        s16x4 l[6], h[6];
#pragma unroll
        for (int k = 0; k < 6; ++k) { l[k] = trd(a0 + (k / 3) * SLOT + (k % 3) * 32); h[k] = trd(a0 + (k / 3) * SLOT + 4608 + (k % 3) * 32); }
        SB();
#pragma unroll
        for (int k = 0; k < 6; ++k) cs[k % 3] = mfma(cat(l[k], h[k]), ones, cs[k % 3]);
      }
      __builtin_amdgcn_s_setprio(0);
      CLOB();
    };
    if (ab) {
      const unsigned v1 = (8 * (lane >> 5) + ((lane >> 3) & 3)) * (D_ * 2) + (((lane & 7) ^ ((q & 3) << 1)) << 4);
      auto w1units = [&](int u0) INL {
#pragma unroll
        for (int e = 0; e < 8; ++e) {
          const int u = u0 + 3 * (e >> 2);
          dma(w1 + 128 * ct + (32 * u + 16 * (e & 1) + 4 * ((e >> 1) & 1)) * (D_ * 2), v1, u * 4096 + (e & 3) * 1024);
        }
      };
      w1units(ld); w1units(ld + 6);
      VMW(8); lds_set(fullw, 1, lane);
      w1units(ld + 12);
      VMW(8); lds_set(fullw, 2, lane);
      DRAIN(); lds_set(fullw, 3, lane);
      wait_free(2);
      const unsigned v2 = (lane >> 3) * (D_ * 2) + (((lane & 7) ^ ((q & 3) << 1)) << 4);
      NOUNROLL for (int u = ld; u < 18; u += 3) {
#pragma unroll
        for (int e = 0; e < 4; ++e) dma(w2 + 128 * ct + (32 * u + 8 * e) * (D_ * 2), v2, 8 * SLOT + u * 4096 + e * 1024);
      }
      DRAIN(); lds_set(fullw, 4, lane);
      wait_free(3);
      NOUNROLL for (int n = 0; n < 8; ++n) w3slice(n);
      DRAIN(); lds_set(fullw, 6, lane);
      wait_full(6);
      colsum3(0, 8);
      wait_free(4);
      NOUNROLL for (int n = 8; n < 11; ++n) w3slice(n);
      wait_free(5);
      NOUNROLL for (int n = 11; n < 16; ++n) w3slice(n);
      w4get(IC<0>{});
      VMW(9); lds_set(fullw, 7, lane);
      wait_full(7);
      colsum3(8, 16);
    } else {
      __builtin_amdgcn_s_sleep(64);
      NOUNROLL for (int n = 0; n < 16; ++n) w3slice(n);
      DRAIN(); lds_set(fullw, 7, lane);
      w4get(IC<0>{});
      wait_full(7);
      colsum3(0, 16);
    }
    lds_set(fullw, 8, lane);
    wait_free(6); wait_full(8);
    w3slice(16); w3slice(17);
    DRAIN(); lds_set(fullw, 9, lane);
    wait_full(9);
    colsum3(16, 18);
    if (t == 0) {
#pragma unroll
      for (int c = 0; c < 3; ++c) *(LAS f32x4*)(L_CS3 + (3 * ld + c) * 64 + q * 16) = cs[c];
    }
    CLOB();
    lds_set(fullw, 10, lane);
    const u64 tag = get_tag();
    auto hround = [&](int p) INL {
      wait_min4(L_RQ, p + 1);
      v4u d[8];
#pragma unroll
      for (int k = 0; k < 8; ++k) d[k] = *(const LAS v4u*)((p ? (8 + p) * SLOT : L_HS) + k * 1024 + lane * 16);
#pragma unroll
      for (int k = 0; k < 8; ++k) ST16(d[k], lane * 16 + k * (144 * 1024), OFF_HH + (8 * g * 144 + fr * 18 + pbase(s) + p) * 1024);
      CLOB();
    };
    auto pround = [&](int p) INL {
      wait_min4(L_RQ, 3 + p + 1);
      const int so = OFF_P + ((g * 4 + s) * 64 + fr) * PUNIT + p * 1024;
      v4u d[7];
#pragma unroll
      for (int k = 0; k < 7; ++k) d[k] = *(const LAS v4u*)(L_PS + p * 8192 + ((fr + 1 + k) & 7) * 1024 + lane * 16);
#pragma unroll
      for (int k = 0; k < 7; ++k) ST16(d[k], lane * 16, so + ((fr + 1 + k) & 7) * (8 * PUNIT));
      DRAIN();
      SIGNAL(FD, p);
    };
    wait_free(7);
    NOUNROLL for (int m = ld; m < 6; m += 3) w4whole(m, dstep(m, s));
    w4get(IC<1>{}); w4get(IC<2>{}); w4get(IC<3>{});
    if (ld > 0) { DRAIN(); lds_set(fullw, 11, lane); }
    hround(ld);
    DRAIN();
    SIGNAL(FC, ld);
    lds_set(fullw, 11, lane);
    w4put(IC<0>{});
    if (ld == 0) w4put(IC<1>{});
    lds_set(fullw, 12, lane);
    wait_free(9); wait_full(10);
    if (ld > 0) w4put(IC<1>{});
    w4put(IC<2>{});
    if (ld == 0) w4put(IC<3>{});
    lds_set(fullw, 13, lane);
    if (ld > 0) { wait_free(10); w4put(IC<3>{}); }
    lds_set(fullw, 14, lane);
    pround(ld);
  } else {
    __builtin_amdgcn_s_setprio(3);
    const u64 tag = __hip_atomic_load((GAS u64*)(ws + OFF_EPOCH), __ATOMIC_RELAXED, __HIP_MEMORY_SCOPE_AGENT) + 1ull;
    if (lane == 0) {
      *(volatile LAS u64*)L_TAG = tag;
      *(volatile LAS unsigned*)(L_RDY + 4 * R_TAG) = 1u;
      __hip_atomic_store(flagp(ws, FS, b, 0), tag, __ATOMIC_RELAXED, __HIP_MEMORY_SCOPE_AGENT);
    }
    {
      const v4u e4 = {EMPTY, EMPTY, EMPTY, EMPTY};
      if (ab) {
        wait_free(3);
#pragma unroll
        for (int k = 0; k < 8; ++k) ST16(e4, lane * 16 + (k & 1) * 1024 + (k >> 1) * (18 * 1024), OFF_X1 + (4 * rt * 18 + 2 * ct) * 1024);
        if (lane < 32) ST16(e4, OFF_STAT + (lane >> 3) * (9 * 128) + (lane & 7) * 16, (4 * rt * 9 + ct) * 128);
        DRAIN();
        lds_set(L_RDY + 4 * R_Z, 1, lane);
      }
    }
    auto poll = [&](GAS u64* p) INL {
      NOUNROLL for (unsigned n = 0; n < GLB_CAP; ++n) {
        const u64 v = __hip_atomic_load(p, __ATOMIC_RELAXED, __HIP_MEMORY_SCOPE_AGENT);
        if (__all(v == tag)) break;
        __builtin_amdgcn_s_sleep(1);
      }
      CLOB();
    };
    auto poll_bits = [&](auto TWO, GAS u64* p0, GAS u64* p1, int word, u64 ma, u64 mb, u64 mc, u64 md) INL {
      unsigned done = 0;
      NOUNROLL for (unsigned n = 0; n < GLB_CAP; ++n) {
        const u64 v0 = __hip_atomic_load(p0, __ATOMIC_RELAXED, __HIP_MEMORY_SCOPE_AGENT);
        u64 v1 = v0;
        if constexpr (TWO.value) v1 = __hip_atomic_load(p1, __ATOMIC_RELAXED, __HIP_MEMORY_SCOPE_AGENT);
        const u64 bad0 = __ballot(v0 != tag), bad1 = __ballot(v1 != tag);
        const unsigned now = ((bad0 & ma) ? 0u : 1u) | ((bad0 & mb) ? 0u : 2u) | ((bad1 & mc) ? 0u : 4u) | ((bad1 & md) ? 0u : 8u);
        if (now != done) { done = now; lds_set(L_RDY + 4 * word, now, lane); }
        if (now == 15u) break;
        __builtin_amdgcn_s_sleep(1);
      }
      lds_set(L_RDY + 4 * word, 15u, lane);
      CLOB();
    };
    constexpr u64 M01 = 0x3333333333333333ull, M23 = 0xccccccccccccccccull;
    const int pl = (lane % 36) >> 2;
    if (ab) poll_bits(IC<0>{}, flagp(ws, FA, j + 8 * ((r & 1) + 2 * pl), lane & 3), nullptr, R_H, M01, M23, M01, M23);
    poll_bits(IC<1>{}, flagp(ws, FB, g + 16 * pl, lane & 3), flagp(ws, FB, g + 8 + 16 * pl, lane & 3), R_X, M01, M23, M01, M23);
    {
      v4u st[9];
      NOUNROLL for (unsigned n = 0; n < TRY_CAP; ++n) { CLOB();
        unsigned m = 0;
#pragma unroll
        for (int k = 0; k < 9; ++k) st[k] = LD16(OFF_STAT + ((lane >> 3) * 9 + k) * 128 + (lane & 7) * 16, g * (8 * 9 * 128));
#pragma unroll
        for (int k = 0; k < 9; ++k) m = mx4(m, st[k]);
        if (!missing(m)) break;
      }
      f32x4 a = __builtin_bit_cast(f32x4, st[0]);
#pragma unroll
      for (int k = 1; k < 9; ++k) a += __builtin_bit_cast(f32x4, st[k]);
      *(LAS f32x4*)(L_LN + lane * 32) = ln_consts(a.x, a.y);
      *(LAS f32x4*)(L_LN + lane * 32 + 16) = ln_consts(a.z, a.w);
      CLOB();
      lds_set(L_RDY + 4 * R_LN, 1, lane);
    }
    if (b == 0) {
      NOUNROLL for (int k = 0; k < 4; ++k) poll(flagp(ws, FS, k * 64 + lane, 0));
      if (lane == 0) __hip_atomic_store((GAS u64*)(ws + OFF_EPOCH), tag, __ATOMIC_RELAXED, __HIP_MEMORY_SCOPE_AGENT);
    }
    auto poll5 = [&](GAS u64* p0, int word, auto NB) INL {
      constexpr unsigned all = (1u << NB.value) - 1u;
      unsigned done = 0;
      NOUNROLL for (unsigned n = 0; n < GLB_CAP; ++n) {
        const u64 bad = __ballot(__hip_atomic_load(p0, __ATOMIC_RELAXED, __HIP_MEMORY_SCOPE_AGENT) != tag);
        unsigned now = 0;
#pragma unroll
        for (int k = 0; k < NB.value; ++k) now |= (bad & (0x0101010101010101ull << k)) ? 0u : 1u << k;
        if (now != done) { done = now; lds_set(L_RDY + 4 * word, now, lane); }
        if (now == all) break;
        __builtin_amdgcn_s_sleep(1);
      }
      lds_set(L_RDY + 4 * word, all, lane);
    };
    poll5(flagp(ws, FC, j + 8 * ((r & ~3) + ((lane >> 3) & 3)), lane & 7), R_HH, IC<7>{});
    const int wr = lane >> 3;
    poll5(flagp(ws, FD, (j & 1) + (wr & 6) + 8 * (((wr & 1) << 4) + (r & 15)), (lane & 7) < 3 ? (lane & 7) : 3 + (fr >> 1)), R_P, IC<5>{});
  }
  if (w < 2 || (w >= 4 && w < 7)) {
    const int p = w < 2 ? w + 3 : w - 4;
    const int T = 8 * g + fr;
    const int u = p < 4 ? pbase(s) + p : (s < 2 ? 4 : 13);
    v4u xv;
    NOUNROLL for (unsigned n = 0; n < TRY_CAP; ++n) { CLOB(); xv = LD16(lane * 16, OFF_X1 + (T * 18 + u) * 1024); if (!missing(mx4(0u, xv))) break; }
    const float rstd = *(const LAS float*)(L_LN + (16 * fr + t) * 16 + 12);
    wait_bit(L_RDY + 4 * R_P, 1u << p);
    const int psrc = OFF_P + ((g * 4 + s) * 8 + fr) * (8 * PUNIT) + p * 1024;
    char* orow = out + (size_t)(16 * T + t) * (D_ * 2) + (32 * u + 4 * q) * 2;
    if (p < 4) {
      v4u pv[8];
#pragma unroll
      for (int k = 1; k < 8; ++k) pv[k] = LD16(lane * 16, psrc + ((fr + k) & 7) * PUNIT);
      pv[0] = *(const LAS v4u*)(L_PS + p * 8192 + fr * 1024 + lane * 16);
      float a[8];
#pragma unroll
      for (int e = 0; e < 8; ++e) a[e] = 0.f;
#pragma unroll
      for (int k = 0; k < 8; ++k)
#pragma unroll
        for (int e = 0; e < 4; ++e) { const unsigned d = pv[k][e]; a[2 * e] += lo16(d); a[2 * e + 1] += hi16(d); }
      unsigned o[4];
#pragma unroll
      for (int e = 0; e < 4; ++e) {
        const unsigned h2 = pk(a[2 * e] * rstd, a[2 * e + 1] * rstd), xe = xv[e];
        o[e] = pk(lo16(xe) + lo16(h2), hi16(xe) + hi16(h2));
      }
      *(GAS v2u*)orow = v2u{o[0], o[1]};
      *(GAS v2u*)(orow + 32) = v2u{o[2], o[3]};
    } else {
      v2u pv[8];
#pragma unroll
      for (int k = 1; k < 8; ++k) pv[k] = LD8(lane * 8, psrc + ((fr + k) & 7) * PUNIT);
      pv[0] = *(const LAS v2u*)(L_PS + 32768 + fr * 512 + lane * 8);
      float a[4] = {0.f, 0.f, 0.f, 0.f};
#pragma unroll
      for (int k = 0; k < 8; ++k) { a[0] += lo16(pv[k].x); a[1] += hi16(pv[k].x); a[2] += lo16(pv[k].y); a[3] += hi16(pv[k].y); }
      const unsigned x0 = (s & 1) ? xv.z : xv.x, x1v = (s & 1) ? xv.w : xv.y;
      const unsigned h0 = pk(a[0] * rstd, a[1] * rstd), h1 = pk(a[2] * rstd, a[3] * rstd);
      *(GAS v2u*)(orow + (s & 1) * 32) = v2u{pk(lo16(x0) + lo16(h0), hi16(x0) + hi16(h0)), pk(lo16(x1v) + lo16(h1), hi16(x1v) + hi16(h1))};
    }
  }
#endif
}
}

extern "C" void kernel_launch(void* const* d_in, const int* in_sizes, int n_in, void* d_out, int out_size, void* d_ws, size_t ws_size,
                              hipStream_t stream) {
  mx::mixer_kernel<<<256, 512, 0, stream>>>((const char*)d_in[0], (const char*)d_in[1], (const char*)d_in[2], (const char*)d_in[3],
                                            (const char*)d_in[4], (char*)d_out, (char*)d_ws);
}
```

```cpp
#include <hip/hip_runtime.h>
#include <utility>

#define LAS __attribute__((address_space(3)))
#define GAS __attribute__((address_space(1)))
#define DI __device__ __forceinline__
typedef unsigned v4u __attribute__((ext_vector_type(4)));
typedef unsigned v2u __attribute__((ext_vector_type(2)));
typedef float f32x4 __attribute__((ext_vector_type(4)));
typedef float f32x2 __attribute__((ext_vector_type(2)));
typedef short bf16x8 __attribute__((ext_vector_type(8)));
typedef short s16x4 __attribute__((ext_vector_type(4)));
typedef __bf16 bf16x2_t __attribute__((ext_vector_type(2)));
typedef unsigned long long u64;

namespace mx {
constexpr int D_ = 576, F_ = 4608;
constexpr int OFF_EPOCH = 0;
constexpr int OFF_FLAGS = 4096;
constexpr int OFF_STAT = 344064;
constexpr int OFF_H1 = 524288;
constexpr int OFF_X1 = OFF_H1 + 64 * 18 * 1024;
constexpr int OFF_HH = OFF_X1 + 64 * 18 * 1024;
constexpr int PUNIT = 4608;
constexpr int OFF_P = OFF_HH + 64 * 144 * 1024;
constexpr int WS_BYTES = OFF_P + 8 * 4 * 8 * 8 * PUNIT;
enum { FS = 0, FA = 1, FB = 2, FC = 3, FD = 4 };
constexpr int SLOT = 9216, MISC = 16 * SLOT;
constexpr int L_FULL = MISC, L_FREE = MISC + 16, L_RQ = MISC + 32, L_RDY = MISC + 48  , L_SY = MISC + 96, L_TAG = MISC + 120;
constexpr int L_DUM = MISC + 128  , L_CS1 = MISC + 512  , L_CS3 = MISC + 1024  , L_LN = MISC + 2048  ,
              L_HS = MISC + 4096  , L_LN1 = L_HS  , L_PS = 4 * SLOT  ;
enum { R_H = 0, R_X = 1, R_HH = 2, R_P = 3, R_TAG = 4, R_LN = 5, R_Z = 6 };
constexpr unsigned EMPTY = 0xffffffffu;
constexpr unsigned TRY_CAP = 1u << 12;
constexpr unsigned LDS_CAP = 1u << 21, GLB_CAP = 1u << 16;
constexpr int PD = 4;

template <class F, int... I> DI void sfor_impl(F&& f, std::integer_sequence<int, I...>) { (f(std::integral_constant<int, I>{}), ...); }
template <int N, class F> DI void sfor(F&& f) { sfor_impl(f, std::make_integer_sequence<int, N>{}); }
template <int V> using IC = std::integral_constant<int, V>;
#define INL __attribute__((always_inline))
#define NOUNROLL _Pragma("nounroll")
#define RARE(x) __builtin_expect(!!(x), 0)
#define SB() __builtin_amdgcn_sched_barrier(0)
#define CLOB() asm volatile("" ::: "memory")
#define DRAIN() asm volatile("s_waitcnt vmcnt(0)" ::: "memory")
#define VMW(n) asm volatile("s_waitcnt vmcnt(%0)" ::"i"(n) : "memory")

DI unsigned rfl(unsigned v) { return __builtin_amdgcn_readfirstlane(v); }
DI f32x4 mfma(bf16x8 a, bf16x8 b, f32x4 c) { return __builtin_amdgcn_mfma_f32_16x16x32_bf16(a, b, c, 0, 0, 0); }
template <bool FIRST> DI void mfa(f32x4& c, bf16x8 a, bf16x8 b) {
  if constexpr (FIRST) asm volatile("v_mfma_f32_16x16x32_bf16 %0, %1, %2, 0" : "=&v"(c) : "v"(a), "v"(b));
  else asm volatile("v_mfma_f32_16x16x32_bf16 %0, %1, %2, %0" : "+v"(c) : "v"(a), "v"(b));
}
#define MFMA_DONE() asm volatile("s_nop 7\n\ts_nop 3" ::: "memory")
DI bf16x8 asfrag(v4u v) { return __builtin_bit_cast(bf16x8, v); }
DI s16x4 trd(unsigned addr) { return __builtin_amdgcn_ds_read_tr16_b64_v4i16((LAS s16x4*)addr); }
DI bf16x8 cat(s16x4 lo, s16x4 hi) { return __builtin_shufflevector(lo, hi, 0, 1, 2, 3, 4, 5, 6, 7); }
DI unsigned pk(float a, float b) { bf16x2_t r = __builtin_convertvector(f32x2{a, b}, bf16x2_t); return __builtin_bit_cast(unsigned, r); }
DI unsigned pkc(float a, float b) { return min(pk(a, b), EMPTY - 1u); }
DI unsigned mx2(unsigned m, unsigned a, unsigned b) { return max(max(m, a), b); }
DI unsigned mx4(unsigned m, v4u d) { return mx2(mx2(m, d.x, d.y), d.z, d.w); }
DI bool missing(unsigned m) { return __any(m == EMPTY); }
DI v4u asv4(bf16x8 f) { return __builtin_bit_cast(v4u, f); }
DI float lo16(unsigned p) { return __uint_as_float(p << 16); }
DI float hi16(unsigned p) { return __uint_as_float(p & 0xffff0000u); }
DI float bfr(float a) { return lo16(pk(a, a)); }
DI float qsum(float v) {
  auto r = __builtin_amdgcn_permlane16_swap(__float_as_uint(v), __float_as_uint(v), false, false);
  float a = __uint_as_float(r[0]) + __uint_as_float(r[1]);
  auto r2 = __builtin_amdgcn_permlane32_swap(__float_as_uint(a), __float_as_uint(a), false, false);
  return __uint_as_float(r2[0]) + __uint_as_float(r2[1]);
}
constexpr float GA = -2.302208198144325f;
constexpr float GB = -0.1029432397357593f;
DI f32x4 ln_consts(float s, float ss) {
  const float m = s * (1.0f / D_);
  float var = __builtin_fmaf(-m, m, ss * (1.0f / D_));
  var = var > 0.f ? var : 0.f;
  const float sd = bfr(__builtin_amdgcn_sqrtf(bfr(bfr(var) + 1e-5f)));
  const float r = __builtin_amdgcn_rcpf(sd);
  return f32x4{-bfr(m), GA * r, GB * r * r * r, r};
}
template <int N> DI void geluN(f32x2 (&z)[N], const float (&a)[N], const float (&b)[N]) {
  f32x2 p[N];
#pragma unroll
  for (int k = 0; k < N; ++k) p[k] = z[k] * z[k];
  SB();
#pragma unroll
  for (int k = 0; k < N; ++k) p[k] = p[k] * b[k] + a[k];
  SB();
#pragma unroll
  for (int k = 0; k < N; ++k) p[k] = z[k] * p[k];
  SB();
#pragma unroll
  for (int k = 0; k < N; ++k) { p[k].x = __builtin_amdgcn_exp2f(p[k].x); p[k].y = __builtin_amdgcn_exp2f(p[k].y); }
  SB();
#pragma unroll
  for (int k = 0; k < N; ++k) p[k] = p[k] + 1.0f;
  SB();
#pragma unroll
  for (int k = 0; k < N; ++k) { p[k].x = __builtin_amdgcn_rcpf(p[k].x); p[k].y = __builtin_amdgcn_rcpf(p[k].y); }
  SB();
#pragma unroll
  for (int k = 0; k < N; ++k) z[k] = z[k] * p[k];
  SB();
}

DI unsigned ldsw(int addr) { return *(volatile LAS unsigned*)addr; }
DI v4u lds4(int addr) { return *(volatile LAS v4u*)addr; }
DI void lds_set(int addr, unsigned v, int lane) { if (lane == 0) *(volatile LAS unsigned*)addr = v; }
DI v4u peek4(int addr) { CLOB(); return *(const LAS v4u*)addr; }
DI unsigned peek(int addr) { CLOB(); return *(const LAS unsigned*)addr; }
DI void post(unsigned addr, unsigned v) { CLOB(); *(LAS unsigned*)addr = v; CLOB(); }
DI void wait_word(int addr, unsigned v) {
  NOUNROLL for (unsigned n = 0; n < LDS_CAP; ++n) { if (rfl(ldsw(addr)) >= v) break; __builtin_amdgcn_s_sleep(1); }
  CLOB();
}
DI unsigned min3(v4u x) { return rfl(min(min(x.x, x.y), x.z)); }
DI unsigned min4(v4u x) { return rfl(min(min(x.x, x.y), min(x.z, x.w))); }
DI void wait_full(unsigned v) {
  NOUNROLL for (unsigned n = 0; n < LDS_CAP; ++n) { if (min3(lds4(L_FULL)) >= v) break; __builtin_amdgcn_s_sleep(1); }
  CLOB();
}
DI void wait_min4(int addr, unsigned v) {
  NOUNROLL for (unsigned n = 0; n < LDS_CAP; ++n) { if (min4(lds4(addr)) >= v) break; __builtin_amdgcn_s_sleep(1); }
  CLOB();
}
DI void wait_free(unsigned v) { wait_min4(L_FREE, v); }
DI void wait_bit(int addr, unsigned bit) {
  NOUNROLL for (unsigned n = 0; n < LDS_CAP; ++n) { if ((rfl(ldsw(addr)) & bit) == bit) break; __builtin_amdgcn_s_sleep(1); }
  CLOB();
}
DI GAS u64* flagp(char* ws, int kind, int b, int wv) { return (GAS u64*)(ws + OFF_FLAGS + (kind * 256 + b) * 128 + wv * 8); }
DI u64 get_tag() {
  wait_word(L_RDY + 4 * R_TAG, 1);
  return ((u64)rfl(ldsw(L_TAG + 4)) << 32) | rfl(ldsw(L_TAG));
}
#define LD16(off, soff) __builtin_amdgcn_raw_buffer_load_b128(rs, (off), (soff), 16)
#define LD8(off, soff) __builtin_amdgcn_raw_buffer_load_b64(rs, (off), (soff), 16)
#define ST16(v, off, soff) do { v4u d_ = (v); asm volatile("" : "+v"(d_)); __builtin_amdgcn_raw_buffer_store_b128(d_, rs, (off), (soff), 16); asm volatile("s_nop 1" ::"v"(d_) : "memory"); } while (0)
#define ST8(v, off, soff) __builtin_amdgcn_raw_buffer_store_b64((v), rs, (off), (soff), 16)
#define SIGNAL(kind, wv) do { CLOB(); ST8((v2u{(unsigned)tag, (unsigned)(tag >> 32)}), lane ? 0x7ffffff0 : 0, OFF_FLAGS + ((kind) * 256 + b) * 128 + (wv) * 8); CLOB(); } while (0)
DI void dma(const char* sbase, unsigned voff, unsigned ldsaddr) {
  asm volatile("s_mov_b32 m0, %0\n\ts_nop 0\n\tglobal_load_lds_dwordx4 %1, %2" ::"s"(ldsaddr), "v"(voff), "s"(sbase) : "memory");
}
DI int pbase(int s) { return (9 * s + (s & 1)) >> 1; }
DI int dstep(int M, int s) {
  if (M < 4) return pbase(s) + M;
  else if (M < 16) return pbase((s + 1 + (M - 4) % 3) & 3) + (M - 4) / 3;
  else return M == 16 ? 4 : 13;
}

__global__ __launch_bounds__(512, 2) void mixer_kernel(const char* __restrict__ x, const char* __restrict__ w1, const char* __restrict__ w2,
                                                       const char* __restrict__ w3, const char* __restrict__ w4, char* __restrict__ out, char* ws) {
#if defined(__HIP_DEVICE_COMPILE__)
  __shared__ __attribute__((aligned(16))) char smem[163840];
  const int tid = threadIdx.x, lane = tid & 63, w = rfl(tid >> 6);
  const int t = lane & 15, q = lane >> 4;
  const int b = blockIdx.x, j = b & 7, r = b >> 3;
  const bool ab = r < 18;
  const int rt = 2 * j + (r & 1), ct = r >> 1;
  const int g = 4 * (j & 1) + ((r >> 2) & 3), fr = (j & 6) + (r >> 4), s = r & 3;
  const __amdgpu_buffer_rsrc_t rs = __builtin_amdgcn_make_buffer_rsrc(ws, 0, WS_BYTES, 0x00020000);
  const bf16x8 ones = {0x3F80, 0x3F80, 0x3F80, 0x3F80, 0x3F80, 0x3F80, 0x3F80, 0x3F80};
  asm volatile("" ::"v"(smem));
  if (tid < 32) *(volatile LAS unsigned*)(MISC + tid * 4) = 0u;
  asm volatile("s_waitcnt lgkmcnt(0)\n\ts_barrier" ::: "memory");
  if (ab) {
    if (w < 4) {
      const char* xsrc = x + (size_t)(4 * rt + w) * (16 * D_ * 2);
#pragma unroll
      for (int e = 0; e < 18; ++e) {
        const unsigned f = (((unsigned)(lane + 64 * e) * 911u) >> 17) & 7u;
        dma(xsrc + e * 1024, (lane * 16) ^ (f << 4), (8 + 2 * w) * SLOT + e * 1024);
      }
    }
  }

  if (w < 4) {
    const int i = w;
    unsigned pa = lane ? L_DUM + 4 * lane : L_FREE + 4 * i;
    asm volatile("" : "+v"(pa));
    const u64 tag = get_tag();
    constexpr int P_FREE = 0, P_RQ = L_RQ - L_FREE, P_SY = L_SY - L_FREE;
    if (ab) {
      unsigned ab4[4];
#pragma unroll
      for (int c = 0; c < 4; ++c) ab4[c] = (4 * q + (t >> 2)) * 128 + ((c ^ ((2 * q + (t >> 3)) & 3)) << 5) + (t & 3) * 8;
      const int T = 4 * rt + i;
      v2u xres[4];
      {
        const int tp = i >> 1, ch = i & 1;
        const unsigned xa = (8 + 2 * i) * SLOT + t * (D_ * 2), xs = ((t >> 1) & 7) << 4;
        unsigned xe[2][2], wb[2][2];
#pragma unroll
        for (int e = 0; e < 2; ++e) {
          xe[0][e] = xa + ((64 * (e ^ tp) + q * 16) ^ xs);
          xe[1][e] = xe[0][e] + (ch ? -2 * SLOT : 2 * SLOT);
          wb[e][0] = (ch ? ab4[2] : ab4[0]) + (e ^ tp) * 4096;
          wb[e][1] = (ch ? ab4[3] : ab4[1]) + (e ^ tp) * 4096;
          asm volatile("" : "+v"(xe[0][e]), "+v"(xe[1][e]), "+v"(wb[e][0]), "+v"(wb[e][1]));
        }
        int dst[4];
        unsigned keep[4], dm[4];
#pragma unroll
        for (int k = 0; k < 4; ++k) {
          const int gt = 4 * ct + 2 * ch + (k & 1), grp = gt / 9;
          const int sh = grp == 0 ? 1 : grp == 1 ? -1 : grp == 2 ? 2 : -2;
          const int ts = 16 * (ch ^ (k >> 1)) + t + sh;
          keep[k] = (ts >= 0 && ts < 32) ? 0xffffffffu : 0u;
          dst[k] = OFF_H1 + ((T & ~1) + ((ts >> 4) & 1)) * (18 * 1024) + ((ts & 15) + 16 * q) * 16 + (k & 1) * 8;
          dm[k] = (q == (t >> 2) && k == (t & 3)) ? 0xffffffffu : 0u;
          asm volatile("" : "+v"(dst[k]), "+v"(keep[k]), "+v"(dm[k]));
        }
        DRAIN();
        post(pa + P_FREE, 1);
        wait_word(L_FREE + 4 * (i ^ 1), 1);
        bf16x8 xf[2][18];
        f32x4 aS = {0, 0, 0, 0}, aG = {0, 0, 0, 0};
#pragma unroll
        for (int p = 0; p < 6; ++p) xf[0][p] = *(const LAS bf16x8*)(xe[0][p & 1] + (p >> 1) * 128);
        SB();
        sfor<18>([&](auto P) INL {
          constexpr int p = P.value;
          aS = mfma(ones, xf[0][p], aS); SB();
          if constexpr (p + 6 < 18) xf[0][p + 6] = *(const LAS bf16x8*)(xe[0][p & 1] + ((p + 6) >> 1) * 128);
          else if constexpr (p + 6 < 22) xres[p - 12] = *(const LAS v2u*)(xa + 128 * ct + ((32 * (p - 12) + 8 * q) ^ xs));
          SB();
          aG = mfma(xf[0][p], xf[0][p], aG); SB();
          xf[1][p] = *(const LAS bf16x8*)(xe[1][p & 1] + (p >> 1) * 128);
          SB();
        });
        CLOB();
        post(pa + P_FREE, 2);
        f32x4 lc[2];
        {
          const float gd = __uint_as_float(((__float_as_uint(aG.x) & dm[0]) | (__float_as_uint(aG.y) & dm[1])) | ((__float_as_uint(aG.z) & dm[2]) | (__float_as_uint(aG.w) & dm[3])));
          lc[0] = ln_consts(aS[0], qsum(gd));
          if (q == 0) *(LAS f32x4*)(L_LN1 + i * 256 + t * 16) = lc[0];
          CLOB();
          post(pa + P_SY, 1);
        }
        SB();
        wait_full(1);
        constexpr int PA = 4;
        s16x4 wl[PA], wh[PA];
#define AADDR(k) (wb[((k) >> 1) & 1][(k) & 1] + ((k) >> 2) * 8192)
#pragma unroll
        for (int k = 0; k < PA; ++k) { wl[k] = trd(AADDR(k)); wh[k] = trd(AADDR(k) + 2048); }
        bf16x8 one2 = ones;
        asm volatile("" : "+v"(one2));
        SB();
        f32x4 acc[2][2], cs[2];
        v4u fv;
        sfor<36>([&](auto K) INL {
          constexpr int k = K.value, p = k >> 1, l = k & 1, nx = k - 1 + PA;
          const bf16x8 cur = cat(wl[k % PA], wh[k % PA]);
          mfa<(p == 0)>(acc[0][l], cur, xf[0][p]); SB();
          if constexpr (nx == 12 || nx == 24) { if (RARE(min3(fv) < nx / 12 + 1)) wait_full(nx / 12 + 1); CLOB(); SB(); }
          if constexpr (k >= 1 && nx < 36) wl[nx % PA] = trd(AADDR(nx));
          SB();
          mfa<(p == 0)>(acc[1][l], cur, xf[1][p]); SB();
          if constexpr (k >= 1 && nx < 36) wh[nx % PA] = trd(AADDR(nx) + 2048);
          if constexpr (nx == 9 || nx == 21) fv = peek4(L_FULL);
          SB();
          if constexpr ((p & 1) == 0) { mfa<(p == 0)>(cs[l], cur, one2); SB(); }
        });
        MFMA_DONE();
        if (t == 0) { *(LAS f32x4*)(L_CS1 + i * 128 + q * 16) = cs[0]; *(LAS f32x4*)(L_CS1 + i * 128 + 64 + q * 16) = cs[1]; }
        CLOB();
        post(pa + P_SY, 2);
        wait_word(L_SY + 4 * (i ^ 1), 1);
        lc[1] = *(const LAS f32x4*)(L_LN1 + (i ^ 1) * 256 + t * 16);
        wait_word(L_SY + 4 * (i ^ 2), 2);
        const f32x4 o0 = *(const LAS f32x4*)(L_CS1 + (i ^ 2) * 128 + q * 16), o1 = *(const LAS f32x4*)(L_CS1 + (i ^ 2) * 128 + 64 + q * 16);
        cs[0] += o0; cs[1] += o1;
        f32x2 z[8];
        float ga8[8], gb8[8];
#pragma unroll
        for (int k = 0; k < 4; ++k) {
          const f32x4 zz = cs[k & 1] * lc[k >> 1].x + acc[k >> 1][k & 1];
          z[2 * k] = f32x2{zz.x, zz.y}; z[2 * k + 1] = f32x2{zz.z, zz.w};
          ga8[2 * k] = ga8[2 * k + 1] = lc[k >> 1].y; gb8[2 * k] = gb8[2 * k + 1] = lc[k >> 1].z;
        }
        SB();
        geluN<8>(z, ga8, gb8);
        unsigned hp[8];
#pragma unroll
        for (int k = 0; k < 8; ++k) { const f32x2 v = z[k] * lc[k >> 2].w; hp[k] = pk(v.x, v.y) & keep[k >> 1]; }
        const int up = 2 * ct + ch;
        if (RARE(up == 4 || up == 13)) {
#pragma unroll
          for (int k = 0; k < 4; ++k) ST8((v2u{hp[2 * k], hp[2 * k + 1]}), dst[k], up * 1024);
        } else {
#pragma unroll
          for (int tt = 0; tt < 2; ++tt) ST16((v4u{hp[4 * tt], hp[4 * tt + 1], hp[4 * tt + 2], hp[4 * tt + 3]}), dst[2 * tt], up * 1024);
        }
        DRAIN();
        SIGNAL(FA, i);
        post(pa + P_FREE, 3);
      }
      {
        wait_full(4);
#pragma unroll
        for (int c = 0; c < 4; ++c) { ab4[c] += 8 * SLOT; asm volatile("" : "+v"(ab4[c])); }
        constexpr int PB = 32;
        s16x4 wl[PB], wh[PB];
#pragma unroll
        for (int k = 0; k < PB; ++k) { wl[k] = trd(ab4[k & 3] + (k >> 2) * 4096); wh[k] = trd(ab4[k & 3] + (k >> 2) * 4096 + 2048); }
        SB();
        f32x2 xr[8];
#pragma unroll
        for (int l = 0; l < 4; ++l) { xr[2 * l] = f32x2{lo16(xres[l].x), hi16(xres[l].x)}; xr[2 * l + 1] = f32x2{lo16(xres[l].y), hi16(xres[l].y)}; }
#pragma unroll
        for (int k = 0; k < 8; ++k) asm volatile("" : "+v"(xr[k]));
        wait_bit(L_RDY + 4 * R_H, 1u << (i >> 1));
        bf16x8 hf[18];
        const int hsrc = OFF_H1 + T * (18 * 1024);
#pragma unroll
        for (int u = 0; u < 9; ++u) hf[u] = asfrag(LD16(lane * 16 + u * 1024, hsrc));
        SB();
        f32x4 acc[4];
        unsigned zv;
        sfor<72>([&](auto K) INL {
          constexpr int k = K.value, u = k >> 2;
          mfa<(k < 4)>(acc[k & 3], cat(wl[k % PB], wh[k % PB]), hf[u]); SB();
          if constexpr (k == 17) post(pa + P_FREE, 4);
          if constexpr (k == 60) zv = peek(L_RDY + 4 * R_Z);
          if constexpr ((k & 1) == 0 && k / 2 + 9 < 18) hf[k / 2 + 9] = asfrag(LD16(lane * 16 + (k / 2 + 9) * 1024, hsrc));
          if constexpr (k + PB < 72) { wl[k % PB] = trd(ab4[k & 3] + ((k + PB) >> 2) * 4096); wh[k % PB] = trd(ab4[k & 3] + ((k + PB) >> 2) * 4096 + 2048); }
          SB();
        });
        MFMA_DONE();
        unsigned xp[8];
#pragma unroll
        for (int l = 0; l < 4; ++l) {
          const f32x2 v0 = xr[2 * l] + f32x2{acc[l].x, acc[l].y}, v1 = xr[2 * l + 1] + f32x2{acc[l].z, acc[l].w};
          xp[2 * l] = pkc(v0.x, v0.y); xp[2 * l + 1] = pkc(v1.x, v1.y);
        }
        if (RARE(rfl(zv) == 0)) wait_word(L_RDY + 4 * R_Z, 1);
        CLOB();
        const int dst = OFF_X1 + (T * 18 + 2 * ct) * 1024;
        ST16((v4u{xp[0], xp[1], xp[2], xp[3]}), lane * 16, dst);
        ST16((v4u{xp[4], xp[5], xp[6], xp[7]}), lane * 16 + 1024, dst);
        SB();
        float sm = 0.f, sq = 0.f;
#pragma unroll
        for (int k = 0; k < 8; ++k) {
          const bf16x2_t v = __builtin_bit_cast(bf16x2_t, xp[k]);
          sm = __builtin_amdgcn_fdot2_f32_bf16(v, bf16x2_t{(__bf16)1.0f, (__bf16)1.0f}, sm, false);
          sq = __builtin_amdgcn_fdot2_f32_bf16(v, v, sq, false);
        }
        sm = qsum(sm); sq = qsum(sq);
        ST8((v2u{min(__float_as_uint(sm), EMPTY - 1u), min(__float_as_uint(sq), EMPTY - 1u)}), q ? 0x7ffffff0 : t * 8, OFF_STAT + (T * 9 + ct) * 128);
        SIGNAL(FB, i);
        post(pa + P_FREE, 5);
      }
    } else {
      const int lt = ((r - 18) * 4 + i) * 64 + lane;
      unsigned sink = 0;
      auto touch = [&](const char* base, unsigned off) INL { asm volatile("global_load_dword %0, %1, %2" : "+v"(sink) : "v"(off), "s"(base) : "memory"); };
#pragma unroll
      for (int k = 0; k < 2; ++k) touch(w1, (5183 - min(lt + 3584 * k, 5183)) * 128);
      touch(x + (size_t)j * (128 * D_ * 2), min(lt, 1151) * 128);
      __builtin_amdgcn_s_sleep(32);
#pragma unroll
      for (int k = 0; k < 2; ++k) touch(w2, min(lt + 3584 * k, 5183) * 128);
      DRAIN();
#pragma unroll
      for (int k = 0; k < 2; ++k) { const int l = min(lt + 3584 * k, 5183); touch(w3 + (j & 6) * (576 * 2), (l / 9) * (F_ * 2) + (l % 9) * 128); }
      DRAIN();
      __builtin_amdgcn_s_sleep(64);
#pragma unroll
      for (int k = 0; k < 3; ++k) touch(w4 + (size_t)(j & 6) * (576 * D_ * 2), min(lt + 3584 * k, 10367) * 128);
      DRAIN();
      asm volatile("" ::"v"(sink));
    }
    unsigned b0 = (4 * q + (t >> 2)) * 288 + (t & 3) * 8, b1 = b0 + 6 * SLOT, b2 = b0 + 12 * SLOT;
    asm volatile("" : "+v"(b0), "+v"(b1), "+v"(b2));
#define WADDR(z, c) ((((z) % 16) < 6 ? b0 : ((z) % 16) < 12 ? b1 : b2) + (((z) % 16) % 6) * SLOT + (c) * 32)
#define FN(i_) ((i_) < 108 ? (i_) / 9 : 12 + ((i_) - 108) % 6)
#define FC(i_) ((i_) < 108 ? (i_) % 9 : ((i_) - 108) / 6)
    const int T0 = 8 * g + 2 * i;
    const int hdst = OFF_HH + (T0 * 144 + fr * 18) * 1024;
    bf16x8 hb[18][2];
    auto hload = [&](auto K) INL {
      constexpr int m = K.value >> 1, tt = K.value & 1;
      hb[m][tt] = asfrag(LD16(lane * 16 + tt * (144 * 1024), hdst + dstep(m, s) * 1024));
    };
    s16x4 lo[PD], hi[PD];
    unsigned rv, hw = L_HS + 2 * i * 1024 + lane * 16, hw2 = 9 * SLOT + 2 * i * 1024 + lane * 16;
    asm volatile("" : "+v"(hw), "+v"(hw2));
    {
      wait_full(6);
#pragma unroll
      for (int k = 0; k < PD; ++k) { lo[k] = trd(WADDR(0, k)); hi[k] = trd(WADDR(0, k) + 4608); }
      f32x4 acc[2][9];
      const int xsrc = OFF_X1 + T0 * (18 * 1024);
      int xo[2] = {lane * 16, lane * 16 + 18 * 1024};
      asm volatile("" : "+v"(xo[0]), "+v"(xo[1]));
      bf16x8 xb[12][2];
      auto xload = [&](auto K) INL {
        constexpr int n = K.value >> 1, tt = K.value & 1;
        xb[n % 12][tt] = asfrag(LD16(xo[tt] + (n & 3) * 1024, xsrc + (n >> 2) * 4096));
      };
      SB();
      auto xfix = [&](auto K0, auto K1) INL {
        NOUNROLL for (unsigned n = 0; n < TRY_CAP; ++n) { CLOB();
          unsigned m = 0;
          sfor<K1.value - K0.value>([&](auto K) INL { xload(IC<K0.value + K.value>{}); });
          sfor<K1.value - K0.value>([&](auto K) INL { constexpr int kk = K0.value + K.value; m = mx4(m, asv4(xb[(kk >> 1) % 12][kk & 1])); });
          if (!missing(m)) break;
        }
      };
      wait_bit(L_RDY + 4 * R_X, 1u << i);
      sfor<10>([&](auto K) INL { xload(K); });
      SB();
      unsigned chk = mx4(mx4(0u, asv4(xb[0][0])), asv4(xb[0][1])), chk2 = 0;
      if (RARE(missing(chk))) xfix(IC<0>{}, IC<2>{});
      SB();
      f32x4 lc[2], csv;
      unsigned hp[2][2][2];
      unsigned la = L_LN + (32 * i + t) * 16, ca = L_CS3 + q * 16;
      asm volatile("" : "+v"(la), "+v"(ca));
      v4u fv;
      u64 bad;
      unsigned sv;
      auto epi = [&](auto Cc) INL {
        constexpr int c = Cc.value;
        f32x2 z[4];
#pragma unroll
        for (int tt = 0; tt < 2; ++tt) {
          const f32x4 zz = csv * lc[tt].x + acc[tt][c];
          z[2 * tt] = f32x2{zz.x, zz.y}; z[2 * tt + 1] = f32x2{zz.z, zz.w};
        }
        if constexpr (c == 3) sv = min4(fv);
        SB();
        const float ga4[4] = {lc[0].y, lc[0].y, lc[1].y, lc[1].y}, gb4[4] = {lc[0].z, lc[0].z, lc[1].z, lc[1].z};
        geluN<4>(z, ga4, gb4);
#pragma unroll
        for (int tt = 0; tt < 2; ++tt) {
          hp[tt][c & 1][0] = pk(z[2 * tt].x, z[2 * tt].y); hp[tt][c & 1][1] = pk(z[2 * tt + 1].x, z[2 * tt + 1].y);
        }
        asm volatile("" : "+v"(hp[0][c & 1][0]), "+v"(hp[0][c & 1][1]), "+v"(hp[1][c & 1][0]), "+v"(hp[1][c & 1][1]));
        if constexpr (c == 3) { if (RARE(sv < 8)) wait_free(8); CLOB(); }
        if constexpr ((c & 1) && c < 8) {
#pragma unroll
          for (int tt = 0; tt < 2; ++tt) {
            const v4u v = {hp[tt][0][0], hp[tt][0][1], hp[tt][1][0], hp[tt][1][1]};
            hb[c >> 1][tt] = asfrag(v);
            if constexpr (c == 1) *(LAS v4u*)(hw + tt * 1024) = v;
            else if constexpr (c < 7) *(LAS v4u*)(hw2 + (c >> 2) * SLOT + tt * 1024) = v;
            else ST16(v, lane * 16 + tt * (144 * 1024), hdst + (pbase(s) + 3) * 1024);
          }
        }
        if constexpr (c == 8) {
#pragma unroll
          for (int tt = 0; tt < 2; ++tt)
            ST8((v2u{hp[tt][0][0], hp[tt][0][1]}), lane * 16 + tt * (144 * 1024) + (s & 1) * 8, hdst + (s < 2 ? 4 : 13) * 1024);
        }
        if constexpr ((c & 1) && c < 7) post(pa + P_RQ, c / 2 + 1);
        SB();
      };
      sfor<162>([&](auto I) INL {
        constexpr int ii = I.value, n = FN(ii), c = FC(ii), nx = ii - 1 + PD;
        const bf16x8 cur = cat(lo[ii % PD], hi[ii % PD]);
        mfa<(n == 0)>(acc[0][c], cur, xb[n % 12][0]); SB();
        constexpr bool ck1 = ii < 99 && (c == 5 || c == 6);
        constexpr bool ck2 = ii >= 72 && ii < 99 && c >= 1 && c <= 4;
        constexpr int k2 = 24 + 4 * (n - 8) + c - 1;
        if constexpr (ii >= 1 && nx < 162) lo[nx % PD] = trd(WADDR(FN(nx), FC(nx)));
        if constexpr (ck1) { chk = mx2(c == 5 ? 0u : chk, asv4(xb[n + 1][c - 5]).x, asv4(xb[n + 1][c - 5]).y); asm volatile("" : "+v"(chk)); }
        if constexpr (ck2) { chk2 = mx2(chk2, asv4(xb[(k2 >> 1) % 12][k2 & 1]).x, asv4(xb[(k2 >> 1) % 12][k2 & 1]).y); asm volatile("" : "+v"(chk2)); }
        if constexpr (ii < 99 && c == 7) { bad = __ballot(chk == EMPTY); asm volatile("" : "+s"(bad)); }
        if constexpr (ii == 99) { bad = __ballot(chk2 == EMPTY); asm volatile("" : "+s"(bad)); }
        if constexpr (ii == 63 || ii == 101) { sv = min3(fv); asm volatile("" : "+s"(sv)); }
        if constexpr (ii == 103) { sv = rfl(rv); asm volatile("" : "+s"(sv)); }
        SB();
        mfa<(n == 0)>(acc[1][c], cur, xb[n % 12][1]); SB();
        if constexpr (ii >= 1 && nx < 162) hi[nx % PD] = trd(WADDR(FN(nx), FC(nx)) + 4608);
        if constexpr (ck1) { chk = mx2(chk, asv4(xb[n + 1][c - 5]).z, asv4(xb[n + 1][c - 5]).w); asm volatile("" : "+v"(chk)); }
        if constexpr (ck2) { chk2 = mx2(chk2, asv4(xb[(k2 >> 1) % 12][k2 & 1]).z, asv4(xb[(k2 >> 1) % 12][k2 & 1]).w); asm volatile("" : "+v"(chk2)); }
        SB();
        if constexpr (ii < 99 && c == 8) { if (RARE(bad != 0)) xfix(IC<2 * n + 2>{}, IC<2 * n + 4>{}); SB(); }
        if constexpr (ii == 100) { if (RARE(bad != 0)) xfix(IC<24>{}, IC<36>{}); SB(); }
        if constexpr (ii < 28 && ii % 2 == 0) { xload(IC<10 + ii / 2>{}); SB(); }
        if constexpr (ii >= 30 && ii < 66 && ii % 3 == 0) { xload(IC<14 + ii / 3>{}); SB(); }
        if constexpr (ii == 18 || ii == 81 || ii == 108 || ii == 159) { post(pa + P_FREE, ii == 18 ? 6 : ii == 81 ? 7 : ii == 108 ? 8 : 9); SB(); }
        if constexpr (ii == 58 || ii == 96) { fv = peek4(L_FULL); SB(); }
        if constexpr (ii == 64) { if (RARE(sv < 7)) wait_full(7); CLOB(); SB(); }
        if constexpr (ii == 97) { rv = peek(L_RDY + 4 * R_LN); SB(); }
        if constexpr (ii == 102) { if (RARE(sv < 10)) wait_full(10); CLOB(); SB(); }
        if constexpr (ii == 104) {
          if (RARE(sv == 0)) wait_word(L_RDY + 4 * R_LN, 1);
          CLOB();
          lc[0] = *(const LAS f32x4*)la; lc[1] = *(const LAS f32x4*)(la + 256);
          SB();
        }
        if constexpr (ii >= 108 && n == 14 && c >= 1) { csv = *(const LAS f32x4*)(ca + (c - 1) * 64); SB(); }
        if constexpr (ii >= 108 && n == 15 && c == 4) { fv = peek4(L_FREE); SB(); }
        if constexpr (ii >= 108 && n == 17 && c >= 1) epi(IC<c - 1>{});
        if constexpr (ii == 150) { fv = peek4(L_FULL); SB(); }
      });
      csv = *(const LAS f32x4*)(ca + 8 * 64);
      if (RARE(min3(fv) < 11)) wait_full(11);
      CLOB();
#pragma unroll
      for (int k = 0; k < PD; ++k) { lo[k] = trd(WADDR(18, k)); hi[k] = trd(WADDR(18, k) + 4608); }
      SB();
      epi(IC<8>{});
    }
    {
      f32x4 acc[2][9];
      unsigned pw = L_PS + 2 * i * 1024 + lane * 16, pw8 = L_PS + 32768 + 2 * i * 512 + lane * 8;
      asm volatile("" : "+v"(pw), "+v"(pw8));
      int po[2];
#pragma unroll
      for (int tt = 0; tt < 2; ++tt) { po[tt] = (2 * i + tt == fr) ? 0x7ff00000 : lane * 8; asm volatile("" : "+v"(po[tt])); }
      const int pdst = OFF_P + (((g * 4 + s) * 8 + 2 * i) * 8 + fr) * PUNIT;
      v4u fv;
      unsigned sv;
      auto epi = [&](auto Cc) INL {
        constexpr int c = Cc.value;
        if constexpr ((c & 1) && c < 8) {
#pragma unroll
          for (int tt = 0; tt < 2; ++tt) {
            const f32x4 a0 = acc[tt][c - 1], a1 = acc[tt][c];
            const v4u v = {pk(a0.x, a0.y), pk(a0.z, a0.w), pk(a1.x, a1.y), pk(a1.z, a1.w)};
            if constexpr (c == 7) ST16(v, 2 * po[tt] , pdst + tt * (8 * PUNIT) + 3 * 1024);
            *(LAS v4u*)(pw + tt * 1024 + (c >> 1) * 8192) = v;
          }
        }
        if constexpr (c == 8) {
#pragma unroll
          for (int tt = 0; tt < 2; ++tt) {
            const f32x4 a0 = acc[tt][8];
            const v2u v = {pk(a0.x, a0.y), pk(a0.z, a0.w)};
            ST8(v, po[tt], pdst + tt * (8 * PUNIT) + 4096);
            *(LAS v2u*)(pw8 + tt * 512) = v;
          }
        }
        if constexpr ((c & 1) && c < 7) post(pa + P_RQ, 3 + c / 2 + 1);
        SB();
      };
      constexpr int HS0 = 16, SIGS = 34, HS1 = 44, HS2 = 70, HS3 = 92;
      sfor<162>([&](auto I) INL {
        constexpr int ii = I.value, n = FN(ii), c = FC(ii), nx = ii - 1 + PD;
        constexpr int G = ii == HS0 ? 0 : ii == HS1 ? 1 : ii == HS2 ? 2 : ii == HS3 ? 3 : -1;
        const bf16x8 cur = cat(lo[ii % PD], hi[ii % PD]);
        mfa<(n == 0)>(acc[0][c], cur, hb[n][0]); SB();
        if constexpr (ii >= 1 && nx < 162) lo[nx % PD] = trd(WADDR(18 + FN(nx), FC(nx)));
        if constexpr (G >= 0) { sv = rfl(rv); asm volatile("" : "+s"(sv)); }
        if constexpr (ii == 48 || ii == 84 || ii == 102) { sv = min3(fv); asm volatile("" : "+s"(sv)); }
        if constexpr (ii == 106) { sv = min4(fv); asm volatile("" : "+s"(sv)); }
        SB();
        mfa<(n == 0)>(acc[1][c], cur, hb[n][1]); SB();
        if constexpr (ii >= 1 && nx < 162) hi[nx % PD] = trd(WADDR(18 + FN(nx), FC(nx)) + 4608);
        SB();
        if constexpr (ii == SIGS) { VMW(6); SIGNAL(FC, 3 + i); SB(); }
        if constexpr (ii + 4 == HS0 || ii + 4 == HS1 || ii + 4 == HS2 || ii + 4 == HS3) { rv = peek(L_RDY + 4 * R_HH); SB(); }
        if constexpr (G >= 0) {
          const unsigned need = G < 3 ? 1u << G : 8u << i;
          if (RARE((sv & need) != need)) wait_bit(L_RDY + 4 * R_HH, need);
          CLOB(); SB();
        }
        if constexpr (ii >= HS0 && ii < HS0 + 6) { hload(IC<8 + ii - HS0>{}); SB(); }
        if constexpr (ii >= HS1 && ii < HS1 + 6) { hload(IC<14 + ii - HS1>{}); SB(); }
        if constexpr (ii >= HS2 && ii < HS2 + 6) { hload(IC<20 + ii - HS2>{}); SB(); }
        if constexpr (ii >= HS3 && ii < HS3 + 10) { hload(IC<26 + ii - HS3>{}); SB(); }
        if constexpr (ii == 18 || ii == 54) { post(pa + P_FREE, ii == 18 ? 10 : 11); SB(); }
        if constexpr (ii == 44 || ii == 79 || ii == 97) { fv = peek4(L_FULL); SB(); }
        if constexpr (ii == 48) { if (RARE(sv < 12)) wait_full(12); CLOB(); SB(); }
        if constexpr (ii == 84) { if (RARE(sv < 13)) wait_full(13); CLOB(); SB(); }
        if constexpr (ii == 102) { if (RARE(sv < 14)) wait_full(14); fv = peek4(L_FREE); SB(); }
        if constexpr (ii == 106) { if (RARE(sv < 11)) wait_free(11); CLOB(); SB(); }
        if constexpr (ii >= 108 && n == 17 && c >= 1) epi(IC<c - 1>{});
      });
      MFMA_DONE();
      epi(IC<8>{});
      asm volatile("s_waitcnt vmcnt(0) lgkmcnt(0)" ::: "memory");
      SIGNAL(FD, 3 + i);
    }
  } else if (w < 7) {
    const int ld = w - 4;
    const unsigned fullw = L_FULL + 4 * ld;
    unsigned v3[3], v4a[9];
#pragma unroll
    for (int e = 0; e < 3; ++e) {
      const int idx = (3 * ld + e) * 64 + lane, row = idx / 18, ch = idx % 18;
      const int sc = (ch + 2 * (s & 1)) % 18;
      v3[e] = row * (F_ * 2) + sc * 16;
    }
#pragma unroll
    for (int e = 0; e < 9; ++e) { const int idx = e * 64 + lane; v4a[e] = (idx / 18) * (D_ * 2) + ((idx % 18 + 2 * (s & 1)) % 18) * 16; }
    const char* w3b = w3 + (576 * fr + 144 * s) * 2;
    const char* w4b = w4 + (size_t)(576 * fr) * (D_ * 2) + 144 * s * 2;
    auto w3slice = [&](int n) INL {
#pragma unroll
      for (int e = 0; e < 3; ++e) dma(w3b + (size_t)n * (32 * F_ * 2), v3[e], (n & 15) * SLOT + (3 * ld + e) * 1024);
    };
    auto w4whole = [&](int m, int u) INL {
      const char* src = w4b + u * (32 * D_ * 2);
#pragma unroll
      for (int e = 0; e < 9; ++e) dma(src, v4a[e], ((18 + m) & 15) * SLOT + e * 1024);
    };
    const __amdgpu_buffer_rsrc_t r4 = __builtin_amdgcn_make_buffer_rsrc((void*)w4b, 0, 576 * D_ * 2 - 144 * s * 2, 0x00020000);
    v4u w4r[4][9];
    auto w4get = [&](auto K) INL {
      const int so = dstep(6 + ld + 3 * K.value, s) * (32 * D_ * 2);
#pragma unroll
      for (int e = 0; e < 9; ++e) { constexpr int eo[9] = {0, 2, 4, 6, 8, 1, 3, 5, 7}; w4r[K.value][eo[e]] = __builtin_amdgcn_raw_buffer_load_b128(r4, v4a[eo[e]], so, 0); }
      CLOB(); SB();
    };
    auto w4put = [&](auto K) INL {
      const unsigned a = ((24 + ld + 3 * K.value) & 15) * SLOT + lane * 16;
#pragma unroll
      for (int e = 0; e < 9; ++e) *(LAS v4u*)(a + e * 1024) = w4r[K.value][e];
      CLOB(); SB();
    };
    f32x4 cs[3] = {{0, 0, 0, 0}, {0, 0, 0, 0}, {0, 0, 0, 0}};
    const unsigned cb = (4 * q + (t >> 2)) * 288 + (t & 3) * 8 + ld * 96;
    auto colsum3 = [&](int n0, int n1) INL {
      __builtin_amdgcn_s_setprio(1);
      NOUNROLL for (int n = n0; n < n1; n += 2) {
        const unsigned a0 = cb + (n & 15) * SLOT;
        s16x4 l[6], h[6];
#pragma unroll
        for (int k = 0; k < 6; ++k) { l[k] = trd(a0 + (k / 3) * SLOT + (k % 3) * 32); h[k] = trd(a0 + (k / 3) * SLOT + 4608 + (k % 3) * 32); }
        SB();
#pragma unroll
        for (int k = 0; k < 6; ++k) cs[k % 3] = mfma(cat(l[k], h[k]), ones, cs[k % 3]);
      }
      __builtin_amdgcn_s_setprio(0);
      CLOB();
    };
    if (ab) {
      const unsigned v1 = (8 * (lane >> 5) + ((lane >> 3) & 3)) * (D_ * 2) + (((lane & 7) ^ ((q & 3) << 1)) << 4);
      auto w1units = [&](int u0) INL {
#pragma unroll
        for (int e = 0; e < 8; ++e) {
          const int u = u0 + 3 * (e >> 2);
          dma(w1 + 128 * ct + (32 * u + 16 * (e & 1) + 4 * ((e >> 1) & 1)) * (D_ * 2), v1, u * 4096 + (e & 3) * 1024);
        }
      };
      w1units(ld); w1units(ld + 6);
      VMW(8); lds_set(fullw, 1, lane);
      w1units(ld + 12);
      VMW(8); lds_set(fullw, 2, lane);
      DRAIN(); lds_set(fullw, 3, lane);
      wait_free(2);
      const unsigned v2 = (lane >> 3) * (D_ * 2) + (((lane & 7) ^ ((q & 3) << 1)) << 4);
      NOUNROLL for (int u = ld; u < 18; u += 3) {
#pragma unroll
        for (int e = 0; e < 4; ++e) dma(w2 + 128 * ct + (32 * u + 8 * e) * (D_ * 2), v2, 8 * SLOT + u * 4096 + e * 1024);
      }
      DRAIN(); lds_set(fullw, 4, lane);
      wait_free(3);
      NOUNROLL for (int n = 0; n < 8; ++n) w3slice(n);
      DRAIN(); lds_set(fullw, 6, lane);
      wait_full(6);
      colsum3(0, 8);
      wait_free(4);
      NOUNROLL for (int n = 8; n < 11; ++n) w3slice(n);
      wait_free(5);
      NOUNROLL for (int n = 11; n < 16; ++n) w3slice(n);
      w4get(IC<0>{});
      VMW(9); lds_set(fullw, 7, lane);
      wait_full(7);
      colsum3(8, 16);
    } else {
      __builtin_amdgcn_s_sleep(64);
      NOUNROLL for (int n = 0; n < 16; ++n) w3slice(n);
      DRAIN(); lds_set(fullw, 7, lane);
      w4get(IC<0>{});
      wait_full(7);
      colsum3(0, 16);
    }
    lds_set(fullw, 8, lane);
    wait_free(6); wait_full(8);
    w3slice(16); w3slice(17);
    DRAIN(); lds_set(fullw, 9, lane);
    wait_full(9);
    colsum3(16, 18);
    if (t == 0) {
#pragma unroll
      for (int c = 0; c < 3; ++c) *(LAS f32x4*)(L_CS3 + (3 * ld + c) * 64 + q * 16) = cs[c];
    }
    CLOB();
    lds_set(fullw, 10, lane);
    const u64 tag = get_tag();
    auto hround = [&](int p) INL {
      wait_min4(L_RQ, p + 1);
      v4u d[8];
#pragma unroll
      for (int k = 0; k < 8; ++k) d[k] = *(const LAS v4u*)((p ? (8 + p) * SLOT : L_HS) + k * 1024 + lane * 16);
#pragma unroll
      for (int k = 0; k < 8; ++k) ST16(d[k], lane * 16 + k * (144 * 1024), OFF_HH + (8 * g * 144 + fr * 18 + pbase(s) + p) * 1024);
      CLOB();
    };
    auto pround = [&](int p) INL {
      wait_min4(L_RQ, 3 + p + 1);
      const int so = OFF_P + ((g * 4 + s) * 64 + fr) * PUNIT + p * 1024;
      v4u d[7];
#pragma unroll
      for (int k = 0; k < 7; ++k) d[k] = *(const LAS v4u*)(L_PS + p * 8192 + ((fr + 1 + k) & 7) * 1024 + lane * 16);
#pragma unroll
      for (int k = 0; k < 7; ++k) ST16(d[k], lane * 16, so + ((fr + 1 + k) & 7) * (8 * PUNIT));
      DRAIN();
      SIGNAL(FD, p);
    };
    wait_free(7);
    NOUNROLL for (int m = ld; m < 6; m += 3) w4whole(m, dstep(m, s));
    w4get(IC<1>{}); w4get(IC<2>{}); w4get(IC<3>{});
    if (ld > 0) { DRAIN(); lds_set(fullw, 11, lane); }
    hround(ld);
    DRAIN();
    SIGNAL(FC, ld);
    lds_set(fullw, 11, lane);
    w4put(IC<0>{});
    if (ld == 0) w4put(IC<1>{});
    lds_set(fullw, 12, lane);
    wait_free(9); wait_full(10);
    if (ld > 0) w4put(IC<1>{});
    w4put(IC<2>{});
    if (ld == 0) w4put(IC<3>{});
    lds_set(fullw, 13, lane);
    if (ld > 0) { wait_free(10); w4put(IC<3>{}); }
    lds_set(fullw, 14, lane);
    pround(ld);
  } else {
    __builtin_amdgcn_s_setprio(3);
    const u64 tag = __hip_atomic_load((GAS u64*)(ws + OFF_EPOCH), __ATOMIC_RELAXED, __HIP_MEMORY_SCOPE_AGENT) + 1ull;
    if (lane == 0) {
      *(volatile LAS u64*)L_TAG = tag;
      *(volatile LAS unsigned*)(L_RDY + 4 * R_TAG) = 1u;
      __hip_atomic_store(flagp(ws, FS, b, 0), tag, __ATOMIC_RELAXED, __HIP_MEMORY_SCOPE_AGENT);
    }
    {
      const v4u e4 = {EMPTY, EMPTY, EMPTY, EMPTY};
      if (ab) {
        wait_free(3);
#pragma unroll
        for (int k = 0; k < 8; ++k) ST16(e4, lane * 16 + (k & 1) * 1024 + (k >> 1) * (18 * 1024), OFF_X1 + (4 * rt * 18 + 2 * ct) * 1024);
        if (lane < 32) ST16(e4, OFF_STAT + (lane >> 3) * (9 * 128) + (lane & 7) * 16, (4 * rt * 9 + ct) * 128);
        DRAIN();
        lds_set(L_RDY + 4 * R_Z, 1, lane);
      }
    }
    auto poll = [&](GAS u64* p) INL {
      NOUNROLL for (unsigned n = 0; n < GLB_CAP; ++n) {
        const u64 v = __hip_atomic_load(p, __ATOMIC_RELAXED, __HIP_MEMORY_SCOPE_AGENT);
        if (__all(v == tag)) break;
        __builtin_amdgcn_s_sleep(1);
      }
      CLOB();
    };
    auto poll_bits = [&](auto TWO, GAS u64* p0, GAS u64* p1, int word, u64 ma, u64 mb, u64 mc, u64 md) INL {
      unsigned done = 0;
      NOUNROLL for (unsigned n = 0; n < GLB_CAP; ++n) {
        const u64 v0 = __hip_atomic_load(p0, __ATOMIC_RELAXED, __HIP_MEMORY_SCOPE_AGENT);
        u64 v1 = v0;
        if constexpr (TWO.value) v1 = __hip_atomic_load(p1, __ATOMIC_RELAXED, __HIP_MEMORY_SCOPE_AGENT);
        const u64 bad0 = __ballot(v0 != tag), bad1 = __ballot(v1 != tag);
        const unsigned now = ((bad0 & ma) ? 0u : 1u) | ((bad0 & mb) ? 0u : 2u) | ((bad1 & mc) ? 0u : 4u) | ((bad1 & md) ? 0u : 8u);
        if (now != done) { done = now; lds_set(L_RDY + 4 * word, now, lane); }
        if (now == 15u) break;
        __builtin_amdgcn_s_sleep(1);
      }
      lds_set(L_RDY + 4 * word, 15u, lane);
      CLOB();
    };
    constexpr u64 M01 = 0x3333333333333333ull, M23 = 0xccccccccccccccccull;
    const int pl = (lane % 36) >> 2;
    if (ab) poll_bits(IC<0>{}, flagp(ws, FA, j + 8 * ((r & 1) + 2 * pl), lane & 3), nullptr, R_H, M01, M23, M01, M23);
    poll_bits(IC<1>{}, flagp(ws, FB, g + 16 * pl, lane & 3), flagp(ws, FB, g + 8 + 16 * pl, lane & 3), R_X, M01, M23, M01, M23);
    {
      v4u st[9];
      NOUNROLL for (unsigned n = 0; n < TRY_CAP; ++n) { CLOB();
        unsigned m = 0;
#pragma unroll
        for (int k = 0; k < 9; ++k) st[k] = LD16(OFF_STAT + ((lane >> 3) * 9 + k) * 128 + (lane & 7) * 16, g * (8 * 9 * 128));
#pragma unroll
        for (int k = 0; k < 9; ++k) m = mx4(m, st[k]);
        if (!missing(m)) break;
      }
      f32x4 a = __builtin_bit_cast(f32x4, st[0]);
#pragma unroll
      for (int k = 1; k < 9; ++k) a += __builtin_bit_cast(f32x4, st[k]);
      *(LAS f32x4*)(L_LN + lane * 32) = ln_consts(a.x, a.y);
      *(LAS f32x4*)(L_LN + lane * 32 + 16) = ln_consts(a.z, a.w);
      CLOB();
      lds_set(L_RDY + 4 * R_LN, 1, lane);
    }
    if (b == 0) {
      NOUNROLL for (int k = 0; k < 4; ++k) poll(flagp(ws, FS, k * 64 + lane, 0));
      if (lane == 0) __hip_atomic_store((GAS u64*)(ws + OFF_EPOCH), tag, __ATOMIC_RELAXED, __HIP_MEMORY_SCOPE_AGENT);
    }
    auto poll5 = [&](GAS u64* p0, int word, auto NB) INL {
      constexpr unsigned all = (1u << NB.value) - 1u;
      unsigned done = 0;
      NOUNROLL for (unsigned n = 0; n < GLB_CAP; ++n) {
        const u64 bad = __ballot(__hip_atomic_load(p0, __ATOMIC_RELAXED, __HIP_MEMORY_SCOPE_AGENT) != tag);
        unsigned now = 0;
#pragma unroll
        for (int k = 0; k < NB.value; ++k) now |= (bad & (0x0101010101010101ull << k)) ? 0u : 1u << k;
        if (now != done) { done = now; lds_set(L_RDY + 4 * word, now, lane); }
        if (now == all) break;
        __builtin_amdgcn_s_sleep(1);
      }
      lds_set(L_RDY + 4 * word, all, lane);
    };
    poll5(flagp(ws, FC, j + 8 * ((r & ~3) + ((lane >> 3) & 3)), lane & 7), R_HH, IC<7>{});
    const int wr = lane >> 3;
    poll5(flagp(ws, FD, (j & 1) + (wr & 6) + 8 * (((wr & 1) << 4) + (r & 15)), (lane & 7) < 3 ? (lane & 7) : 3 + (fr >> 1)), R_P, IC<5>{});
  }
  if (w < 2 || (w >= 4 && w < 7)) {
    const int p = w < 2 ? w + 3 : w - 4;
    const int T = 8 * g + fr;
    const int u = p < 4 ? pbase(s) + p : (s < 2 ? 4 : 13);
    v4u xv;
    NOUNROLL for (unsigned n = 0; n < TRY_CAP; ++n) { CLOB(); xv = LD16(lane * 16, OFF_X1 + (T * 18 + u) * 1024); if (!missing(mx4(0u, xv))) break; }
    const float rstd = *(const LAS float*)(L_LN + (16 * fr + t) * 16 + 12);
    wait_bit(L_RDY + 4 * R_P, 1u << p);
    const int psrc = OFF_P + ((g * 4 + s) * 8 + fr) * (8 * PUNIT) + p * 1024;
    const __amdgpu_buffer_rsrc_t ro = __builtin_amdgcn_make_buffer_rsrc(out, 0, 1024 * D_ * 2, 0x00020000);
    const int orow = (16 * T + t) * (D_ * 2) + (32 * u + 4 * q) * 2;
    if (p < 4) {
      v4u pv[8];
#pragma unroll
      for (int k = 1; k < 8; ++k) pv[k] = LD16(lane * 16, psrc + ((fr + k) & 7) * PUNIT);
      pv[0] = *(const LAS v4u*)(L_PS + p * 8192 + fr * 1024 + lane * 16);
      float a[8];
#pragma unroll
      for (int e = 0; e < 8; ++e) a[e] = 0.f;
#pragma unroll
      for (int k = 0; k < 8; ++k)
#pragma unroll
        for (int e = 0; e < 4; ++e) { const unsigned d = pv[k][e]; a[2 * e] += lo16(d); a[2 * e + 1] += hi16(d); }
      unsigned o[4];
#pragma unroll
      for (int e = 0; e < 4; ++e) {
        const unsigned h2 = pk(a[2 * e] * rstd, a[2 * e + 1] * rstd), xe = xv[e];
        o[e] = pk(lo16(xe) + lo16(h2), hi16(xe) + hi16(h2));
      }
      __builtin_amdgcn_raw_buffer_store_b64(v2u{o[0], o[1]}, ro, orow, 0, 16);
      __builtin_amdgcn_raw_buffer_store_b64(v2u{o[2], o[3]}, ro, orow + 32, 0, 16);
    } else {
      v2u pv[8];
#pragma unroll
      for (int k = 1; k < 8; ++k) pv[k] = LD8(lane * 8, psrc + ((fr + k) & 7) * PUNIT);
      pv[0] = *(const LAS v2u*)(L_PS + 32768 + fr * 512 + lane * 8);
      float a[4] = {0.f, 0.f, 0.f, 0.f};
#pragma unroll
      for (int k = 0; k < 8; ++k) { a[0] += lo16(pv[k].x); a[1] += hi16(pv[k].x); a[2] += lo16(pv[k].y); a[3] += hi16(pv[k].y); }
      const unsigned x0 = (s & 1) ? xv.z : xv.x, x1v = (s & 1) ? xv.w : xv.y;
      const unsigned h0 = pk(a[0] * rstd, a[1] * rstd), h1 = pk(a[2] * rstd, a[3] * rstd);
      __builtin_amdgcn_raw_buffer_store_b64(v2u{pk(lo16(x0) + lo16(h0), hi16(x0) + hi16(h0)), pk(lo16(x1v) + lo16(h1), hi16(x1v) + hi16(h1))}, ro, orow + (s & 1) * 32, 0, 16);
    }
  }
#endif
}
}

extern "C" void kernel_launch(void* const* d_in, const int* in_sizes, int n_in, void* d_out, int out_size, void* d_ws, size_t ws_size,
                              hipStream_t stream) {
  mx::mixer_kernel<<<256, 512, 0, stream>>>((const char*)d_in[0], (const char*)d_in[1], (const char*)d_in[2], (const char*)d_in[3],
                                            (const char*)d_in[4], (char*)d_out, (char*)d_ws);
}
```

```cpp
#include <hip/hip_runtime.h>
#include <utility>

#define LAS __attribute__((address_space(3)))
#define GAS __attribute__((address_space(1)))
#define DI __device__ __forceinline__
typedef unsigned v4u __attribute__((ext_vector_type(4)));
typedef unsigned v2u __attribute__((ext_vector_type(2)));
typedef float f32x4 __attribute__((ext_vector_type(4)));
typedef float f32x2 __attribute__((ext_vector_type(2)));
typedef short bf16x8 __attribute__((ext_vector_type(8)));
typedef short s16x4 __attribute__((ext_vector_type(4)));
typedef __bf16 bf16x2_t __attribute__((ext_vector_type(2)));
typedef unsigned long long u64;

namespace mx {
constexpr int D_ = 576, F_ = 4608;
constexpr int OFF_EPOCH = 0;
constexpr int OFF_FLAGS = 4096;
constexpr int OFF_STAT = 344064;
constexpr int OFF_H1 = 524288;
constexpr int OFF_X1 = OFF_H1 + 64 * 18 * 1024;
constexpr int OFF_HH = OFF_X1 + 64 * 18 * 1024;
constexpr int PUNIT = 4608;
constexpr int OFF_P = OFF_HH + 64 * 144 * 1024;
constexpr int WS_BYTES = OFF_P + 8 * 4 * 8 * 8 * PUNIT;
enum { FS = 0, FA = 1, FB = 2, FC = 3, FD = 4 };
constexpr int SLOT = 9216, MISC = 16 * SLOT;
constexpr int L_FULL = MISC, L_FREE = MISC + 16, L_RQ = MISC + 32, L_RDY = MISC + 48  , L_SY = MISC + 96, L_TAG = MISC + 120;
constexpr int L_DUM = MISC + 128  , L_CS1 = MISC + 512  , L_CS3 = MISC + 1024  , L_LN = MISC + 2048  ,
              L_HS = MISC + 4096  , L_LN1 = L_HS  , L_PS = 4 * SLOT  ;
enum { R_H = 0, R_X = 1, R_HH = 2, R_P = 3, R_TAG = 4, R_LN = 5, R_Z = 6 };
constexpr unsigned EMPTY = 0xffffffffu;
constexpr unsigned TRY_CAP = 1u << 12;
constexpr unsigned LDS_CAP = 1u << 21, GLB_CAP = 1u << 16;
constexpr int PD = 4;

template <class F, int... I> DI void sfor_impl(F&& f, std::integer_sequence<int, I...>) { (f(std::integral_constant<int, I>{}), ...); }
template <int N, class F> DI void sfor(F&& f) { sfor_impl(f, std::make_integer_sequence<int, N>{}); }
template <int V> using IC = std::integral_constant<int, V>;
#define INL __attribute__((always_inline))
#define NOUNROLL _Pragma("nounroll")
#define RARE(x) __builtin_expect(!!(x), 0)
#define SB() __builtin_amdgcn_sched_barrier(0)
#define CLOB() asm volatile("" ::: "memory")
#define DRAIN() asm volatile("s_waitcnt vmcnt(0)" ::: "memory")
#define VMW(n) asm volatile("s_waitcnt vmcnt(%0)" ::"i"(n) : "memory")

DI unsigned rfl(unsigned v) { return __builtin_amdgcn_readfirstlane(v); }
DI f32x4 mfma(bf16x8 a, bf16x8 b, f32x4 c) { return __builtin_amdgcn_mfma_f32_16x16x32_bf16(a, b, c, 0, 0, 0); }
template <bool FIRST> DI void mfa(f32x4& c, bf16x8 a, bf16x8 b) {
  if constexpr (FIRST) asm volatile("v_mfma_f32_16x16x32_bf16 %0, %1, %2, 0" : "=&v"(c) : "v"(a), "v"(b));
  else asm volatile("v_mfma_f32_16x16x32_bf16 %0, %1, %2, %0" : "+v"(c) : "v"(a), "v"(b));
}
#define MFMA_DONE() asm volatile("s_nop 7\n\ts_nop 3" ::: "memory")
DI void pin(f32x4& v) { asm volatile("" : "+v"(v)); }
DI bf16x8 asfrag(v4u v) { return __builtin_bit_cast(bf16x8, v); }
DI s16x4 trd(unsigned addr) { return __builtin_amdgcn_ds_read_tr16_b64_v4i16((LAS s16x4*)addr); }
DI bf16x8 cat(s16x4 lo, s16x4 hi) { return __builtin_shufflevector(lo, hi, 0, 1, 2, 3, 4, 5, 6, 7); }
DI unsigned pk(float a, float b) { bf16x2_t r = __builtin_convertvector(f32x2{a, b}, bf16x2_t); return __builtin_bit_cast(unsigned, r); }
DI unsigned pkc(float a, float b) { return min(pk(a, b), EMPTY - 1u); }
DI unsigned mx2(unsigned m, unsigned a, unsigned b) { return max(max(m, a), b); }
DI unsigned mx4(unsigned m, v4u d) { return mx2(mx2(m, d.x, d.y), d.z, d.w); }
DI bool missing(unsigned m) { return __any(m == EMPTY); }
DI v4u asv4(bf16x8 f) { return __builtin_bit_cast(v4u, f); }
DI float lo16(unsigned p) { return __uint_as_float(p << 16); }
DI float hi16(unsigned p) { return __uint_as_float(p & 0xffff0000u); }
DI float bfr(float a) { return lo16(pk(a, a)); }
DI float qsum(float v) {
  auto r = __builtin_amdgcn_permlane16_swap(__float_as_uint(v), __float_as_uint(v), false, false);
  float a = __uint_as_float(r[0]) + __uint_as_float(r[1]);
  auto r2 = __builtin_amdgcn_permlane32_swap(__float_as_uint(a), __float_as_uint(a), false, false);
  return __uint_as_float(r2[0]) + __uint_as_float(r2[1]);
}
constexpr float GA = -2.302208198144325f;
constexpr float GB = -0.1029432397357593f;
DI f32x4 ln_consts(float s, float ss) {
  const float m = s * (1.0f / D_);
  float var = __builtin_fmaf(-m, m, ss * (1.0f / D_));
  var = var > 0.f ? var : 0.f;
  const float sd = bfr(__builtin_amdgcn_sqrtf(bfr(bfr(var) + 1e-5f)));
  const float r = __builtin_amdgcn_rcpf(sd);
  return f32x4{-bfr(m), GA * r, GB * r * r * r, r};
}
template <int N> DI void geluN(f32x2 (&z)[N], const float (&a)[N], const float (&b)[N]) {
  f32x2 p[N];
#pragma unroll
  for (int k = 0; k < N; ++k) p[k] = z[k] * z[k];
  SB();
#pragma unroll
  for (int k = 0; k < N; ++k) p[k] = p[k] * b[k] + a[k];
  SB();
#pragma unroll
  for (int k = 0; k < N; ++k) p[k] = z[k] * p[k];
  SB();
#pragma unroll
  for (int k = 0; k < N; ++k) { p[k].x = __builtin_amdgcn_exp2f(p[k].x); p[k].y = __builtin_amdgcn_exp2f(p[k].y); }
  SB();
#pragma unroll
  for (int k = 0; k < N; ++k) p[k] = p[k] + 1.0f;
  SB();
#pragma unroll
  for (int k = 0; k < N; ++k) { p[k].x = __builtin_amdgcn_rcpf(p[k].x); p[k].y = __builtin_amdgcn_rcpf(p[k].y); }
  SB();
#pragma unroll
  for (int k = 0; k < N; ++k) z[k] = z[k] * p[k];
  SB();
}

DI unsigned ldsw(int addr) { return *(volatile LAS unsigned*)addr; }
DI v4u lds4(int addr) { return *(volatile LAS v4u*)addr; }
DI void lds_set(int addr, unsigned v, int lane) { if (lane == 0) *(volatile LAS unsigned*)addr = v; }
DI v4u peek4(int addr) { CLOB(); return *(const LAS v4u*)addr; }
DI unsigned peek(int addr) { CLOB(); return *(const LAS unsigned*)addr; }
DI void post(unsigned addr, unsigned v) { CLOB(); *(LAS unsigned*)addr = v; CLOB(); }
DI void wait_word(int addr, unsigned v) {
  NOUNROLL for (unsigned n = 0; n < LDS_CAP; ++n) { if (rfl(ldsw(addr)) >= v) break; __builtin_amdgcn_s_sleep(1); }
  CLOB();
}
DI unsigned min3(v4u x) { return rfl(min(min(x.x, x.y), x.z)); }
DI unsigned min4(v4u x) { return rfl(min(min(x.x, x.y), min(x.z, x.w))); }
DI void wait_full(unsigned v) {
  NOUNROLL for (unsigned n = 0; n < LDS_CAP; ++n) { if (min3(lds4(L_FULL)) >= v) break; __builtin_amdgcn_s_sleep(1); }
  CLOB();
}
DI void wait_min4(int addr, unsigned v) {
  NOUNROLL for (unsigned n = 0; n < LDS_CAP; ++n) { if (min4(lds4(addr)) >= v) break; __builtin_amdgcn_s_sleep(1); }
  CLOB();
}
DI void wait_free(unsigned v) { wait_min4(L_FREE, v); }
DI void wait_bit(int addr, unsigned bit) {
  NOUNROLL for (unsigned n = 0; n < LDS_CAP; ++n) { if ((rfl(ldsw(addr)) & bit) == bit) break; __builtin_amdgcn_s_sleep(1); }
  CLOB();
}
DI GAS u64* flagp(char* ws, int kind, int b, int wv) { return (GAS u64*)(ws + OFF_FLAGS + (kind * 256 + b) * 128 + wv * 8); }
DI u64 get_tag() {
  wait_word(L_RDY + 4 * R_TAG, 1);
  return ((u64)rfl(ldsw(L_TAG + 4)) << 32) | rfl(ldsw(L_TAG));
}
#define LD16(off, soff) __builtin_amdgcn_raw_buffer_load_b128(rs, (off), (soff), 16)
#define LD8(off, soff) __builtin_amdgcn_raw_buffer_load_b64(rs, (off), (soff), 16)
#define ST16(v, off, soff) do { v4u d_ = (v); asm volatile("" : "+v"(d_)); __builtin_amdgcn_raw_buffer_store_b128(d_, rs, (off), (soff), 16); asm volatile("s_nop 1" ::"v"(d_) : "memory"); } while (0)
#define ST8(v, off, soff) __builtin_amdgcn_raw_buffer_store_b64((v), rs, (off), (soff), 16)
#define SIGNAL(kind, wv) do { CLOB(); ST8((v2u{(unsigned)tag, (unsigned)(tag >> 32)}), lane ? 0x7ffffff0 : 0, OFF_FLAGS + ((kind) * 256 + b) * 128 + (wv) * 8); CLOB(); } while (0)
DI void dma(const char* sbase, unsigned voff, unsigned ldsaddr) {
  asm volatile("s_mov_b32 m0, %0\n\ts_nop 0\n\tglobal_load_lds_dwordx4 %1, %2" ::"s"(ldsaddr), "v"(voff), "s"(sbase) : "memory");
}
DI int pbase(int s) { return (9 * s + (s & 1)) >> 1; }
DI int dstep(int M, int s) {
  if (M < 4) return pbase(s) + M;
  else if (M < 16) return pbase((s + 1 + (M - 4) % 3) & 3) + (M - 4) / 3;
  else return M == 16 ? 4 : 13;
}

__global__ __launch_bounds__(512, 2) void mixer_kernel(const char* __restrict__ x, const char* __restrict__ w1, const char* __restrict__ w2,
                                                       const char* __restrict__ w3, const char* __restrict__ w4, char* __restrict__ out, char* ws) {
#if defined(__HIP_DEVICE_COMPILE__)
  __shared__ __attribute__((aligned(16))) char smem[163840];
  const int tid = threadIdx.x, lane = tid & 63, w = rfl(tid >> 6);
  const int t = lane & 15, q = lane >> 4;
  const int b = blockIdx.x, j = b & 7, r = b >> 3;
  const bool ab = r < 18;
  const int rt = 2 * j + (r & 1), ct = r >> 1;
  const int g = 4 * (j & 1) + ((r >> 2) & 3), fr = (j & 6) + (r >> 4), s = r & 3;
  const __amdgpu_buffer_rsrc_t rs = __builtin_amdgcn_make_buffer_rsrc(ws, 0, WS_BYTES, 0x00020000);
  const bf16x8 ones = {0x3F80, 0x3F80, 0x3F80, 0x3F80, 0x3F80, 0x3F80, 0x3F80, 0x3F80};
  asm volatile("" ::"v"(smem));
  if (tid < 32) *(volatile LAS unsigned*)(MISC + tid * 4) = 0u;
  asm volatile("s_waitcnt lgkmcnt(0)\n\ts_barrier" ::: "memory");
  if (ab) {
    if (w < 4) {
      const char* xsrc = x + (size_t)(4 * rt + w) * (16 * D_ * 2);
#pragma unroll
      for (int e = 0; e < 18; ++e) {
        const unsigned f = (((unsigned)(lane + 64 * e) * 911u) >> 17) & 7u;
        dma(xsrc + e * 1024, (lane * 16) ^ (f << 4), (8 + 2 * w) * SLOT + e * 1024);
      }
    }
  }

  if (w < 4) {
    const int i = w;
    unsigned pa = lane ? L_DUM + 4 * lane : L_FREE + 4 * i;
    asm volatile("" : "+v"(pa));
    const u64 tag = get_tag();
    constexpr int P_FREE = 0, P_RQ = L_RQ - L_FREE, P_SY = L_SY - L_FREE;
    if (ab) {
      unsigned ab4[4];
#pragma unroll
      for (int c = 0; c < 4; ++c) ab4[c] = (4 * q + (t >> 2)) * 128 + ((c ^ ((2 * q + (t >> 3)) & 3)) << 5) + (t & 3) * 8;
      const int T = 4 * rt + i;
      v2u xres[4];
      {
        const int tp = i >> 1, ch = i & 1;
        const unsigned xa = (8 + 2 * i) * SLOT + t * (D_ * 2), xs = ((t >> 1) & 7) << 4;
        unsigned xe[2][2], wb[2][2];
#pragma unroll
        for (int e = 0; e < 2; ++e) {
          xe[0][e] = xa + ((64 * (e ^ tp) + q * 16) ^ xs);
          xe[1][e] = xe[0][e] + (ch ? -2 * SLOT : 2 * SLOT);
          wb[e][0] = (ch ? ab4[2] : ab4[0]) + (e ^ tp) * 4096;
          wb[e][1] = (ch ? ab4[3] : ab4[1]) + (e ^ tp) * 4096;
          asm volatile("" : "+v"(xe[0][e]), "+v"(xe[1][e]), "+v"(wb[e][0]), "+v"(wb[e][1]));
        }
        int dst[4];
        unsigned keep[4], dm[4];
#pragma unroll
        for (int k = 0; k < 4; ++k) {
          const int gt = 4 * ct + 2 * ch + (k & 1), grp = gt / 9;
          const int sh = grp == 0 ? 1 : grp == 1 ? -1 : grp == 2 ? 2 : -2;
          const int ts = 16 * (ch ^ (k >> 1)) + t + sh;
          keep[k] = (ts >= 0 && ts < 32) ? 0xffffffffu : 0u;
          dst[k] = OFF_H1 + ((T & ~1) + ((ts >> 4) & 1)) * (18 * 1024) + ((ts & 15) + 16 * q) * 16 + (k & 1) * 8;
          dm[k] = (q == (t >> 2) && k == (t & 3)) ? 0xffffffffu : 0u;
          asm volatile("" : "+v"(dst[k]), "+v"(keep[k]), "+v"(dm[k]));
        }
        DRAIN();
        post(pa + P_FREE, 1);
        wait_word(L_FREE + 4 * (i ^ 1), 1);
        bf16x8 xf[2][18];
        f32x4 aS = {0, 0, 0, 0}, aG = {0, 0, 0, 0};
#pragma unroll
        for (int p = 0; p < 6; ++p) xf[0][p] = *(const LAS bf16x8*)(xe[0][p & 1] + (p >> 1) * 128);
        SB();
        sfor<18>([&](auto P) INL {
          constexpr int p = P.value;
          aS = mfma(ones, xf[0][p], aS); SB();
          if constexpr (p + 6 < 18) xf[0][p + 6] = *(const LAS bf16x8*)(xe[0][p & 1] + ((p + 6) >> 1) * 128);
          else if constexpr (p + 6 < 22) xres[p - 12] = *(const LAS v2u*)(xa + 128 * ct + ((32 * (p - 12) + 8 * q) ^ xs));
          SB();
          aG = mfma(xf[0][p], xf[0][p], aG); SB();
          xf[1][p] = *(const LAS bf16x8*)(xe[1][p & 1] + (p >> 1) * 128);
          SB();
        });
        CLOB();
        post(pa + P_FREE, 2);
        f32x4 lc[2];
        {
          const float gd = __uint_as_float(((__float_as_uint(aG.x) & dm[0]) | (__float_as_uint(aG.y) & dm[1])) | ((__float_as_uint(aG.z) & dm[2]) | (__float_as_uint(aG.w) & dm[3])));
          lc[0] = ln_consts(aS[0], qsum(gd));
          if (q == 0) *(LAS f32x4*)(L_LN1 + i * 256 + t * 16) = lc[0];
          CLOB();
          post(pa + P_SY, 1);
        }
        SB();
        wait_full(1);
        constexpr int PA = 4;
        s16x4 wl[PA], wh[PA];
#define AADDR(k) (wb[((k) >> 1) & 1][(k) & 1] + ((k) >> 2) * 8192)
#pragma unroll
        for (int k = 0; k < PA; ++k) { wl[k] = trd(AADDR(k)); wh[k] = trd(AADDR(k) + 2048); }
        bf16x8 one2 = ones;
        asm volatile("" : "+v"(one2));
        SB();
        f32x4 acc[2][2], cs[2];
        v4u fv;
        sfor<36>([&](auto K) INL {
          constexpr int k = K.value, p = k >> 1, l = k & 1, nx = k - 1 + PA;
          const bf16x8 cur = cat(wl[k % PA], wh[k % PA]);
          mfa<(p == 0)>(acc[0][l], cur, xf[0][p]); SB();
          if constexpr (nx == 12 || nx == 24) { if (RARE(min3(fv) < nx / 12 + 1)) wait_full(nx / 12 + 1); CLOB(); SB(); }
          if constexpr (k >= 1 && nx < 36) wl[nx % PA] = trd(AADDR(nx));
          SB();
          mfa<(p == 0)>(acc[1][l], cur, xf[1][p]); SB();
          if constexpr (k >= 1 && nx < 36) wh[nx % PA] = trd(AADDR(nx) + 2048);
          if constexpr (nx == 9 || nx == 21) fv = peek4(L_FULL);
          SB();
          if constexpr ((p & 1) == 0) { mfa<(p == 0)>(cs[l], cur, one2); SB(); }
        });
        MFMA_DONE();
        pin(cs[0]); pin(cs[1]); pin(acc[0][0]); pin(acc[0][1]); pin(acc[1][0]); pin(acc[1][1]);
        if (t == 0) { *(LAS f32x4*)(L_CS1 + i * 128 + q * 16) = cs[0]; *(LAS f32x4*)(L_CS1 + i * 128 + 64 + q * 16) = cs[1]; }
        CLOB();
        post(pa + P_SY, 2);
        wait_word(L_SY + 4 * (i ^ 1), 1);
        lc[1] = *(const LAS f32x4*)(L_LN1 + (i ^ 1) * 256 + t * 16);
        wait_word(L_SY + 4 * (i ^ 2), 2);
        const f32x4 o0 = *(const LAS f32x4*)(L_CS1 + (i ^ 2) * 128 + q * 16), o1 = *(const LAS f32x4*)(L_CS1 + (i ^ 2) * 128 + 64 + q * 16);
        cs[0] += o0; cs[1] += o1;
        f32x2 z[8];
        float ga8[8], gb8[8];
#pragma unroll
        for (int k = 0; k < 4; ++k) {
          const f32x4 zz = cs[k & 1] * lc[k >> 1].x + acc[k >> 1][k & 1];
          z[2 * k] = f32x2{zz.x, zz.y}; z[2 * k + 1] = f32x2{zz.z, zz.w};
          ga8[2 * k] = ga8[2 * k + 1] = lc[k >> 1].y; gb8[2 * k] = gb8[2 * k + 1] = lc[k >> 1].z;
        }
        SB();
        geluN<8>(z, ga8, gb8);
        unsigned hp[8];
#pragma unroll
        for (int k = 0; k < 8; ++k) { const f32x2 v = z[k] * lc[k >> 2].w; hp[k] = pk(v.x, v.y) & keep[k >> 1]; }
        const int up = 2 * ct + ch;
        if (RARE(up == 4 || up == 13)) {
#pragma unroll
          for (int k = 0; k < 4; ++k) ST8((v2u{hp[2 * k], hp[2 * k + 1]}), dst[k], up * 1024);
        } else {
#pragma unroll
          for (int tt = 0; tt < 2; ++tt) ST16((v4u{hp[4 * tt], hp[4 * tt + 1], hp[4 * tt + 2], hp[4 * tt + 3]}), dst[2 * tt], up * 1024);
        }
        DRAIN();
        SIGNAL(FA, i);
        post(pa + P_FREE, 3);
      }
      {
        wait_full(4);
#pragma unroll
        for (int c = 0; c < 4; ++c) { ab4[c] += 8 * SLOT; asm volatile("" : "+v"(ab4[c])); }
        constexpr int PB = 32;
        s16x4 wl[PB], wh[PB];
#pragma unroll
        for (int k = 0; k < PB; ++k) { wl[k] = trd(ab4[k & 3] + (k >> 2) * 4096); wh[k] = trd(ab4[k & 3] + (k >> 2) * 4096 + 2048); }
        SB();
        f32x2 xr[8];
#pragma unroll
        for (int l = 0; l < 4; ++l) { xr[2 * l] = f32x2{lo16(xres[l].x), hi16(xres[l].x)}; xr[2 * l + 1] = f32x2{lo16(xres[l].y), hi16(xres[l].y)}; }
#pragma unroll
        for (int k = 0; k < 8; ++k) asm volatile("" : "+v"(xr[k]));
        wait_bit(L_RDY + 4 * R_H, 1u << (i >> 1));
        bf16x8 hf[18];
        const int hsrc = OFF_H1 + T * (18 * 1024);
#pragma unroll
        for (int u = 0; u < 9; ++u) hf[u] = asfrag(LD16(lane * 16 + u * 1024, hsrc));
        SB();
        f32x4 acc[4];
        unsigned zv;
        sfor<72>([&](auto K) INL {
          constexpr int k = K.value, u = k >> 2;
          mfa<(k < 4)>(acc[k & 3], cat(wl[k % PB], wh[k % PB]), hf[u]); SB();
          if constexpr (k == 17) post(pa + P_FREE, 4);
          if constexpr (k == 60) zv = peek(L_RDY + 4 * R_Z);
          if constexpr ((k & 1) == 0 && k / 2 + 9 < 18) hf[k / 2 + 9] = asfrag(LD16(lane * 16 + (k / 2 + 9) * 1024, hsrc));
          if constexpr (k + PB < 72) { wl[k % PB] = trd(ab4[k & 3] + ((k + PB) >> 2) * 4096); wh[k % PB] = trd(ab4[k & 3] + ((k + PB) >> 2) * 4096 + 2048); }
          SB();
        });
        MFMA_DONE();
#pragma unroll
        for (int l = 0; l < 4; ++l) pin(acc[l]);
        unsigned xp[8];
#pragma unroll
        for (int l = 0; l < 4; ++l) {
          const f32x2 v0 = xr[2 * l] + f32x2{acc[l].x, acc[l].y}, v1 = xr[2 * l + 1] + f32x2{acc[l].z, acc[l].w};
          xp[2 * l] = pkc(v0.x, v0.y); xp[2 * l + 1] = pkc(v1.x, v1.y);
        }
        if (RARE(rfl(zv) == 0)) wait_word(L_RDY + 4 * R_Z, 1);
        CLOB();
        const int dst = OFF_X1 + (T * 18 + 2 * ct) * 1024;
        ST16((v4u{xp[0], xp[1], xp[2], xp[3]}), lane * 16, dst);
        ST16((v4u{xp[4], xp[5], xp[6], xp[7]}), lane * 16 + 1024, dst);
        SB();
        float sm = 0.f, sq = 0.f;
#pragma unroll
        for (int k = 0; k < 8; ++k) {
          const bf16x2_t v = __builtin_bit_cast(bf16x2_t, xp[k]);
          sm = __builtin_amdgcn_fdot2_f32_bf16(v, bf16x2_t{(__bf16)1.0f, (__bf16)1.0f}, sm, false);
          sq = __builtin_amdgcn_fdot2_f32_bf16(v, v, sq, false);
        }
        sm = qsum(sm); sq = qsum(sq);
        ST8((v2u{min(__float_as_uint(sm), EMPTY - 1u), min(__float_as_uint(sq), EMPTY - 1u)}), q ? 0x7ffffff0 : t * 8, OFF_STAT + (T * 9 + ct) * 128);
        SIGNAL(FB, i);
        post(pa + P_FREE, 5);
      }
    } else {
      const int lt = ((r - 18) * 4 + i) * 64 + lane;
      unsigned sink = 0;
      auto touch = [&](const char* base, unsigned off) INL { asm volatile("global_load_dword %0, %1, %2" : "+v"(sink) : "v"(off), "s"(base) : "memory"); };
#pragma unroll
      for (int k = 0; k < 2; ++k) touch(w1, (5183 - min(lt + 3584 * k, 5183)) * 128);
      touch(x + (size_t)j * (128 * D_ * 2), min(lt, 1151) * 128);
      __builtin_amdgcn_s_sleep(32);
#pragma unroll
      for (int k = 0; k < 2; ++k) touch(w2, min(lt + 3584 * k, 5183) * 128);
      DRAIN();
#pragma unroll
      for (int k = 0; k < 2; ++k) { const int l = min(lt + 3584 * k, 5183); touch(w3 + (j & 6) * (576 * 2), (l / 9) * (F_ * 2) + (l % 9) * 128); }
      DRAIN();
      __builtin_amdgcn_s_sleep(64);
#pragma unroll
      for (int k = 0; k < 3; ++k) touch(w4 + (size_t)(j & 6) * (576 * D_ * 2), min(lt + 3584 * k, 10367) * 128);
      DRAIN();
      asm volatile("" ::"v"(sink));
    }
    unsigned b0 = (4 * q + (t >> 2)) * 288 + (t & 3) * 8, b1 = b0 + 6 * SLOT, b2 = b0 + 12 * SLOT;
    asm volatile("" : "+v"(b0), "+v"(b1), "+v"(b2));
#define WADDR(z, c) ((((z) % 16) < 6 ? b0 : ((z) % 16) < 12 ? b1 : b2) + (((z) % 16) % 6) * SLOT + (c) * 32)
#define FN(i_) ((i_) < 108 ? (i_) / 9 : 12 + ((i_) - 108) % 6)
#define FC(i_) ((i_) < 108 ? (i_) % 9 : ((i_) - 108) / 6)
    const int T0 = 8 * g + 2 * i;
    const int hdst = OFF_HH + (T0 * 144 + fr * 18) * 1024;
    bf16x8 hb[18][2];
    auto hload = [&](auto K) INL {
      constexpr int m = K.value >> 1, tt = K.value & 1;
      hb[m][tt] = asfrag(LD16(lane * 16 + tt * (144 * 1024), hdst + dstep(m, s) * 1024));
    };
    s16x4 lo[PD], hi[PD];
    unsigned rv, hw = L_HS + 2 * i * 1024 + lane * 16, hw2 = 9 * SLOT + 2 * i * 1024 + lane * 16;
    asm volatile("" : "+v"(hw), "+v"(hw2));
    {
      wait_full(6);
#pragma unroll
      for (int k = 0; k < PD; ++k) { lo[k] = trd(WADDR(0, k)); hi[k] = trd(WADDR(0, k) + 4608); }
      f32x4 acc[2][9];
      const int xsrc = OFF_X1 + T0 * (18 * 1024);
      int xo[2] = {lane * 16, lane * 16 + 18 * 1024};
      asm volatile("" : "+v"(xo[0]), "+v"(xo[1]));
      bf16x8 xb[12][2];
      auto xload = [&](auto K) INL {
        constexpr int n = K.value >> 1, tt = K.value & 1;
        xb[n % 12][tt] = asfrag(LD16(xo[tt] + (n & 3) * 1024, xsrc + (n >> 2) * 4096));
      };
      SB();
      auto xfix = [&](auto K0, auto K1) INL {
        NOUNROLL for (unsigned n = 0; n < TRY_CAP; ++n) { CLOB();
          unsigned m = 0;
          sfor<K1.value - K0.value>([&](auto K) INL { xload(IC<K0.value + K.value>{}); });
          sfor<K1.value - K0.value>([&](auto K) INL { constexpr int kk = K0.value + K.value; m = mx4(m, asv4(xb[(kk >> 1) % 12][kk & 1])); });
          if (!missing(m)) break;
        }
      };
      wait_bit(L_RDY + 4 * R_X, 1u << i);
      sfor<10>([&](auto K) INL { xload(K); });
      SB();
      unsigned chk = mx4(mx4(0u, asv4(xb[0][0])), asv4(xb[0][1])), chk2 = 0;
      if (RARE(missing(chk))) xfix(IC<0>{}, IC<2>{});
      SB();
      f32x4 lc[2], csv;
      unsigned hp[2][2][2];
      unsigned la = L_LN + (32 * i + t) * 16, ca = L_CS3 + q * 16;
      asm volatile("" : "+v"(la), "+v"(ca));
      v4u fv;
      u64 bad;
      unsigned sv;
      auto epi = [&](auto Cc) INL {
        constexpr int c = Cc.value;
        pin(acc[0][c]); pin(acc[1][c]);
        f32x2 z[4];
#pragma unroll
        for (int tt = 0; tt < 2; ++tt) {
          const f32x4 zz = csv * lc[tt].x + acc[tt][c];
          z[2 * tt] = f32x2{zz.x, zz.y}; z[2 * tt + 1] = f32x2{zz.z, zz.w};
        }
        if constexpr (c == 3) sv = min4(fv);
        SB();
        const float ga4[4] = {lc[0].y, lc[0].y, lc[1].y, lc[1].y}, gb4[4] = {lc[0].z, lc[0].z, lc[1].z, lc[1].z};
        geluN<4>(z, ga4, gb4);
#pragma unroll
        for (int tt = 0; tt < 2; ++tt) {
          hp[tt][c & 1][0] = pk(z[2 * tt].x, z[2 * tt].y); hp[tt][c & 1][1] = pk(z[2 * tt + 1].x, z[2 * tt + 1].y);
        }
        asm volatile("" : "+v"(hp[0][c & 1][0]), "+v"(hp[0][c & 1][1]), "+v"(hp[1][c & 1][0]), "+v"(hp[1][c & 1][1]));
        if constexpr (c == 3) { if (RARE(sv < 8)) wait_free(8); CLOB(); }
        if constexpr ((c & 1) && c < 8) {
#pragma unroll
          for (int tt = 0; tt < 2; ++tt) {
            const v4u v = {hp[tt][0][0], hp[tt][0][1], hp[tt][1][0], hp[tt][1][1]};
            hb[c >> 1][tt] = asfrag(v);
            if constexpr (c == 1) *(LAS v4u*)(hw + tt * 1024) = v;
            else if constexpr (c < 7) *(LAS v4u*)(hw2 + (c >> 2) * SLOT + tt * 1024) = v;
            else ST16(v, lane * 16 + tt * (144 * 1024), hdst + (pbase(s) + 3) * 1024);
          }
        }
        if constexpr (c == 8) {
#pragma unroll
          for (int tt = 0; tt < 2; ++tt)
            ST8((v2u{hp[tt][0][0], hp[tt][0][1]}), lane * 16 + tt * (144 * 1024) + (s & 1) * 8, hdst + (s < 2 ? 4 : 13) * 1024);
        }
        if constexpr ((c & 1) && c < 7) post(pa + P_RQ, c / 2 + 1);
        SB();
      };
      sfor<162>([&](auto I) INL {
        constexpr int ii = I.value, n = FN(ii), c = FC(ii), nx = ii - 1 + PD;
        const bf16x8 cur = cat(lo[ii % PD], hi[ii % PD]);
        mfa<(n == 0)>(acc[0][c], cur, xb[n % 12][0]); SB();
        constexpr bool ck1 = ii < 99 && (c == 5 || c == 6);
        constexpr bool ck2 = ii >= 72 && ii < 99 && c >= 1 && c <= 4;
        constexpr int k2 = 24 + 4 * (n - 8) + c - 1;
        if constexpr (ii >= 1 && nx < 162) lo[nx % PD] = trd(WADDR(FN(nx), FC(nx)));
        if constexpr (ck1) { chk = mx2(c == 5 ? 0u : chk, asv4(xb[n + 1][c - 5]).x, asv4(xb[n + 1][c - 5]).y); asm volatile("" : "+v"(chk)); }
        if constexpr (ck2) { chk2 = mx2(chk2, asv4(xb[(k2 >> 1) % 12][k2 & 1]).x, asv4(xb[(k2 >> 1) % 12][k2 & 1]).y); asm volatile("" : "+v"(chk2)); }
        if constexpr (ii < 99 && c == 7) { bad = __ballot(chk == EMPTY); asm volatile("" : "+s"(bad)); }
        if constexpr (ii == 99) { bad = __ballot(chk2 == EMPTY); asm volatile("" : "+s"(bad)); }
        if constexpr (ii == 63 || ii == 101) { sv = min3(fv); asm volatile("" : "+s"(sv)); }
        if constexpr (ii == 103) { sv = rfl(rv); asm volatile("" : "+s"(sv)); }
        SB();
        mfa<(n == 0)>(acc[1][c], cur, xb[n % 12][1]); SB();
        if constexpr (ii >= 1 && nx < 162) hi[nx % PD] = trd(WADDR(FN(nx), FC(nx)) + 4608);
        if constexpr (ck1) { chk = mx2(chk, asv4(xb[n + 1][c - 5]).z, asv4(xb[n + 1][c - 5]).w); asm volatile("" : "+v"(chk)); }
        if constexpr (ck2) { chk2 = mx2(chk2, asv4(xb[(k2 >> 1) % 12][k2 & 1]).z, asv4(xb[(k2 >> 1) % 12][k2 & 1]).w); asm volatile("" : "+v"(chk2)); }
        SB();
        if constexpr (ii < 99 && c == 8) { if (RARE(bad != 0)) xfix(IC<2 * n + 2>{}, IC<2 * n + 4>{}); SB(); }
        if constexpr (ii == 100) { if (RARE(bad != 0)) xfix(IC<24>{}, IC<36>{}); SB(); }
        if constexpr (ii < 28 && ii % 2 == 0) { xload(IC<10 + ii / 2>{}); SB(); }
        if constexpr (ii >= 30 && ii < 66 && ii % 3 == 0) { xload(IC<14 + ii / 3>{}); SB(); }
        if constexpr (ii == 18 || ii == 81 || ii == 108 || ii == 159) { post(pa + P_FREE, ii == 18 ? 6 : ii == 81 ? 7 : ii == 108 ? 8 : 9); SB(); }
        if constexpr (ii == 58 || ii == 96) { fv = peek4(L_FULL); SB(); }
        if constexpr (ii == 64) { if (RARE(sv < 7)) wait_full(7); CLOB(); SB(); }
        if constexpr (ii == 97) { rv = peek(L_RDY + 4 * R_LN); SB(); }
        if constexpr (ii == 102) { if (RARE(sv < 10)) wait_full(10); CLOB(); SB(); }
        if constexpr (ii == 104) {
          if (RARE(sv == 0)) wait_word(L_RDY + 4 * R_LN, 1);
          CLOB();
          lc[0] = *(const LAS f32x4*)la; lc[1] = *(const LAS f32x4*)(la + 256);
          SB();
        }
        if constexpr (ii >= 108 && n == 14 && c >= 1) { csv = *(const LAS f32x4*)(ca + (c - 1) * 64); SB(); }
        if constexpr (ii >= 108 && n == 15 && c == 4) { fv = peek4(L_FREE); SB(); }
        if constexpr (ii >= 108 && n == 17 && c >= 1) epi(IC<c - 1>{});
        if constexpr (ii == 150) { fv = peek4(L_FULL); SB(); }
      });
      csv = *(const LAS f32x4*)(ca + 8 * 64);
      if (RARE(min3(fv) < 11)) wait_full(11);
      CLOB();
#pragma unroll
      for (int k = 0; k < PD; ++k) { lo[k] = trd(WADDR(18, k)); hi[k] = trd(WADDR(18, k) + 4608); }
      SB();
      epi(IC<8>{});
    }
    {
      f32x4 acc[2][9];
      unsigned pw = L_PS + 2 * i * 1024 + lane * 16, pw8 = L_PS + 32768 + 2 * i * 512 + lane * 8;
      asm volatile("" : "+v"(pw), "+v"(pw8));
      int po[2];
#pragma unroll
      for (int tt = 0; tt < 2; ++tt) { po[tt] = (2 * i + tt == fr) ? 0x7ff00000 : lane * 8; asm volatile("" : "+v"(po[tt])); }
      const int pdst = OFF_P + (((g * 4 + s) * 8 + 2 * i) * 8 + fr) * PUNIT;
      v4u fv;
      unsigned sv;
      auto epi = [&](auto Cc) INL {
        constexpr int c = Cc.value;
        if constexpr ((c & 1) && c < 8) {
#pragma unroll
          for (int tt = 0; tt < 2; ++tt) {
            pin(acc[tt][c - 1]); pin(acc[tt][c]);
            const f32x4 a0 = acc[tt][c - 1], a1 = acc[tt][c];
            const v4u v = {pk(a0.x, a0.y), pk(a0.z, a0.w), pk(a1.x, a1.y), pk(a1.z, a1.w)};
            if constexpr (c == 7) ST16(v, 2 * po[tt] , pdst + tt * (8 * PUNIT) + 3 * 1024);
            *(LAS v4u*)(pw + tt * 1024 + (c >> 1) * 8192) = v;
          }
        }
        if constexpr (c == 8) {
#pragma unroll
          for (int tt = 0; tt < 2; ++tt) {
            const f32x4 a0 = acc[tt][8];
            const v2u v = {pk(a0.x, a0.y), pk(a0.z, a0.w)};
            ST8(v, po[tt], pdst + tt * (8 * PUNIT) + 4096);
            *(LAS v2u*)(pw8 + tt * 512) = v;
          }
        }
        if constexpr ((c & 1) && c < 7) post(pa + P_RQ, 3 + c / 2 + 1);
        SB();
      };
      constexpr int HS0 = 16, SIGS = 34, HS1 = 44, HS2 = 70, HS3 = 92;
      sfor<162>([&](auto I) INL {
        constexpr int ii = I.value, n = FN(ii), c = FC(ii), nx = ii - 1 + PD;
        constexpr int G = ii == HS0 ? 0 : ii == HS1 ? 1 : ii == HS2 ? 2 : ii == HS3 ? 3 : -1;
        const bf16x8 cur = cat(lo[ii % PD], hi[ii % PD]);
        mfa<(n == 0)>(acc[0][c], cur, hb[n][0]); SB();
        if constexpr (ii >= 1 && nx < 162) lo[nx % PD] = trd(WADDR(18 + FN(nx), FC(nx)));
        if constexpr (G >= 0) { sv = rfl(rv); asm volatile("" : "+s"(sv)); }
        if constexpr (ii == 48 || ii == 84 || ii == 102) { sv = min3(fv); asm volatile("" : "+s"(sv)); }
        if constexpr (ii == 106) { sv = min4(fv); asm volatile("" : "+s"(sv)); }
        SB();
        mfa<(n == 0)>(acc[1][c], cur, hb[n][1]); SB();
        if constexpr (ii >= 1 && nx < 162) hi[nx % PD] = trd(WADDR(18 + FN(nx), FC(nx)) + 4608);
        SB();
        if constexpr (ii == SIGS) { VMW(6); SIGNAL(FC, 3 + i); SB(); }
        if constexpr (ii + 4 == HS0 || ii + 4 == HS1 || ii + 4 == HS2 || ii + 4 == HS3) { rv = peek(L_RDY + 4 * R_HH); SB(); }
        if constexpr (G >= 0) {
          const unsigned need = G < 3 ? 1u << G : 8u << i;
          if (RARE((sv & need) != need)) wait_bit(L_RDY + 4 * R_HH, need);
          CLOB(); SB();
        }
        if constexpr (ii >= HS0 && ii < HS0 + 6) { hload(IC<8 + ii - HS0>{}); SB(); }
        if constexpr (ii >= HS1 && ii < HS1 + 6) { hload(IC<14 + ii - HS1>{}); SB(); }
        if constexpr (ii >= HS2 && ii < HS2 + 6) { hload(IC<20 + ii - HS2>{}); SB(); }
        if constexpr (ii >= HS3 && ii < HS3 + 10) { hload(IC<26 + ii - HS3>{}); SB(); }
        if constexpr (ii == 18 || ii == 54) { post(pa + P_FREE, ii == 18 ? 10 : 11); SB(); }
        if constexpr (ii == 44 || ii == 79 || ii == 97) { fv = peek4(L_FULL); SB(); }
        if constexpr (ii == 48) { if (RARE(sv < 12)) wait_full(12); CLOB(); SB(); }
        if constexpr (ii == 84) { if (RARE(sv < 13)) wait_full(13); CLOB(); SB(); }
        if constexpr (ii == 102) { if (RARE(sv < 14)) wait_full(14); fv = peek4(L_FREE); SB(); }
        if constexpr (ii == 106) { if (RARE(sv < 11)) wait_free(11); CLOB(); SB(); }
        if constexpr (ii >= 108 && n == 17 && c >= 1) epi(IC<c - 1>{});
      });
      MFMA_DONE();
      pin(acc[0][8]); pin(acc[1][8]);
      epi(IC<8>{});
      asm volatile("s_waitcnt vmcnt(0) lgkmcnt(0)" ::: "memory");
      SIGNAL(FD, 3 + i);
    }
  } else if (w < 7) {
    const int ld = w - 4;
    const unsigned fullw = L_FULL + 4 * ld;
    unsigned v3[3], v4a[9];
#pragma unroll
    for (int e = 0; e < 3; ++e) {
      const int idx = (3 * ld + e) * 64 + lane, row = idx / 18, ch = idx % 18;
      const int sc = (ch + 2 * (s & 1)) % 18;
      v3[e] = row * (F_ * 2) + sc * 16;
    }
#pragma unroll
    for (int e = 0; e < 9; ++e) { const int idx = e * 64 + lane; v4a[e] = (idx / 18) * (D_ * 2) + ((idx % 18 + 2 * (s & 1)) % 18) * 16; }
    const char* w3b = w3 + (576 * fr + 144 * s) * 2;
    const char* w4b = w4 + (size_t)(576 * fr) * (D_ * 2) + 144 * s * 2;
    auto w3slice = [&](int n) INL {
#pragma unroll
      for (int e = 0; e < 3; ++e) dma(w3b + (size_t)n * (32 * F_ * 2), v3[e], (n & 15) * SLOT + (3 * ld + e) * 1024);
    };
    auto w4whole = [&](int m, int u) INL {
      const char* src = w4b + u * (32 * D_ * 2);
#pragma unroll
      for (int e = 0; e < 9; ++e) dma(src, v4a[e], ((18 + m) & 15) * SLOT + e * 1024);
    };
    const __amdgpu_buffer_rsrc_t r4 = __builtin_amdgcn_make_buffer_rsrc((void*)w4b, 0, 576 * D_ * 2 - 144 * s * 2, 0x00020000);
    v4u w4r[4][9];
    auto w4get = [&](auto K) INL {
      const int so = dstep(6 + ld + 3 * K.value, s) * (32 * D_ * 2);
#pragma unroll
      for (int e = 0; e < 9; ++e) { constexpr int eo[9] = {0, 2, 4, 6, 8, 1, 3, 5, 7}; w4r[K.value][eo[e]] = __builtin_amdgcn_raw_buffer_load_b128(r4, v4a[eo[e]], so, 0); }
      CLOB(); SB();
    };
    auto w4put = [&](auto K) INL {
      const unsigned a = ((24 + ld + 3 * K.value) & 15) * SLOT + lane * 16;
#pragma unroll
      for (int e = 0; e < 9; ++e) *(LAS v4u*)(a + e * 1024) = w4r[K.value][e];
      CLOB(); SB();
    };
    f32x4 cs[3] = {{0, 0, 0, 0}, {0, 0, 0, 0}, {0, 0, 0, 0}};
    const unsigned cb = (4 * q + (t >> 2)) * 288 + (t & 3) * 8 + ld * 96;
    auto colsum3 = [&](int n0, int n1) INL {
      __builtin_amdgcn_s_setprio(1);
      NOUNROLL for (int n = n0; n < n1; n += 2) {
        const unsigned a0 = cb + (n & 15) * SLOT;
        s16x4 l[6], h[6];
#pragma unroll
        for (int k = 0; k < 6; ++k) { l[k] = trd(a0 + (k / 3) * SLOT + (k % 3) * 32); h[k] = trd(a0 + (k / 3) * SLOT + 4608 + (k % 3) * 32); }
        SB();
#pragma unroll
        for (int k = 0; k < 6; ++k) cs[k % 3] = mfma(cat(l[k], h[k]), ones, cs[k % 3]);
      }
      __builtin_amdgcn_s_setprio(0);
      CLOB();
    };
    if (ab) {
      const unsigned v1 = (8 * (lane >> 5) + ((lane >> 3) & 3)) * (D_ * 2) + (((lane & 7) ^ ((q & 3) << 1)) << 4);
      auto w1units = [&](int u0) INL {
#pragma unroll
        for (int e = 0; e < 8; ++e) {
          const int u = u0 + 3 * (e >> 2);
          dma(w1 + 128 * ct + (32 * u + 16 * (e & 1) + 4 * ((e >> 1) & 1)) * (D_ * 2), v1, u * 4096 + (e & 3) * 1024);
        }
      };
      w1units(ld); w1units(ld + 6);
      VMW(8); lds_set(fullw, 1, lane);
      w1units(ld + 12);
      VMW(8); lds_set(fullw, 2, lane);
      DRAIN(); lds_set(fullw, 3, lane);
      wait_free(2);
      const unsigned v2 = (lane >> 3) * (D_ * 2) + (((lane & 7) ^ ((q & 3) << 1)) << 4);
      NOUNROLL for (int u = ld; u < 18; u += 3) {
#pragma unroll
        for (int e = 0; e < 4; ++e) dma(w2 + 128 * ct + (32 * u + 8 * e) * (D_ * 2), v2, 8 * SLOT + u * 4096 + e * 1024);
      }
      DRAIN(); lds_set(fullw, 4, lane);
      wait_free(3);
      NOUNROLL for (int n = 0; n < 8; ++n) w3slice(n);
      DRAIN(); lds_set(fullw, 6, lane);
      wait_full(6);
      colsum3(0, 8);
      wait_free(4);
      NOUNROLL for (int n = 8; n < 11; ++n) w3slice(n);
      wait_free(5);
      NOUNROLL for (int n = 11; n < 16; ++n) w3slice(n);
      w4get(IC<0>{});
      VMW(9); lds_set(fullw, 7, lane);
      wait_full(7);
      colsum3(8, 16);
    } else {
      __builtin_amdgcn_s_sleep(64);
      NOUNROLL for (int n = 0; n < 16; ++n) w3slice(n);
      DRAIN(); lds_set(fullw, 7, lane);
      w4get(IC<0>{});
      wait_full(7);
      colsum3(0, 16);
    }
    lds_set(fullw, 8, lane);
    wait_free(6); wait_full(8);
    w3slice(16); w3slice(17);
    DRAIN(); lds_set(fullw, 9, lane);
    wait_full(9);
    colsum3(16, 18);
    if (t == 0) {
#pragma unroll
      for (int c = 0; c < 3; ++c) *(LAS f32x4*)(L_CS3 + (3 * ld + c) * 64 + q * 16) = cs[c];
    }
    CLOB();
    lds_set(fullw, 10, lane);
    const u64 tag = get_tag();
    auto hround = [&](int p) INL {
      wait_min4(L_RQ, p + 1);
      v4u d[8];
#pragma unroll
      for (int k = 0; k < 8; ++k) d[k] = *(const LAS v4u*)((p ? (8 + p) * SLOT : L_HS) + k * 1024 + lane * 16);
#pragma unroll
      for (int k = 0; k < 8; ++k) ST16(d[k], lane * 16 + k * (144 * 1024), OFF_HH + (8 * g * 144 + fr * 18 + pbase(s) + p) * 1024);
      CLOB();
    };
    auto pround = [&](int p) INL {
      wait_min4(L_RQ, 3 + p + 1);
      const int so = OFF_P + ((g * 4 + s) * 64 + fr) * PUNIT + p * 1024;
      v4u d[7];
#pragma unroll
      for (int k = 0; k < 7; ++k) d[k] = *(const LAS v4u*)(L_PS + p * 8192 + ((fr + 1 + k) & 7) * 1024 + lane * 16);
#pragma unroll
      for (int k = 0; k < 7; ++k) ST16(d[k], lane * 16, so + ((fr + 1 + k) & 7) * (8 * PUNIT));
      DRAIN();
      SIGNAL(FD, p);
    };
    wait_free(7);
    NOUNROLL for (int m = ld; m < 6; m += 3) w4whole(m, dstep(m, s));
    w4get(IC<1>{}); w4get(IC<2>{}); w4get(IC<3>{});
    if (ld > 0) { DRAIN(); lds_set(fullw, 11, lane); }
    hround(ld);
    DRAIN();
    SIGNAL(FC, ld);
    lds_set(fullw, 11, lane);
    w4put(IC<0>{});
    if (ld == 0) w4put(IC<1>{});
    lds_set(fullw, 12, lane);
    wait_free(9); wait_full(10);
    if (ld > 0) w4put(IC<1>{});
    w4put(IC<2>{});
    if (ld == 0) w4put(IC<3>{});
    lds_set(fullw, 13, lane);
    if (ld > 0) { wait_free(10); w4put(IC<3>{}); }
    lds_set(fullw, 14, lane);
    pround(ld);
  } else {
    __builtin_amdgcn_s_setprio(3);
    const u64 tag = __hip_atomic_load((GAS u64*)(ws + OFF_EPOCH), __ATOMIC_RELAXED, __HIP_MEMORY_SCOPE_AGENT) + 1ull;
    if (lane == 0) {
      *(volatile LAS u64*)L_TAG = tag;
      *(volatile LAS unsigned*)(L_RDY + 4 * R_TAG) = 1u;
      __hip_atomic_store(flagp(ws, FS, b, 0), tag, __ATOMIC_RELAXED, __HIP_MEMORY_SCOPE_AGENT);
    }
    {
      const v4u e4 = {EMPTY, EMPTY, EMPTY, EMPTY};
      if (ab) {
        wait_free(3);
#pragma unroll
        for (int k = 0; k < 8; ++k) ST16(e4, lane * 16 + (k & 1) * 1024 + (k >> 1) * (18 * 1024), OFF_X1 + (4 * rt * 18 + 2 * ct) * 1024);
        if (lane < 32) ST16(e4, OFF_STAT + (lane >> 3) * (9 * 128) + (lane & 7) * 16, (4 * rt * 9 + ct) * 128);
        DRAIN();
        lds_set(L_RDY + 4 * R_Z, 1, lane);
      }
    }
    auto poll = [&](GAS u64* p) INL {
      NOUNROLL for (unsigned n = 0; n < GLB_CAP; ++n) {
        const u64 v = __hip_atomic_load(p, __ATOMIC_RELAXED, __HIP_MEMORY_SCOPE_AGENT);
        if (__all(v == tag)) break;
        __builtin_amdgcn_s_sleep(1);
      }
      CLOB();
    };
    auto poll_bits = [&](auto TWO, GAS u64* p0, GAS u64* p1, int word, u64 ma, u64 mb, u64 mc, u64 md) INL {
      unsigned done = 0;
      NOUNROLL for (unsigned n = 0; n < GLB_CAP; ++n) {
        const u64 v0 = __hip_atomic_load(p0, __ATOMIC_RELAXED, __HIP_MEMORY_SCOPE_AGENT);
        u64 v1 = v0;
        if constexpr (TWO.value) v1 = __hip_atomic_load(p1, __ATOMIC_RELAXED, __HIP_MEMORY_SCOPE_AGENT);
        const u64 bad0 = __ballot(v0 != tag), bad1 = __ballot(v1 != tag);
        const unsigned now = ((bad0 & ma) ? 0u : 1u) | ((bad0 & mb) ? 0u : 2u) | ((bad1 & mc) ? 0u : 4u) | ((bad1 & md) ? 0u : 8u);
        if (now != done) { done = now; lds_set(L_RDY + 4 * word, now, lane); }
        if (now == 15u) break;
        __builtin_amdgcn_s_sleep(1);
      }
      lds_set(L_RDY + 4 * word, 15u, lane);
      CLOB();
    };
    constexpr u64 M01 = 0x3333333333333333ull, M23 = 0xccccccccccccccccull;
    const int pl = (lane % 36) >> 2;
    if (ab) poll_bits(IC<0>{}, flagp(ws, FA, j + 8 * ((r & 1) + 2 * pl), lane & 3), nullptr, R_H, M01, M23, M01, M23);
    poll_bits(IC<1>{}, flagp(ws, FB, g + 16 * pl, lane & 3), flagp(ws, FB, g + 8 + 16 * pl, lane & 3), R_X, M01, M23, M01, M23);
    {
      v4u st[9];
      NOUNROLL for (unsigned n = 0; n < TRY_CAP; ++n) { CLOB();
        unsigned m = 0;
#pragma unroll
        for (int k = 0; k < 9; ++k) st[k] = LD16(OFF_STAT + ((lane >> 3) * 9 + k) * 128 + (lane & 7) * 16, g * (8 * 9 * 128));
#pragma unroll
        for (int k = 0; k < 9; ++k) m = mx4(m, st[k]);
        if (!missing(m)) break;
      }
      f32x4 a = __builtin_bit_cast(f32x4, st[0]);
#pragma unroll
      for (int k = 1; k < 9; ++k) a += __builtin_bit_cast(f32x4, st[k]);
      *(LAS f32x4*)(L_LN + lane * 32) = ln_consts(a.x, a.y);
      *(LAS f32x4*)(L_LN + lane * 32 + 16) = ln_consts(a.z, a.w);
      CLOB();
      lds_set(L_RDY + 4 * R_LN, 1, lane);
    }
    if (b == 0) {
      NOUNROLL for (int k = 0; k < 4; ++k) poll(flagp(ws, FS, k * 64 + lane, 0));
      if (lane == 0) __hip_atomic_store((GAS u64*)(ws + OFF_EPOCH), tag, __ATOMIC_RELAXED, __HIP_MEMORY_SCOPE_AGENT);
    }
    auto poll5 = [&](GAS u64* p0, int word, auto NB) INL {
      constexpr unsigned all = (1u << NB.value) - 1u;
      unsigned done = 0;
      NOUNROLL for (unsigned n = 0; n < GLB_CAP; ++n) {
        const u64 bad = __ballot(__hip_atomic_load(p0, __ATOMIC_RELAXED, __HIP_MEMORY_SCOPE_AGENT) != tag);
        unsigned now = 0;
#pragma unroll
        for (int k = 0; k < NB.value; ++k) now |= (bad & (0x0101010101010101ull << k)) ? 0u : 1u << k;
        if (now != done) { done = now; lds_set(L_RDY + 4 * word, now, lane); }
        if (now == all) break;
        __builtin_amdgcn_s_sleep(1);
      }
      lds_set(L_RDY + 4 * word, all, lane);
    };
    poll5(flagp(ws, FC, j + 8 * ((r & ~3) + ((lane >> 3) & 3)), lane & 7), R_HH, IC<7>{});
    const int wr = lane >> 3;
    poll5(flagp(ws, FD, (j & 1) + (wr & 6) + 8 * (((wr & 1) << 4) + (r & 15)), (lane & 7) < 3 ? (lane & 7) : 3 + (fr >> 1)), R_P, IC<5>{});
  }
  if (w < 2 || (w >= 4 && w < 7)) {
    const int p = w < 2 ? w + 3 : w - 4;
    const int T = 8 * g + fr;
    const int u = p < 4 ? pbase(s) + p : (s < 2 ? 4 : 13);
    v4u xv;
    NOUNROLL for (unsigned n = 0; n < TRY_CAP; ++n) { CLOB(); xv = LD16(lane * 16, OFF_X1 + (T * 18 + u) * 1024); if (!missing(mx4(0u, xv))) break; }
    const float rstd = *(const LAS float*)(L_LN + (16 * fr + t) * 16 + 12);
    wait_bit(L_RDY + 4 * R_P, 1u << p);
    const int psrc = OFF_P + ((g * 4 + s) * 8 + fr) * (8 * PUNIT) + p * 1024;
    char* orow = out + (size_t)(16 * T + t) * (D_ * 2) + (32 * u + 4 * q) * 2;
    if (p < 4) {
      v4u pv[8];
#pragma unroll
      for (int k = 1; k < 8; ++k) pv[k] = LD16(lane * 16, psrc + ((fr + k) & 7) * PUNIT);
      pv[0] = *(const LAS v4u*)(L_PS + p * 8192 + fr * 1024 + lane * 16);
      float a[8];
#pragma unroll
      for (int e = 0; e < 8; ++e) a[e] = 0.f;
#pragma unroll
      for (int k = 0; k < 8; ++k)
#pragma unroll
        for (int e = 0; e < 4; ++e) { const unsigned d = pv[k][e]; a[2 * e] += lo16(d); a[2 * e + 1] += hi16(d); }
      unsigned o[4];
#pragma unroll
      for (int e = 0; e < 4; ++e) {
        const unsigned h2 = pk(a[2 * e] * rstd, a[2 * e + 1] * rstd), xe = xv[e];
        o[e] = pk(lo16(xe) + lo16(h2), hi16(xe) + hi16(h2));
      }
      *(GAS v2u*)orow = v2u{o[0], o[1]};
      *(GAS v2u*)(orow + 32) = v2u{o[2], o[3]};
    } else {
      v2u pv[8];
#pragma unroll
      for (int k = 1; k < 8; ++k) pv[k] = LD8(lane * 8, psrc + ((fr + k) & 7) * PUNIT);
      pv[0] = *(const LAS v2u*)(L_PS + 32768 + fr * 512 + lane * 8);
      float a[4] = {0.f, 0.f, 0.f, 0.f};
#pragma unroll
      for (int k = 0; k < 8; ++k) { a[0] += lo16(pv[k].x); a[1] += hi16(pv[k].x); a[2] += lo16(pv[k].y); a[3] += hi16(pv[k].y); }
      const unsigned x0 = (s & 1) ? xv.z : xv.x, x1v = (s & 1) ? xv.w : xv.y;
      const unsigned h0 = pk(a[0] * rstd, a[1] * rstd), h1 = pk(a[2] * rstd, a[3] * rstd);
      *(GAS v2u*)(orow + (s & 1) * 32) = v2u{pk(lo16(x0) + lo16(h0), hi16(x0) + hi16(h0)), pk(lo16(x1v) + lo16(h1), hi16(x1v) + hi16(h1))};
    }
  }
#endif
}
}

extern "C" void kernel_launch(void* const* d_in, const int* in_sizes, int n_in, void* d_out, int out_size, void* d_ws, size_t ws_size,
                              hipStream_t stream) {
  mx::mixer_kernel<<<256, 512, 0, stream>>>((const char*)d_in[0], (const char*)d_in[1], (const char*)d_in[2], (const char*)d_in[3],
                                            (const char*)d_in[4], (char*)d_out, (char*)d_ws);
}
```
